```python
import torch

def _softmax(x, axis=-1):
    return torch.softmax(x, dim=axis)
def _gelu(x):
    return torch.nn.functional.gelu(x, approximate="tanh")
def _silu(x):
    return torch.nn.functional.silu(x)
def _sigmoid(x):
    return torch.sigmoid(x)
def _layernorm(x, axis=-1, eps=1e-5):
    m = x.mean(dim=axis, keepdim=True)
    v = x.var(dim=axis, keepdim=True, unbiased=False)
    return (x - m) / torch.sqrt(v + eps)
def _scatter_add(tgt, idx, src, axis=0):
    return tgt.clone().index_add_(axis, idx, src)
def _segment_sum(src, seg, n):
    out = torch.zeros((n,) + src.shape[1:], dtype=src.dtype)
    return out.index_add_(0, seg, src)
def _pad(x, pw):
    flat = tuple(v for pair in reversed(pw) for v in pair)
    return torch.nn.functional.pad(x, flat)

kernel_name = 'gated_mix__ssm_conv_s4__moe_arch_mod__prompt'

def setup_inputs():

    _w = (lambda a: (lambda t: t.to(torch.bfloat16) if t.is_floating_point() else t)(torch.as_tensor(a)))
    def _A_setup():
        B, L, D, N = 1, 2048, 576, 128
        A_re = -(lambda _x: torch.exp(torch.as_tensor(_x)))((lambda *s: torch.randn(*s, dtype=torch.bfloat16) * 3.2)(D, N))
        A_im = torch.pi * (lambda *s: torch.randn(*s, dtype=torch.bfloat16) * 0.05)(D, N)
        B_re = (lambda *s: torch.randn(*s, dtype=torch.bfloat16) * 0.2)(D, N)
        B_im = (lambda *s: torch.randn(*s, dtype=torch.bfloat16) * 0.2)(D, N)
        C_re = (lambda *s: torch.randn(*s, dtype=torch.bfloat16) * 0.2)(D, N)
        C_im = (lambda *s: torch.randn(*s, dtype=torch.bfloat16) * 0.2)(D, N)
        log_dt = (lambda *s: torch.randn(*s, dtype=torch.bfloat16) * 0.05)(D) * 0.5 - 4.0
        Dskip = (lambda *s: torch.randn(*s, dtype=torch.bfloat16) * 0.1)(D)
        return {
            "u": _w((lambda *s: torch.randn(*s, dtype=torch.bfloat16) * 0.05)(B, L, D)),
            "A_re": _w(A_re),
            "A_im": _w(A_im),
            "B_re": _w(B_re),
            "B_im": _w(B_im),
            "C_re": _w(C_re),
            "C_im": _w(C_im),
            "log_dt": _w(log_dt),
            "Dskip": _w(Dskip),
        }
    _A_in = _A_setup()

    def _B_setup():
        B, S, D, F, C = 1, 2048, 576, 4608, 8
        return {
            "x": _w((lambda *s: torch.randn(*s, dtype=torch.bfloat16) * 0.05)(B, S, D)),
            "w_r": _w((lambda *s: torch.randn(*s, dtype=torch.bfloat16) * 3.2)(D)),
            "w_gate": _w((lambda *s: torch.randn(*s, dtype=torch.bfloat16) * 0.1)(D, F)),
            "w_up": _w((lambda *s: torch.randn(*s, dtype=torch.bfloat16) * 0.1)(D, F)),
            "w_down": _w((lambda *s: torch.randn(*s, dtype=torch.bfloat16) * 0.1)(F, D)),
        }
    _B_in = _B_setup()

    return {
        "A_u": _A_in["u"],
        "A_A_re": _A_in["A_re"],
        "A_A_im": _A_in["A_im"],
        "A_B_re": _A_in["B_re"],
        "A_B_im": _A_in["B_im"],
        "A_C_re": _A_in["C_re"],
        "A_C_im": _A_in["C_im"],
        "A_log_dt": _A_in["log_dt"],
        "A_Dskip": _A_in["Dskip"],
        "B_x": _B_in["x"],
        "B_w_r": _B_in["w_r"],
        "B_w_gate": _B_in["w_gate"],
        "B_w_up": _B_in["w_up"],
        "B_w_down": _B_in["w_down"],
        "gate_w": _w((lambda *s: torch.randn(*s, dtype=torch.bfloat16) * 0.05)(576) * 16.0),
    }

def reference(A_u, A_A_re, A_A_im, A_B_re, A_B_im, A_C_re, A_C_im, A_log_dt, A_Dskip, B_x, B_w_r, B_w_gate, B_w_up, B_w_down, gate_w):
    def _A_ref(u, A_re, A_im, B_re, B_im, C_re, C_im, log_dt, Dskip):
        Bsz, L, D = u.shape
        dt = torch.exp(log_dt)[:, None]
        A = A_re + 1j * A_im
        Bc = B_re + 1j * B_im
        Cc = C_re + 1j * C_im
        logdA = dt * A
        dB = dt * Bc
        l = (lambda x, dt: x.to(dt))(torch.arange(L), A_re.dtype)
        P = torch.exp(logdA[:, :, None] * l[None, None, :])
        K = torch.einsum("dn,dnl,dn->dl", Cc, P, dB).real
        L2 = 2 * L
        Kp = _pad(K, ((0, 0), (0, L)))
        up = (lambda x, *d: x.permute(*d))(u, 0, 2, 1)
        up = _pad(up, ((0, 0), (0, 0), (0, L)))
        Kf = (lambda x, axes=(-2,-1): torch.fft.rfft2(x.float(), dim=axes))(Kp[:, None, :], axes=(-2, -1))
        Uf = (lambda x, axes=(-2,-1): torch.fft.rfft2(x.float(), dim=axes))(up[:, :, None, :], axes=(-2, -1))
        Yf = Uf * Kf[None, :, :, :]
        y = (lambda x, s, axes=(-2,-1): torch.fft.irfft2(x, s=s, dim=axes).to(torch.bfloat16))(Yf, (1, L2), axes=(-2, -1))[:, :, 0, :L]
        y = (lambda x, *d: x.permute(*d))(y, 0, 2, 1) + u * Dskip[None, None, :]
        return y

    def _B_ref(x, w_r, w_gate, w_up, w_down):
        B, S, D = x.shape
        C = 8
        r = x @ w_r
        idx = torch.argsort(-r, axis=-1)[:, :C]
        idx_e = (lambda x, n, axis: torch.repeat_interleave(x, n, dim=axis))(idx.reshape(B, C, 1), D, axis=2)
        x_sel = (lambda x, i, axis: torch.take_along_dim(x, i, dim=axis))(x, idx_e, axis=1)
        g = _silu(x_sel @ w_gate)
        u = x_sel @ w_up
        y = (g * u) @ w_down
        r_sel = _sigmoid((lambda x, i, axis: torch.take_along_dim(x, i, dim=axis))(r, idx, axis=1))
        y = y * r_sel.reshape(B, C, 1)
        b_off = (torch.arange(B) * S).reshape(B, 1)
        flat_idx = (idx + b_off).reshape(B * C)
        zero = (x * 0.0).reshape(B * S, D)
        upd = _scatter_add(zero, flat_idx, y.reshape(B * C, D), axis=0)
        return x + upd.reshape(B, S, D)

    x = A_u
    g = _sigmoid((x * gate_w).sum(-1, keepdims=True))
    a = _A_ref(x, A_A_re, A_A_im, A_B_re, A_B_im, A_C_re, A_C_im, A_log_dt, A_Dskip)
    b = _B_ref(x, B_w_r, B_w_gate, B_w_up, B_w_down)
    return g * a + (1 - g) * b
```

```cpp
#include <hip/hip_runtime.h>
#include <stdint.h>

typedef unsigned int u32;
typedef unsigned long long u64;
typedef unsigned short u16;
typedef u32 v4u __attribute__((ext_vector_type(4)));
typedef u32 v2u __attribute__((ext_vector_type(2)));
typedef u32 v8u __attribute__((ext_vector_type(8)));
typedef u32 v16u __attribute__((ext_vector_type(16)));
typedef float v4f __attribute__((ext_vector_type(4)));
typedef float v2f __attribute__((ext_vector_type(2)));
typedef short v8s __attribute__((ext_vector_type(8)));
typedef __bf16 v2bf __attribute__((ext_vector_type(2)));
#define GAS __attribute__((address_space(1)))
#define LAS __attribute__((address_space(3)))
#define DI __device__ __forceinline__
#define RLX_AGENT __ATOMIC_RELAXED, __HIP_MEMORY_SCOPE_AGENT
#define SB() __builtin_amdgcn_sched_barrier(0)
#define MEMFENCE() asm volatile("" ::: "memory")
#define PIN(x) asm volatile("" : "+v"(x))
#define VMWAIT(n) asm volatile("s_waitcnt vmcnt(%0)" ::"n"(n) : "memory")

#define CODE_KB 24
#define WIN 6
#define WIN0 6
#define PART_AT 570
#define PART_K 3
#define WDD 6
#define HWALL 0
#define HWREV 0
#define RBQ 1
#define HWC 4u
#define NWE 6
#define PG0 192
#define PGN 64
#define HWAVES 5
#define HT0 1
#define HWIN 2
#define HR0 320
#define PART_GAP 2
#define RED_DELAY0 4
#define WDE 0
#define SLOW0 128
#define SEL_AT 215
#define TREF 1
#define SEL_GAP 3
#define POLL_GAP 1
#define BUMP_DELAY 16
#define NM_LIST 4, 4, 10, 11, 11, 10, 11, 11
#define NH_LIST 2, 2, 2, 2, 1, 0, 1, 1
#define NI_LIST 16, 16, 28, 21, 16, 0, 16, 15
constexpr int TB = 248;
constexpr int RB = 250;

constexpr int L_ = 2048, D_ = 576, N_ = 128, F_ = 4608;
constexpr int NTHR = 768;
constexpr int SPIN_CAP = 1 << 17;
constexpr u32 OOB = 0x7ffffff0u;
__device__ __forceinline__ unsigned tag_of(unsigned epoch) { return epoch == 0xffffffffu ? 1u : epoch + 1u; }

constexpr u32 WS_EPOCH = 0;
constexpr u32 WS_SEL = 1024;
constexpr u32 WS_T0 = 2048;
constexpr u32 WS_KEYS = 4096;
constexpr u32 WS_CAND = WS_KEYS + 16384;
constexpr u32 WS_ACK = WS_CAND + 8 * 128 * 128;
constexpr u32 WS_PART = WS_ACK + 256 * 128;
constexpr u32 WS_BYTES = WS_PART + 144 * 72 * 128;

constexpr int TR = 2080;
constexpr int LD_TB = 0;
constexpr int ROW_T1 = 16, ROW_FL = 26, ROW_FU = 27, ROW_T0 = 28, ROW_T2 = 44;
constexpr int U_PAD = 256, U_CH = 2 * (U_PAD + 2048);
constexpr int LD_U = LD_TB + 60 * TR;
constexpr int PA = 550, PB = 545, KA_CP = 1216, KA_CH = 2 * KA_CP;
constexpr int LD_KA = LD_U + 4 * U_CH;
constexpr int G_CH = 896;
constexpr int LD_G = LD_KA + 4 * KA_CH;
constexpr int LD_BD = LD_G + 4 * G_CH;
constexpr int LD_CONV_END = LD_BD + 5120;
constexpr int FL_IT = 64;
constexpr int LD_TAG = 288;
constexpr int LD_SCR = 512;
constexpr int LD_XS = 1024 + 2 * 9216;
constexpr int LD_WD = LD_XS + 9216;
constexpr int LD_RED = LD_WD + 9 * 8192;
constexpr int HB_ROW = 144;
constexpr int LD_HB = LD_RED + 6 * 4096;
constexpr int LD_MOE_END = LD_HB + 8 * HB_ROW;
constexpr int LDS_BYTES = LD_CONV_END > LD_MOE_END ? LD_CONV_END : LD_MOE_END;
static_assert(LDS_BYTES > 81920 && LDS_BYTES <= 163840, "lds: one workgroup per CU");
static_assert(LD_BD % 128 == 0 && LD_MOE_END <= LD_KA && LD_U % 16 == 0 && LD_KA % 16 == 0 && LD_G % 16 == 0 && LD_WD % 1024 == 0, "align");

struct Params {
    const u16 *x, *Are, *Aim, *Bre, *Bim, *Cre, *Cim, *logdt, *dskip, *wr, *wg, *wu, *wd, *gw;
    u16* out;
    unsigned char* ws;
};
struct Args { Params p; unsigned role[256]; };
constexpr int nconv_of(int label) { return label < 2 ? 24 : 16; }
constexpr Args mk_roles() {
    Args A{};
    const int nm[8] = {NM_LIST}, nh[8] = {NH_LIST}, ni[8] = {NI_LIST};
    int m0 = 0, i0 = 0;
    bool ok = true;
    for (int x = 0; x < 8; ++x) {
        const int nc = nconv_of(x), first = x * 32, w = 32 - nc;
        for (int ii = 0; ii < nc; ++ii) A.role[first + ii] = (unsigned)(m0 | nm[x] << 8 | (65536 / nm[x] + 1) << 16);
        const int rest = ni[x] - (w - nm[x]) * nh[x];
        ok = ok && rest >= 0;
        for (int ii = 31; ii >= nc; --ii) {
            const int cnt = ii - nc >= nm[x] ? nh[x] : rest / nm[x] + (ii - nc < rest % nm[x] ? 1 : 0);
            A.role[first + ii] = (unsigned)((ii - nc < nm[x] ? m0 + ii - nc + 1 : 0) | (cnt ? i0 : 0) << 8 | cnt << 16);
            i0 += cnt; ok = ok && cnt <= 3;
        }
        m0 += nm[x];
    }
    A.role[255] |= (m0 != 72 || i0 != 128 || !ok) ? 0x80000000u : 0u;
    return A;
}
static_assert(!(mk_roles().role[255] >> 31) && (mk_roles().role[(RB & 7) * 32 + (RB >> 3)] & 255) == 0, "roles");

#define EXIT() do { VMWAIT(0); __builtin_amdgcn_endpgm(); } while (0)

typedef LAS unsigned char* lptr;
typedef const LAS unsigned char* clptr;
DI float bf_lo(u32 v) { return __uint_as_float(v << 16); }
DI float bf_hi(u32 v) { return __uint_as_float(v & 0xffff0000u); }
DI u32 pk_bf16(float a, float b) { v2f f = {a, b}; return __builtin_bit_cast(u32, __builtin_convertvector(f, v2bf)); }
DI float dot2(u32 a, u32 b, float c) { return __builtin_amdgcn_fdot2_f32_bf16(__builtin_bit_cast(v2bf, a), __builtin_bit_cast(v2bf, b), c, false); }
DI float sigmoidf_(float s) { return __builtin_amdgcn_rcpf(1.f + __builtin_amdgcn_exp2f(-1.44269504f * s)); }
template <int CTRL, int RM> DI u32 dpp0(u32 v) { return (u32)__builtin_amdgcn_update_dpp(0, (int)v, CTRL, RM, 0xf, true); }
template <int CTRL, int RM> DI float dppf(float v) { return __uint_as_float(dpp0<CTRL, RM>(__float_as_uint(v))); }
DI u32 umax(u32 a, u32 b) { return a > b ? a : b; }
DI u32 umin(u32 a, u32 b) { return a < b ? a : b; }
DI int fresh_lane() { int l; asm volatile("v_mbcnt_lo_u32_b32 %0, -1, 0\n\tv_mbcnt_hi_u32_b32 %0, -1, %0" : "=v"(l)); return l; }
DI u32 rfl(u32 v) { return (u32)__builtin_amdgcn_readfirstlane((int)v); }
DI u32 rdl(u32 v, int l) { return (u32)__builtin_amdgcn_readlane((int)v, l); }
DI u32 wave_max(u32 v) {
    v = umax(v, dpp0<0x111, 0xf>(v)); v = umax(v, dpp0<0x112, 0xf>(v)); v = umax(v, dpp0<0x114, 0xf>(v)); v = umax(v, dpp0<0x118, 0xf>(v));
    v = umax(v, dpp0<0x142, 0xa>(v)); v = umax(v, dpp0<0x143, 0xc>(v));
    return rdl(v, 63);
}
DI u32 med3u(u32 a, u32 b, u32 c) { return umax(umin(a, b), umin(umax(a, b), c)); }
DI u32 perm_lo(u32 hi, u32 lo) { return __builtin_amdgcn_perm(hi, lo, 0x05040100u); }
DI u32 perm_hi(u32 hi, u32 lo) { return __builtin_amdgcn_perm(hi, lo, 0x07060302u); }
typedef __bf16 v8bf __attribute__((ext_vector_type(8)));
typedef __bf16 v16bf __attribute__((ext_vector_type(16)));
DI v4f smfmac(v4u a, v8u b, v4f c, u32 idx) { return __builtin_amdgcn_smfmac_f32_16x16x64_bf16(__builtin_bit_cast(v8bf, a), __builtin_bit_cast(v16bf, b), c, (int)idx, 0, 0); }
DI __amdgpu_buffer_rsrc_t mk_rsrc(const void* p, u32 n) { return __builtin_amdgcn_make_buffer_rsrc((void*)p, 0, n, 0x00020000); }
DI __amdgpu_buffer_rsrc_t ws_rsrc(const Params& P) { return mk_rsrc(P.ws, WS_BYTES); }
DI v4u ld_sc1(__amdgpu_buffer_rsrc_t r, u32 off) { return __builtin_amdgcn_raw_buffer_load_b128(r, off, 0, 16); }
DI v2u ld2_sc1(__amdgpu_buffer_rsrc_t r, u32 off) { return __builtin_amdgcn_raw_buffer_load_b64(r, off, 0, 16); }
DI void st_sc1(__amdgpu_buffer_rsrc_t r, u32 off, v4u v) { __builtin_amdgcn_raw_buffer_store_b128(v, r, off, 0, 16); }
DI void st2_sc1(__amdgpu_buffer_rsrc_t r, u32 off, v2u v) { __builtin_amdgcn_raw_buffer_store_b64(v, r, off, 0, 16); }
DI float g_of(u32 p) { return (float)__builtin_bit_cast(_Float16, (u16)p); }
DI bool tag_ok(v4u v, u32 tag) { return (v[1] == tag) & (v[3] == tag); }
DI v2u tr_read(clptr p) {
    typedef short v4s __attribute__((ext_vector_type(4)));
    return __builtin_bit_cast(v2u, __builtin_amdgcn_ds_read_tr16_b64_v4i16((LAS v4s*)p));
}
DI void fmac_(float& acc, float a, float b) { asm("v_fmac_f32 %0, %1, %2" : "+v"(acc) : "v"(a), "v"(b)); }
DI float mul_(float a, float b) { float r; asm("v_mul_f32 %0, %1, %2" : "=v"(r) : "v"(a), "v"(b)); return r; }
#define W16(p) (*(volatile LAS u16*)(p))
#define W32(p) (*(volatile LAS u32*)(p))
#define W64(p) (*(volatile LAS v2u*)(p))
#define W128(p) (*(volatile LAS v4u*)(p))

template <int SLEEP> DI void wait_ge(clptr p, u32 target) {
    u32 v, n;
    asm volatile("s_mov_b32 %1, 0x100000\n"
                 "Lw%=:\n\tds_read_b32 %0, %2\n\ts_waitcnt lgkmcnt(0)\n\tv_cmp_gt_u32 vcc, %3, %0\n\ts_cbranch_vccz Ld%=\n\t"
                 "s_sleep %4\n\ts_sub_u32 %1, %1, 1\n\ts_cbranch_scc0 Lw%=\n"
                 "Ld%=:"
                 : "=&v"(v), "=&s"(n) : "v"(p), "s"(target), "n"(SLEEP) : "memory", "vcc", "scc");
}
DI void flag1(lptr p, u32 v) {
    asm volatile("s_mov_b64 exec, 1\n\tds_write_b32 %0, %1\n\ts_mov_b64 exec, -1" ::"v"(p), "v"(v) : "memory");
}
template <int OFF> DI void put_row(u32 v) { asm volatile("ds_write_addtid_b32 %0 offset:%1" ::"v"(v), "n"(OFF) : "memory"); }
DI void wg_barrier() { asm volatile("s_waitcnt lgkmcnt(0)\n\ts_barrier" ::: "memory"); }

DI void touch(const void* base, u32 off) {
    asm volatile("s_movk_i32 m0, %2\n\ts_nop 0\n\tglobal_load_lds_dword %0, %1" ::"v"(off), "s"(base), "n"(LD_SCR) : "memory");
}
template <int STEP> DI void dma_rows(const void* base, u32 off, u32 dst) {
#define DR(q) "global_load_lds_dwordx4 %0, %1 offset:128*" #q "\n\ts_addk_i32 m0, %3\n\ts_nop 0\n\t"
    asm volatile("s_mov_b32 m0, %2\n\ts_nop 0\n\t" DR(0) DR(1) DR(2) DR(3) DR(4) DR(5) DR(6) DR(7) "global_load_lds_dwordx4 %0, %1 offset:1024" ::"v"(off), "s"(base), "s"(dst), "n"(STEP - 128) : "memory", "scc");
}
DI int cg_of(int xcd, int ii) { return ii < 16 ? 16 * xcd + ii : 112 + 8 * xcd + ii; }
struct Lab { u32 m0, n, mg; };
DI Lab lab_of(u32 w) { return {w & 255u, (w >> 8) & 255u, w >> 16}; }
DI u32 wd_unit(Lab lb, u32 unit) {
    const u32 j = (unit * lb.mg) >> 16, cu = unit - lb.n * j;
    return (lb.m0 + cu) * 73728u + j * 8192u;
}

DI u32 from_lane(u32 v, int l) { return (u32)__builtin_amdgcn_ds_bpermute(4 * l, (int)v); }

DI void conv_final(const Params& P, lptr sm, int cg, int xcd, u32 tag, v4u sg) {
    const int lane = fresh_lane();
    const __amdgpu_buffer_rsrc_t rws = ws_rsrc(P), ro = mk_rsrc(P.out, L_ * D_ * 2);
    const int j = (lane & 4) + (lane >> 4), cc = lane & 3;
    u32 po = WS_PART + (u32)cg * (72u * 128u) + (u32)lane * 16u;
    PIN(po);
#pragma unroll 1
    for (int spin = 0; spin < SPIN_CAP && !__all(tag_ok(sg, tag)); ++spin) {
        __builtin_amdgcn_s_sleep(4);
        MEMFENCE();
        sg = ld_sc1(rws, WS_SEL + (u32)xcd * 128u + (u32)(lane & 7) * 16u);
    }
    const u32 sel = from_lane(sg[0], j), gk = from_lane(sg[2], j), tok = 2047u - (sel & 0x7ffu);
    const float rs = sigmoidf_(bf_hi(sel ^ ((~(u32)((int)sel >> 31)) | 0x80000000u)));
    const float x = bf_lo((u32)W16(sm + LD_U + cc * U_CH + 2 * U_PAD + tok * 2u));
    wait_ge<1>(sm + LD_TB + ROW_FL * TR + (lane & 3) * 516 + (lane & 4) * 64, 18);
    const u32 c = (tok >> 4) & 15u;
    clptr ap = sm + LD_TB + (tok >> 7) * TR + cc * 512 + (c & 7u) * 64u + (((tok >> 2) ^ (c >> 1)) & 3u) * 16u + (tok & 3u) * 4u;
    const float a2 = *(const volatile LAS float*)(ap + (ROW_T0 - 4) * TR);
    const float a = *(const volatile LAS float*)ap + (tok >= 512u ? a2 : 0.f);
    const float g = g_of(gk);
    float gs = (1.f - g) * rs, base = x + g * (a - x);
    u32 oo = (lane & 9) ? OOB : (tok * D_ + (u32)(4 * cg + cc)) * 2u;
    clptr fw = sm + LD_TB + ROW_FU * TR + (lane & 3) * 516 + 256;
    PIN(base); PIN(gs); PIN(oo); PIN(fw);
    u32 po2 = po + 5120u;
    const u32 at = rfl(gk >> 16);
    PIN(po2);
#pragma unroll 1
    for (int w = 0; w < 4096; ++w) {
        if ((short)((u32)__builtin_amdgcn_s_memrealtime() - at) >= 0) break;
        __builtin_amdgcn_s_sleep(1);
    }
    float s0, s1, s2, s3;
    u32 n;
#define PLD(k, a, o) "global_load_dwordx4 v[64+4*" #k ":67+4*" #k "], " a ", %[ws] offset:" #o " sc1\n\t"
#define PAD(k, w) "s_waitcnt vmcnt(" #w ")\n\tv_dot2c_f32_bf16 %[s0], %[lo], v[64+4*" #k "]\n\tv_dot2c_f32_bf16 %[s1], %[hi], v[64+4*" #k "]\n\t" \
                  "v_dot2c_f32_bf16 %[s2], %[lo], v[66+4*" #k "]\n\tv_dot2c_f32_bf16 %[s3], %[hi], v[66+4*" #k "]\n\t" \
                  "v_bitop3_b32 v[65+4*" #k "], v[65+4*" #k "], v[67+4*" #k "], %[tag] bitop3:0x7e\n\t"
    asm volatile("s_mov_b32 %[n], 0x20000\n\tv_mov_b32 v100, 1\n"
                 "Lr%=:\n\tv_cmp_ne_u32 vcc, 0, v100\n\tv_mov_b32 %[s0], 0\n\tv_mov_b32 %[s1], 0\n\tv_mov_b32 %[s2], 0\n\tv_mov_b32 %[s3], 0\n\ts_mov_b64 exec, vcc\n\t"
                 PLD(0, "%[p0]", 0) PLD(1, "%[p0]", 1024) PLD(2, "%[p0]", 2048) PLD(3, "%[p0]", 3072) PLD(4, "%[p1]", -1024)
                 PLD(5, "%[p1]", 0) PLD(6, "%[p1]", 1024) PLD(7, "%[p1]", 2048) PLD(8, "%[p1]", 3072) "s_mov_b64 exec, -1\n\t"
                 PAD(0, 8) PAD(1, 7) PAD(2, 6) "v_or3_b32 v100, v65, v69, v73\n\t" PAD(3, 5) PAD(4, 4) "v_or3_b32 v100, v100, v77, v81\n\t"
                 PAD(5, 3) PAD(6, 2) "v_or3_b32 v100, v100, v85, v89\n\t" PAD(7, 1) PAD(8, 0) "v_or3_b32 v100, v100, v93, v97\n\t"
                 "v_cmp_ne_u32 vcc, 0, v100\n\ts_cbranch_vccz Le%=\n\t"
                 "v_mov_b32 v65, %[tag]\n\tv_mov_b32 v69, %[tag]\n\tv_mov_b32 v73, %[tag]\n\tv_mov_b32 v77, %[tag]\n\tv_mov_b32 v81, %[tag]\n\t"
                 "v_mov_b32 v85, %[tag]\n\tv_mov_b32 v89, %[tag]\n\tv_mov_b32 v93, %[tag]\n\tv_mov_b32 v97, %[tag]\n\t"
                 "s_sleep %[gap]\n\ts_sub_u32 %[n], %[n], 1\n\ts_cbranch_scc0 Lr%=\n"
                 "Le%=:\n\ts_nop 1"
                 : [s0] "=&v"(s0), [s1] "=&v"(s1), [s2] "=&v"(s2), [s3] "=&v"(s3), [n] "=&s"(n)
                 : [p0] "v"(po), [p1] "v"(po2), [ws] "s"(P.ws), [tag] "s"(tag), [lo] "s"(0x00003f80u), [hi] "s"(0x3f800000u), [gap] "n"(PART_GAP)
                 : "memory", "vcc", "scc", "v64", "v65", "v66", "v67", "v68", "v69", "v70", "v71", "v72", "v73", "v74", "v75", "v76", "v77", "v78", "v79", "v80", "v81", "v82", "v83",
                   "v84", "v85", "v86", "v87", "v88", "v89", "v90", "v91", "v92", "v93", "v94", "v95", "v96", "v97", "v98", "v99", "v100");
    wait_ge<0>(fw, 20);
    const auto a3 = __builtin_amdgcn_permlane32_swap(__float_as_uint(s0), __float_as_uint(s2), false, false);
    const auto b2 = __builtin_amdgcn_permlane32_swap(__float_as_uint(s1), __float_as_uint(s3), false, false);
    const auto e = __builtin_amdgcn_permlane16_swap(__float_as_uint(__uint_as_float(a3[0]) + __uint_as_float(a3[1])), __float_as_uint(__uint_as_float(b2[0]) + __uint_as_float(b2[1])), false, false);
    float y = __uint_as_float(e[0]) + __uint_as_float(e[1]);
    y += dppf<0x128, 0xf>(y);
    const float o = base + gs * y;
    __builtin_amdgcn_raw_buffer_store_b32(pk_bf16(o, dppf<0xB1, 0xf>(o)), ro, oo, 0, 0);
}

struct ConvTab { u32 v[(12 * 5 + 5) * 256]; };
constexpr ConvTab mk_tab() {
    ConvTab t{};
    for (int kind = 0; kind < 3; ++kind) for (int ch = 0; ch < 4; ++ch) for (int lane = 0; lane < 64; ++lane) {
        const int c = lane & 15, kg = lane >> 4;
        const int ka = LD_KA + ch * KA_CH, gq = LD_G + ch * G_CH;
        const int kb = ka + ((c & 1) ? KA_CP + 2 * PB : 2 * PA) - 2 * (c + 16 - 8 * kg) - 64 * 15;
        u32 ad[20] = {};
        if (kind < 2) {
            ad[0] = LD_TB + ROW_FU * TR + ch * 512;
            ad[1] = LD_U + ch * U_CH + 2 * (U_PAD - 16 + 16 * c + 8 * kg) - 7 * 64;
            ad[2] = LD_TB + ROW_FU * TR + (lane & 3) * 516 + 256;
            ad[3] = LD_TB + (ROW_T0 + (c >> 3)) * TR + ch * 512 + 64 * (c & 7) + 16 * (kg ^ ((c >> 1) & 3));
            ad[4] = kind ? gq + 4 * (15 - c + 4 * kg) : kb;
            ad[5] = kind ? kb - 64 : LD_TB + (c >> 3) * TR + ch * 512 + 64 * (c & 7) + 16 * (kg ^ ((c >> 1) & 3));
            ad[6] = kind ? LD_BD + lane * 16 : LD_TB + ROW_FL * TR + ch * 512 + 256;
            ad[7] = gq;
        } else {
            ad[0] = LD_TB + ROW_FL * TR + ch * 512 + (lane & 1) * 260;
            ad[1] = LD_TB + c * TR + ch * 512 + kg * 16;
            ad[2] = LD_TB + (ROW_T1 + (c & 3) + (c >> 3) + 5 * ((c >> 2) & 1)) * TR + ch * 512 + kg * 16;
            ad[3] = ka + 2 * (PA - 3 - (4 * (kg & 1) + 128 * (kg >> 1) + 8 * c));
            ad[4] = ka + (kg < 2 ? 2 * (PA - 6) : KA_CP + 2 * (PB - 9)) - 2 * (256 + 128 * (kg & 1) + 8 * c);
            ad[5] = gq + (kg < 2 ? (c & 1) * 256 + 4 * (47 - 24) + 2 * kg : 512 + 4 * (63 - 24) + 2 * (c & 1) - 128 * (kg - 2)) - 4 * (c >> 1);
            ad[6] = kg < 2 ? 0x01000302u : 0x03020302u;
            ad[7] = kg < 2 ? 0x01000100u : 0x01000302u;
            ad[8] = lane == 0 ? 0x05040100u : ad[7];
            ad[9] = (128 * ch + 2 * lane) * (D_ * 2);
            ad[10] = LD_U + 2 * (U_PAD + 128 * ch + 2 * lane);
            ad[11] = LD_TB + ch * TR + 64 * (lane >> 3) + 16 * (((lane >> 1) ^ (lane >> 4)) & 3) + 8 * (lane & 1);
            ad[12] = WS_KEYS + (64 * ch + lane) * 16;
            ad[13] = LD_TB + ROW_FL * TR + (lane & 3) * 516;
            ad[14] = WS_PART + (18 * ch + (lane < 17 ? lane : 17)) * 128;
            ad[15] = LD_TB + ROW_FL * TR + (lane & 3) * 516 + (lane & 4) * 64;
            ad[16] = lane * 128;
            ad[17] = lane < 32 ? ka + (lane >> 4) * KA_CP + (c < 6 ? c * 16 : 1088 + (c & 3) * 16) : LD_U + ch * U_CH + (lane - 32) * 16;
            ad[18] = gq + (lane < 55 ? lane : 55) * 16;
            ad[19] = ka;
        }
        for (int k = 0; k < 20; ++k) t.v[((kind * 4 + ch) * 5 + (k >> 2)) * 256 + lane * 4 + (k & 3)] = ad[k];
    }
    const int lo[5] = {3, 6, 5, 4, 3}, hi[5] = {5, 10, 11, 12, 4};
    for (int k = 0; k < 5; ++k) for (int lane = 0; lane < 64; ++lane) for (int e = 0; e < 8; ++e) {
        const int l = (lane & 15) + 16 - 8 * (lane >> 4) - e;
        if (l >= lo[k] && l <= hi[k]) t.v[(60 + k) * 256 + lane * 4 + (e >> 1)] |= (k < 4 ? 0x3f80u : 0xffffu) << (16 * (e & 1));
    }
    return t;
}
static __device__ const ConvTab conv_tab = mk_tab();
#define CONV_A_ASM \
    "s_setprio 1\n" \
    "s_waitcnt vmcnt(4)\n" \
    "v_mul_f32_e32 v0, 0x3fb8aa3b, v70\n" \
    "v_exp_f32_e32 v0, v0\n" \
    "v_mul_f32_e32 v1, 0x3fb8aa3b, v64\n" \
    "v_cvt_pk_bf16_f32 v0, 0, v0\n" \
    "v_mul_f32_e32 v2, 0.15915494, v65\n" \
    "v_mul_f32_e32 v1, v0, v1\n" \
    "v_mul_f32_e32 v2, v0, v2\n" \
    "v_mul_f32_e32 v4, 0x41000000, v2\n" \
    "v_mul_f32_e32 v3, 0x41000000, v1\n" \
    "v_fract_f32_e32 v4, v4\n" \
    "v_exp_f32_e32 v3, v3\n" \
    "v_cos_f32_e32 v8, v4\n" \
    "v_sin_f32_e32 v9, v4\n" \
    "v_mul_f32_e32 v66, v0, v66\n" \
    "v_mul_f32_e64 v8, v3, v8\n" \
    "v_mul_f32_e64 v9, v3, v9\n" \
    "v_pk_mul_f32 v[18:19], v[8:9], v[8:9] op_sel_hi:[0,1]\n" \
    "v_cvt_pk_bf16_f32 v30, v8, v9\n" \
    "v_pk_fma_f32 v[14:15], v[8:9], v[8:9], v[18:19] op_sel:[1,1,0] op_sel_hi:[0,1,1] neg_lo:[0,1,0]\n" \
    "ds_write_addtid_b32 v30 offset:2080\n" \
    "v_pk_mul_f32 v[18:19], v[8:9], v[14:15] op_sel_hi:[0,1]\n" \
    "v_cvt_pk_bf16_f32 v31, v14, v15\n" \
    "v_pk_fma_f32 v[14:15], v[14:15], v[8:9], v[18:19] op_sel:[1,1,0] op_sel_hi:[0,1,1] neg_lo:[0,1,0]\n" \
    "ds_write_addtid_b32 v31 offset:4160\n" \
    "v_pk_mul_f32 v[18:19], v[8:9], v[14:15] op_sel_hi:[0,1]\n" \
    "v_cvt_pk_bf16_f32 v30, v14, v15\n" \
    "v_pk_fma_f32 v[14:15], v[14:15], v[8:9], v[18:19] op_sel:[1,1,0] op_sel_hi:[0,1,1] neg_lo:[0,1,0]\n" \
    "ds_write_addtid_b32 v30 offset:6240\n" \
    "v_pk_mul_f32 v[18:19], v[8:9], v[14:15] op_sel_hi:[0,1]\n" \
    "v_cvt_pk_bf16_f32 v31, v14, v15\n" \
    "v_pk_fma_f32 v[14:15], v[14:15], v[8:9], v[18:19] op_sel:[1,1,0] op_sel_hi:[0,1,1] neg_lo:[0,1,0]\n" \
    "ds_write_addtid_b32 v31 offset:8320\n" \
    "v_mul_f32_e32 v4, 0x43000000, v2\n" \
    "v_mul_f32_e32 v3, 0x43000000, v1\n" \
    "v_fract_f32_e32 v4, v4\n" \
    "v_exp_f32_e32 v3, v3\n" \
    "v_cos_f32_e32 v12, v4\n" \
    "v_sin_f32_e32 v13, v4\n" \
    "v_pk_mul_f32 v[18:19], v[8:9], v[14:15] op_sel_hi:[0,1]\n" \
    "v_cvt_pk_bf16_f32 v30, v14, v15\n" \
    "v_pk_fma_f32 v[14:15], v[14:15], v[8:9], v[18:19] op_sel:[1,1,0] op_sel_hi:[0,1,1] neg_lo:[0,1,0]\n" \
    "ds_write_addtid_b32 v30 offset:10400\n" \
    "v_pk_mul_f32 v[18:19], v[8:9], v[14:15] op_sel_hi:[0,1]\n" \
    "v_cvt_pk_bf16_f32 v31, v14, v15\n" \
    "v_pk_fma_f32 v[14:15], v[14:15], v[8:9], v[18:19] op_sel:[1,1,0] op_sel_hi:[0,1,1] neg_lo:[0,1,0]\n" \
    "ds_write_addtid_b32 v31 offset:12480\n" \
    "v_mul_f32_e64 v12, v3, v12\n" \
    "v_mul_f32_e64 v13, -v3, v13\n" \
    "v_pk_mul_f32 v[18:19], v[8:9], v[14:15] op_sel_hi:[0,1]\n" \
    "v_cvt_pk_bf16_f32 v30, v14, v15\n" \
    "v_pk_fma_f32 v[14:15], v[14:15], v[8:9], v[18:19] op_sel:[1,1,0] op_sel_hi:[0,1,1] neg_lo:[0,1,0]\n" \
    "ds_write_addtid_b32 v30 offset:14560\n" \
    "v_pk_mul_f32 v[18:19], v[8:9], v[14:15] op_sel_hi:[0,1]\n" \
    "v_cvt_pk_bf16_f32 v31, v14, v15\n" \
    "v_pk_fma_f32 v[14:15], v[14:15], v[8:9], v[18:19] op_sel:[1,1,0] op_sel_hi:[0,1,1] neg_lo:[0,1,0]\n" \
    "ds_write_addtid_b32 v31 offset:16640\n" \
    "v_fract_f32_e32 v6, v2\n" \
    "v_exp_f32_e32 v5, v1\n" \
    "v_cos_f32_e32 v10, v6\n" \
    "v_sin_f32_e32 v11, v6\n" \
    "v_pk_mul_f32 v[18:19], v[8:9], v[14:15] op_sel_hi:[0,1]\n" \
    "v_cvt_pk_bf16_f32 v30, v14, v15\n" \
    "v_pk_fma_f32 v[14:15], v[14:15], v[8:9], v[18:19] op_sel:[1,1,0] op_sel_hi:[0,1,1] neg_lo:[0,1,0]\n" \
    "ds_write_addtid_b32 v30 offset:18720\n" \
    "v_pk_mul_f32 v[18:19], v[8:9], v[14:15] op_sel_hi:[0,1]\n" \
    "v_cvt_pk_bf16_f32 v31, v14, v15\n" \
    "v_pk_fma_f32 v[14:15], v[14:15], v[8:9], v[18:19] op_sel:[1,1,0] op_sel_hi:[0,1,1] neg_lo:[0,1,0]\n" \
    "ds_write_addtid_b32 v31 offset:20800\n" \
    "v_mul_f32_e64 v10, v5, v10\n" \
    "v_mul_f32_e64 v11, -v5, v11\n" \
    "v_pk_mul_f32 v[18:19], v[8:9], v[14:15] op_sel_hi:[0,1]\n" \
    "v_cvt_pk_bf16_f32 v30, v14, v15\n" \
    "v_pk_fma_f32 v[14:15], v[14:15], v[8:9], v[18:19] op_sel:[1,1,0] op_sel_hi:[0,1,1] neg_lo:[0,1,0]\n" \
    "ds_write_addtid_b32 v30 offset:22880\n" \
    "v_mul_f32_e64 v67, v0, v67\n" \
    "v_mul_f32_e64 v16, v68, v66\n" \
    "v_mul_f32_e64 v17, -v68, v67\n" \
    "v_fma_f32 v16, -v69, v67, v16\n" \
    "v_fma_f32 v17, -v69, v66, v17\n" \
    "v_pk_mul_f32 v[18:19], v[8:9], v[14:15] op_sel_hi:[0,1]\n" \
    "v_cvt_pk_bf16_f32 v31, v14, v15\n" \
    "v_pk_fma_f32 v[14:15], v[14:15], v[8:9], v[18:19] op_sel:[1,1,0] op_sel_hi:[0,1,1] neg_lo:[0,1,0]\n" \
    "ds_write_addtid_b32 v31 offset:24960\n" \
    "v_pk_mul_f32 v[18:19], v[8:9], v[14:15] op_sel_hi:[0,1]\n" \
    "v_cvt_pk_bf16_f32 v30, v14, v15\n" \
    "v_pk_fma_f32 v[14:15], v[14:15], v[8:9], v[18:19] op_sel:[1,1,0] op_sel_hi:[0,1,1] neg_lo:[0,1,0]\n" \
    "ds_write_addtid_b32 v30 offset:27040\n" \
    "v_pk_mul_f32 v[18:19], v[8:9], v[14:15] op_sel_hi:[0,1]\n" \
    "v_cvt_pk_bf16_f32 v31, v14, v15\n" \
    "v_pk_fma_f32 v[14:15], v[14:15], v[8:9], v[18:19] op_sel:[1,1,0] op_sel_hi:[0,1,1] neg_lo:[0,1,0]\n" \
    "ds_write_addtid_b32 v31 offset:29120\n" \
    "v_pk_mul_f32 v[18:19], v[12:13], v[16:17] op_sel_hi:[0,1]\n" \
    "v_cvt_pk_bf16_f32 v30, v14, v15\n" \
    "v_pk_fma_f32 v[20:21], v[16:17], v[12:13], v[18:19] op_sel:[1,1,0] op_sel_hi:[0,1,1] neg_lo:[0,1,0]\n" \
    "ds_write_addtid_b32 v30 offset:31200\n" \
    "v_mov_b32_e32 v61, 1\n" \
    "ds_write_addtid_b32 v61 offset:54080\n" \
    "s_add_u32 m0, %[m0a], 54080\n" \
    "v_cvt_pk_bf16_f32 v31, v16, v17\n" \
    "ds_write_addtid_b32 v31 offset:4160\n" \
    "v_pk_mul_f32 v[18:19], v[10:11], v[16:17] op_sel_hi:[0,1]\n" \
    "v_cvt_pk_bf16_f32 v30, v20, v21\n" \
    "v_pk_fma_f32 v[16:17], v[16:17], v[10:11], v[18:19] op_sel:[1,1,0] op_sel_hi:[0,1,1] neg_lo:[0,1,0]\n" \
    "ds_write_addtid_b32 v30 offset:20800\n" \
    "v_pk_mul_f32 v[18:19], v[12:13], v[16:17] op_sel_hi:[0,1]\n" \
    "v_cvt_pk_bf16_f32 v31, v16, v17\n" \
    "v_pk_fma_f32 v[14:15], v[16:17], v[12:13], v[18:19] op_sel:[1,1,0] op_sel_hi:[0,1,1] neg_lo:[0,1,0]\n" \
    "ds_write_addtid_b32 v31 offset:6240\n" \
    "v_pk_mul_f32 v[18:19], v[10:11], v[16:17] op_sel_hi:[0,1]\n" \
    "v_cvt_pk_bf16_f32 v30, v14, v15\n" \
    "v_pk_fma_f32 v[16:17], v[16:17], v[10:11], v[18:19] op_sel:[1,1,0] op_sel_hi:[0,1,1] neg_lo:[0,1,0]\n" \
    "ds_write_addtid_b32 v30 offset:22880\n" \
    "v_pk_mul_f32 v[18:19], v[12:13], v[16:17] op_sel_hi:[0,1]\n" \
    "v_cvt_pk_bf16_f32 v31, v16, v17\n" \
    "v_pk_fma_f32 v[22:23], v[16:17], v[12:13], v[18:19] op_sel:[1,1,0] op_sel_hi:[0,1,1] neg_lo:[0,1,0]\n" \
    "ds_write_addtid_b32 v31 offset:8320\n" \
    "v_pk_mul_f32 v[18:19], v[10:11], v[16:17] op_sel_hi:[0,1]\n" \
    "v_cvt_pk_bf16_f32 v30, v22, v23\n" \
    "v_pk_fma_f32 v[16:17], v[16:17], v[10:11], v[18:19] op_sel:[1,1,0] op_sel_hi:[0,1,1] neg_lo:[0,1,0]\n" \
    "ds_write_addtid_b32 v30 offset:24960\n" \
    "v_pk_mul_f32 v[18:19], v[12:13], v[16:17] op_sel_hi:[0,1]\n" \
    "v_cvt_pk_bf16_f32 v31, v16, v17\n" \
    "v_pk_fma_f32 v[14:15], v[16:17], v[12:13], v[18:19] op_sel:[1,1,0] op_sel_hi:[0,1,1] neg_lo:[0,1,0]\n" \
    "ds_write_addtid_b32 v31 offset:10400\n" \
    "v_pk_mul_f32 v[18:19], v[10:11], v[16:17] op_sel_hi:[0,1]\n" \
    "v_cvt_pk_bf16_f32 v30, v14, v15\n" \
    "v_pk_fma_f32 v[16:17], v[16:17], v[10:11], v[18:19] op_sel:[1,1,0] op_sel_hi:[0,1,1] neg_lo:[0,1,0]\n" \
    "ds_write_addtid_b32 v30 offset:27040\n" \
    "v_pk_mul_f32 v[18:19], v[12:13], v[16:17] op_sel_hi:[0,1]\n" \
    "v_cvt_pk_bf16_f32 v31, v16, v17\n" \
    "v_pk_fma_f32 v[24:25], v[16:17], v[12:13], v[18:19] op_sel:[1,1,0] op_sel_hi:[0,1,1] neg_lo:[0,1,0]\n" \
    "ds_write_addtid_b32 v31 offset:12480\n" \
    "v_pk_mul_f32 v[18:19], v[10:11], v[16:17] op_sel_hi:[0,1]\n" \
    "v_cvt_pk_bf16_f32 v30, v24, v25\n" \
    "v_pk_fma_f32 v[16:17], v[16:17], v[10:11], v[18:19] op_sel:[1,1,0] op_sel_hi:[0,1,1] neg_lo:[0,1,0]\n" \
    "ds_write_addtid_b32 v30 offset:29120\n" \
    "v_pk_mul_f32 v[18:19], v[12:13], v[16:17] op_sel_hi:[0,1]\n" \
    "v_cvt_pk_bf16_f32 v31, v16, v17\n" \
    "v_pk_fma_f32 v[14:15], v[16:17], v[12:13], v[18:19] op_sel:[1,1,0] op_sel_hi:[0,1,1] neg_lo:[0,1,0]\n" \
    "ds_write_addtid_b32 v31 offset:14560\n" \
    "v_pk_mul_f32 v[18:19], v[10:11], v[16:17] op_sel_hi:[0,1]\n" \
    "v_cvt_pk_bf16_f32 v30, v14, v15\n" \
    "v_pk_fma_f32 v[16:17], v[16:17], v[10:11], v[18:19] op_sel:[1,1,0] op_sel_hi:[0,1,1] neg_lo:[0,1,0]\n" \
    "ds_write_addtid_b32 v30 offset:31200\n" \
    "v_pk_mul_f32 v[18:19], v[12:13], v[16:17] op_sel_hi:[0,1]\n" \
    "v_cvt_pk_bf16_f32 v31, v16, v17\n" \
    "v_pk_fma_f32 v[26:27], v[16:17], v[12:13], v[18:19] op_sel:[1,1,0] op_sel_hi:[0,1,1] neg_lo:[0,1,0]\n" \
    "ds_write_addtid_b32 v31 offset:16640\n" \
    "v_pk_mul_f32 v[18:19], v[10:11], v[16:17] op_sel_hi:[0,1]\n" \
    "v_cvt_pk_bf16_f32 v30, v26, v27\n" \
    "v_pk_fma_f32 v[16:17], v[16:17], v[10:11], v[18:19] op_sel:[1,1,0] op_sel_hi:[0,1,1] neg_lo:[0,1,0]\n" \
    "ds_write_addtid_b32 v30 offset:33280\n" \
    "v_pk_mul_f32 v[18:19], v[12:13], v[16:17] op_sel_hi:[0,1]\n" \
    "v_cvt_pk_bf16_f32 v31, v16, v17\n" \
    "v_pk_fma_f32 v[14:15], v[16:17], v[12:13], v[18:19] op_sel:[1,1,0] op_sel_hi:[0,1,1] neg_lo:[0,1,0]\n" \
    "ds_write_addtid_b32 v31 offset:18720\n" \
    "s_waitcnt vmcnt(0)\n" \
    "v_pk_mul_f32 v[18:19], v[12:13], v[20:21] op_sel_hi:[0,1]\n" \
    "v_cvt_pk_bf16_f32 v30, v14, v15\n" \
    "v_pk_fma_f32 v[20:21], v[20:21], v[12:13], v[18:19] op_sel:[1,1,0] op_sel_hi:[0,1,1] neg_lo:[0,1,0]\n" \
    "ds_write_addtid_b32 v30 offset:35360\n" \
    "v_mov_b32_e32 v61, 2\n" \
    "ds_write_addtid_b32 v61 offset:0\n" \
    "s_mov_b32 m0, %[m0a]\n" \
    "v_pk_mul_f32 v[18:19], v[12:13], v[22:23] op_sel_hi:[0,1]\n" \
    "v_cvt_pk_bf16_f32 v31, v20, v21\n" \
    "v_pk_fma_f32 v[22:23], v[22:23], v[12:13], v[18:19] op_sel:[1,1,0] op_sel_hi:[0,1,1] neg_lo:[0,1,0]\n" \
    "ds_write_addtid_b32 v31 offset:33280\n" \
    "v_pk_mul_f32 v[18:19], v[12:13], v[24:25] op_sel_hi:[0,1]\n" \
    "v_cvt_pk_bf16_f32 v30, v22, v23\n" \
    "v_pk_fma_f32 v[24:25], v[24:25], v[12:13], v[18:19] op_sel:[1,1,0] op_sel_hi:[0,1,1] neg_lo:[0,1,0]\n" \
    "ds_write_addtid_b32 v30 offset:35360\n" \
    "v_pk_mul_f32 v[18:19], v[12:13], v[26:27] op_sel_hi:[0,1]\n" \
    "v_cvt_pk_bf16_f32 v31, v24, v25\n" \
    "v_pk_fma_f32 v[26:27], v[26:27], v[12:13], v[18:19] op_sel:[1,1,0] op_sel_hi:[0,1,1] neg_lo:[0,1,0]\n" \
    "ds_write_addtid_b32 v31 offset:37440\n" \
    "v_pk_mul_f32 v[18:19], v[10:11], v[14:15] op_sel_hi:[0,1]\n" \
    "v_cvt_pk_bf16_f32 v30, v26, v27\n" \
    "v_pk_fma_f32 v[28:29], v[14:15], v[10:11], v[18:19] op_sel:[1,1,0] op_sel_hi:[0,1,1] neg_lo:[0,1,0]\n" \
    "ds_write_addtid_b32 v30 offset:39520\n" \
    "v_pk_mul_f32 v[18:19], v[12:13], v[28:29] op_sel_hi:[0,1]\n" \
    "v_pk_fma_f32 v[28:29], v[28:29], v[12:13], v[18:19] op_sel:[1,1,0] op_sel_hi:[0,1,1] neg_lo:[0,1,0]\n" \
    "v_pk_mul_f32 v[18:19], v[12:13], v[20:21] op_sel_hi:[0,1]\n" \
    "v_cvt_pk_bf16_f32 v31, v28, v29\n" \
    "v_pk_fma_f32 v[20:21], v[20:21], v[12:13], v[18:19] op_sel:[1,1,0] op_sel_hi:[0,1,1] neg_lo:[0,1,0]\n" \
    "ds_write_addtid_b32 v31 offset:41600\n" \
    "v_pk_mul_f32 v[18:19], v[12:13], v[22:23] op_sel_hi:[0,1]\n" \
    "v_cvt_pk_bf16_f32 v30, v20, v21\n" \
    "v_pk_fma_f32 v[22:23], v[22:23], v[12:13], v[18:19] op_sel:[1,1,0] op_sel_hi:[0,1,1] neg_lo:[0,1,0]\n" \
    "ds_write_addtid_b32 v30 offset:43680\n" \
    "v_pk_mul_f32 v[18:19], v[12:13], v[24:25] op_sel_hi:[0,1]\n" \
    "v_cvt_pk_bf16_f32 v31, v22, v23\n" \
    "v_pk_fma_f32 v[24:25], v[24:25], v[12:13], v[18:19] op_sel:[1,1,0] op_sel_hi:[0,1,1] neg_lo:[0,1,0]\n" \
    "ds_write_addtid_b32 v31 offset:45760\n" \
    "v_pk_mul_f32 v[18:19], v[12:13], v[26:27] op_sel_hi:[0,1]\n" \
    "v_cvt_pk_bf16_f32 v30, v24, v25\n" \
    "v_pk_fma_f32 v[26:27], v[26:27], v[12:13], v[18:19] op_sel:[1,1,0] op_sel_hi:[0,1,1] neg_lo:[0,1,0]\n" \
    "ds_write_addtid_b32 v30 offset:47840\n" \
    "v_pk_mul_f32 v[18:19], v[12:13], v[28:29] op_sel_hi:[0,1]\n" \
    "v_cvt_pk_bf16_f32 v31, v26, v27\n" \
    "v_pk_fma_f32 v[28:29], v[28:29], v[12:13], v[18:19] op_sel:[1,1,0] op_sel_hi:[0,1,1] neg_lo:[0,1,0]\n" \
    "ds_write_addtid_b32 v31 offset:49920\n" \
    "v_pk_mul_f32 v[18:19], v[12:13], v[24:25] op_sel_hi:[0,1]\n" \
    "v_cvt_pk_bf16_f32 v30, v28, v29\n" \
    "v_pk_fma_f32 v[24:25], v[24:25], v[12:13], v[18:19] op_sel:[1,1,0] op_sel_hi:[0,1,1] neg_lo:[0,1,0]\n" \
    "ds_write_addtid_b32 v30 offset:52000\n" \
    "v_mov_b32_e32 v61, 3\n" \
    "ds_write_addtid_b32 v61 offset:54080\n" \
    "s_add_u32 m0, %[m0a], 62400\n" \
    "v_pk_mul_f32 v[18:19], v[12:13], v[28:29] op_sel_hi:[0,1]\n" \
    "v_cvt_pk_bf16_f32 v31, v24, v25\n" \
    "v_pk_fma_f32 v[28:29], v[28:29], v[12:13], v[18:19] op_sel:[1,1,0] op_sel_hi:[0,1,1] neg_lo:[0,1,0]\n" \
    "ds_write_addtid_b32 v31 offset:29120\n" \
    "v_pk_mul_f32 v[18:19], v[12:13], v[24:25] op_sel_hi:[0,1]\n" \
    "v_cvt_pk_bf16_f32 v30, v28, v29\n" \
    "v_pk_fma_f32 v[24:25], v[24:25], v[12:13], v[18:19] op_sel:[1,1,0] op_sel_hi:[0,1,1] neg_lo:[0,1,0]\n" \
    "ds_write_addtid_b32 v30 offset:37440\n" \
    "v_pk_mul_f32 v[18:19], v[12:13], v[28:29] op_sel_hi:[0,1]\n" \
    "v_cvt_pk_bf16_f32 v31, v24, v25\n" \
    "v_pk_fma_f32 v[28:29], v[28:29], v[12:13], v[18:19] op_sel:[1,1,0] op_sel_hi:[0,1,1] neg_lo:[0,1,0]\n" \
    "ds_write_addtid_b32 v31 offset:31200\n" \
    "v_pk_mul_f32 v[18:19], v[12:13], v[24:25] op_sel_hi:[0,1]\n" \
    "v_cvt_pk_bf16_f32 v30, v28, v29\n" \
    "v_pk_fma_f32 v[24:25], v[24:25], v[12:13], v[18:19] op_sel:[1,1,0] op_sel_hi:[0,1,1] neg_lo:[0,1,0]\n" \
    "ds_write_addtid_b32 v30 offset:39520\n" \
    "v_pk_mul_f32 v[18:19], v[12:13], v[28:29] op_sel_hi:[0,1]\n" \
    "v_cvt_pk_bf16_f32 v31, v24, v25\n" \
    "v_pk_fma_f32 v[28:29], v[28:29], v[12:13], v[18:19] op_sel:[1,1,0] op_sel_hi:[0,1,1] neg_lo:[0,1,0]\n" \
    "ds_write_addtid_b32 v31 offset:33280\n" \
    "v_pk_mul_f32 v[18:19], v[12:13], v[24:25] op_sel_hi:[0,1]\n" \
    "v_cvt_pk_bf16_f32 v30, v28, v29\n" \
    "v_pk_fma_f32 v[24:25], v[24:25], v[12:13], v[18:19] op_sel:[1,1,0] op_sel_hi:[0,1,1] neg_lo:[0,1,0]\n" \
    "ds_write_addtid_b32 v30 offset:41600\n" \
    "v_pk_mul_f32 v[18:19], v[12:13], v[28:29] op_sel_hi:[0,1]\n" \
    "v_cvt_pk_bf16_f32 v31, v24, v25\n" \
    "v_pk_fma_f32 v[28:29], v[28:29], v[12:13], v[18:19] op_sel:[1,1,0] op_sel_hi:[0,1,1] neg_lo:[0,1,0]\n" \
    "ds_write_addtid_b32 v31 offset:35360\n" \
    "v_pk_mul_f32 v[18:19], v[12:13], v[28:29] op_sel_hi:[0,1]\n" \
    "v_cvt_pk_bf16_f32 v30, v28, v29\n" \
    "v_pk_fma_f32 v[28:29], v[28:29], v[12:13], v[18:19] op_sel:[1,1,0] op_sel_hi:[0,1,1] neg_lo:[0,1,0]\n" \
    "ds_write_addtid_b32 v30 offset:43680\n" \
    "v_pk_mul_f32 v[18:19], v[12:13], v[28:29] op_sel_hi:[0,1]\n" \
    "v_cvt_pk_bf16_f32 v31, v28, v29\n" \
    "v_pk_fma_f32 v[28:29], v[28:29], v[12:13], v[18:19] op_sel:[1,1,0] op_sel_hi:[0,1,1] neg_lo:[0,1,0]\n" \
    "ds_write_addtid_b32 v31 offset:45760\n" \
    "v_pk_mul_f32 v[18:19], v[12:13], v[28:29] op_sel_hi:[0,1]\n" \
    "v_cvt_pk_bf16_f32 v30, v28, v29\n" \
    "v_pk_fma_f32 v[28:29], v[28:29], v[12:13], v[18:19] op_sel:[1,1,0] op_sel_hi:[0,1,1] neg_lo:[0,1,0]\n" \
    "ds_write_addtid_b32 v30 offset:47840\n" \
    "ds_read_b32 v60, v32\n" \
    "v_pk_mul_f32 v[18:19], v[12:13], v[28:29] op_sel_hi:[0,1]\n" \
    "v_cvt_pk_bf16_f32 v31, v28, v29\n" \
    "v_pk_fma_f32 v[28:29], v[28:29], v[12:13], v[18:19] op_sel:[1,1,0] op_sel_hi:[0,1,1] neg_lo:[0,1,0]\n" \
    "ds_write_addtid_b32 v31 offset:49920\n" \
    "v_pk_mul_f32 v[18:19], v[12:13], v[28:29] op_sel_hi:[0,1]\n" \
    "v_cvt_pk_bf16_f32 v30, v28, v29\n" \
    "v_pk_fma_f32 v[28:29], v[28:29], v[12:13], v[18:19] op_sel:[1,1,0] op_sel_hi:[0,1,1] neg_lo:[0,1,0]\n" \
    "ds_write_addtid_b32 v30 offset:52000\n" \
    "v_pk_mul_f32 v[18:19], v[12:13], v[28:29] op_sel_hi:[0,1]\n" \
    "v_cvt_pk_bf16_f32 v31, v28, v29\n" \
    "v_pk_fma_f32 v[28:29], v[28:29], v[12:13], v[18:19] op_sel:[1,1,0] op_sel_hi:[0,1,1] neg_lo:[0,1,0]\n" \
    "ds_write_addtid_b32 v31 offset:54080\n" \
    "s_waitcnt lgkmcnt(3)\n" \
    "v_cmp_gt_u32_e32 vcc, 1, v60\n" \
    "s_cbranch_vccz Lae0_%=\n" \
    "s_mov_b32 s90, 0x100000\n" \
    "Lal0_%=:\n" \
    "s_sleep 1\n" \
    "ds_read_b32 v60, v32 offset:0\n" \
    "s_waitcnt lgkmcnt(0)\n" \
    "v_cmp_gt_u32_e32 vcc, 1, v60\n" \
    "s_cbranch_vccz Lae0_%=\n" \
    "s_sub_u32 s90, s90, 1\n" \
    "s_cbranch_scc0 Lal0_%=\n" \
    "Lae0_%=:\n" \
    "ds_read2_b32 v[64:65], v36 offset0:240 offset1:241\n" \
    "ds_read2_b32 v[66:67], v36 offset0:242 offset1:243\n" \
    "ds_read2_b32 v[68:69], v36 offset0:224 offset1:225\n" \
    "ds_read2_b32 v[70:71], v36 offset0:226 offset1:227\n" \
    "v_pk_mul_f32 v[18:19], v[12:13], v[28:29] op_sel_hi:[0,1]\n" \
    "v_cvt_pk_bf16_f32 v30, v28, v29\n" \
    "v_pk_fma_f32 v[28:29], v[28:29], v[12:13], v[18:19] op_sel:[1,1,0] op_sel_hi:[0,1,1] neg_lo:[0,1,0]\n" \
    "ds_write_addtid_b32 v30 offset:56160\n" \
    "ds_read2_b32 v[72:73], v36 offset0:208 offset1:209\n" \
    "ds_read2_b32 v[74:75], v36 offset0:210 offset1:211\n" \
    "ds_read2_b32 v[76:77], v36 offset0:192 offset1:193\n" \
    "ds_read2_b32 v[78:79], v36 offset0:194 offset1:195\n" \
    "v_pk_mul_f32 v[18:19], v[12:13], v[28:29] op_sel_hi:[0,1]\n" \
    "v_cvt_pk_bf16_f32 v31, v28, v29\n" \
    "v_pk_fma_f32 v[28:29], v[28:29], v[12:13], v[18:19] op_sel:[1,1,0] op_sel_hi:[0,1,1] neg_lo:[0,1,0]\n" \
    "ds_write_addtid_b32 v31 offset:58240\n" \
    "ds_read2_b32 v[80:81], v36 offset0:176 offset1:177\n" \
    "ds_read2_b32 v[82:83], v36 offset0:178 offset1:179\n" \
    "ds_read2_b32 v[84:85], v36 offset0:160 offset1:161\n" \
    "ds_read2_b32 v[86:87], v36 offset0:162 offset1:163\n" \
    "v_cvt_pk_bf16_f32 v30, v28, v29\n" \
    "ds_write_addtid_b32 v30 offset:60320\n" \
    "s_mov_b32 m0, %[m0a]\n" \
    "v_mov_b32_e32 v61, 4\n" \
    "ds_write_addtid_b32 v61 offset:54080\n" \
    "ds_read2_b32 v[88:89], v36 offset0:144 offset1:145\n" \
    "ds_read2_b32 v[90:91], v36 offset0:146 offset1:147\n" \
    "ds_read2_b32 v[92:93], v36 offset0:128 offset1:129\n" \
    "ds_read2_b32 v[94:95], v36 offset0:130 offset1:131\n" \
    "s_setprio 3\n" \
    "s_mov_b32 s90, 0x100000\n" \
    "Lal1_%=:\n" \
    "ds_read_b32 v62, v34 offset:0\n" \
    "s_waitcnt lgkmcnt(0)\n" \
    "v_cmp_gt_u32_e32 vcc, 1, v62\n" \
    "s_cbranch_vccz Lae1_%=\n" \
    "s_sleep 1\n" \
    "s_sub_u32 s90, s90, 1\n" \
    "s_cbranch_scc0 Lal1_%=\n" \
    "Lae1_%=:\n" \
    "ds_read_b128 v[156:159], v33 offset:448\n" \
    "ds_read_b128 v[152:155], v33 offset:384\n" \
    "ds_read_b128 v[148:151], v33 offset:320\n" \
    "ds_read_b128 v[144:147], v33 offset:256\n" \
    "ds_read_b128 v[140:143], v33 offset:192\n" \
    "ds_read_b128 v[136:139], v33 offset:128\n" \
    "ds_read_b128 v[132:135], v33 offset:64\n" \
    "ds_read_b128 v[128:131], v33 offset:0\n" \
    "ds_read_b32 v60, v32\n" \
    "s_waitcnt lgkmcnt(8)\n" \
    "v_mfma_f32_16x16x32_bf16 v[0:3], v[64:67], v[156:159], 0\n" \
    "s_waitcnt lgkmcnt(7)\n" \
    "v_mfma_f32_16x16x32_bf16 v[0:3], v[68:71], v[152:155], v[0:3]\n" \
    "s_waitcnt lgkmcnt(6)\n" \
    "v_mfma_f32_16x16x32_bf16 v[0:3], v[72:75], v[148:151], v[0:3]\n" \
    "s_waitcnt lgkmcnt(0)\n" \
    "v_cmp_gt_u32_e32 vcc, 2, v60\n" \
    "s_cbranch_vccz Lae2_%=\n" \
    "s_mov_b32 s90, 0x100000\n" \
    "Lal2_%=:\n" \
    "s_sleep 1\n" \
    "ds_read_b32 v60, v32 offset:0\n" \
    "s_waitcnt lgkmcnt(0)\n" \
    "v_cmp_gt_u32_e32 vcc, 2, v60\n" \
    "s_cbranch_vccz Lae2_%=\n" \
    "s_sub_u32 s90, s90, 1\n" \
    "s_cbranch_scc0 Lal2_%=\n" \
    "Lae2_%=:\n" \
    "v_mfma_f32_16x16x32_bf16 v[0:3], v[76:79], v[144:147], v[0:3]\n" \
    "ds_read2_b32 v[96:97], v36 offset0:112 offset1:113\n" \
    "ds_read2_b32 v[98:99], v36 offset0:114 offset1:115\n" \
    "ds_read2_b32 v[100:101], v36 offset0:96 offset1:97\n" \
    "ds_read2_b32 v[102:103], v36 offset0:98 offset1:99\n" \
    "v_mfma_f32_16x16x32_bf16 v[0:3], v[80:83], v[140:143], v[0:3]\n" \
    "ds_read2_b32 v[104:105], v36 offset0:80 offset1:81\n" \
    "ds_read2_b32 v[106:107], v36 offset0:82 offset1:83\n" \
    "ds_read2_b32 v[108:109], v36 offset0:64 offset1:65\n" \
    "ds_read2_b32 v[110:111], v36 offset0:66 offset1:67\n" \
    "v_mfma_f32_16x16x32_bf16 v[0:3], v[84:87], v[136:139], v[0:3]\n" \
    "ds_read2_b32 v[112:113], v36 offset0:48 offset1:49\n" \
    "ds_read2_b32 v[114:115], v36 offset0:50 offset1:51\n" \
    "ds_read2_b32 v[116:117], v36 offset0:32 offset1:33\n" \
    "ds_read2_b32 v[118:119], v36 offset0:34 offset1:35\n" \
    "v_mfma_f32_16x16x32_bf16 v[0:3], v[88:91], v[132:135], v[0:3]\n" \
    "ds_read2_b32 v[120:121], v36 offset0:16 offset1:17\n" \
    "ds_read2_b32 v[122:123], v36 offset0:18 offset1:19\n" \
    "ds_read2_b32 v[124:125], v36 offset0:0 offset1:1\n" \
    "ds_read2_b32 v[126:127], v36 offset0:2 offset1:3\n" \
    "v_mfma_f32_16x16x32_bf16 v[0:3], v[92:95], v[128:131], v[0:3]\n" \
    "s_waitcnt lgkmcnt(14)\n" \
    "v_mfma_f32_16x16x32_bf16 v[4:7], v[96:99], v[156:159], 0\n" \
    "ds_read_b128 v[156:159], v33 offset:960\n" \
    "s_waitcnt lgkmcnt(13)\n" \
    "v_mfma_f32_16x16x32_bf16 v[4:7], v[100:103], v[152:155], v[4:7]\n" \
    "ds_read_b128 v[152:155], v33 offset:896\n" \
    "ds_read_b32 v62, v34\n" \
    "s_waitcnt lgkmcnt(13)\n" \
    "v_mfma_f32_16x16x32_bf16 v[4:7], v[104:107], v[148:151], v[4:7]\n" \
    "ds_read_b128 v[148:151], v33 offset:832\n" \
    "s_waitcnt lgkmcnt(12)\n" \
    "v_mfma_f32_16x16x32_bf16 v[4:7], v[108:111], v[144:147], v[4:7]\n" \
    "ds_read_b128 v[144:147], v33 offset:768\n" \
    "s_waitcnt lgkmcnt(11)\n" \
    "v_mfma_f32_16x16x32_bf16 v[4:7], v[112:115], v[140:143], v[4:7]\n" \
    "ds_read_b128 v[140:143], v33 offset:704\n" \
    "s_waitcnt lgkmcnt(10)\n" \
    "v_mfma_f32_16x16x32_bf16 v[4:7], v[116:119], v[136:139], v[4:7]\n" \
    "ds_read_b128 v[136:139], v33 offset:640\n" \
    "s_waitcnt lgkmcnt(9)\n" \
    "v_mfma_f32_16x16x32_bf16 v[4:7], v[120:123], v[132:135], v[4:7]\n" \
    "ds_read_b128 v[132:135], v33 offset:576\n" \
    "s_waitcnt lgkmcnt(8)\n" \
    "v_mfma_f32_16x16x32_bf16 v[4:7], v[124:127], v[128:131], v[4:7]\n" \
    "ds_read_b128 v[128:131], v33 offset:512\n" \
    "s_waitcnt lgkmcnt(6)\n" \
    "v_cmp_gt_u32_e32 vcc, 3, v62\n" \
    "s_cbranch_vccnz Las3_%=\n" \
    "Lae3_%=:\n" \
    "s_waitcnt lgkmcnt(4)\n" \
    "v_mfma_f32_16x16x32_bf16 v[4:7], v[64:67], v[156:159], v[4:7]\n" \
    "v_mfma_f32_16x16x32_bf16 v[8:11], v[96:99], v[156:159], 0\n" \
    "ds_read_b128 v[156:159], v33 offset:1472\n" \
    "v_mfma_f32_16x16x32_bf16 v[4:7], v[68:71], v[152:155], v[4:7]\n" \
    "v_mfma_f32_16x16x32_bf16 v[8:11], v[100:103], v[152:155], v[8:11]\n" \
    "ds_read_b128 v[152:155], v33 offset:1408\n" \
    "v_mov_b32_e64 v61, 11\n" \
    "v_mfma_f32_16x16x32_bf16 v[4:7], v[72:75], v[148:151], v[4:7]\n" \
    "ds_write_b128 v37, v[0:3] offset:0\n" \
    "ds_write_addtid_b32 v61 offset:54080\n" \
    "v_mfma_f32_16x16x32_bf16 v[8:11], v[104:107], v[148:151], v[8:11]\n" \
    "ds_read_b128 v[148:151], v33 offset:1344\n" \
    "v_mfma_f32_16x16x32_bf16 v[4:7], v[76:79], v[144:147], v[4:7]\n" \
    "v_mfma_f32_16x16x32_bf16 v[8:11], v[108:111], v[144:147], v[8:11]\n" \
    "ds_read_b128 v[144:147], v33 offset:1280\n" \
    "s_waitcnt lgkmcnt(6)\n" \
    "v_mfma_f32_16x16x32_bf16 v[4:7], v[80:83], v[140:143], v[4:7]\n" \
    "v_mfma_f32_16x16x32_bf16 v[8:11], v[112:115], v[140:143], v[8:11]\n" \
    "ds_read_b128 v[140:143], v33 offset:1216\n" \
    "v_mfma_f32_16x16x32_bf16 v[4:7], v[84:87], v[136:139], v[4:7]\n" \
    "v_mfma_f32_16x16x32_bf16 v[8:11], v[116:119], v[136:139], v[8:11]\n" \
    "ds_read_b128 v[136:139], v33 offset:1152\n" \
    "v_mfma_f32_16x16x32_bf16 v[4:7], v[88:91], v[132:135], v[4:7]\n" \
    "v_mfma_f32_16x16x32_bf16 v[8:11], v[120:123], v[132:135], v[8:11]\n" \
    "ds_read_b128 v[132:135], v33 offset:1088\n" \
    "v_mfma_f32_16x16x32_bf16 v[4:7], v[92:95], v[128:131], v[4:7]\n" \
    "v_mfma_f32_16x16x32_bf16 v[8:11], v[124:127], v[128:131], v[8:11]\n" \
    "ds_read_b128 v[128:131], v33 offset:1024\n" \
    "s_waitcnt lgkmcnt(4)\n" \
    "v_mfma_f32_16x16x32_bf16 v[8:11], v[64:67], v[156:159], v[8:11]\n" \
    "v_mfma_f32_16x16x32_bf16 v[12:15], v[96:99], v[156:159], 0\n" \
    "ds_read_b128 v[156:159], v33 offset:1984\n" \
    "v_mfma_f32_16x16x32_bf16 v[8:11], v[68:71], v[152:155], v[8:11]\n" \
    "v_mfma_f32_16x16x32_bf16 v[12:15], v[100:103], v[152:155], v[12:15]\n" \
    "ds_read_b128 v[152:155], v33 offset:1920\n" \
    "v_mov_b32_e64 v61, 12\n" \
    "v_mfma_f32_16x16x32_bf16 v[8:11], v[72:75], v[148:151], v[8:11]\n" \
    "ds_write_b128 v37, v[4:7] offset:4160\n" \
    "ds_write_addtid_b32 v61 offset:54080\n" \
    "v_mfma_f32_16x16x32_bf16 v[12:15], v[104:107], v[148:151], v[12:15]\n" \
    "ds_read_b128 v[148:151], v33 offset:1856\n" \
    "v_mfma_f32_16x16x32_bf16 v[8:11], v[76:79], v[144:147], v[8:11]\n" \
    "v_mfma_f32_16x16x32_bf16 v[12:15], v[108:111], v[144:147], v[12:15]\n" \
    "ds_read_b128 v[144:147], v33 offset:1792\n" \
    "s_waitcnt lgkmcnt(6)\n" \
    "v_mfma_f32_16x16x32_bf16 v[8:11], v[80:83], v[140:143], v[8:11]\n" \
    "v_mfma_f32_16x16x32_bf16 v[12:15], v[112:115], v[140:143], v[12:15]\n" \
    "ds_read_b128 v[140:143], v33 offset:1728\n" \
    "v_mfma_f32_16x16x32_bf16 v[8:11], v[84:87], v[136:139], v[8:11]\n" \
    "v_mfma_f32_16x16x32_bf16 v[12:15], v[116:119], v[136:139], v[12:15]\n" \
    "ds_read_b128 v[136:139], v33 offset:1664\n" \
    "v_mfma_f32_16x16x32_bf16 v[8:11], v[88:91], v[132:135], v[8:11]\n" \
    "ds_read_b32 v62, v34\n" \
    "v_mfma_f32_16x16x32_bf16 v[12:15], v[120:123], v[132:135], v[12:15]\n" \
    "ds_read_b128 v[132:135], v33 offset:1600\n" \
    "v_mfma_f32_16x16x32_bf16 v[8:11], v[92:95], v[128:131], v[8:11]\n" \
    "v_mfma_f32_16x16x32_bf16 v[12:15], v[124:127], v[128:131], v[12:15]\n" \
    "ds_read_b128 v[128:131], v33 offset:1536\n" \
    "s_waitcnt lgkmcnt(2)\n" \
    "v_cmp_gt_u32_e32 vcc, 5, v62\n" \
    "s_cbranch_vccnz Las4_%=\n" \
    "Lae4_%=:\n" \
    "v_mfma_f32_16x16x32_bf16 v[12:15], v[64:67], v[156:159], v[12:15]\n" \
    "v_mfma_f32_16x16x32_bf16 v[16:19], v[96:99], v[156:159], 0\n" \
    "ds_read_b128 v[156:159], v33 offset:2496\n" \
    "v_mfma_f32_16x16x32_bf16 v[12:15], v[68:71], v[152:155], v[12:15]\n" \
    "v_mfma_f32_16x16x32_bf16 v[16:19], v[100:103], v[152:155], v[16:19]\n" \
    "ds_read_b128 v[152:155], v33 offset:2432\n" \
    "v_mov_b32_e64 v61, 13\n" \
    "v_mfma_f32_16x16x32_bf16 v[12:15], v[72:75], v[148:151], v[12:15]\n" \
    "ds_write_b128 v37, v[8:11] offset:8320\n" \
    "ds_write_addtid_b32 v61 offset:54080\n" \
    "v_mfma_f32_16x16x32_bf16 v[16:19], v[104:107], v[148:151], v[16:19]\n" \
    "ds_read_b128 v[148:151], v33 offset:2368\n" \
    "v_mfma_f32_16x16x32_bf16 v[12:15], v[76:79], v[144:147], v[12:15]\n" \
    "v_mfma_f32_16x16x32_bf16 v[16:19], v[108:111], v[144:147], v[16:19]\n" \
    "ds_read_b128 v[144:147], v33 offset:2304\n" \
    "s_waitcnt lgkmcnt(6)\n" \
    "v_mfma_f32_16x16x32_bf16 v[12:15], v[80:83], v[140:143], v[12:15]\n" \
    "v_mfma_f32_16x16x32_bf16 v[16:19], v[112:115], v[140:143], v[16:19]\n" \
    "ds_read_b128 v[140:143], v33 offset:2240\n" \
    "v_mfma_f32_16x16x32_bf16 v[12:15], v[84:87], v[136:139], v[12:15]\n" \
    "v_mfma_f32_16x16x32_bf16 v[16:19], v[116:119], v[136:139], v[16:19]\n" \
    "ds_read_b128 v[136:139], v33 offset:2176\n" \
    "v_mfma_f32_16x16x32_bf16 v[12:15], v[88:91], v[132:135], v[12:15]\n" \
    "v_mfma_f32_16x16x32_bf16 v[16:19], v[120:123], v[132:135], v[16:19]\n" \
    "ds_read_b128 v[132:135], v33 offset:2112\n" \
    "v_mfma_f32_16x16x32_bf16 v[12:15], v[92:95], v[128:131], v[12:15]\n" \
    "v_mfma_f32_16x16x32_bf16 v[16:19], v[124:127], v[128:131], v[16:19]\n" \
    "ds_read_b128 v[128:131], v33 offset:2048\n" \
    "s_waitcnt lgkmcnt(4)\n" \
    "v_mfma_f32_16x16x32_bf16 v[16:19], v[64:67], v[156:159], v[16:19]\n" \
    "v_mfma_f32_16x16x32_bf16 v[20:23], v[96:99], v[156:159], 0\n" \
    "ds_read_b128 v[156:159], v33 offset:3008\n" \
    "v_mfma_f32_16x16x32_bf16 v[16:19], v[68:71], v[152:155], v[16:19]\n" \
    "v_mfma_f32_16x16x32_bf16 v[20:23], v[100:103], v[152:155], v[20:23]\n" \
    "ds_read_b128 v[152:155], v33 offset:2944\n" \
    "v_mov_b32_e64 v61, 14\n" \
    "v_mfma_f32_16x16x32_bf16 v[16:19], v[72:75], v[148:151], v[16:19]\n" \
    "ds_write_b128 v37, v[12:15] offset:12480\n" \
    "ds_write_addtid_b32 v61 offset:54080\n" \
    "v_mfma_f32_16x16x32_bf16 v[20:23], v[104:107], v[148:151], v[20:23]\n" \
    "ds_read_b128 v[148:151], v33 offset:2880\n" \
    "v_mfma_f32_16x16x32_bf16 v[16:19], v[76:79], v[144:147], v[16:19]\n" \
    "v_mfma_f32_16x16x32_bf16 v[20:23], v[108:111], v[144:147], v[20:23]\n" \
    "ds_read_b128 v[144:147], v33 offset:2816\n" \
    "s_waitcnt lgkmcnt(6)\n" \
    "v_mfma_f32_16x16x32_bf16 v[16:19], v[80:83], v[140:143], v[16:19]\n" \
    "v_mfma_f32_16x16x32_bf16 v[20:23], v[112:115], v[140:143], v[20:23]\n" \
    "ds_read_b128 v[140:143], v33 offset:2752\n" \
    "v_mfma_f32_16x16x32_bf16 v[16:19], v[84:87], v[136:139], v[16:19]\n" \
    "v_mfma_f32_16x16x32_bf16 v[20:23], v[116:119], v[136:139], v[20:23]\n" \
    "ds_read_b128 v[136:139], v33 offset:2688\n" \
    "v_mfma_f32_16x16x32_bf16 v[16:19], v[88:91], v[132:135], v[16:19]\n" \
    "ds_read_b32 v62, v34\n" \
    "v_mfma_f32_16x16x32_bf16 v[20:23], v[120:123], v[132:135], v[20:23]\n" \
    "ds_read_b128 v[132:135], v33 offset:2624\n" \
    "v_mfma_f32_16x16x32_bf16 v[16:19], v[92:95], v[128:131], v[16:19]\n" \
    "v_mfma_f32_16x16x32_bf16 v[20:23], v[124:127], v[128:131], v[20:23]\n" \
    "ds_read_b128 v[128:131], v33 offset:2560\n" \
    "s_waitcnt lgkmcnt(2)\n" \
    "v_cmp_gt_u32_e32 vcc, 7, v62\n" \
    "s_cbranch_vccnz Las5_%=\n" \
    "Lae5_%=:\n" \
    "v_mfma_f32_16x16x32_bf16 v[20:23], v[64:67], v[156:159], v[20:23]\n" \
    "v_mfma_f32_16x16x32_bf16 v[24:27], v[96:99], v[156:159], 0\n" \
    "ds_read_b128 v[156:159], v33 offset:3520\n" \
    "v_mfma_f32_16x16x32_bf16 v[20:23], v[68:71], v[152:155], v[20:23]\n" \
    "v_mfma_f32_16x16x32_bf16 v[24:27], v[100:103], v[152:155], v[24:27]\n" \
    "ds_read_b128 v[152:155], v33 offset:3456\n" \
    "v_mov_b32_e64 v61, 15\n" \
    "v_mfma_f32_16x16x32_bf16 v[20:23], v[72:75], v[148:151], v[20:23]\n" \
    "ds_write_b128 v37, v[16:19] offset:16640\n" \
    "ds_write_addtid_b32 v61 offset:54080\n" \
    "v_mfma_f32_16x16x32_bf16 v[24:27], v[104:107], v[148:151], v[24:27]\n" \
    "ds_read_b128 v[148:151], v33 offset:3392\n" \
    "v_mfma_f32_16x16x32_bf16 v[20:23], v[76:79], v[144:147], v[20:23]\n" \
    "v_mfma_f32_16x16x32_bf16 v[24:27], v[108:111], v[144:147], v[24:27]\n" \
    "ds_read_b128 v[144:147], v33 offset:3328\n" \
    "s_waitcnt lgkmcnt(6)\n" \
    "v_mfma_f32_16x16x32_bf16 v[20:23], v[80:83], v[140:143], v[20:23]\n" \
    "v_mfma_f32_16x16x32_bf16 v[24:27], v[112:115], v[140:143], v[24:27]\n" \
    "ds_read_b128 v[140:143], v33 offset:3264\n" \
    "v_mfma_f32_16x16x32_bf16 v[20:23], v[84:87], v[136:139], v[20:23]\n" \
    "v_mfma_f32_16x16x32_bf16 v[24:27], v[116:119], v[136:139], v[24:27]\n" \
    "ds_read_b128 v[136:139], v33 offset:3200\n" \
    "v_mfma_f32_16x16x32_bf16 v[20:23], v[88:91], v[132:135], v[20:23]\n" \
    "v_mfma_f32_16x16x32_bf16 v[24:27], v[120:123], v[132:135], v[24:27]\n" \
    "ds_read_b128 v[132:135], v33 offset:3136\n" \
    "v_mfma_f32_16x16x32_bf16 v[20:23], v[92:95], v[128:131], v[20:23]\n" \
    "v_mfma_f32_16x16x32_bf16 v[24:27], v[124:127], v[128:131], v[24:27]\n" \
    "ds_read_b128 v[128:131], v33 offset:3072\n" \
    "s_waitcnt lgkmcnt(4)\n" \
    "v_mfma_f32_16x16x32_bf16 v[24:27], v[64:67], v[156:159], v[24:27]\n" \
    "v_mfma_f32_16x16x32_bf16 v[28:31], v[96:99], v[156:159], 0\n" \
    "ds_read_b128 v[156:159], v33 offset:4032\n" \
    "v_mfma_f32_16x16x32_bf16 v[24:27], v[68:71], v[152:155], v[24:27]\n" \
    "v_mfma_f32_16x16x32_bf16 v[28:31], v[100:103], v[152:155], v[28:31]\n" \
    "ds_read_b128 v[152:155], v33 offset:3968\n" \
    "v_mov_b32_e64 v61, 16\n" \
    "v_mfma_f32_16x16x32_bf16 v[24:27], v[72:75], v[148:151], v[24:27]\n" \
    "ds_write_b128 v37, v[20:23] offset:20800\n" \
    "ds_write_addtid_b32 v61 offset:54080\n" \
    "v_mfma_f32_16x16x32_bf16 v[28:31], v[104:107], v[148:151], v[28:31]\n" \
    "ds_read_b128 v[148:151], v33 offset:3904\n" \
    "v_mfma_f32_16x16x32_bf16 v[24:27], v[76:79], v[144:147], v[24:27]\n" \
    "v_mfma_f32_16x16x32_bf16 v[28:31], v[108:111], v[144:147], v[28:31]\n" \
    "ds_read_b128 v[144:147], v33 offset:3840\n" \
    "s_waitcnt lgkmcnt(6)\n" \
    "v_mfma_f32_16x16x32_bf16 v[24:27], v[80:83], v[140:143], v[24:27]\n" \
    "v_mfma_f32_16x16x32_bf16 v[28:31], v[112:115], v[140:143], v[28:31]\n" \
    "ds_read_b128 v[140:143], v33 offset:3776\n" \
    "v_mfma_f32_16x16x32_bf16 v[24:27], v[84:87], v[136:139], v[24:27]\n" \
    "v_mfma_f32_16x16x32_bf16 v[28:31], v[116:119], v[136:139], v[28:31]\n" \
    "ds_read_b128 v[136:139], v33 offset:3712\n" \
    "v_mfma_f32_16x16x32_bf16 v[24:27], v[88:91], v[132:135], v[24:27]\n" \
    "v_mfma_f32_16x16x32_bf16 v[28:31], v[120:123], v[132:135], v[28:31]\n" \
    "ds_read_b128 v[132:135], v33 offset:3648\n" \
    "v_mfma_f32_16x16x32_bf16 v[24:27], v[92:95], v[128:131], v[24:27]\n" \
    "v_mfma_f32_16x16x32_bf16 v[28:31], v[124:127], v[128:131], v[28:31]\n" \
    "ds_read_b128 v[128:131], v33 offset:3584\n" \
    "s_waitcnt lgkmcnt(4)\n" \
    "v_mfma_f32_16x16x32_bf16 v[28:31], v[64:67], v[156:159], v[28:31]\n" \
    "v_mfma_f32_16x16x32_bf16 v[28:31], v[68:71], v[152:155], v[28:31]\n" \
    "v_mfma_f32_16x16x32_bf16 v[28:31], v[72:75], v[148:151], v[28:31]\n" \
    "v_mov_b32_e64 v61, 17\n" \
    "v_mfma_f32_16x16x32_bf16 v[28:31], v[76:79], v[144:147], v[28:31]\n" \
    "ds_write_b128 v37, v[24:27] offset:24960\n" \
    "ds_write_addtid_b32 v61 offset:54080\n" \
    "s_waitcnt lgkmcnt(2)\n" \
    "v_mfma_f32_16x16x32_bf16 v[28:31], v[80:83], v[140:143], v[28:31]\n" \
    "v_mfma_f32_16x16x32_bf16 v[28:31], v[84:87], v[136:139], v[28:31]\n" \
    "v_mfma_f32_16x16x32_bf16 v[28:31], v[88:91], v[132:135], v[28:31]\n" \
    "v_mfma_f32_16x16x32_bf16 v[28:31], v[92:95], v[128:131], v[28:31]\n" \
    "v_mov_b32_e64 v61, 18\n" \
    "s_nop 6\n" \
    "ds_write_b128 v37, v[28:31] offset:29120\n" \
    "ds_write_addtid_b32 v61 offset:54080\n" \
    "s_branch Lax_%=\n" \
    "Las3_%=:\n" \
    "s_mov_b32 s90, 0x100000\n" \
    "Lal3_%=:\n" \
    "ds_read_b32 v62, v34 offset:0\n" \
    "s_waitcnt lgkmcnt(0)\n" \
    "v_cmp_gt_u32_e32 vcc, 3, v62\n" \
    "s_cbranch_vccz Lae3_%=\n" \
    "s_sub_u32 s90, s90, 1\n" \
    "s_cbranch_scc0 Lal3_%=\n" \
    "s_branch Lae3_%=\n" \
    "Las4_%=:\n" \
    "s_mov_b32 s90, 0x100000\n" \
    "Lal4_%=:\n" \
    "ds_read_b32 v62, v34 offset:0\n" \
    "s_waitcnt lgkmcnt(0)\n" \
    "v_cmp_gt_u32_e32 vcc, 5, v62\n" \
    "s_cbranch_vccz Lae4_%=\n" \
    "s_sub_u32 s90, s90, 1\n" \
    "s_cbranch_scc0 Lal4_%=\n" \
    "s_branch Lae4_%=\n" \
    "Las5_%=:\n" \
    "s_mov_b32 s90, 0x100000\n" \
    "Lal5_%=:\n" \
    "ds_read_b32 v62, v34 offset:0\n" \
    "s_waitcnt lgkmcnt(0)\n" \
    "v_cmp_gt_u32_e32 vcc, 7, v62\n" \
    "s_cbranch_vccz Lae5_%=\n" \
    "s_sub_u32 s90, s90, 1\n" \
    "s_cbranch_scc0 Lal5_%=\n" \
    "s_branch Lae5_%=\n" \
    "Lax_%=:\n"
#define CONV_A_CLOB "v0", "v1", "v2", "v3", "v4", "v5", "v6", "v7", "v8", "v9", "v10", "v11", "v12", "v13", "v14", "v15", "v16", "v17", "v18", "v19", "v20", "v21", "v22", "v23", "v24", "v25", "v26", "v27", "v28", "v29", "v30", "v31", "v32", "v33", "v34", "v36", "v37", "v60", "v61", "v62", "v64", "v65", "v66", "v67", "v68", "v69", "v70", "v71", "v72", "v73", "v74", "v75", "v76", "v77", "v78", "v79", "v80", "v81", "v82", "v83", "v84", "v85", "v86", "v87", "v88", "v89", "v90", "v91", "v92", "v93", "v94", "v95", "v96", "v97", "v98", "v99", "v100", "v101", "v102", "v103", "v104", "v105", "v106", "v107", "v108", "v109", "v110", "v111", "v112", "v113", "v114", "v115", "v116", "v117", "v118", "v119", "v120", "v121", "v122", "v123", "v124", "v125", "v126", "v127", "v128", "v129", "v130", "v131", "v132", "v133", "v134", "v135", "v136", "v137", "v138", "v139", "v140", "v141", "v142", "v143", "v144", "v145", "v146", "v147", "v148", "v149", "v150", "v151", "v152", "v153", "v154", "v155", "v156", "v157", "v158", "v159"
#define CONV_B_ASM \
    "s_mov_b32 s92, 0x07060302\n" \
    "s_setprio 2\n" \
    "s_waitcnt vmcnt(4)\n" \
    "v_mul_f32_e32 v0, 0x3fb8aa3b, v70\n" \
    "v_exp_f32_e32 v0, v0\n" \
    "v_mul_f32_e32 v1, 0x3fb8aa3b, v64\n" \
    "v_cvt_pk_bf16_f32 v0, 0, v0\n" \
    "v_mul_f32_e32 v2, 0.15915494, v65\n" \
    "v_mul_f32_e32 v1, v0, v1\n" \
    "v_mul_f32_e32 v2, v0, v2\n" \
    "v_mul_f32_e32 v4, 0x41000000, v2\n" \
    "v_mul_f32_e32 v3, 0x41000000, v1\n" \
    "v_fract_f32_e32 v4, v4\n" \
    "v_exp_f32_e32 v3, v3\n" \
    "v_cos_f32_e32 v8, v4\n" \
    "v_sin_f32_e32 v9, v4\n" \
    "v_mul_f32_e32 v66, v0, v66\n" \
    "v_mul_f32_e64 v8, v3, v8\n" \
    "v_mul_f32_e64 v9, v3, v9\n" \
    "v_pk_mul_f32 v[18:19], v[8:9], v[8:9] op_sel_hi:[0,1]\n" \
    "v_cvt_pk_bf16_f32 v30, v8, v9\n" \
    "v_pk_fma_f32 v[14:15], v[8:9], v[8:9], v[18:19] op_sel:[1,1,0] op_sel_hi:[0,1,1] neg_lo:[0,1,0]\n" \
    "ds_write_addtid_b32 v30 offset:2080\n" \
    "v_pk_mul_f32 v[18:19], v[8:9], v[14:15] op_sel_hi:[0,1]\n" \
    "v_cvt_pk_bf16_f32 v31, v14, v15\n" \
    "v_pk_fma_f32 v[14:15], v[14:15], v[8:9], v[18:19] op_sel:[1,1,0] op_sel_hi:[0,1,1] neg_lo:[0,1,0]\n" \
    "ds_write_addtid_b32 v31 offset:4160\n" \
    "v_pk_mul_f32 v[18:19], v[8:9], v[14:15] op_sel_hi:[0,1]\n" \
    "v_cvt_pk_bf16_f32 v30, v14, v15\n" \
    "v_pk_fma_f32 v[14:15], v[14:15], v[8:9], v[18:19] op_sel:[1,1,0] op_sel_hi:[0,1,1] neg_lo:[0,1,0]\n" \
    "ds_write_addtid_b32 v30 offset:6240\n" \
    "v_pk_mul_f32 v[18:19], v[8:9], v[14:15] op_sel_hi:[0,1]\n" \
    "v_cvt_pk_bf16_f32 v31, v14, v15\n" \
    "v_pk_fma_f32 v[14:15], v[14:15], v[8:9], v[18:19] op_sel:[1,1,0] op_sel_hi:[0,1,1] neg_lo:[0,1,0]\n" \
    "ds_write_addtid_b32 v31 offset:8320\n" \
    "v_mul_f32_e32 v4, 0x43000000, v2\n" \
    "v_mul_f32_e32 v3, 0x43000000, v1\n" \
    "v_fract_f32_e32 v4, v4\n" \
    "v_exp_f32_e32 v3, v3\n" \
    "v_cos_f32_e32 v12, v4\n" \
    "v_sin_f32_e32 v13, v4\n" \
    "v_pk_mul_f32 v[18:19], v[8:9], v[14:15] op_sel_hi:[0,1]\n" \
    "v_cvt_pk_bf16_f32 v30, v14, v15\n" \
    "v_pk_fma_f32 v[14:15], v[14:15], v[8:9], v[18:19] op_sel:[1,1,0] op_sel_hi:[0,1,1] neg_lo:[0,1,0]\n" \
    "ds_write_addtid_b32 v30 offset:10400\n" \
    "v_pk_mul_f32 v[18:19], v[8:9], v[14:15] op_sel_hi:[0,1]\n" \
    "v_cvt_pk_bf16_f32 v31, v14, v15\n" \
    "v_pk_fma_f32 v[14:15], v[14:15], v[8:9], v[18:19] op_sel:[1,1,0] op_sel_hi:[0,1,1] neg_lo:[0,1,0]\n" \
    "ds_write_addtid_b32 v31 offset:12480\n" \
    "v_mul_f32_e64 v12, v3, v12\n" \
    "v_mul_f32_e64 v13, -v3, v13\n" \
    "v_pk_mul_f32 v[18:19], v[8:9], v[14:15] op_sel_hi:[0,1]\n" \
    "v_cvt_pk_bf16_f32 v30, v14, v15\n" \
    "v_pk_fma_f32 v[14:15], v[14:15], v[8:9], v[18:19] op_sel:[1,1,0] op_sel_hi:[0,1,1] neg_lo:[0,1,0]\n" \
    "ds_write_addtid_b32 v30 offset:14560\n" \
    "v_pk_mul_f32 v[18:19], v[8:9], v[14:15] op_sel_hi:[0,1]\n" \
    "v_cvt_pk_bf16_f32 v31, v14, v15\n" \
    "v_pk_fma_f32 v[14:15], v[14:15], v[8:9], v[18:19] op_sel:[1,1,0] op_sel_hi:[0,1,1] neg_lo:[0,1,0]\n" \
    "ds_write_addtid_b32 v31 offset:16640\n" \
    "v_fract_f32_e32 v6, v2\n" \
    "v_exp_f32_e32 v5, v1\n" \
    "v_cos_f32_e32 v10, v6\n" \
    "v_sin_f32_e32 v11, v6\n" \
    "v_pk_mul_f32 v[18:19], v[8:9], v[14:15] op_sel_hi:[0,1]\n" \
    "v_cvt_pk_bf16_f32 v30, v14, v15\n" \
    "v_pk_fma_f32 v[14:15], v[14:15], v[8:9], v[18:19] op_sel:[1,1,0] op_sel_hi:[0,1,1] neg_lo:[0,1,0]\n" \
    "ds_write_addtid_b32 v30 offset:18720\n" \
    "v_pk_mul_f32 v[18:19], v[8:9], v[14:15] op_sel_hi:[0,1]\n" \
    "v_cvt_pk_bf16_f32 v31, v14, v15\n" \
    "v_pk_fma_f32 v[14:15], v[14:15], v[8:9], v[18:19] op_sel:[1,1,0] op_sel_hi:[0,1,1] neg_lo:[0,1,0]\n" \
    "ds_write_addtid_b32 v31 offset:20800\n" \
    "v_mul_f32_e64 v10, v5, v10\n" \
    "v_mul_f32_e64 v11, -v5, v11\n" \
    "v_pk_mul_f32 v[18:19], v[8:9], v[14:15] op_sel_hi:[0,1]\n" \
    "v_cvt_pk_bf16_f32 v30, v14, v15\n" \
    "v_pk_fma_f32 v[14:15], v[14:15], v[8:9], v[18:19] op_sel:[1,1,0] op_sel_hi:[0,1,1] neg_lo:[0,1,0]\n" \
    "ds_write_addtid_b32 v30 offset:22880\n" \
    "v_mul_f32_e64 v67, v0, v67\n" \
    "v_mul_f32_e64 v16, v68, v66\n" \
    "v_mul_f32_e64 v17, -v68, v67\n" \
    "v_fma_f32 v16, -v69, v67, v16\n" \
    "v_fma_f32 v17, -v69, v66, v17\n" \
    "v_pk_mul_f32 v[18:19], v[8:9], v[14:15] op_sel_hi:[0,1]\n" \
    "v_cvt_pk_bf16_f32 v31, v14, v15\n" \
    "v_pk_fma_f32 v[14:15], v[14:15], v[8:9], v[18:19] op_sel:[1,1,0] op_sel_hi:[0,1,1] neg_lo:[0,1,0]\n" \
    "ds_write_addtid_b32 v31 offset:24960\n" \
    "v_pk_mul_f32 v[18:19], v[8:9], v[14:15] op_sel_hi:[0,1]\n" \
    "v_cvt_pk_bf16_f32 v30, v14, v15\n" \
    "v_pk_fma_f32 v[14:15], v[14:15], v[8:9], v[18:19] op_sel:[1,1,0] op_sel_hi:[0,1,1] neg_lo:[0,1,0]\n" \
    "ds_write_addtid_b32 v30 offset:27040\n" \
    "v_pk_mul_f32 v[18:19], v[8:9], v[14:15] op_sel_hi:[0,1]\n" \
    "v_cvt_pk_bf16_f32 v31, v14, v15\n" \
    "v_pk_fma_f32 v[14:15], v[14:15], v[8:9], v[18:19] op_sel:[1,1,0] op_sel_hi:[0,1,1] neg_lo:[0,1,0]\n" \
    "ds_write_addtid_b32 v31 offset:29120\n" \
    "v_pk_mul_f32 v[18:19], v[12:13], v[16:17] op_sel_hi:[0,1]\n" \
    "v_cvt_pk_bf16_f32 v30, v14, v15\n" \
    "v_pk_fma_f32 v[20:21], v[16:17], v[12:13], v[18:19] op_sel:[1,1,0] op_sel_hi:[0,1,1] neg_lo:[0,1,0]\n" \
    "ds_write_addtid_b32 v30 offset:31200\n" \
    "v_mov_b32_e32 v61, 1\n" \
    "ds_write_addtid_b32 v61 offset:54080\n" \
    "s_add_u32 m0, %[m0a], 54080\n" \
    "v_cvt_pk_bf16_f32 v31, v16, v17\n" \
    "ds_write_addtid_b32 v31 offset:4160\n" \
    "v_pk_mul_f32 v[18:19], v[10:11], v[16:17] op_sel_hi:[0,1]\n" \
    "v_cvt_pk_bf16_f32 v30, v20, v21\n" \
    "v_pk_fma_f32 v[16:17], v[16:17], v[10:11], v[18:19] op_sel:[1,1,0] op_sel_hi:[0,1,1] neg_lo:[0,1,0]\n" \
    "ds_write_addtid_b32 v30 offset:20800\n" \
    "v_pk_mul_f32 v[18:19], v[12:13], v[16:17] op_sel_hi:[0,1]\n" \
    "v_cvt_pk_bf16_f32 v31, v16, v17\n" \
    "v_pk_fma_f32 v[14:15], v[16:17], v[12:13], v[18:19] op_sel:[1,1,0] op_sel_hi:[0,1,1] neg_lo:[0,1,0]\n" \
    "ds_write_addtid_b32 v31 offset:6240\n" \
    "v_pk_mul_f32 v[18:19], v[10:11], v[16:17] op_sel_hi:[0,1]\n" \
    "v_cvt_pk_bf16_f32 v30, v14, v15\n" \
    "v_pk_fma_f32 v[16:17], v[16:17], v[10:11], v[18:19] op_sel:[1,1,0] op_sel_hi:[0,1,1] neg_lo:[0,1,0]\n" \
    "ds_write_addtid_b32 v30 offset:22880\n" \
    "v_pk_mul_f32 v[18:19], v[12:13], v[16:17] op_sel_hi:[0,1]\n" \
    "v_cvt_pk_bf16_f32 v31, v16, v17\n" \
    "v_pk_fma_f32 v[22:23], v[16:17], v[12:13], v[18:19] op_sel:[1,1,0] op_sel_hi:[0,1,1] neg_lo:[0,1,0]\n" \
    "ds_write_addtid_b32 v31 offset:8320\n" \
    "v_pk_mul_f32 v[18:19], v[10:11], v[16:17] op_sel_hi:[0,1]\n" \
    "v_cvt_pk_bf16_f32 v30, v22, v23\n" \
    "v_pk_fma_f32 v[16:17], v[16:17], v[10:11], v[18:19] op_sel:[1,1,0] op_sel_hi:[0,1,1] neg_lo:[0,1,0]\n" \
    "ds_write_addtid_b32 v30 offset:24960\n" \
    "v_pk_mul_f32 v[18:19], v[12:13], v[16:17] op_sel_hi:[0,1]\n" \
    "v_cvt_pk_bf16_f32 v31, v16, v17\n" \
    "v_pk_fma_f32 v[14:15], v[16:17], v[12:13], v[18:19] op_sel:[1,1,0] op_sel_hi:[0,1,1] neg_lo:[0,1,0]\n" \
    "ds_write_addtid_b32 v31 offset:10400\n" \
    "v_pk_mul_f32 v[18:19], v[10:11], v[16:17] op_sel_hi:[0,1]\n" \
    "v_cvt_pk_bf16_f32 v30, v14, v15\n" \
    "v_pk_fma_f32 v[16:17], v[16:17], v[10:11], v[18:19] op_sel:[1,1,0] op_sel_hi:[0,1,1] neg_lo:[0,1,0]\n" \
    "ds_write_addtid_b32 v30 offset:27040\n" \
    "v_pk_mul_f32 v[18:19], v[12:13], v[16:17] op_sel_hi:[0,1]\n" \
    "v_cvt_pk_bf16_f32 v31, v16, v17\n" \
    "v_pk_fma_f32 v[24:25], v[16:17], v[12:13], v[18:19] op_sel:[1,1,0] op_sel_hi:[0,1,1] neg_lo:[0,1,0]\n" \
    "ds_write_addtid_b32 v31 offset:12480\n" \
    "v_pk_mul_f32 v[18:19], v[10:11], v[16:17] op_sel_hi:[0,1]\n" \
    "v_cvt_pk_bf16_f32 v30, v24, v25\n" \
    "v_pk_fma_f32 v[16:17], v[16:17], v[10:11], v[18:19] op_sel:[1,1,0] op_sel_hi:[0,1,1] neg_lo:[0,1,0]\n" \
    "ds_write_addtid_b32 v30 offset:29120\n" \
    "v_pk_mul_f32 v[18:19], v[12:13], v[16:17] op_sel_hi:[0,1]\n" \
    "v_cvt_pk_bf16_f32 v31, v16, v17\n" \
    "v_pk_fma_f32 v[14:15], v[16:17], v[12:13], v[18:19] op_sel:[1,1,0] op_sel_hi:[0,1,1] neg_lo:[0,1,0]\n" \
    "ds_write_addtid_b32 v31 offset:14560\n" \
    "v_pk_mul_f32 v[18:19], v[10:11], v[16:17] op_sel_hi:[0,1]\n" \
    "v_cvt_pk_bf16_f32 v30, v14, v15\n" \
    "v_pk_fma_f32 v[16:17], v[16:17], v[10:11], v[18:19] op_sel:[1,1,0] op_sel_hi:[0,1,1] neg_lo:[0,1,0]\n" \
    "ds_write_addtid_b32 v30 offset:31200\n" \
    "v_pk_mul_f32 v[18:19], v[12:13], v[16:17] op_sel_hi:[0,1]\n" \
    "v_cvt_pk_bf16_f32 v31, v16, v17\n" \
    "v_pk_fma_f32 v[26:27], v[16:17], v[12:13], v[18:19] op_sel:[1,1,0] op_sel_hi:[0,1,1] neg_lo:[0,1,0]\n" \
    "ds_write_addtid_b32 v31 offset:16640\n" \
    "v_pk_mul_f32 v[18:19], v[10:11], v[16:17] op_sel_hi:[0,1]\n" \
    "v_cvt_pk_bf16_f32 v30, v26, v27\n" \
    "v_pk_fma_f32 v[16:17], v[16:17], v[10:11], v[18:19] op_sel:[1,1,0] op_sel_hi:[0,1,1] neg_lo:[0,1,0]\n" \
    "ds_write_addtid_b32 v30 offset:33280\n" \
    "v_pk_mul_f32 v[18:19], v[12:13], v[16:17] op_sel_hi:[0,1]\n" \
    "v_cvt_pk_bf16_f32 v31, v16, v17\n" \
    "v_pk_fma_f32 v[14:15], v[16:17], v[12:13], v[18:19] op_sel:[1,1,0] op_sel_hi:[0,1,1] neg_lo:[0,1,0]\n" \
    "ds_write_addtid_b32 v31 offset:18720\n" \
    "s_waitcnt vmcnt(0)\n" \
    "v_readfirstlane_b32 %[ep], v72\n" \
    "v_pk_mul_f32 v[18:19], v[12:13], v[20:21] op_sel_hi:[0,1]\n" \
    "v_cvt_pk_bf16_f32 v30, v14, v15\n" \
    "v_pk_fma_f32 v[20:21], v[20:21], v[12:13], v[18:19] op_sel:[1,1,0] op_sel_hi:[0,1,1] neg_lo:[0,1,0]\n" \
    "ds_write_addtid_b32 v30 offset:35360\n" \
    "v_mov_b32_e32 v61, 2\n" \
    "ds_write_addtid_b32 v61 offset:0\n" \
    "s_mov_b32 m0, %[m0a]\n" \
    "v_pk_mul_f32 v[18:19], v[12:13], v[22:23] op_sel_hi:[0,1]\n" \
    "v_cvt_pk_bf16_f32 v31, v20, v21\n" \
    "v_pk_fma_f32 v[22:23], v[22:23], v[12:13], v[18:19] op_sel:[1,1,0] op_sel_hi:[0,1,1] neg_lo:[0,1,0]\n" \
    "ds_write_addtid_b32 v31 offset:33280\n" \
    "v_pk_mul_f32 v[18:19], v[12:13], v[24:25] op_sel_hi:[0,1]\n" \
    "v_cvt_pk_bf16_f32 v30, v22, v23\n" \
    "v_pk_fma_f32 v[24:25], v[24:25], v[12:13], v[18:19] op_sel:[1,1,0] op_sel_hi:[0,1,1] neg_lo:[0,1,0]\n" \
    "ds_write_addtid_b32 v30 offset:35360\n" \
    "v_pk_mul_f32 v[18:19], v[12:13], v[26:27] op_sel_hi:[0,1]\n" \
    "v_cvt_pk_bf16_f32 v31, v24, v25\n" \
    "v_pk_fma_f32 v[26:27], v[26:27], v[12:13], v[18:19] op_sel:[1,1,0] op_sel_hi:[0,1,1] neg_lo:[0,1,0]\n" \
    "ds_write_addtid_b32 v31 offset:37440\n" \
    "v_pk_mul_f32 v[18:19], v[10:11], v[14:15] op_sel_hi:[0,1]\n" \
    "v_cvt_pk_bf16_f32 v30, v26, v27\n" \
    "v_pk_fma_f32 v[28:29], v[14:15], v[10:11], v[18:19] op_sel:[1,1,0] op_sel_hi:[0,1,1] neg_lo:[0,1,0]\n" \
    "ds_write_addtid_b32 v30 offset:39520\n" \
    "v_pk_mul_f32 v[18:19], v[12:13], v[28:29] op_sel_hi:[0,1]\n" \
    "v_pk_fma_f32 v[28:29], v[28:29], v[12:13], v[18:19] op_sel:[1,1,0] op_sel_hi:[0,1,1] neg_lo:[0,1,0]\n" \
    "v_pk_mul_f32 v[18:19], v[12:13], v[20:21] op_sel_hi:[0,1]\n" \
    "v_cvt_pk_bf16_f32 v31, v28, v29\n" \
    "v_pk_fma_f32 v[20:21], v[20:21], v[12:13], v[18:19] op_sel:[1,1,0] op_sel_hi:[0,1,1] neg_lo:[0,1,0]\n" \
    "ds_write_addtid_b32 v31 offset:41600\n" \
    "v_pk_mul_f32 v[18:19], v[12:13], v[22:23] op_sel_hi:[0,1]\n" \
    "v_cvt_pk_bf16_f32 v30, v20, v21\n" \
    "v_pk_fma_f32 v[22:23], v[22:23], v[12:13], v[18:19] op_sel:[1,1,0] op_sel_hi:[0,1,1] neg_lo:[0,1,0]\n" \
    "ds_write_addtid_b32 v30 offset:43680\n" \
    "v_pk_mul_f32 v[18:19], v[12:13], v[24:25] op_sel_hi:[0,1]\n" \
    "v_cvt_pk_bf16_f32 v31, v22, v23\n" \
    "v_pk_fma_f32 v[24:25], v[24:25], v[12:13], v[18:19] op_sel:[1,1,0] op_sel_hi:[0,1,1] neg_lo:[0,1,0]\n" \
    "ds_write_addtid_b32 v31 offset:45760\n" \
    "v_pk_mul_f32 v[18:19], v[12:13], v[26:27] op_sel_hi:[0,1]\n" \
    "v_cvt_pk_bf16_f32 v30, v24, v25\n" \
    "v_pk_fma_f32 v[26:27], v[26:27], v[12:13], v[18:19] op_sel:[1,1,0] op_sel_hi:[0,1,1] neg_lo:[0,1,0]\n" \
    "ds_write_addtid_b32 v30 offset:47840\n" \
    "v_pk_mul_f32 v[18:19], v[12:13], v[28:29] op_sel_hi:[0,1]\n" \
    "v_cvt_pk_bf16_f32 v31, v26, v27\n" \
    "v_pk_fma_f32 v[28:29], v[28:29], v[12:13], v[18:19] op_sel:[1,1,0] op_sel_hi:[0,1,1] neg_lo:[0,1,0]\n" \
    "ds_write_addtid_b32 v31 offset:49920\n" \
    "v_pk_mul_f32 v[18:19], v[12:13], v[24:25] op_sel_hi:[0,1]\n" \
    "v_cvt_pk_bf16_f32 v30, v28, v29\n" \
    "v_pk_fma_f32 v[24:25], v[24:25], v[12:13], v[18:19] op_sel:[1,1,0] op_sel_hi:[0,1,1] neg_lo:[0,1,0]\n" \
    "ds_write_addtid_b32 v30 offset:52000\n" \
    "v_mov_b32_e32 v61, 3\n" \
    "ds_write_addtid_b32 v61 offset:54080\n" \
    "s_add_u32 m0, %[m0a], 62400\n" \
    "v_pk_mul_f32 v[18:19], v[12:13], v[28:29] op_sel_hi:[0,1]\n" \
    "v_cvt_pk_bf16_f32 v31, v24, v25\n" \
    "v_pk_fma_f32 v[28:29], v[28:29], v[12:13], v[18:19] op_sel:[1,1,0] op_sel_hi:[0,1,1] neg_lo:[0,1,0]\n" \
    "ds_write_addtid_b32 v31 offset:29120\n" \
    "v_pk_mul_f32 v[18:19], v[12:13], v[24:25] op_sel_hi:[0,1]\n" \
    "v_cvt_pk_bf16_f32 v30, v28, v29\n" \
    "v_pk_fma_f32 v[24:25], v[24:25], v[12:13], v[18:19] op_sel:[1,1,0] op_sel_hi:[0,1,1] neg_lo:[0,1,0]\n" \
    "ds_write_addtid_b32 v30 offset:37440\n" \
    "v_pk_mul_f32 v[18:19], v[12:13], v[28:29] op_sel_hi:[0,1]\n" \
    "v_cvt_pk_bf16_f32 v31, v24, v25\n" \
    "v_pk_fma_f32 v[28:29], v[28:29], v[12:13], v[18:19] op_sel:[1,1,0] op_sel_hi:[0,1,1] neg_lo:[0,1,0]\n" \
    "ds_write_addtid_b32 v31 offset:31200\n" \
    "v_pk_mul_f32 v[18:19], v[12:13], v[24:25] op_sel_hi:[0,1]\n" \
    "v_cvt_pk_bf16_f32 v30, v28, v29\n" \
    "v_pk_fma_f32 v[24:25], v[24:25], v[12:13], v[18:19] op_sel:[1,1,0] op_sel_hi:[0,1,1] neg_lo:[0,1,0]\n" \
    "ds_write_addtid_b32 v30 offset:39520\n" \
    "v_pk_mul_f32 v[18:19], v[12:13], v[28:29] op_sel_hi:[0,1]\n" \
    "v_cvt_pk_bf16_f32 v31, v24, v25\n" \
    "v_pk_fma_f32 v[28:29], v[28:29], v[12:13], v[18:19] op_sel:[1,1,0] op_sel_hi:[0,1,1] neg_lo:[0,1,0]\n" \
    "ds_write_addtid_b32 v31 offset:33280\n" \
    "v_pk_mul_f32 v[18:19], v[12:13], v[24:25] op_sel_hi:[0,1]\n" \
    "v_cvt_pk_bf16_f32 v30, v28, v29\n" \
    "v_pk_fma_f32 v[24:25], v[24:25], v[12:13], v[18:19] op_sel:[1,1,0] op_sel_hi:[0,1,1] neg_lo:[0,1,0]\n" \
    "ds_write_addtid_b32 v30 offset:41600\n" \
    "v_pk_mul_f32 v[18:19], v[12:13], v[28:29] op_sel_hi:[0,1]\n" \
    "v_cvt_pk_bf16_f32 v31, v24, v25\n" \
    "v_pk_fma_f32 v[28:29], v[28:29], v[12:13], v[18:19] op_sel:[1,1,0] op_sel_hi:[0,1,1] neg_lo:[0,1,0]\n" \
    "ds_write_addtid_b32 v31 offset:35360\n" \
    "v_pk_mul_f32 v[18:19], v[12:13], v[28:29] op_sel_hi:[0,1]\n" \
    "v_cvt_pk_bf16_f32 v30, v28, v29\n" \
    "v_pk_fma_f32 v[28:29], v[28:29], v[12:13], v[18:19] op_sel:[1,1,0] op_sel_hi:[0,1,1] neg_lo:[0,1,0]\n" \
    "ds_write_addtid_b32 v30 offset:43680\n" \
    "v_pk_mul_f32 v[18:19], v[12:13], v[28:29] op_sel_hi:[0,1]\n" \
    "v_cvt_pk_bf16_f32 v31, v28, v29\n" \
    "v_pk_fma_f32 v[28:29], v[28:29], v[12:13], v[18:19] op_sel:[1,1,0] op_sel_hi:[0,1,1] neg_lo:[0,1,0]\n" \
    "ds_write_addtid_b32 v31 offset:45760\n" \
    "v_pk_mul_f32 v[18:19], v[12:13], v[28:29] op_sel_hi:[0,1]\n" \
    "v_cvt_pk_bf16_f32 v30, v28, v29\n" \
    "v_pk_fma_f32 v[28:29], v[28:29], v[12:13], v[18:19] op_sel:[1,1,0] op_sel_hi:[0,1,1] neg_lo:[0,1,0]\n" \
    "ds_write_addtid_b32 v30 offset:47840\n" \
    "v_pk_mul_f32 v[18:19], v[12:13], v[28:29] op_sel_hi:[0,1]\n" \
    "v_cvt_pk_bf16_f32 v31, v28, v29\n" \
    "v_pk_fma_f32 v[28:29], v[28:29], v[12:13], v[18:19] op_sel:[1,1,0] op_sel_hi:[0,1,1] neg_lo:[0,1,0]\n" \
    "ds_write_addtid_b32 v31 offset:49920\n" \
    "v_pk_mul_f32 v[18:19], v[12:13], v[28:29] op_sel_hi:[0,1]\n" \
    "v_cvt_pk_bf16_f32 v30, v28, v29\n" \
    "v_pk_fma_f32 v[28:29], v[28:29], v[12:13], v[18:19] op_sel:[1,1,0] op_sel_hi:[0,1,1] neg_lo:[0,1,0]\n" \
    "ds_write_addtid_b32 v30 offset:52000\n" \
    "v_pk_mul_f32 v[18:19], v[12:13], v[28:29] op_sel_hi:[0,1]\n" \
    "v_cvt_pk_bf16_f32 v31, v28, v29\n" \
    "v_pk_fma_f32 v[28:29], v[28:29], v[12:13], v[18:19] op_sel:[1,1,0] op_sel_hi:[0,1,1] neg_lo:[0,1,0]\n" \
    "ds_write_addtid_b32 v31 offset:54080\n" \
    "v_pk_mul_f32 v[18:19], v[12:13], v[28:29] op_sel_hi:[0,1]\n" \
    "v_cvt_pk_bf16_f32 v30, v28, v29\n" \
    "v_pk_fma_f32 v[28:29], v[28:29], v[12:13], v[18:19] op_sel:[1,1,0] op_sel_hi:[0,1,1] neg_lo:[0,1,0]\n" \
    "ds_write_addtid_b32 v30 offset:56160\n" \
    "v_pk_mul_f32 v[18:19], v[12:13], v[28:29] op_sel_hi:[0,1]\n" \
    "v_cvt_pk_bf16_f32 v31, v28, v29\n" \
    "v_pk_fma_f32 v[28:29], v[28:29], v[12:13], v[18:19] op_sel:[1,1,0] op_sel_hi:[0,1,1] neg_lo:[0,1,0]\n" \
    "ds_write_addtid_b32 v31 offset:58240\n" \
    "v_cvt_pk_bf16_f32 v30, v28, v29\n" \
    "ds_write_addtid_b32 v30 offset:60320\n" \
    "s_mov_b32 m0, %[m0a]\n" \
    "v_mov_b32_e32 v61, 4\n" \
    "ds_write_addtid_b32 v61 offset:54080\n" \
    "s_setprio 3\n" \
    "ds_read_b32 v63, v34\n" \
    "ds_read_b128 v[152:155], v38 offset:1024\n" \
    "ds_read_b128 v[160:163], v38 offset:3072\n" \
    "ds_read_b128 v[148:151], v38 offset:0\n" \
    "ds_read_b128 v[156:159], v38 offset:2048\n" \
    "ds_read_b128 v[164:167], v38 offset:4096\n" \
    "s_waitcnt lgkmcnt(5)\n" \
    "v_cmp_gt_u32_e32 vcc, 2, v63\n" \
    "s_cbranch_vccz Lbe0_%=\n" \
    "s_mov_b32 s90, 0x100000\n" \
    "Lbl0_%=:\n" \
    "s_sleep 1\n" \
    "ds_read_b32 v63, v34 offset:0\n" \
    "ds_read_b128 v[152:155], v38 offset:1024\n" \
    "ds_read_b128 v[160:163], v38 offset:3072\n" \
    "ds_read_b128 v[148:151], v38 offset:0\n" \
    "ds_read_b128 v[156:159], v38 offset:2048\n" \
    "ds_read_b128 v[164:167], v38 offset:4096\n" \
    "s_waitcnt lgkmcnt(0)\n" \
    "v_cmp_gt_u32_e32 vcc, 2, v63\n" \
    "s_cbranch_vccz Lbe0_%=\n" \
    "s_sub_u32 s90, s90, 1\n" \
    "s_cbranch_scc0 Lbl0_%=\n" \
    "Lbe0_%=:\n" \
    "ds_read_b128 v[108:111], v33 offset:448\n" \
    "ds_read_b128 v[112:115], v33 offset:432\n" \
    "ds_read_b128 v[116:119], v33 offset:960\n" \
    "ds_read_b128 v[120:123], v33 offset:944\n" \
    "s_waitcnt lgkmcnt(3)\n" \
    "v_mfma_f32_16x16x32_bf16 v[48:51], v[108:111], v[152:155], 0\n" \
    "ds_read_b32 v60, v32\n" \
    "s_waitcnt lgkmcnt(3)\n" \
    "v_mfma_f32_16x16x32_bf16 v[52:55], v[112:115], v[152:155], 0\n" \
    "v_mfma_f32_16x16x32_bf16 v[56:59], v[112:115], v[160:163], 0\n" \
    "v_mfma_f32_16x16x32_bf16 v[0:3], v[108:111], v[148:151], 0\n" \
    "v_mfma_f32_16x16x32_bf16 v[4:7], v[112:115], v[148:151], 0\n" \
    "v_mfma_f32_16x16x32_bf16 v[40:43], v[108:111], v[156:159], 0\n" \
    "s_waitcnt lgkmcnt(2)\n" \
    "v_mfma_f32_16x16x32_bf16 v[8:11], v[116:119], v[148:151], 0\n" \
    "s_nop 3\n" \
    "v_cvt_pk_bf16_f32 v0, v0, v48\n" \
    "v_cvt_pk_bf16_f32 v1, v1, v49\n" \
    "v_cvt_pk_bf16_f32 v2, v2, v50\n" \
    "v_cvt_pk_bf16_f32 v3, v3, v51\n" \
    "s_waitcnt lgkmcnt(1)\n" \
    "v_mfma_f32_16x16x32_bf16 v[12:15], v[120:123], v[148:151], 0\n" \
    "v_cvt_pk_bf16_f32 v4, v4, v52\n" \
    "v_cvt_pk_bf16_f32 v5, v5, v53\n" \
    "v_cvt_pk_bf16_f32 v6, v6, v54\n" \
    "v_cvt_pk_bf16_f32 v7, v7, v55\n" \
    "v_mfma_f32_16x16x32_bf16 v[44:47], v[116:119], v[156:159], 0\n" \
    "v_cvt_pk_bf16_f32 v40, v40, v56\n" \
    "v_cvt_pk_bf16_f32 v41, v41, v57\n" \
    "v_cvt_pk_bf16_f32 v42, v42, v58\n" \
    "v_cvt_pk_bf16_f32 v43, v43, v59\n" \
    "v_mfma_f32_16x16x32_bf16 v[48:51], v[116:119], v[152:155], 0\n" \
    "v_mfma_f32_16x16x32_bf16 v[52:55], v[120:123], v[152:155], 0\n" \
    "v_mfma_f32_16x16x32_bf16 v[56:59], v[120:123], v[160:163], 0\n" \
    "s_waitcnt lgkmcnt(0)\n" \
    "v_cmp_gt_u32_e32 vcc, 2, v60\n" \
    "s_cbranch_vccz Lbe1_%=\n" \
    "s_mov_b32 s90, 0x100000\n" \
    "Lbl1_%=:\n" \
    "s_sleep 1\n" \
    "ds_read_b32 v60, v32 offset:0\n" \
    "s_waitcnt lgkmcnt(0)\n" \
    "v_cmp_gt_u32_e32 vcc, 2, v60\n" \
    "s_cbranch_vccz Lbe1_%=\n" \
    "s_sub_u32 s90, s90, 1\n" \
    "s_cbranch_scc0 Lbl1_%=\n" \
    "Lbe1_%=:\n" \
    "ds_read2_b32 v[104:105], v37 offset0:0 offset1:1\n" \
    "ds_read2_b32 v[106:107], v37 offset0:2 offset1:3\n" \
    "s_waitcnt lgkmcnt(0)\n" \
    "v_mfma_f32_16x16x32_bf16 v[132:135], v[104:107], v[108:111], 0\n" \
    "v_mfma_f32_16x16x32_bf16 v[136:139], v[104:107], v[116:119], 0\n" \
    "ds_read_b32 v60, v32\n" \
    "s_waitcnt lgkmcnt(0)\n" \
    "v_cmp_gt_u32_e32 vcc, 3, v60\n" \
    "s_cbranch_vccz Lbe2_%=\n" \
    "s_mov_b32 s90, 0x100000\n" \
    "Lbl2_%=:\n" \
    "s_sleep 1\n" \
    "ds_read_b32 v60, v32 offset:0\n" \
    "s_waitcnt lgkmcnt(0)\n" \
    "v_cmp_gt_u32_e32 vcc, 3, v60\n" \
    "s_cbranch_vccz Lbe2_%=\n" \
    "s_sub_u32 s90, s90, 1\n" \
    "s_cbranch_scc0 Lbl2_%=\n" \
    "Lbe2_%=:\n" \
    "ds_read_b32 v62, v39 offset:320\n" \
    "ds_read_b32 v63, v34\n" \
    "ds_read2_b32 v[64:65], v36 offset0:32 offset1:33\n" \
    "ds_read2_b32 v[66:67], v36 offset0:34 offset1:35\n" \
    "ds_read2_b32 v[76:77], v36 offset0:96 offset1:97\n" \
    "ds_read2_b32 v[78:79], v36 offset0:98 offset1:99\n" \
    "ds_read2_b32 v[68:69], v36 offset0:16 offset1:17\n" \
    "ds_read2_b32 v[70:71], v36 offset0:18 offset1:19\n" \
    "ds_read2_b32 v[80:81], v36 offset0:80 offset1:81\n" \
    "ds_read2_b32 v[82:83], v36 offset0:82 offset1:83\n" \
    "ds_read2_b32 v[72:73], v36 offset0:0 offset1:1\n" \
    "ds_read2_b32 v[74:75], v36 offset0:2 offset1:3\n" \
    "ds_read2_b32 v[84:85], v36 offset0:64 offset1:65\n" \
    "ds_read2_b32 v[86:87], v36 offset0:66 offset1:67\n" \
    "ds_read2_b32 v[88:89], v36 offset0:176 offset1:177\n" \
    "ds_read2_b32 v[90:91], v36 offset0:178 offset1:179\n" \
    "ds_read2_b32 v[92:93], v36 offset0:160 offset1:161\n" \
    "ds_read2_b32 v[94:95], v36 offset0:162 offset1:163\n" \
    "ds_read2_b32 v[96:97], v36 offset0:144 offset1:145\n" \
    "ds_read2_b32 v[98:99], v36 offset0:146 offset1:147\n" \
    "ds_read2_b32 v[100:101], v36 offset0:128 offset1:129\n" \
    "ds_read2_b32 v[102:103], v36 offset0:130 offset1:131\n" \
    "s_waitcnt lgkmcnt(14)\n" \
    "v_perm_b32 v62, v62, v62, s92\n" \
    "v_and_b32_e64 v164, v62, v164\n" \
    "v_and_b32_e64 v165, v62, v165\n" \
    "v_and_b32_e64 v166, v62, v166\n" \
    "v_and_b32_e64 v167, v62, v167\n" \
    "s_nop 0\n" \
    "s_nop 0\n" \
    "v_mfma_f32_16x16x32_bf16 v[140:143], v[164:167], v[108:111], 0\n" \
    "v_mfma_f32_16x16x32_bf16 v[124:127], v[164:167], v[116:119], 0\n" \
    "v_cmp_gt_u32_e32 vcc, 3, v63\n" \
    "s_cbranch_vccnz Lbs3_%=\n" \
    "Lbe3_%=:\n" \
    "ds_read_b128 v[108:111], v33 offset:1472\n" \
    "ds_read_b128 v[112:115], v33 offset:1456\n" \
    "s_setprio 2\n" \
    "v_mfma_f32_16x16x32_bf16 v[132:135], v[0:3], v[64:67], v[132:135]\n" \
    "v_cvt_pk_bf16_f32 v8, v8, v48\n" \
    "v_cvt_pk_bf16_f32 v9, v9, v49\n" \
    "v_cvt_pk_bf16_f32 v10, v10, v50\n" \
    "v_cvt_pk_bf16_f32 v11, v11, v51\n" \
    "v_cvt_pk_bf16_f32 v12, v12, v52\n" \
    "v_cvt_pk_bf16_f32 v13, v13, v53\n" \
    "v_cvt_pk_bf16_f32 v14, v14, v54\n" \
    "v_cvt_pk_bf16_f32 v15, v15, v55\n" \
    "v_cvt_pk_bf16_f32 v44, v44, v56\n" \
    "v_cvt_pk_bf16_f32 v45, v45, v57\n" \
    "v_cvt_pk_bf16_f32 v46, v46, v58\n" \
    "v_cvt_pk_bf16_f32 v47, v47, v59\n" \
    "ds_read_b32 v63, v34\n" \
    "v_mfma_f32_16x16x32_bf16 v[132:135], v[4:7], v[76:79], v[132:135]\n" \
    "s_waitcnt lgkmcnt(2)\n" \
    "v_mfma_f32_16x16x32_bf16 v[128:131], v[164:167], v[108:111], 0\n" \
    "v_mfma_f32_16x16x32_bf16 v[140:143], v[104:107], v[108:111], v[140:143]\n" \
    "v_mfma_f32_16x16x32_bf16 v[16:19], v[108:111], v[148:151], 0\n" \
    "s_waitcnt lgkmcnt(1)\n" \
    "v_mfma_f32_16x16x32_bf16 v[20:23], v[112:115], v[148:151], 0\n" \
    "v_mfma_f32_16x16x32_bf16 v[144:147], v[108:111], v[156:159], 0\n" \
    "v_mfma_f32_16x16x32_bf16 v[48:51], v[108:111], v[152:155], 0\n" \
    "v_mfma_f32_16x16x32_bf16 v[52:55], v[112:115], v[152:155], 0\n" \
    "v_mfma_f32_16x16x32_bf16 v[56:59], v[112:115], v[160:163], 0\n" \
    "s_waitcnt lgkmcnt(0)\n" \
    "v_cmp_gt_u32_e32 vcc, 4, v63\n" \
    "s_cbranch_vccnz Lbs4_%=\n" \
    "Lbe4_%=:\n" \
    "ds_read_b128 v[116:119], v33 offset:1984\n" \
    "ds_read_b128 v[120:123], v33 offset:1968\n" \
    "v_mfma_f32_16x16x32_bf16 v[136:139], v[8:11], v[64:67], v[136:139]\n" \
    "v_mov_b32_e64 v61, 13\n" \
    "ds_write_b128 v35, v[132:135] offset:0\n" \
    "ds_write_addtid_b32 v61 offset:54080\n" \
    "v_cvt_pk_bf16_f32 v16, v16, v48\n" \
    "v_cvt_pk_bf16_f32 v17, v17, v49\n" \
    "v_cvt_pk_bf16_f32 v18, v18, v50\n" \
    "v_cvt_pk_bf16_f32 v19, v19, v51\n" \
    "v_cvt_pk_bf16_f32 v20, v20, v52\n" \
    "v_cvt_pk_bf16_f32 v21, v21, v53\n" \
    "v_cvt_pk_bf16_f32 v22, v22, v54\n" \
    "v_cvt_pk_bf16_f32 v23, v23, v55\n" \
    "v_cvt_pk_bf16_f32 v144, v144, v56\n" \
    "v_cvt_pk_bf16_f32 v145, v145, v57\n" \
    "v_cvt_pk_bf16_f32 v146, v146, v58\n" \
    "v_cvt_pk_bf16_f32 v147, v147, v59\n" \
    "ds_read_b32 v63, v34\n" \
    "v_mfma_f32_16x16x32_bf16 v[136:139], v[12:15], v[76:79], v[136:139]\n" \
    "v_mfma_f32_16x16x32_bf16 v[136:139], v[0:3], v[68:71], v[136:139]\n" \
    "v_mfma_f32_16x16x32_bf16 v[136:139], v[4:7], v[80:83], v[136:139]\n" \
    "s_waitcnt lgkmcnt(4)\n" \
    "v_mfma_f32_16x16x32_bf16 v[132:135], v[164:167], v[116:119], 0\n" \
    "v_mfma_f32_16x16x32_bf16 v[124:127], v[104:107], v[116:119], v[124:127]\n" \
    "v_mfma_f32_16x16x32_bf16 v[24:27], v[116:119], v[148:151], 0\n" \
    "s_waitcnt lgkmcnt(3)\n" \
    "v_mfma_f32_16x16x32_bf16 v[28:31], v[120:123], v[148:151], 0\n" \
    "v_mfma_f32_16x16x32_bf16 v[164:167], v[116:119], v[156:159], 0\n" \
    "v_mfma_f32_16x16x32_bf16 v[48:51], v[116:119], v[152:155], 0\n" \
    "v_mfma_f32_16x16x32_bf16 v[52:55], v[120:123], v[152:155], 0\n" \
    "v_mfma_f32_16x16x32_bf16 v[56:59], v[120:123], v[160:163], 0\n" \
    "s_waitcnt lgkmcnt(0)\n" \
    "v_cmp_gt_u32_e32 vcc, 5, v63\n" \
    "s_cbranch_vccnz Lbs5_%=\n" \
    "Lbe5_%=:\n" \
    "ds_read_b128 v[108:111], v33 offset:2496\n" \
    "ds_read_b128 v[112:115], v33 offset:2480\n" \
    "v_mfma_f32_16x16x32_bf16 v[140:143], v[16:19], v[64:67], v[140:143]\n" \
    "v_mov_b32_e64 v61, 14\n" \
    "ds_write_b128 v35, v[136:139] offset:4160\n" \
    "ds_write_addtid_b32 v61 offset:54080\n" \
    "v_mfma_f32_16x16x32_bf16 v[140:143], v[20:23], v[76:79], v[140:143]\n" \
    "v_cvt_pk_bf16_f32 v24, v24, v48\n" \
    "v_cvt_pk_bf16_f32 v25, v25, v49\n" \
    "v_cvt_pk_bf16_f32 v26, v26, v50\n" \
    "v_mfma_f32_16x16x32_bf16 v[140:143], v[8:11], v[68:71], v[140:143]\n" \
    "v_cvt_pk_bf16_f32 v27, v27, v51\n" \
    "v_cvt_pk_bf16_f32 v28, v28, v52\n" \
    "v_cvt_pk_bf16_f32 v29, v29, v53\n" \
    "v_mfma_f32_16x16x32_bf16 v[140:143], v[12:15], v[80:83], v[140:143]\n" \
    "v_cvt_pk_bf16_f32 v30, v30, v54\n" \
    "v_cvt_pk_bf16_f32 v31, v31, v55\n" \
    "v_cvt_pk_bf16_f32 v164, v164, v56\n" \
    "v_mfma_f32_16x16x32_bf16 v[140:143], v[0:3], v[72:75], v[140:143]\n" \
    "v_cvt_pk_bf16_f32 v165, v165, v57\n" \
    "v_cvt_pk_bf16_f32 v166, v166, v58\n" \
    "v_cvt_pk_bf16_f32 v167, v167, v59\n" \
    "v_mfma_f32_16x16x32_bf16 v[140:143], v[4:7], v[84:87], v[140:143]\n" \
    "ds_read_b32 v63, v34\n" \
    "v_mfma_f32_16x16x32_bf16 v[140:143], v[40:43], v[88:91], v[140:143]\n" \
    "s_waitcnt lgkmcnt(4)\n" \
    "v_mfma_f32_16x16x32_bf16 v[128:131], v[104:107], v[108:111], v[128:131]\n" \
    "v_mfma_f32_16x16x32_bf16 v[0:3], v[108:111], v[148:151], 0\n" \
    "s_waitcnt lgkmcnt(3)\n" \
    "v_mfma_f32_16x16x32_bf16 v[4:7], v[112:115], v[148:151], 0\n" \
    "v_mfma_f32_16x16x32_bf16 v[48:51], v[108:111], v[152:155], 0\n" \
    "v_mfma_f32_16x16x32_bf16 v[52:55], v[112:115], v[152:155], 0\n" \
    "s_waitcnt lgkmcnt(0)\n" \
    "v_cmp_gt_u32_e32 vcc, 6, v63\n" \
    "s_cbranch_vccnz Lbs6_%=\n" \
    "Lbe6_%=:\n" \
    "ds_read_b128 v[116:119], v33 offset:3008\n" \
    "ds_read_b128 v[120:123], v33 offset:2992\n" \
    "v_mfma_f32_16x16x32_bf16 v[124:127], v[24:27], v[64:67], v[124:127]\n" \
    "v_mov_b32_e64 v61, 15\n" \
    "ds_write_b128 v35, v[140:143] offset:8320\n" \
    "v_mfma_f32_16x16x32_bf16 v[124:127], v[28:31], v[76:79], v[124:127]\n" \
    "ds_write_addtid_b32 v61 offset:54080\n" \
    "v_cvt_pk_bf16_f32 v0, v0, v48\n" \
    "v_mfma_f32_16x16x32_bf16 v[124:127], v[16:19], v[68:71], v[124:127]\n" \
    "v_cvt_pk_bf16_f32 v1, v1, v49\n" \
    "v_cvt_pk_bf16_f32 v2, v2, v50\n" \
    "v_mfma_f32_16x16x32_bf16 v[124:127], v[20:23], v[80:83], v[124:127]\n" \
    "v_cvt_pk_bf16_f32 v3, v3, v51\n" \
    "v_cvt_pk_bf16_f32 v4, v4, v52\n" \
    "v_mfma_f32_16x16x32_bf16 v[124:127], v[8:11], v[72:75], v[124:127]\n" \
    "v_cvt_pk_bf16_f32 v5, v5, v53\n" \
    "v_cvt_pk_bf16_f32 v6, v6, v54\n" \
    "v_mfma_f32_16x16x32_bf16 v[124:127], v[12:15], v[84:87], v[124:127]\n" \
    "v_cvt_pk_bf16_f32 v7, v7, v55\n" \
    "v_mfma_f32_16x16x32_bf16 v[124:127], v[44:47], v[88:91], v[124:127]\n" \
    "v_mfma_f32_16x16x32_bf16 v[124:127], v[40:43], v[92:95], v[124:127]\n" \
    "s_waitcnt lgkmcnt(3)\n" \
    "v_mfma_f32_16x16x32_bf16 v[132:135], v[104:107], v[116:119], v[132:135]\n" \
    "v_mfma_f32_16x16x32_bf16 v[8:11], v[116:119], v[148:151], 0\n" \
    "s_waitcnt lgkmcnt(2)\n" \
    "v_mfma_f32_16x16x32_bf16 v[12:15], v[120:123], v[148:151], 0\n" \
    "v_mfma_f32_16x16x32_bf16 v[48:51], v[116:119], v[152:155], 0\n" \
    "v_mfma_f32_16x16x32_bf16 v[52:55], v[120:123], v[152:155], 0\n" \
    "v_and_b32_e32 v112, 0x70, v38\n" \
    "global_load_dwordx4 v[108:111], v112, %[sg] sc1\n" \
    "v_mfma_f32_16x16x32_bf16 v[128:131], v[0:3], v[64:67], v[128:131]\n" \
    "v_mov_b32_e64 v61, 16\n" \
    "ds_write_b128 v35, v[124:127] offset:12480\n" \
    "v_mfma_f32_16x16x32_bf16 v[128:131], v[4:7], v[76:79], v[128:131]\n" \
    "ds_write_addtid_b32 v61 offset:54080\n" \
    "v_cvt_pk_bf16_f32 v8, v8, v48\n" \
    "v_mfma_f32_16x16x32_bf16 v[128:131], v[24:27], v[68:71], v[128:131]\n" \
    "v_cvt_pk_bf16_f32 v9, v9, v49\n" \
    "v_cvt_pk_bf16_f32 v10, v10, v50\n" \
    "v_mfma_f32_16x16x32_bf16 v[128:131], v[28:31], v[80:83], v[128:131]\n" \
    "v_cvt_pk_bf16_f32 v11, v11, v51\n" \
    "v_cvt_pk_bf16_f32 v12, v12, v52\n" \
    "v_mfma_f32_16x16x32_bf16 v[128:131], v[16:19], v[72:75], v[128:131]\n" \
    "v_cvt_pk_bf16_f32 v13, v13, v53\n" \
    "v_cvt_pk_bf16_f32 v14, v14, v54\n" \
    "v_mfma_f32_16x16x32_bf16 v[128:131], v[20:23], v[84:87], v[128:131]\n" \
    "v_cvt_pk_bf16_f32 v15, v15, v55\n" \
    "v_mfma_f32_16x16x32_bf16 v[128:131], v[144:147], v[88:91], v[128:131]\n" \
    "v_mfma_f32_16x16x32_bf16 v[128:131], v[44:47], v[92:95], v[128:131]\n" \
    "v_mfma_f32_16x16x32_bf16 v[128:131], v[40:43], v[96:99], v[128:131]\n" \
    "v_mfma_f32_16x16x32_bf16 v[132:135], v[8:11], v[64:67], v[132:135]\n" \
    "v_mov_b32_e64 v61, 17\n" \
    "s_nop 5\n" \
    "ds_write_b128 v35, v[128:131] offset:16640\n" \
    "v_mfma_f32_16x16x32_bf16 v[132:135], v[12:15], v[76:79], v[132:135]\n" \
    "ds_write_addtid_b32 v61 offset:54080\n" \
    "v_mfma_f32_16x16x32_bf16 v[132:135], v[0:3], v[68:71], v[132:135]\n" \
    "v_mfma_f32_16x16x32_bf16 v[132:135], v[4:7], v[80:83], v[132:135]\n" \
    "v_mfma_f32_16x16x32_bf16 v[132:135], v[24:27], v[72:75], v[132:135]\n" \
    "v_mfma_f32_16x16x32_bf16 v[132:135], v[28:31], v[84:87], v[132:135]\n" \
    "v_mfma_f32_16x16x32_bf16 v[132:135], v[164:167], v[88:91], v[132:135]\n" \
    "v_mfma_f32_16x16x32_bf16 v[132:135], v[144:147], v[92:95], v[132:135]\n" \
    "v_mfma_f32_16x16x32_bf16 v[132:135], v[44:47], v[96:99], v[132:135]\n" \
    "v_mfma_f32_16x16x32_bf16 v[132:135], v[40:43], v[100:103], v[132:135]\n" \
    "v_mov_b32_e64 v61, 18\n" \
    "s_nop 6\n" \
    "ds_write_b128 v35, v[132:135] offset:20800\n" \
    "ds_write_addtid_b32 v61 offset:54080\n" \
    "s_setprio 0\n" \
    "s_waitcnt vmcnt(0)\n" \
    "s_branch Lbx_%=\n" \
    "Lbs3_%=:\n" \
    "s_mov_b32 s90, 0x100000\n" \
    "Lbl3_%=:\n" \
    "ds_read_b32 v63, v34 offset:0\n" \
    "s_waitcnt lgkmcnt(0)\n" \
    "v_cmp_gt_u32_e32 vcc, 3, v63\n" \
    "s_cbranch_vccz Lbe3_%=\n" \
    "s_sub_u32 s90, s90, 1\n" \
    "s_cbranch_scc0 Lbl3_%=\n" \
    "s_branch Lbe3_%=\n" \
    "Lbs4_%=:\n" \
    "s_mov_b32 s90, 0x100000\n" \
    "Lbl4_%=:\n" \
    "ds_read_b32 v63, v34 offset:0\n" \
    "s_waitcnt lgkmcnt(0)\n" \
    "v_cmp_gt_u32_e32 vcc, 4, v63\n" \
    "s_cbranch_vccz Lbe4_%=\n" \
    "s_sub_u32 s90, s90, 1\n" \
    "s_cbranch_scc0 Lbl4_%=\n" \
    "s_branch Lbe4_%=\n" \
    "Lbs5_%=:\n" \
    "s_mov_b32 s90, 0x100000\n" \
    "Lbl5_%=:\n" \
    "ds_read_b32 v63, v34 offset:0\n" \
    "s_waitcnt lgkmcnt(0)\n" \
    "v_cmp_gt_u32_e32 vcc, 5, v63\n" \
    "s_cbranch_vccz Lbe5_%=\n" \
    "s_sub_u32 s90, s90, 1\n" \
    "s_cbranch_scc0 Lbl5_%=\n" \
    "s_branch Lbe5_%=\n" \
    "Lbs6_%=:\n" \
    "s_mov_b32 s90, 0x100000\n" \
    "Lbl6_%=:\n" \
    "ds_read_b32 v63, v34 offset:0\n" \
    "s_waitcnt lgkmcnt(0)\n" \
    "v_cmp_gt_u32_e32 vcc, 6, v63\n" \
    "s_cbranch_vccz Lbe6_%=\n" \
    "s_sub_u32 s90, s90, 1\n" \
    "s_cbranch_scc0 Lbl6_%=\n" \
    "s_branch Lbe6_%=\n" \
    "Lbx_%=:\n"
#define CONV_B_CLOB "v0", "v1", "v2", "v3", "v4", "v5", "v6", "v7", "v8", "v9", "v10", "v11", "v12", "v13", "v14", "v15", "v16", "v17", "v18", "v19", "v20", "v21", "v22", "v23", "v24", "v25", "v26", "v27", "v28", "v29", "v30", "v31", "v32", "v33", "v34", "v35", "v36", "v37", "v38", "v39", "v40", "v41", "v42", "v43", "v44", "v45", "v46", "v47", "v48", "v49", "v50", "v51", "v52", "v53", "v54", "v55", "v56", "v57", "v58", "v59", "v60", "v61", "v62", "v63", "v64", "v65", "v66", "v67", "v68", "v69", "v70", "v71", "v72", "v73", "v74", "v75", "v76", "v77", "v78", "v79", "v80", "v81", "v82", "v83", "v84", "v85", "v86", "v87", "v88", "v89", "v90", "v91", "v92", "v93", "v94", "v95", "v96", "v97", "v98", "v99", "v100", "v101", "v102", "v103", "v104", "v105", "v106", "v107", "v112", "v113", "v114", "v115", "v116", "v117", "v118", "v119", "v120", "v121", "v122", "v123", "v124", "v125", "v126", "v127", "v128", "v129", "v130", "v131", "v132", "v133", "v134", "v135", "v136", "v137", "v138", "v139", "v140", "v141", "v142", "v143", "v144", "v145", "v146", "v147", "v148", "v149", "v150", "v151", "v152", "v153", "v154", "v155", "v156", "v157", "v158", "v159", "v160", "v161", "v162", "v163", "v164", "v165", "v166", "v167"
#define CONV_C_ASM \
    "s_setprio 3\n" \
    "s_mov_b64 exec, %[pm]\n" \
    "global_load_dword v63, v62, %[xb]\n" \
    "s_mov_b64 exec, -1\n" \
    "s_mov_b32 s91, 0x05040100\n" \
    "s_mov_b32 s92, 0x07060302\n" \
    "s_mov_b64 s[96:97], %[o0]\n" \
    "s_mov_b64 s[94:95], %[x0]\n" \
    "v_mov_b64 v[160:161], 0\n" \
    "v_mov_b64 v[162:163], 0\n" \
    "s_waitcnt vmcnt(6)\n" \
    "s_mov_b32 s93, m0\n" \
    "v_lshrrev_b32_e32 v0, 3, v48\n" \
    "s_mov_b32 m0, %[bl]\n" \
    "s_sub_u32 s88, %[bl], s93\n" \
    "s_sub_u32 s88, s88, s93\n" \
    "global_load_lds_dwordx4 v0, %[bs]\n" \
    "s_add_u32 m0, s88, 4096\n" \
    "s_lshl_b32 s88, s93, 1\n" \
    "s_sub_u32 s88, 4096, s88\n" \
    "v_add_u32_e32 v0, s88, v0\n" \
    "global_load_lds_dwordx4 v0, %[bs]\n" \
    "s_mov_b32 m0, s93\n" \
    "ds_write_b128 v49, v[160:163]\n" \
    "ds_write_b128 v50, v[160:163]\n" \
    "s_waitcnt vmcnt(3)\n" \
    "v_add_u32_e32 v56, 91520, v33\n" \
    "s_mov_b64 exec, 1\n" \
    "v_mov_b32_e32 v160, v71\n" \
    "s_mov_b64 exec, -1\n" \
    "v_readfirstlane_b32 %[ep], v72\n" \
    "s_nop 2\n" \
    "s_add_u32 s98, %[ep], 1\n" \
    "s_max_u32 s98, s98, 1\n" \
    "s_mov_b32 s99, %[tg]\n" \
    "global_load_dwordx2 v[12:13], v41, s[94:95] offset:0\n" \
    "s_mov_b32 s90, 0x100000\n" \
    "Lcl0_%=:\n" \
    "ds_read_b32 v60, v32 offset:0\n" \
    "s_waitcnt lgkmcnt(0)\n" \
    "v_cmp_gt_u32_e32 vcc, 1, v60\n" \
    "s_cbranch_vccz Lce0_%=\n" \
    "s_sleep 2\n" \
    "s_sub_u32 s90, s90, 1\n" \
    "s_cbranch_scc0 Lcl0_%=\n" \
    "Lce0_%=:\n" \
    "ds_read_b128 v[96:99], v33 offset:0\n" \
    "ds_read_b128 v[100:103], v33 offset:64\n" \
    "ds_read_b128 v[104:107], v33 offset:128\n" \
    "ds_read_b128 v[108:111], v33 offset:192\n" \
    "ds_read_b128 v[112:115], v33 offset:256\n" \
    "ds_read_b128 v[116:119], v33 offset:320\n" \
    "ds_read_b128 v[120:123], v33 offset:384\n" \
    "ds_read_b128 v[124:127], v33 offset:448\n" \
    "global_load_dwordx2 v[14:15], v41, s[94:95] offset:1152\n" \
    "s_add_u32 s94, s94, 589824\n" \
    "s_addc_u32 s95, s95, 0\n" \
    "global_load_dwordx2 v[16:17], v41, s[94:95] offset:0\n" \
    "global_load_dwordx2 v[18:19], v41, s[94:95] offset:1152\n" \
    "s_add_u32 s94, s94, 589824\n" \
    "s_addc_u32 s95, s95, 0\n" \
    "s_mov_b32 s90, 0x100000\n" \
    "Lcl1_%=:\n" \
    "ds_read_b32 v60, v32 offset:0\n" \
    "s_waitcnt lgkmcnt(0)\n" \
    "v_cmp_gt_u32_e32 vcc, 2, v60\n" \
    "s_cbranch_vccz Lce1_%=\n" \
    "s_sleep 1\n" \
    "s_sub_u32 s90, s90, 1\n" \
    "s_cbranch_scc0 Lcl1_%=\n" \
    "Lce1_%=:\n" \
    "ds_read_b128 v[128:131], v33 offset:58240\n" \
    "ds_read_b128 v[132:135], v33 offset:58304\n" \
    "ds_read_b128 v[136:139], v33 offset:58368\n" \
    "ds_read_b128 v[140:143], v33 offset:58432\n" \
    "ds_read_b128 v[144:147], v33 offset:58496\n" \
    "ds_read_b128 v[148:151], v33 offset:58560\n" \
    "ds_read_b128 v[152:155], v33 offset:58624\n" \
    "ds_read_b128 v[156:159], v33 offset:58688\n" \
    "s_waitcnt lgkmcnt(6)\n" \
    "v_mfma_f32_16x16x32_bf16 v[160:163], v[128:131], v[96:99], v[160:163]\n" \
    "v_mfma_f32_16x16x32_bf16 v[160:163], v[132:135], v[100:103], v[160:163]\n" \
    "s_waitcnt lgkmcnt(4)\n" \
    "v_mfma_f32_16x16x32_bf16 v[160:163], v[136:139], v[104:107], v[160:163]\n" \
    "v_mfma_f32_16x16x32_bf16 v[160:163], v[140:143], v[108:111], v[160:163]\n" \
    "s_waitcnt lgkmcnt(2)\n" \
    "v_mfma_f32_16x16x32_bf16 v[160:163], v[144:147], v[112:115], v[160:163]\n" \
    "v_mfma_f32_16x16x32_bf16 v[160:163], v[148:151], v[116:119], v[160:163]\n" \
    "s_waitcnt lgkmcnt(0)\n" \
    "v_mfma_f32_16x16x32_bf16 v[160:163], v[152:155], v[120:123], v[160:163]\n" \
    "v_mfma_f32_16x16x32_bf16 v[160:163], v[156:159], v[124:127], v[160:163]\n" \
    "s_nop 7\n" \
    "v_cvt_pk_bf16_f32 v0, v163, v162\n" \
    "v_cvt_pk_bf16_f32 v1, v161, v160\n" \
    "v_cvt_pk_bf16_f32 v2, v162, v161\n" \
    "ds_write_b16 v35, v0\n" \
    "ds_write_b32 v35, v2 offset:2\n" \
    "ds_write_b16_d16_hi v35, v1 offset:6\n" \
    "ds_write_b32 v35, v0 offset:1206\n" \
    "ds_write_b32 v35, v1 offset:1210\n" \
    "v_readlane_b32 s93, v0, 63\n" \
    "v_mov_b32_e32 v61, 1\n" \
    "ds_write_addtid_b32 v61 offset:56160\n" \
    "global_load_dwordx2 v[20:21], v41, s[94:95] offset:0\n" \
    "global_load_dwordx2 v[22:23], v41, s[94:95] offset:1152\n" \
    "s_add_u32 s94, s94, 589824\n" \
    "s_addc_u32 s95, s95, 0\n" \
    "s_mov_b32 s90, 0x100000\n" \
    "Lcl2_%=:\n" \
    "ds_read_b32 v60, v32 offset:0\n" \
    "s_waitcnt lgkmcnt(0)\n" \
    "v_cmp_gt_u32_e32 vcc, 3, v60\n" \
    "s_cbranch_vccz Lce2_%=\n" \
    "s_sleep 1\n" \
    "s_sub_u32 s90, s90, 1\n" \
    "s_cbranch_scc0 Lcl2_%=\n" \
    "Lce2_%=:\n" \
    "ds_read_b128 v[128:131], v34 offset:0\n" \
    "ds_read_b128 v[132:135], v34 offset:64\n" \
    "ds_read_b128 v[136:139], v34 offset:128\n" \
    "ds_read_b128 v[140:143], v34 offset:192\n" \
    "ds_read_b128 v[144:147], v34 offset:256\n" \
    "ds_read_b128 v[148:151], v34 offset:320\n" \
    "ds_read_b128 v[152:155], v34 offset:384\n" \
    "ds_read_b128 v[156:159], v34 offset:448\n" \
    "s_waitcnt lgkmcnt(6)\n" \
    "v_mfma_f32_16x16x32_bf16 v[160:163], v[128:131], v[96:99], 0\n" \
    "v_mfma_f32_16x16x32_bf16 v[160:163], v[132:135], v[100:103], v[160:163]\n" \
    "s_waitcnt lgkmcnt(4)\n" \
    "v_mfma_f32_16x16x32_bf16 v[160:163], v[136:139], v[104:107], v[160:163]\n" \
    "v_mfma_f32_16x16x32_bf16 v[160:163], v[140:143], v[108:111], v[160:163]\n" \
    "s_waitcnt lgkmcnt(2)\n" \
    "v_mfma_f32_16x16x32_bf16 v[160:163], v[144:147], v[112:115], v[160:163]\n" \
    "v_mfma_f32_16x16x32_bf16 v[160:163], v[148:151], v[116:119], v[160:163]\n" \
    "s_waitcnt lgkmcnt(0)\n" \
    "v_mfma_f32_16x16x32_bf16 v[160:163], v[152:155], v[120:123], v[160:163]\n" \
    "v_mfma_f32_16x16x32_bf16 v[160:163], v[156:159], v[124:127], v[160:163]\n" \
    "ds_read_b32 v60, v32\n" \
    "s_nop 6\n" \
    "v_cvt_pk_bf16_f32 v1, v160, v161\n" \
    "v_cvt_pk_bf16_f32 v2, v162, v163\n" \
    "v_perm_b32 v6, v1, v1, v38\n" \
    "v_perm_b32 v7, s93, v1, v40\n" \
    "v_perm_b32 v4, v2, v2, v38\n" \
    "v_perm_b32 v5, v2, v2, v39\n" \
    "ds_write_b128 v36, v[4:7]\n" \
    "s_mov_b64 exec, 1\n" \
    "ds_write_b16 v51, v1 offset:1792\n" \
    "ds_write_b16 v51, v1 offset:1794\n" \
    "s_brev_b64 exec, 1\n" \
    "ds_write2_b32 v51, v4, v4 offset0:18 offset1:19\n" \
    "ds_write_b16 v51, v4 offset:1278\n" \
    "s_mov_b64 exec, -1\n" \
    "v_mov_b32_e32 v61, 2\n" \
    "ds_write_addtid_b32 v61 offset:56160\n" \
    "global_load_dwordx2 v[24:25], v41, s[94:95] offset:0\n" \
    "global_load_dwordx2 v[26:27], v41, s[94:95] offset:1152\n" \
    "v_mov_b32_e32 v61, 2\n" \
    "s_waitcnt vmcnt(6)\n" \
    "v_perm_b32 v8, v14, v12, s91\n" \
    "v_perm_b32 v9, v14, v12, s92\n" \
    "v_perm_b32 v10, v15, v13, s91\n" \
    "v_perm_b32 v11, v15, v13, s92\n" \
    "ds_write2st64_b32 v42, v8, v9 offset0:0 offset1:18\n" \
    "ds_write2st64_b32 v42, v10, v11 offset0:36 offset1:54\n" \
    "ds_write_addtid_b32 v61 offset:56416\n" \
    "s_waitcnt lgkmcnt(9)\n" \
    "v_cmp_gt_u32_e32 vcc, 4, v60\n" \
    "s_cbranch_vccz Lce3_%=\n" \
    "s_mov_b32 s90, 0x100000\n" \
    "Lcl3_%=:\n" \
    "s_sleep 1\n" \
    "ds_read_b32 v60, v32 offset:0\n" \
    "s_waitcnt lgkmcnt(0)\n" \
    "v_cmp_gt_u32_e32 vcc, 4, v60\n" \
    "s_cbranch_vccz Lce3_%=\n" \
    "s_sub_u32 s90, s90, 1\n" \
    "s_cbranch_scc0 Lcl3_%=\n" \
    "Lce3_%=:\n" \
    "ds_read_b128 v[128:131], v56 offset:0\n" \
    "ds_read_b128 v[132:135], v56 offset:64\n" \
    "ds_read_b128 v[136:139], v56 offset:128\n" \
    "ds_read_b128 v[140:143], v56 offset:192\n" \
    "ds_read_b128 v[144:147], v56 offset:256\n" \
    "ds_read_b128 v[148:151], v56 offset:320\n" \
    "ds_read_b128 v[152:155], v56 offset:384\n" \
    "ds_read_b128 v[156:159], v56 offset:448\n" \
    "s_waitcnt lgkmcnt(6)\n" \
    "v_mfma_f32_16x16x32_bf16 v[160:163], v[128:131], v[96:99], 0\n" \
    "v_mfma_f32_16x16x32_bf16 v[160:163], v[132:135], v[100:103], v[160:163]\n" \
    "s_waitcnt lgkmcnt(4)\n" \
    "v_mfma_f32_16x16x32_bf16 v[160:163], v[136:139], v[104:107], v[160:163]\n" \
    "v_mfma_f32_16x16x32_bf16 v[160:163], v[140:143], v[108:111], v[160:163]\n" \
    "s_waitcnt lgkmcnt(2)\n" \
    "v_mfma_f32_16x16x32_bf16 v[160:163], v[144:147], v[112:115], v[160:163]\n" \
    "v_mfma_f32_16x16x32_bf16 v[160:163], v[148:151], v[116:119], v[160:163]\n" \
    "s_waitcnt lgkmcnt(0)\n" \
    "v_mfma_f32_16x16x32_bf16 v[160:163], v[152:155], v[120:123], v[160:163]\n" \
    "v_mfma_f32_16x16x32_bf16 v[160:163], v[156:159], v[124:127], v[160:163]\n" \
    "s_nop 7\n" \
    "v_cvt_pk_bf16_f32 v0, v160, v161\n" \
    "v_cvt_pk_bf16_f32 v1, v162, v163\n" \
    "ds_write_b16 v37, v0 offset:96\n" \
    "ds_write_b16_d16_hi v37, v0 offset:64\n" \
    "ds_write_b16 v37, v1 offset:32\n" \
    "ds_write_b16_d16_hi v37, v1\n" \
    "v_mov_b32_e32 v61, 3\n" \
    "ds_write_addtid_b32 v61 offset:56160\n" \
    "s_mov_b32 s90, 0x100000\n" \
    "Lcl4_%=:\n" \
    "ds_read_b32 v60, v45 offset:2336\n" \
    "s_waitcnt lgkmcnt(0)\n" \
    "v_cmp_gt_u32_e32 vcc, 2, v60\n" \
    "s_cbranch_vccz Lce4_%=\n" \
    "s_sleep 1\n" \
    "s_sub_u32 s90, s90, 1\n" \
    "s_cbranch_scc0 Lcl4_%=\n" \
    "Lce4_%=:\n" \
    "v_mov_b32_e32 v0, %[ak]\n" \
    "v_mov_b32_e32 v1, s98\n" \
    "s_mov_b32 exec_lo, %[am]\n" \
    "s_mov_b32 exec_hi, 0\n" \
    "global_store_dword v0, v1, %[ws] sc1\n" \
    "s_mov_b64 exec, -1\n" \
    "global_load_dwordx4 v[80:83], v44, %[ws] sc1\n" \
    "v_add_u32_e32 v44, 0x1000, v44\n" \
    "global_load_dwordx4 v[84:87], v44, %[ws] sc1\n" \
    "v_add_u32_e32 v44, 0x1000, v44\n" \
    "global_load_dwordx4 v[88:91], v44, %[ws] sc1\n" \
    "v_add_u32_e32 v44, 0x1000, v44\n" \
    "global_load_dwordx4 v[92:95], v44, %[ws] sc1\n" \
    "v_add_u32_e32 v44, 0xffffd000, v44\n" \
    "v_mov_b32_e32 v61, 4\n" \
    "s_waitcnt vmcnt(9)\n" \
    "v_perm_b32 v8, v18, v16, s91\n" \
    "v_perm_b32 v9, v18, v16, s92\n" \
    "v_perm_b32 v10, v19, v17, s91\n" \
    "v_perm_b32 v11, v19, v17, s92\n" \
    "ds_write2st64_b32 v42, v8, v9 offset0:4 offset1:22\n" \
    "ds_write2st64_b32 v42, v10, v11 offset0:40 offset1:58\n" \
    "ds_write_addtid_b32 v61 offset:56416\n" \
    "v_mov_b32_e32 v61, 6\n" \
    "s_waitcnt vmcnt(7)\n" \
    "v_perm_b32 v8, v22, v20, s91\n" \
    "v_perm_b32 v9, v22, v20, s92\n" \
    "v_perm_b32 v10, v23, v21, s91\n" \
    "v_perm_b32 v11, v23, v21, s92\n" \
    "ds_write2st64_b32 v42, v8, v9 offset0:8 offset1:26\n" \
    "ds_write2st64_b32 v42, v10, v11 offset0:44 offset1:62\n" \
    "ds_write_addtid_b32 v61 offset:56416\n" \
    "v_mov_b32_e32 v61, 8\n" \
    "s_waitcnt vmcnt(5)\n" \
    "v_perm_b32 v8, v26, v24, s91\n" \
    "v_perm_b32 v9, v26, v24, s92\n" \
    "v_perm_b32 v10, v27, v25, s91\n" \
    "v_perm_b32 v11, v27, v25, s92\n" \
    "ds_write2st64_b32 v42, v8, v9 offset0:12 offset1:30\n" \
    "ds_write2st64_b32 v42, v10, v11 offset0:48 offset1:66\n" \
    "ds_write_addtid_b32 v61 offset:56416\n" \
    "global_load_dword v63, v48, %[wd]\n" \
    "s_mov_b32 s93, 0xffff0000\n" \
    "v_add_u32_e32 v58, 58240, v43\n" \
    "s_waitcnt vmcnt(4)\n" \
    "v_cmp_ne_u32_e32 vcc, s98, v81\n" \
    "v_cmp_ne_u32_e64 s[88:89], s98, v83\n" \
    "s_or_b64 vcc, vcc, s[88:89]\n" \
    "s_cbranch_scc1 Lcs5_%=\n" \
    "Lce5_%=:\n" \
    "v_cvt_f32_f16_e32 v104, v80\n" \
    "v_cvt_f32_f16_e32 v105, v82\n" \
    "v_lshlrev_b32_e32 v96, 16, v12\n" \
    "v_and_b32_e32 v97, s93, v12\n" \
    "v_lshlrev_b32_e32 v98, 16, v13\n" \
    "v_and_b32_e32 v99, s93, v13\n" \
    "v_lshlrev_b32_e32 v100, 16, v14\n" \
    "v_and_b32_e32 v101, s93, v14\n" \
    "v_lshlrev_b32_e32 v102, 16, v15\n" \
    "v_and_b32_e32 v103, s93, v15\n" \
    "v_sub_f32_e32 v106, 1.0, v104\n" \
    "v_sub_f32_e32 v107, 1.0, v105\n" \
    "v_mul_f32_e32 v96, v106, v96\n" \
    "v_mul_f32_e32 v97, v106, v97\n" \
    "v_mul_f32_e32 v98, v106, v98\n" \
    "v_mul_f32_e32 v99, v106, v99\n" \
    "v_mul_f32_e32 v100, v107, v100\n" \
    "v_mul_f32_e32 v101, v107, v101\n" \
    "v_mul_f32_e32 v102, v107, v102\n" \
    "v_mul_f32_e32 v103, v107, v103\n" \
    "s_mov_b32 s90, 0x100000\n" \
    "Lcl6_%=:\n" \
    "ds_read_b32 v60, v45 offset:0\n" \
    "ds_read_b64 v[64:65], v43 offset:0\n" \
    "ds_read_b64 v[66:67], v43 offset:512\n" \
    "ds_read_b64 v[68:69], v43 offset:1024\n" \
    "ds_read_b64 v[70:71], v43 offset:1536\n" \
    "s_waitcnt lgkmcnt(0)\n" \
    "v_cmp_gt_u32_e32 vcc, s99, v60\n" \
    "s_cbranch_vccz Lce6_%=\n" \
    "s_sleep 2\n" \
    "s_sub_u32 s90, s90, 1\n" \
    "s_cbranch_scc0 Lcl6_%=\n" \
    "Lce6_%=:\n" \
    "v_fmac_f32_e32 v96, v104, v64\n" \
    "v_fmac_f32_e32 v97, v104, v66\n" \
    "v_fmac_f32_e32 v98, v104, v68\n" \
    "v_fmac_f32_e32 v99, v104, v70\n" \
    "v_cvt_pk_bf16_f32 v108, v96, v97\n" \
    "v_cvt_pk_bf16_f32 v109, v98, v99\n" \
    "global_store_dwordx2 v41, v[108:109], s[96:97] offset:0\n" \
    "v_fmac_f32_e32 v100, v105, v65\n" \
    "v_fmac_f32_e32 v101, v105, v67\n" \
    "v_fmac_f32_e32 v102, v105, v69\n" \
    "v_fmac_f32_e32 v103, v105, v71\n" \
    "v_cvt_pk_bf16_f32 v110, v100, v101\n" \
    "v_cvt_pk_bf16_f32 v111, v102, v103\n" \
    "global_store_dwordx2 v41, v[110:111], s[96:97] offset:1152\n" \
    "s_add_u32 s96, s96, 589824\n" \
    "s_addc_u32 s97, s97, 0\n" \
    "s_add_u32 s99, s99, 2\n" \
    "s_waitcnt vmcnt(5)\n" \
    "v_cmp_ne_u32_e32 vcc, s98, v85\n" \
    "v_cmp_ne_u32_e64 s[88:89], s98, v87\n" \
    "s_or_b64 vcc, vcc, s[88:89]\n" \
    "s_cbranch_scc1 Lcs7_%=\n" \
    "Lce7_%=:\n" \
    "v_cvt_f32_f16_e32 v104, v84\n" \
    "v_cvt_f32_f16_e32 v105, v86\n" \
    "v_lshlrev_b32_e32 v96, 16, v16\n" \
    "v_and_b32_e32 v97, s93, v16\n" \
    "v_lshlrev_b32_e32 v98, 16, v17\n" \
    "v_and_b32_e32 v99, s93, v17\n" \
    "v_lshlrev_b32_e32 v100, 16, v18\n" \
    "v_and_b32_e32 v101, s93, v18\n" \
    "v_lshlrev_b32_e32 v102, 16, v19\n" \
    "v_and_b32_e32 v103, s93, v19\n" \
    "v_sub_f32_e32 v106, 1.0, v104\n" \
    "v_sub_f32_e32 v107, 1.0, v105\n" \
    "v_mul_f32_e32 v96, v106, v96\n" \
    "v_mul_f32_e32 v97, v106, v97\n" \
    "v_mul_f32_e32 v98, v106, v98\n" \
    "v_mul_f32_e32 v99, v106, v99\n" \
    "v_mul_f32_e32 v100, v107, v100\n" \
    "v_mul_f32_e32 v101, v107, v101\n" \
    "v_mul_f32_e32 v102, v107, v102\n" \
    "v_mul_f32_e32 v103, v107, v103\n" \
    "s_mov_b32 s90, 0x100000\n" \
    "Lcl8_%=:\n" \
    "ds_read_b32 v60, v47 offset:0\n" \
    "ds_read_b64 v[64:65], v43 offset:8320\n" \
    "ds_read_b64 v[66:67], v43 offset:8832\n" \
    "ds_read_b64 v[68:69], v43 offset:9344\n" \
    "ds_read_b64 v[70:71], v43 offset:9856\n" \
    "ds_read_b64 v[72:73], v58 offset:0\n" \
    "ds_read_b64 v[74:75], v58 offset:512\n" \
    "ds_read_b64 v[76:77], v58 offset:1024\n" \
    "ds_read_b64 v[78:79], v58 offset:1536\n" \
    "s_waitcnt lgkmcnt(0)\n" \
    "v_cmp_gt_u32_e32 vcc, s99, v60\n" \
    "s_cbranch_vccz Lce8_%=\n" \
    "s_sleep 2\n" \
    "s_sub_u32 s90, s90, 1\n" \
    "s_cbranch_scc0 Lcl8_%=\n" \
    "Lce8_%=:\n" \
    "v_fmac_f32_e32 v96, v104, v64\n" \
    "v_fmac_f32_e32 v97, v104, v66\n" \
    "v_fmac_f32_e32 v98, v104, v68\n" \
    "v_fmac_f32_e32 v99, v104, v70\n" \
    "v_fmac_f32_e32 v96, v104, v72\n" \
    "v_fmac_f32_e32 v97, v104, v74\n" \
    "v_fmac_f32_e32 v98, v104, v76\n" \
    "v_fmac_f32_e32 v99, v104, v78\n" \
    "v_cvt_pk_bf16_f32 v112, v96, v97\n" \
    "v_cvt_pk_bf16_f32 v113, v98, v99\n" \
    "global_store_dwordx2 v41, v[112:113], s[96:97] offset:0\n" \
    "v_fmac_f32_e32 v100, v105, v65\n" \
    "v_fmac_f32_e32 v101, v105, v67\n" \
    "v_fmac_f32_e32 v102, v105, v69\n" \
    "v_fmac_f32_e32 v103, v105, v71\n" \
    "v_fmac_f32_e32 v100, v105, v73\n" \
    "v_fmac_f32_e32 v101, v105, v75\n" \
    "v_fmac_f32_e32 v102, v105, v77\n" \
    "v_fmac_f32_e32 v103, v105, v79\n" \
    "v_cvt_pk_bf16_f32 v114, v100, v101\n" \
    "v_cvt_pk_bf16_f32 v115, v102, v103\n" \
    "global_store_dwordx2 v41, v[114:115], s[96:97] offset:1152\n" \
    "s_add_u32 s96, s96, 589824\n" \
    "s_addc_u32 s97, s97, 0\n" \
    "s_add_u32 s99, s99, 2\n" \
    "s_waitcnt vmcnt(6)\n" \
    "v_cmp_ne_u32_e32 vcc, s98, v89\n" \
    "v_cmp_ne_u32_e64 s[88:89], s98, v91\n" \
    "s_or_b64 vcc, vcc, s[88:89]\n" \
    "s_cbranch_scc1 Lcs9_%=\n" \
    "Lce9_%=:\n" \
    "v_cvt_f32_f16_e32 v104, v88\n" \
    "v_cvt_f32_f16_e32 v105, v90\n" \
    "v_lshlrev_b32_e32 v96, 16, v20\n" \
    "v_and_b32_e32 v97, s93, v20\n" \
    "v_lshlrev_b32_e32 v98, 16, v21\n" \
    "v_and_b32_e32 v99, s93, v21\n" \
    "v_lshlrev_b32_e32 v100, 16, v22\n" \
    "v_and_b32_e32 v101, s93, v22\n" \
    "v_lshlrev_b32_e32 v102, 16, v23\n" \
    "v_and_b32_e32 v103, s93, v23\n" \
    "v_sub_f32_e32 v106, 1.0, v104\n" \
    "v_sub_f32_e32 v107, 1.0, v105\n" \
    "v_mul_f32_e32 v96, v106, v96\n" \
    "v_mul_f32_e32 v97, v106, v97\n" \
    "v_mul_f32_e32 v98, v106, v98\n" \
    "v_mul_f32_e32 v99, v106, v99\n" \
    "v_mul_f32_e32 v100, v107, v100\n" \
    "v_mul_f32_e32 v101, v107, v101\n" \
    "v_mul_f32_e32 v102, v107, v102\n" \
    "v_mul_f32_e32 v103, v107, v103\n" \
    "s_mov_b32 s90, 0x100000\n" \
    "Lcl10_%=:\n" \
    "ds_read_b32 v60, v47 offset:0\n" \
    "ds_read_b64 v[64:65], v43 offset:16640\n" \
    "ds_read_b64 v[66:67], v43 offset:17152\n" \
    "ds_read_b64 v[68:69], v43 offset:17664\n" \
    "ds_read_b64 v[70:71], v43 offset:18176\n" \
    "ds_read_b64 v[72:73], v58 offset:8320\n" \
    "ds_read_b64 v[74:75], v58 offset:8832\n" \
    "ds_read_b64 v[76:77], v58 offset:9344\n" \
    "ds_read_b64 v[78:79], v58 offset:9856\n" \
    "s_waitcnt lgkmcnt(0)\n" \
    "v_cmp_gt_u32_e32 vcc, s99, v60\n" \
    "s_cbranch_vccz Lce10_%=\n" \
    "s_sleep 2\n" \
    "s_sub_u32 s90, s90, 1\n" \
    "s_cbranch_scc0 Lcl10_%=\n" \
    "Lce10_%=:\n" \
    "v_fmac_f32_e32 v96, v104, v64\n" \
    "v_fmac_f32_e32 v97, v104, v66\n" \
    "v_fmac_f32_e32 v98, v104, v68\n" \
    "v_fmac_f32_e32 v99, v104, v70\n" \
    "v_fmac_f32_e32 v96, v104, v72\n" \
    "v_fmac_f32_e32 v97, v104, v74\n" \
    "v_fmac_f32_e32 v98, v104, v76\n" \
    "v_fmac_f32_e32 v99, v104, v78\n" \
    "v_cvt_pk_bf16_f32 v108, v96, v97\n" \
    "v_cvt_pk_bf16_f32 v109, v98, v99\n" \
    "global_store_dwordx2 v41, v[108:109], s[96:97] offset:0\n" \
    "v_fmac_f32_e32 v100, v105, v65\n" \
    "v_fmac_f32_e32 v101, v105, v67\n" \
    "v_fmac_f32_e32 v102, v105, v69\n" \
    "v_fmac_f32_e32 v103, v105, v71\n" \
    "v_fmac_f32_e32 v100, v105, v73\n" \
    "v_fmac_f32_e32 v101, v105, v75\n" \
    "v_fmac_f32_e32 v102, v105, v77\n" \
    "v_fmac_f32_e32 v103, v105, v79\n" \
    "v_cvt_pk_bf16_f32 v110, v100, v101\n" \
    "v_cvt_pk_bf16_f32 v111, v102, v103\n" \
    "global_store_dwordx2 v41, v[110:111], s[96:97] offset:1152\n" \
    "s_add_u32 s96, s96, 589824\n" \
    "s_addc_u32 s97, s97, 0\n" \
    "s_add_u32 s99, s99, 2\n" \
    "s_waitcnt vmcnt(7)\n" \
    "v_cmp_ne_u32_e32 vcc, s98, v93\n" \
    "v_cmp_ne_u32_e64 s[88:89], s98, v95\n" \
    "s_or_b64 vcc, vcc, s[88:89]\n" \
    "s_cbranch_scc1 Lcs11_%=\n" \
    "Lce11_%=:\n" \
    "v_cvt_f32_f16_e32 v104, v92\n" \
    "v_cvt_f32_f16_e32 v105, v94\n" \
    "v_lshlrev_b32_e32 v96, 16, v24\n" \
    "v_and_b32_e32 v97, s93, v24\n" \
    "v_lshlrev_b32_e32 v98, 16, v25\n" \
    "v_and_b32_e32 v99, s93, v25\n" \
    "v_lshlrev_b32_e32 v100, 16, v26\n" \
    "v_and_b32_e32 v101, s93, v26\n" \
    "v_lshlrev_b32_e32 v102, 16, v27\n" \
    "v_and_b32_e32 v103, s93, v27\n" \
    "v_sub_f32_e32 v106, 1.0, v104\n" \
    "v_sub_f32_e32 v107, 1.0, v105\n" \
    "v_mul_f32_e32 v96, v106, v96\n" \
    "v_mul_f32_e32 v97, v106, v97\n" \
    "v_mul_f32_e32 v98, v106, v98\n" \
    "v_mul_f32_e32 v99, v106, v99\n" \
    "v_mul_f32_e32 v100, v107, v100\n" \
    "v_mul_f32_e32 v101, v107, v101\n" \
    "v_mul_f32_e32 v102, v107, v102\n" \
    "v_mul_f32_e32 v103, v107, v103\n" \
    "s_mov_b32 s90, 0x100000\n" \
    "Lcl12_%=:\n" \
    "ds_read_b32 v60, v47 offset:0\n" \
    "ds_read_b64 v[64:65], v43 offset:24960\n" \
    "ds_read_b64 v[66:67], v43 offset:25472\n" \
    "ds_read_b64 v[68:69], v43 offset:25984\n" \
    "ds_read_b64 v[70:71], v43 offset:26496\n" \
    "ds_read_b64 v[72:73], v58 offset:16640\n" \
    "ds_read_b64 v[74:75], v58 offset:17152\n" \
    "ds_read_b64 v[76:77], v58 offset:17664\n" \
    "ds_read_b64 v[78:79], v58 offset:18176\n" \
    "s_waitcnt lgkmcnt(0)\n" \
    "v_cmp_gt_u32_e32 vcc, s99, v60\n" \
    "s_cbranch_vccz Lce12_%=\n" \
    "s_sub_u32 s90, s90, 1\n" \
    "s_cbranch_scc0 Lcl12_%=\n" \
    "Lce12_%=:\n" \
    "v_fmac_f32_e32 v96, v104, v64\n" \
    "v_fmac_f32_e32 v97, v104, v66\n" \
    "v_fmac_f32_e32 v98, v104, v68\n" \
    "v_fmac_f32_e32 v99, v104, v70\n" \
    "v_fmac_f32_e32 v96, v104, v72\n" \
    "v_fmac_f32_e32 v97, v104, v74\n" \
    "v_fmac_f32_e32 v98, v104, v76\n" \
    "v_fmac_f32_e32 v99, v104, v78\n" \
    "v_cvt_pk_bf16_f32 v112, v96, v97\n" \
    "v_cvt_pk_bf16_f32 v113, v98, v99\n" \
    "global_store_dwordx2 v41, v[112:113], s[96:97] offset:0\n" \
    "v_fmac_f32_e32 v100, v105, v65\n" \
    "v_fmac_f32_e32 v101, v105, v67\n" \
    "v_fmac_f32_e32 v102, v105, v69\n" \
    "v_fmac_f32_e32 v103, v105, v71\n" \
    "v_fmac_f32_e32 v100, v105, v73\n" \
    "v_fmac_f32_e32 v101, v105, v75\n" \
    "v_fmac_f32_e32 v102, v105, v77\n" \
    "v_fmac_f32_e32 v103, v105, v79\n" \
    "v_cvt_pk_bf16_f32 v114, v100, v101\n" \
    "v_cvt_pk_bf16_f32 v115, v102, v103\n" \
    "global_store_dwordx2 v41, v[114:115], s[96:97] offset:1152\n" \
    "v_mov_b32_e32 v61, 20\n" \
    "s_waitcnt vmcnt(0)\n" \
    "ds_write_addtid_b32 v61 offset:56416\n" \
    "s_branch Lcx_%=\n" \
    "Lcs5_%=:\n" \
    "s_mov_b32 s90, 0x20000\n" \
    "v_add_u32_e32 v0, 0, v44\n" \
    "Lcl5_%=:\n" \
    "s_sleep 4\n" \
    "global_load_dwordx4 v[80:83], v0, %[ws] sc1\n" \
    "s_waitcnt vmcnt(0)\n" \
    "v_cmp_ne_u32_e32 vcc, s98, v81\n" \
    "v_cmp_ne_u32_e64 s[88:89], s98, v83\n" \
    "s_nop 2\n" \
    "s_or_b64 vcc, vcc, s[88:89]\n" \
    "s_cbranch_scc0 Lce5_%=\n" \
    "s_sub_u32 s90, s90, 1\n" \
    "s_cbranch_scc0 Lcl5_%=\n" \
    "s_branch Lce5_%=\n" \
    "Lcs7_%=:\n" \
    "s_mov_b32 s90, 0x20000\n" \
    "v_add_u32_e32 v0, 4096, v44\n" \
    "Lcl7_%=:\n" \
    "s_sleep 4\n" \
    "global_load_dwordx4 v[84:87], v0, %[ws] sc1\n" \
    "s_waitcnt vmcnt(0)\n" \
    "v_cmp_ne_u32_e32 vcc, s98, v85\n" \
    "v_cmp_ne_u32_e64 s[88:89], s98, v87\n" \
    "s_nop 2\n" \
    "s_or_b64 vcc, vcc, s[88:89]\n" \
    "s_cbranch_scc0 Lce7_%=\n" \
    "s_sub_u32 s90, s90, 1\n" \
    "s_cbranch_scc0 Lcl7_%=\n" \
    "s_branch Lce7_%=\n" \
    "Lcs9_%=:\n" \
    "s_mov_b32 s90, 0x20000\n" \
    "v_add_u32_e32 v0, 8192, v44\n" \
    "Lcl9_%=:\n" \
    "s_sleep 4\n" \
    "global_load_dwordx4 v[88:91], v0, %[ws] sc1\n" \
    "s_waitcnt vmcnt(0)\n" \
    "v_cmp_ne_u32_e32 vcc, s98, v89\n" \
    "v_cmp_ne_u32_e64 s[88:89], s98, v91\n" \
    "s_nop 2\n" \
    "s_or_b64 vcc, vcc, s[88:89]\n" \
    "s_cbranch_scc0 Lce9_%=\n" \
    "s_sub_u32 s90, s90, 1\n" \
    "s_cbranch_scc0 Lcl9_%=\n" \
    "s_branch Lce9_%=\n" \
    "Lcs11_%=:\n" \
    "s_mov_b32 s90, 0x20000\n" \
    "v_add_u32_e32 v0, 12288, v44\n" \
    "Lcl11_%=:\n" \
    "s_sleep 4\n" \
    "global_load_dwordx4 v[92:95], v0, %[ws] sc1\n" \
    "s_waitcnt vmcnt(0)\n" \
    "v_cmp_ne_u32_e32 vcc, s98, v93\n" \
    "v_cmp_ne_u32_e64 s[88:89], s98, v95\n" \
    "s_nop 2\n" \
    "s_or_b64 vcc, vcc, s[88:89]\n" \
    "s_cbranch_scc0 Lce11_%=\n" \
    "s_sub_u32 s90, s90, 1\n" \
    "s_cbranch_scc0 Lcl11_%=\n" \
    "s_branch Lce11_%=\n" \
    "Lcx_%=:\n"
#define CONV_C_CLOB "v0", "v1", "v2", "v4", "v5", "v6", "v7", "v8", "v9", "v10", "v11", "v12", "v13", "v14", "v15", "v16", "v17", "v18", "v19", "v20", "v21", "v22", "v23", "v24", "v25", "v26", "v27", "v32", "v33", "v34", "v35", "v36", "v37", "v38", "v39", "v40", "v41", "v42", "v43", "v44", "v45", "v47", "v48", "v49", "v50", "v51", "v56", "v58", "v60", "v61", "v62", "v63", "v64", "v65", "v66", "v67", "v68", "v69", "v70", "v71", "v72", "v73", "v74", "v75", "v76", "v77", "v78", "v79", "v80", "v81", "v82", "v83", "v84", "v85", "v86", "v87", "v88", "v89", "v90", "v91", "v92", "v93", "v94", "v95", "v96", "v97", "v98", "v99", "v100", "v101", "v102", "v103", "v104", "v105", "v106", "v107", "v108", "v109", "v110", "v111", "v112", "v113", "v114", "v115", "v116", "v117", "v118", "v119", "v120", "v121", "v122", "v123", "v124", "v125", "v126", "v127", "v128", "v129", "v130", "v131", "v132", "v133", "v134", "v135", "v136", "v137", "v138", "v139", "v140", "v141", "v142", "v143", "v144", "v145", "v146", "v147", "v148", "v149", "v150", "v151", "v152", "v153", "v154", "v155", "v156", "v157", "v158", "v159", "v160", "v161", "v162", "v163"
#define ITEM_ASM \
    "v_mbcnt_lo_u32_b32 v53, -1, 0\n" \
    "v_mbcnt_hi_u32_b32 v53, -1, v53\n" \
    "s_lshl_b32 s88, %[it], 7\n" \
    "s_add_u32 s89, s88, %[kb]\n" \
    "s_add_u32 s88, s88, %[cb]\n" \
    "v_and_b32_e32 v28, 3, v53\n" \
    "v_bfe_u32 v31, v53, 2, 2\n" \
    "v_lshrrev_b32_e32 v52, 4, v53\n" \
    "v_lshlrev_b32_e32 v30, 3, v31\n" \
    "v_lshl_or_b32 v30, v28, 5, v30\n" \
    "v_add_u32_e32 v30, s89, v30\n" \
    "v_lshlrev_b32_e32 v28, 4, v28\n" \
    "v_lshl_or_b32 v28, v52, 6, v28\n" \
    "v_and_b32_e32 v52, 1, v31\n" \
    "v_mul_u32_u24_e32 v52, 1152, v52\n" \
    "s_mov_b32 s89, %[wl]\n" \
    "v_add3_u32 v28, v28, v52, s89\n" \
    "v_lshlrev_b32_e32 v29, 14, v53\n" \
    "v_add_u32_e32 v29, s88, v29\n" \
    "v_sub_u32_e32 v31, 15, v31\n" \
    "v_mov_b64 v[16:17], 0\n" \
    "v_mov_b64 v[18:19], 0\n" \
    "v_mov_b64 v[20:21], 0\n" \
    "v_mov_b64 v[22:23], 0\n" \
    "s_mov_b32 vcc_lo, 0xaaaaaaaa\n" \
    "v_mov_b32_e32 v52, 0x07060302\n" \
    "s_mov_b32 s88, 0x05040100\n" \
    "v_mov_b32_e32 v53, s88\n" \
    "v_cndmask_b32_e32 v52, v53, v52, vcc\n" \
    "s_mov_b32 s88, 0x80008000\n" \
    "s_mov_b32 s89, 0xffff0000\n" \
    "s_mov_b32 s92, 0x07060304\n" \
    "s_mov_b32 s93, 0x07060204\n" \
    "s_mov_b32 vcc_lo, 0xcccccccc\n" \
    "v_mov_b32_e32 v91, %[fl]\n" \
    "v_mov_b32_e32 v92, 1\n" \
    "s_waitcnt vmcnt(18)\n" \
    "ds_read_b128 v[0:3], v28 offset:0\n" \
    "ds_read_b128 v[4:7], v28 offset:256\n" \
    "ds_read_b128 v[8:11], v28 offset:512\n" \
    "ds_read_b128 v[12:15], v28 offset:768\n" \
    "s_mov_b32 exec_hi, 0\n" \
    "ds_read_b128 v[16:19], v28 offset:1024\n" \
    "s_mov_b64 exec, -1\n" \
    "s_mov_b32 exec_lo, 0\n" \
    "ds_read_b128 v[20:23], v28 offset:896\n" \
    "s_mov_b32 exec_lo, -1\n" \
    "v_readfirstlane_b32 s98, v72\n" \
    "v_add_u32_e32 v54, -4, v31\n" \
    "v_add_u32_e32 v55, -8, v31\n" \
    "v_add_u32_e32 v90, -12, v31\n" \
    "s_add_u32 s98, s98, 1\n" \
    "s_max_u32 s98, s98, 1\n" \
    "v_mov_b32_e32 v25, s98\n" \
    "v_mov_b32_e32 v27, s98\n" \
    "s_waitcnt vmcnt(17)\n" \
    "s_waitcnt lgkmcnt(5)\n" \
    "v_mfma_f32_16x16x32_bf16 v[74:77], v[0:3], v[96:99], 0\n" \
    "s_waitcnt vmcnt(16)\n" \
    "v_mfma_f32_16x16x32_bf16 v[78:81], v[0:3], v[112:115], 0\n" \
    "s_waitcnt vmcnt(15)\n" \
    "v_mfma_f32_16x16x32_bf16 v[82:85], v[0:3], v[128:131], 0\n" \
    "s_waitcnt vmcnt(14)\n" \
    "v_mfma_f32_16x16x32_bf16 v[86:89], v[0:3], v[144:147], 0\n" \
    "s_waitcnt vmcnt(13)\n" \
    "s_waitcnt lgkmcnt(4)\n" \
    "v_mfma_f32_16x16x32_bf16 v[74:77], v[4:7], v[100:103], v[74:77]\n" \
    "s_waitcnt vmcnt(12)\n" \
    "v_mfma_f32_16x16x32_bf16 v[78:81], v[4:7], v[116:119], v[78:81]\n" \
    "s_waitcnt vmcnt(11)\n" \
    "v_mfma_f32_16x16x32_bf16 v[82:85], v[4:7], v[132:135], v[82:85]\n" \
    "s_waitcnt vmcnt(10)\n" \
    "v_mfma_f32_16x16x32_bf16 v[86:89], v[4:7], v[148:151], v[86:89]\n" \
    "s_waitcnt vmcnt(9)\n" \
    "s_waitcnt lgkmcnt(3)\n" \
    "v_mfma_f32_16x16x32_bf16 v[74:77], v[8:11], v[104:107], v[74:77]\n" \
    "s_waitcnt vmcnt(8)\n" \
    "v_mfma_f32_16x16x32_bf16 v[78:81], v[8:11], v[120:123], v[78:81]\n" \
    "s_waitcnt vmcnt(7)\n" \
    "v_mfma_f32_16x16x32_bf16 v[82:85], v[8:11], v[136:139], v[82:85]\n" \
    "s_waitcnt vmcnt(6)\n" \
    "v_mfma_f32_16x16x32_bf16 v[86:89], v[8:11], v[152:155], v[86:89]\n" \
    "s_waitcnt vmcnt(5)\n" \
    "s_waitcnt lgkmcnt(2)\n" \
    "v_mfma_f32_16x16x32_bf16 v[74:77], v[12:15], v[108:111], v[74:77]\n" \
    "s_waitcnt vmcnt(4)\n" \
    "v_mfma_f32_16x16x32_bf16 v[78:81], v[12:15], v[124:127], v[78:81]\n" \
    "s_waitcnt vmcnt(3)\n" \
    "v_mfma_f32_16x16x32_bf16 v[82:85], v[12:15], v[140:143], v[82:85]\n" \
    "s_waitcnt vmcnt(2)\n" \
    "v_mfma_f32_16x16x32_bf16 v[86:89], v[12:15], v[156:159], v[86:89]\n" \
    "s_waitcnt vmcnt(1)\n" \
    "s_waitcnt lgkmcnt(1)\n" \
    "v_mfma_f32_16x16x32_bf16 v[74:77], v[16:19], v[160:163], v[74:77]\n" \
    "s_waitcnt lgkmcnt(0)\n" \
    "v_mfma_f32_16x16x32_bf16 v[78:81], v[20:23], v[160:163], v[78:81]\n" \
    "s_waitcnt vmcnt(0)\n" \
    "v_mfma_f32_16x16x32_bf16 v[82:85], v[16:19], v[164:167], v[82:85]\n" \
    "v_mfma_f32_16x16x32_bf16 v[86:89], v[20:23], v[164:167], v[86:89]\n" \
    "s_nop 2\n" \
    "v_mov_b32_dpp v0, v74 quad_perm:[0,0,0,0] row_mask:0xf bank_mask:0xf\n" \
    "s_nop 0\n" \
    "v_mov_b32_dpp v1, v78 quad_perm:[0,0,0,0] row_mask:0xf bank_mask:0xf\n" \
    "s_nop 0\n" \
    "v_mov_b32_dpp v2, v82 quad_perm:[0,0,0,0] row_mask:0xf bank_mask:0xf\n" \
    "v_mov_b32_dpp v3, v86 quad_perm:[0,0,0,0] row_mask:0xf bank_mask:0xf\n" \
    "v_add_f32_dpp v0, v75, v0 quad_perm:[1,1,1,1] row_mask:0xf bank_mask:0xf\n" \
    "v_add_f32_dpp v1, v79, v1 quad_perm:[1,1,1,1] row_mask:0xf bank_mask:0xf\n" \
    "v_add_f32_dpp v2, v83, v2 quad_perm:[1,1,1,1] row_mask:0xf bank_mask:0xf\n" \
    "v_add_f32_dpp v3, v87, v3 quad_perm:[1,1,1,1] row_mask:0xf bank_mask:0xf\n" \
    "v_add_f32_dpp v0, v76, v0 quad_perm:[2,2,2,2] row_mask:0xf bank_mask:0xf\n" \
    "v_add_f32_dpp v1, v80, v1 quad_perm:[2,2,2,2] row_mask:0xf bank_mask:0xf\n" \
    "v_add_f32_dpp v2, v84, v2 quad_perm:[2,2,2,2] row_mask:0xf bank_mask:0xf\n" \
    "v_add_f32_dpp v3, v88, v3 quad_perm:[2,2,2,2] row_mask:0xf bank_mask:0xf\n" \
    "v_add_f32_dpp v0, v77, v0 quad_perm:[3,3,3,3] row_mask:0xf bank_mask:0xf\n" \
    "v_add_f32_dpp v1, v81, v1 quad_perm:[3,3,3,3] row_mask:0xf bank_mask:0xf\n" \
    "v_add_f32_dpp v2, v85, v2 quad_perm:[3,3,3,3] row_mask:0xf bank_mask:0xf\n" \
    "v_add_f32_dpp v3, v89, v3 quad_perm:[3,3,3,3] row_mask:0xf bank_mask:0xf\n" \
    "v_max_f32_e32 v4, 0xff800000, v0\n" \
    "v_max_f32_e32 v5, 0xff800000, v1\n" \
    "v_max_f32_e32 v6, 0xff800000, v2\n" \
    "v_max_f32_e32 v7, 0xff800000, v3\n" \
    "v_cvt_pk_bf16_f32 v8, v4, v5\n" \
    "v_cvt_pk_bf16_f32 v9, v6, v7\n" \
    "v_pk_ashrrev_i16 v4, 15, v8 op_sel_hi:[0,1]\n" \
    "v_pk_ashrrev_i16 v5, 15, v9 op_sel_hi:[0,1]\n" \
    "v_bitop3_b32 v8, v8, v4, s88 bitop3:0x1e\n" \
    "v_bitop3_b32 v9, v9, v5, s88 bitop3:0x1e\n" \
    "v_lshl_or_b32 v4, v8, 16, v31\n" \
    "v_and_or_b32 v5, v8, s89, v54\n" \
    "v_lshl_or_b32 v6, v9, 16, v55\n" \
    "v_and_or_b32 v7, v9, s89, v90\n" \
    "v_max_u32_e32 v10, v4, v5\n" \
    "v_max_u32_e32 v12, v6, v7\n" \
    "v_min_u32_e32 v11, v4, v5\n" \
    "v_min_u32_e32 v13, v6, v7\n" \
    "v_max_u32_e32 v14, v10, v12\n" \
    "v_min_u32_e32 v17, v10, v12\n" \
    "v_min_u32_e32 v18, v10, v13\n" \
    "v_max3_u32 v15, v17, v11, v13\n" \
    "v_min_u32_e32 v16, v11, v12\n" \
    "v_max_u32_e32 v16, v16, v18\n" \
    "v_min_u32_dpp v4, v14, v14 row_ror:4 row_mask:0xf bank_mask:0xf\n" \
    "v_mov_b32_dpp v5, v15 row_ror:4 row_mask:0xf bank_mask:0xf\n" \
    "v_max_u32_dpp v10, v14, v14 row_ror:4 row_mask:0xf bank_mask:0xf\n" \
    "v_min_u32_dpp v17, v14, v15 row_ror:4 row_mask:0xf bank_mask:0xf\n" \
    "v_max_u32_dpp v6, v16, v16 row_ror:4 row_mask:0xf bank_mask:0xf\n" \
    "v_max3_u32 v11, v4, v15, v5\n" \
    "v_min_u32_e32 v18, v14, v5\n" \
    "v_max3_u32 v12, v17, v18, v6\n" \
    "v_min_u32_dpp v4, v10, v10 row_ror:8 row_mask:0xf bank_mask:0xf\n" \
    "v_mov_b32_dpp v5, v11 row_ror:8 row_mask:0xf bank_mask:0xf\n" \
    "v_max_u32_dpp v14, v10, v10 row_ror:8 row_mask:0xf bank_mask:0xf\n" \
    "v_min_u32_dpp v17, v10, v11 row_ror:8 row_mask:0xf bank_mask:0xf\n" \
    "v_max_u32_dpp v6, v12, v12 row_ror:8 row_mask:0xf bank_mask:0xf\n" \
    "v_max3_u32 v15, v4, v11, v5\n" \
    "v_min_u32_e32 v18, v10, v5\n" \
    "v_max3_u32 v16, v17, v18, v6\n" \
    "s_mov_b64 exec, 0xff\n" \
    "v_perm_b32 v24, v14, v16, s92\n" \
    "v_perm_b32 v26, v15, v16, s93\n" \
    "global_store_dwordx4 v29, v[24:27], %[ws] sc1\n" \
    "ds_write_b32 v91, v92\n" \
    "s_mov_b64 exec, -1\n" \
    "v_mul_f32_e32 v0, 0xbfb8aa3b, v0\n" \
    "v_mul_f32_e32 v1, 0xbfb8aa3b, v1\n" \
    "v_mul_f32_e32 v2, 0xbfb8aa3b, v2\n" \
    "v_mul_f32_e32 v3, 0xbfb8aa3b, v3\n" \
    "v_exp_f32_e32 v0, v0\n" \
    "v_exp_f32_e32 v1, v1\n" \
    "v_exp_f32_e32 v2, v2\n" \
    "v_exp_f32_e32 v3, v3\n" \
    "v_add_f32_e32 v0, 1.0, v0\n" \
    "v_add_f32_e32 v1, 1.0, v1\n" \
    "v_add_f32_e32 v2, 1.0, v2\n" \
    "v_add_f32_e32 v3, 1.0, v3\n" \
    "v_rcp_f32_e32 v0, v0\n" \
    "v_rcp_f32_e32 v1, v1\n" \
    "v_rcp_f32_e32 v2, v2\n" \
    "v_rcp_f32_e32 v3, v3\n" \
    "v_cvt_f16_f32_e32 v0, v0\n" \
    "v_cvt_f16_f32_e32 v1, v1\n" \
    "v_cvt_f16_f32_e32 v2, v2\n" \
    "v_cvt_f16_f32_e32 v3, v3\n" \
    "v_pack_b32_f16 v0, v0, v1\n" \
    "v_pack_b32_f16 v2, v2, v3\n" \
    "v_cndmask_b32_e32 v8, v8, v9, vcc\n" \
    "v_cndmask_b32_e32 v0, v0, v2, vcc\n" \
    "v_mov_b32_e32 v11, s98\n" \
    "s_nop 0\n" \
    "v_permlane16_swap_b32 v0, v1\n" \
    "v_perm_b32 v10, v8, v1, v52\n" \
    "s_mov_b64 exec, 0xffff\n" \
    "global_store_dwordx2 v30, v[10:11], %[ws] sc1\n" \
    "s_mov_b64 exec, -1\n"
#define ITEM_CLOB "v0", "v1", "v2", "v3", "v4", "v5", "v6", "v7", "v8", "v9", "v10", "v11", "v12", "v13", "v14", "v15", "v16", "v17", "v18", "v19", "v20", "v21", "v22", "v23", "v24", "v25", "v26", "v27", "v28", "v29", "v30", "v31", "v52", "v53", "v54", "v55", "v72", "v74", "v75", "v76", "v77", "v78", "v79", "v80", "v81", "v82", "v83", "v84", "v85", "v86", "v87", "v88", "v89", "v90", "v91", "v92", "v96", "v97", "v98", "v99", "v100", "v101", "v102", "v103", "v104", "v105", "v106", "v107", "v108", "v109", "v110", "v111", "v112", "v113", "v114", "v115", "v116", "v117", "v118", "v119", "v120", "v121", "v122", "v123", "v124", "v125", "v126", "v127", "v128", "v129", "v130", "v131", "v132", "v133", "v134", "v135", "v136", "v137", "v138", "v139", "v140", "v141", "v142", "v143", "v144", "v145", "v146", "v147", "v148", "v149", "v150", "v151", "v152", "v153", "v154", "v155", "v156", "v157", "v158", "v159", "v160", "v161", "v162", "v163", "v164", "v165", "v166", "v167"
#define FFN_ASM \
    "v_mov_b64 v[96:97], 0\n" \
    "v_mov_b64 v[98:99], 0\n" \
    "v_mov_b64 v[100:101], 0\n" \
    "v_mov_b64 v[102:103], 0\n" \
    "v_mov_b64 v[104:105], 0\n" \
    "v_mov_b64 v[106:107], 0\n" \
    "v_mov_b64 v[108:109], 0\n" \
    "v_mov_b64 v[110:111], 0\n" \
    "v_mov_b64 v[112:113], 0\n" \
    "v_mov_b64 v[114:115], 0\n" \
    "v_mov_b64 v[116:117], 0\n" \
    "v_mov_b64 v[118:119], 0\n" \
    "v_mov_b64 v[120:121], 0\n" \
    "v_mov_b64 v[122:123], 0\n" \
    "s_waitcnt lgkmcnt(0)\n" \
    "s_barrier\n" \
    "ds_read_b128 v[124:127], %[xa] offset:0\n" \
    "ds_read_b128 v[128:131], %[xa] offset:3072\n" \
    "ds_read_b128 v[132:135], %[xa] offset:6144\n" \
    "ds_read_b32 v136, v112 offset:%[tg]\n" \
    "s_waitcnt lgkmcnt(3)\n" \
    "v_smfmac_f32_16x16x64_bf16 v[96:99], v[124:127], v[0:7], %[idx]\n" \
    "v_smfmac_f32_16x16x64_bf16 v[100:103], v[124:127], v[8:15], %[idx]\n" \
    "v_smfmac_f32_16x16x64_bf16 v[104:107], v[124:127], v[16:23], %[idx]\n" \
    "v_smfmac_f32_16x16x64_bf16 v[108:111], v[124:127], v[24:31], %[idx]\n" \
    "s_waitcnt lgkmcnt(2)\n" \
    "v_smfmac_f32_16x16x64_bf16 v[96:99], v[128:131], v[32:39], %[idx]\n" \
    "v_smfmac_f32_16x16x64_bf16 v[100:103], v[128:131], v[40:47], %[idx]\n" \
    "v_smfmac_f32_16x16x64_bf16 v[104:107], v[128:131], v[48:55], %[idx]\n" \
    "v_smfmac_f32_16x16x64_bf16 v[108:111], v[128:131], v[56:63], %[idx]\n" \
    "s_waitcnt lgkmcnt(1)\n" \
    "v_smfmac_f32_16x16x64_bf16 v[96:99], v[132:135], v[64:71], %[idx]\n" \
    "v_smfmac_f32_16x16x64_bf16 v[100:103], v[132:135], v[72:79], %[idx]\n" \
    "v_smfmac_f32_16x16x64_bf16 v[104:107], v[132:135], v[80:87], %[idx]\n" \
    "v_smfmac_f32_16x16x64_bf16 v[108:111], v[132:135], v[88:95], %[idx]\n" \
    "s_nop 4\n" \
    "ds_write_b128 %[rdw], v[96:99] offset:0\n" \
    "ds_write_b128 %[rdw], v[100:103] offset:1024\n" \
    "ds_write_b128 %[rdw], v[104:107] offset:2048\n" \
    "ds_write_b128 %[rdw], v[108:111] offset:3072\n" \
    "s_cmp_gt_u32 %[wv], 3\n" \
    "s_waitcnt lgkmcnt(4)\n" \
    "v_mov_b32_e32 v73, v136\n" \
    "v_mov_b32_e32 v75, v136\n" \
    "v_mov_b32_e32 v77, v136\n" \
    "v_mov_b32_e32 v79, v136\n" \
    "v_mov_b32_e32 v81, v136\n" \
    "v_mov_b32_e32 v83, v136\n" \
    "s_waitcnt lgkmcnt(0)\n" \
    "s_barrier\n" \
    "s_cbranch_scc1 Lfs_%=\n" \
    "ds_read_b64 v[48:49], %[rdr] offset:0\n" \
    "ds_read_b64 v[50:51], %[rdr] offset:4096\n" \
    "ds_read_b64 v[52:53], %[rdr] offset:8192\n" \
    "ds_read_b64 v[54:55], %[rdr] offset:12288\n" \
    "ds_read_b64 v[56:57], %[rdr] offset:16384\n" \
    "ds_read_b64 v[58:59], %[rdr] offset:20480\n" \
    "ds_read_b64 v[60:61], %[rdr] offset:128\n" \
    "ds_read_b64 v[62:63], %[rdr] offset:4224\n" \
    "ds_read_b64 v[64:65], %[rdr] offset:8320\n" \
    "ds_read_b64 v[66:67], %[rdr] offset:12416\n" \
    "ds_read_b64 v[68:69], %[rdr] offset:16512\n" \
    "ds_read_b64 v[70:71], %[rdr] offset:20608\n" \
    "s_waitcnt lgkmcnt(10)\n" \
    "v_pk_add_f32 v[48:49], v[48:49], v[50:51]\n" \
    "s_waitcnt lgkmcnt(8)\n" \
    "v_pk_add_f32 v[52:53], v[52:53], v[54:55]\n" \
    "s_waitcnt lgkmcnt(6)\n" \
    "v_pk_add_f32 v[56:57], v[56:57], v[58:59]\n" \
    "v_pk_add_f32 v[48:49], v[48:49], v[52:53]\n" \
    "s_waitcnt lgkmcnt(4)\n" \
    "v_pk_add_f32 v[60:61], v[60:61], v[62:63]\n" \
    "v_pk_add_f32 v[48:49], v[48:49], v[56:57]\n" \
    "s_waitcnt lgkmcnt(2)\n" \
    "v_pk_add_f32 v[64:65], v[64:65], v[66:67]\n" \
    "v_mul_f32_e32 v50, 0xbfb8aa3b, v48\n" \
    "v_mul_f32_e32 v51, 0xbfb8aa3b, v49\n" \
    "v_exp_f32_e32 v50, v50\n" \
    "v_exp_f32_e32 v51, v51\n" \
    "s_waitcnt lgkmcnt(0)\n" \
    "v_pk_add_f32 v[68:69], v[68:69], v[70:71]\n" \
    "v_add_f32_e32 v50, 1.0, v50\n" \
    "v_add_f32_e32 v51, 1.0, v51\n" \
    "v_rcp_f32_e32 v50, v50\n" \
    "v_rcp_f32_e32 v51, v51\n" \
    "v_pk_add_f32 v[60:61], v[60:61], v[64:65]\n" \
    "v_pk_add_f32 v[60:61], v[60:61], v[68:69]\n" \
    "v_pk_mul_f32 v[48:49], v[48:49], v[50:51]\n" \
    "v_pk_mul_f32 v[48:49], v[48:49], v[60:61]\n" \
    "v_cvt_pk_bf16_f32 v48, v48, v49\n" \
    "ds_write_b16 %[hbw], v48\n" \
    "ds_write_b16_d16_hi %[hbw], v48 offset:144\n" \
    "s_branch Lfj_%=\n" \
    "Lfs_%=:\n" \
    "Lfj_%=:\n" \
    "s_waitcnt vmcnt(0) lgkmcnt(0)\n" \
    "s_barrier\n" \
    "ds_read_b128 v[84:87], %[hs]\n" \
    "ds_read_b128 v[88:91], %[hs] offset:64\n" \
    "ds_read_b64_tr_b16 v[0:1], %[d00] offset:0\n" \
    "ds_read_b64_tr_b16 v[2:3], %[d01] offset:256\n" \
    "ds_read_b64_tr_b16 v[4:5], %[d00] offset:2048\n" \
    "ds_read_b64_tr_b16 v[6:7], %[d01] offset:2304\n" \
    "ds_read_b64_tr_b16 v[8:9], %[d10] offset:0\n" \
    "ds_read_b64_tr_b16 v[10:11], %[d11] offset:256\n" \
    "ds_read_b64_tr_b16 v[12:13], %[d10] offset:2048\n" \
    "ds_read_b64_tr_b16 v[14:15], %[d11] offset:2304\n" \
    "ds_read_b64_tr_b16 v[16:17], %[d20] offset:0\n" \
    "ds_read_b64_tr_b16 v[18:19], %[d21] offset:256\n" \
    "ds_read_b64_tr_b16 v[20:21], %[d20] offset:2048\n" \
    "ds_read_b64_tr_b16 v[22:23], %[d21] offset:2304\n" \
    "ds_read_b64_tr_b16 v[24:25], %[d00] offset:4096\n" \
    "ds_read_b64_tr_b16 v[26:27], %[d01] offset:4352\n" \
    "ds_read_b64_tr_b16 v[28:29], %[d00] offset:6144\n" \
    "ds_read_b64_tr_b16 v[30:31], %[d01] offset:6400\n" \
    "ds_read_b64_tr_b16 v[32:33], %[d10] offset:4096\n" \
    "ds_read_b64_tr_b16 v[34:35], %[d11] offset:4352\n" \
    "ds_read_b64_tr_b16 v[36:37], %[d10] offset:6144\n" \
    "ds_read_b64_tr_b16 v[38:39], %[d11] offset:6400\n" \
    "ds_read_b64_tr_b16 v[40:41], %[d20] offset:4096\n" \
    "ds_read_b64_tr_b16 v[42:43], %[d21] offset:4352\n" \
    "ds_read_b64_tr_b16 v[44:45], %[d20] offset:6144\n" \
    "ds_read_b64_tr_b16 v[46:47], %[d21] offset:6400\n" \
    "s_waitcnt lgkmcnt(14)\n" \
    "v_smfmac_f32_16x16x64_bf16 v[112:115], v[84:87], v[0:7], %[idx]\n" \
    "v_smfmac_f32_16x16x64_bf16 v[116:119], v[84:87], v[8:15], %[idx]\n" \
    "s_waitcnt lgkmcnt(12)\n" \
    "v_smfmac_f32_16x16x64_bf16 v[120:123], v[84:87], v[16:23], %[idx]\n" \
    "s_waitcnt lgkmcnt(8)\n" \
    "v_smfmac_f32_16x16x64_bf16 v[112:115], v[88:91], v[24:31], %[idx]\n" \
    "s_waitcnt lgkmcnt(4)\n" \
    "v_smfmac_f32_16x16x64_bf16 v[116:119], v[88:91], v[32:39], %[idx]\n" \
    "s_waitcnt lgkmcnt(0)\n" \
    "v_smfmac_f32_16x16x64_bf16 v[120:123], v[88:91], v[40:47], %[idx]\n" \
    "s_nop 3\n" \
    "v_cvt_pk_bf16_f32 v72, v112, v113\n" \
    "v_cvt_pk_bf16_f32 v74, v114, v115\n" \
    "global_store_dwordx4 %[po0], v[72:75], %[ws] sc1\n" \
    "v_cvt_pk_bf16_f32 v76, v116, v117\n" \
    "v_cvt_pk_bf16_f32 v78, v118, v119\n" \
    "global_store_dwordx4 %[po1], v[76:79], %[ws] sc1\n" \
    "v_cvt_pk_bf16_f32 v80, v120, v121\n" \
    "v_cvt_pk_bf16_f32 v82, v122, v123\n" \
    "global_store_dwordx4 %[po2], v[80:83], %[ws] sc1\n"
#define FFN_CLOB "v96", "v97", "v98", "v99", "v100", "v101", "v102", "v103", "v104", "v105", "v106", "v107", "v108", "v109", "v110", "v111", "v112", "v113", "v114", "v115", "v116", "v117", "v118", "v119", "v120", "v121", "v122", "v123", "v124", "v125", "v126", "v127", "v128", "v129", "v130", "v131", "v132", "v133", "v134", "v135", "v136"
#define SEL_ASM \
    "s_mov_b32 %[ps], 0\n" \
    "s_branch Lsn_%=\n" \
    "Lsg_%=:\n" \
    "s_mov_b32 %[ps], 4096\n" \
    "Lsw_%=:\n" \
    "s_memrealtime s[92:93]\n" \
    "s_waitcnt lgkmcnt(0)\n" \
    "s_sub_u32 s92, s92, %[at]\n" \
    "s_cmp_lt_i32 s92, 0\n" \
    "s_cbranch_scc0 Lsr_%=\n" \
    "s_sleep 1\n" \
    "s_sub_u32 %[ps], %[ps], 1\n" \
    "s_cbranch_scc0 Lsw_%=\n" \
    "Lsr_%=:\n" \
    "s_mov_b32 %[ps], 0x20000\n" \
    "Lsp_%=:\n" \
    "global_load_dwordx4 v[0:3], %[co], %[ws] sc1\n" \
    "global_load_dwordx4 v[4:7], %[co2], %[ws] sc1\n" \
    "s_mov_b64 exec, -1\n" \
    "s_waitcnt vmcnt(1)\n" \
    "v_bitop3_b32 v10, v1, v3, %[tag] bitop3:0x7e\n" \
    "s_waitcnt vmcnt(0)\n" \
    "v_bitop3_b32 v11, v5, v7, %[tag] bitop3:0x7e\n" \
    "v_or_b32_e32 v10, v10, v11\n" \
    "v_cmp_ne_u32_e32 vcc, 0, v10\n" \
    "s_cbranch_vccz Lsn_%=\n" \
    "s_sleep %[gap]\n" \
    "s_mov_b64 exec, vcc\n" \
    "s_sub_u32 %[ps], %[ps], 1\n" \
    "s_cbranch_scc0 Lsp_%=\n" \
    "s_mov_b64 exec, -1\n" \
    "s_mov_b32 %[ps], 1\n" \
    "Lsn_%=:\n" \
    "v_and_or_b32 v15, v0, %[mk], %[f0]\n" \
    "v_and_or_b32 v17, v4, %[mk], %[fi]\n" \
    "v_and_or_b32 v16, v2, %[mk], %[f0]\n" \
    "v_and_or_b32 v18, v6, %[mk], %[fi]\n" \
    "v_max_u32_e32 v8, v15, v17\n" \
    "v_min_u32_e32 v9, v15, v17\n" \
    "v_max3_u32 v9, v9, v16, v18\n" \
    "v_mov_b32_dpp v10, v8 quad_perm:[1,0,3,2] row_mask:0xf bank_mask:0xf\n" \
    "v_med3_u32 v8, v8, v10, %[c1]\n" \
    "v_mov_b32_dpp v11, v9 quad_perm:[1,0,3,2] row_mask:0xf bank_mask:0xf\n" \
    "v_med3_u32 v9, v9, v11, %[c1]\n" \
    "v_mov_b32_dpp v10, v8 quad_perm:[3,2,1,0] row_mask:0xf bank_mask:0xf\n" \
    "v_med3_u32 v8, v8, v10, %[c2]\n" \
    "v_mov_b32_dpp v11, v9 quad_perm:[3,2,1,0] row_mask:0xf bank_mask:0xf\n" \
    "v_med3_u32 v9, v9, v11, %[c2]\n" \
    "v_mov_b32_dpp v10, v8 quad_perm:[1,0,3,2] row_mask:0xf bank_mask:0xf\n" \
    "v_med3_u32 v8, v8, v10, %[c1]\n" \
    "v_mov_b32_dpp v11, v9 quad_perm:[1,0,3,2] row_mask:0xf bank_mask:0xf\n" \
    "v_med3_u32 v9, v9, v11, %[c1]\n" \
    "v_mov_b32_dpp v10, v8 row_half_mirror row_mask:0xf bank_mask:0xf\n" \
    "v_med3_u32 v8, v8, v10, %[c4]\n" \
    "v_max_u32_dpp v9, v9, v9 row_half_mirror row_mask:0xf bank_mask:0xf\n" \
    "v_perm_b32 v19, v0, v2, %[pm]\n" \
    "v_mov_b32_dpp v10, v8 quad_perm:[2,3,0,1] row_mask:0xf bank_mask:0xf\n" \
    "v_med3_u32 v8, v8, v10, %[c2]\n" \
    "v_mov_b32_dpp v11, v9 quad_perm:[2,3,0,1] row_mask:0xf bank_mask:0xf\n" \
    "v_med3_u32 v9, v9, v11, %[c2]\n" \
    "v_mov_b32_dpp v10, v8 quad_perm:[1,0,3,2] row_mask:0xf bank_mask:0xf\n" \
    "v_med3_u32 v8, v8, v10, %[c1]\n" \
    "v_mov_b32_dpp v11, v9 quad_perm:[1,0,3,2] row_mask:0xf bank_mask:0xf\n" \
    "v_med3_u32 v9, v9, v11, %[c1]\n" \
    "v_max_u32_dpp v8, v8, v8 row_mirror row_mask:0xf bank_mask:0xf\n" \
    "s_nop 0\n" \
    "v_max_u32_dpp v9, v9, v9 row_mirror row_mask:0xf bank_mask:0xf\n" \
    "v_max_u32_dpp v10, v8, v8 row_shl:4 row_mask:0xf bank_mask:0x5\n" \
    "v_min_u32_dpp v10, v8, v8 row_shr:4 row_mask:0xf bank_mask:0xa\n" \
    "v_mov_b32_dpp v11, v9 quad_perm:[2,3,0,1] row_mask:0xf bank_mask:0xf\n" \
    "v_med3_u32 v9, v9, v11, %[c2]\n" \
    "v_mov_b32_dpp v8, v10 quad_perm:[2,3,0,1] row_mask:0xf bank_mask:0xf\n" \
    "v_med3_u32 v10, v10, v8, %[c2]\n" \
    "v_mov_b32_dpp v11, v9 quad_perm:[1,0,3,2] row_mask:0xf bank_mask:0xf\n" \
    "v_med3_u32 v9, v9, v11, %[c1]\n" \
    "v_mov_b32_dpp v8, v10 quad_perm:[1,0,3,2] row_mask:0xf bank_mask:0xf\n" \
    "v_med3_u32 v10, v10, v8, %[c1]\n" \
    "v_mov_b32_e32 v13, v9\n" \
    "v_perm_b32 v20, v4, v6, %[pm]\n" \
    "v_mov_b32_e32 v12, v10\n" \
    "v_permlane16_swap_b32 v9, v13\n" \
    "v_min_u32_e32 v15, v15, v18\n" \
    "v_permlane16_swap_b32 v10, v12\n" \
    "v_max_u32_dpp v9, v13, v9 quad_perm:[3,2,1,0] row_mask:0xf bank_mask:0xf\n" \
    "v_min_u32_e32 v16, v16, v17\n" \
    "v_max_u32_dpp v10, v12, v10 row_half_mirror row_mask:0xf bank_mask:0xf\n" \
    "v_mov_b32_dpp v11, v9 quad_perm:[2,3,0,1] row_mask:0xf bank_mask:0xf\n" \
    "v_med3_u32 v9, v9, v11, %[c2]\n" \
    "v_mov_b32_dpp v8, v10 row_ror:4 row_mask:0xf bank_mask:0xf\n" \
    "v_med3_u32 v10, v10, v8, %[c4]\n" \
    "v_mov_b32_dpp v11, v9 quad_perm:[1,0,3,2] row_mask:0xf bank_mask:0xf\n" \
    "v_med3_u32 v9, v9, v11, %[c1]\n" \
    "v_mov_b32_dpp v8, v10 quad_perm:[2,3,0,1] row_mask:0xf bank_mask:0xf\n" \
    "v_med3_u32 v10, v10, v8, %[c2]\n" \
    "v_mov_b32_e32 v13, v9\n" \
    "v_max3_u32 v19, v19, v20, v15\n" \
    "v_mov_b32_dpp v8, v10 quad_perm:[1,0,3,2] row_mask:0xf bank_mask:0xf\n" \
    "v_med3_u32 v10, v10, v8, %[c1]\n" \
    "v_permlane32_swap_b32 v9, v13\n" \
    "v_max_u32_e32 %[tc], v19, v16\n" \
    "v_mov_b32_e32 v12, v10\n" \
    "v_max_u32_dpp v9, v13, v9 quad_perm:[3,2,1,0] row_mask:0xf bank_mask:0xf\n" \
    "s_nop 0\n" \
    "v_permlane32_swap_b32 v10, v12\n" \
    "v_mov_b32_dpp v11, v9 quad_perm:[2,3,0,1] row_mask:0xf bank_mask:0xf\n" \
    "v_med3_u32 v9, v9, v11, %[c2]\n" \
    "v_max_u32_dpp v10, v12, v10 row_half_mirror row_mask:0xf bank_mask:0xf\n" \
    "s_nop 0\n" \
    "v_mov_b32_dpp v11, v9 quad_perm:[1,0,3,2] row_mask:0xf bank_mask:0xf\n" \
    "v_med3_u32 v9, v9, v11, %[c1]\n" \
    "v_mov_b32_dpp v8, v10 row_ror:4 row_mask:0xf bank_mask:0xf\n" \
    "v_med3_u32 v10, v10, v8, %[c4]\n" \
    "v_and_b32_e32 v9, v9, %[c4]\n" \
    "s_nop 0\n" \
    "v_mov_b32_dpp v8, v10 quad_perm:[2,3,0,1] row_mask:0xf bank_mask:0xf\n" \
    "v_med3_u32 v10, v10, v8, %[c2]\n" \
    "s_nop 1\n" \
    "v_mov_b32_dpp v8, v10 quad_perm:[1,0,3,2] row_mask:0xf bank_mask:0xf\n" \
    "v_med3_u32 v10, v10, v8, %[c1]\n" \
    "v_max_u32_dpp v10, v9, v10 row_half_mirror row_mask:0xf bank_mask:0xf\n" \
    "s_cmp_eq_u32 %[ps], 0\n" \
    "s_cbranch_scc1 Lsg_%=\n" \
    "v_mov_b32_dpp v14, v10 row_newbcast:0 row_mask:0x1 bank_mask:0x3\n" \
    "v_mov_b32_dpp v14, v10 row_newbcast:1 row_mask:0x1 bank_mask:0xc\n" \
    "v_mov_b32_dpp v14, v10 row_newbcast:2 row_mask:0x2 bank_mask:0x3\n" \
    "v_mov_b32_dpp v14, v10 row_newbcast:3 row_mask:0x2 bank_mask:0xc\n" \
    "v_mov_b32_dpp v14, v10 row_newbcast:4 row_mask:0x4 bank_mask:0x3\n" \
    "v_mov_b32_dpp v14, v10 row_newbcast:5 row_mask:0x4 bank_mask:0xc\n" \
    "v_mov_b32_dpp v14, v10 row_newbcast:6 row_mask:0x8 bank_mask:0x3\n" \
    "v_mov_b32_dpp v14, v10 row_newbcast:7 row_mask:0x8 bank_mask:0xc\n" \
    "s_mov_b32 m0, %[xs]\n" \
    "v_and_b32_e32 v14, 0x7ff, v14\n" \
    "v_mul_u32_u24_e32 v14, 1152, v14\n" \
    "v_sub_u32_e32 v14, %[xk], v14\n" \
    "s_cmp_eq_u32 %[moe], 0\n" \
    "s_cbranch_scc1 Lse_%=\n" \
    "global_load_lds_dwordx4 v14, %[x] offset:0\n" \
    "s_addk_i32 m0, 896\n" \
    "s_nop 0\n" \
    "global_load_lds_dwordx4 v14, %[x] offset:128\n" \
    "s_addk_i32 m0, 896\n" \
    "s_nop 0\n" \
    "global_load_lds_dwordx4 v14, %[x] offset:256\n" \
    "s_addk_i32 m0, 896\n" \
    "s_nop 0\n" \
    "global_load_lds_dwordx4 v14, %[x] offset:384\n" \
    "s_addk_i32 m0, 896\n" \
    "s_nop 0\n" \
    "global_load_lds_dwordx4 v14, %[x] offset:512\n" \
    "s_addk_i32 m0, 896\n" \
    "s_nop 0\n" \
    "global_load_lds_dwordx4 v14, %[x] offset:640\n" \
    "s_addk_i32 m0, 896\n" \
    "s_nop 0\n" \
    "global_load_lds_dwordx4 v14, %[x] offset:768\n" \
    "s_addk_i32 m0, 896\n" \
    "s_nop 0\n" \
    "global_load_lds_dwordx4 v14, %[x] offset:896\n" \
    "s_addk_i32 m0, 896\n" \
    "s_nop 0\n" \
    "global_load_lds_dwordx4 v14, %[x] offset:1024\n" \
    "Lse_%=:\n" \
    "v_mov_b32_e32 %[g8], v10\n"
#define SEL_CLOB "v0", "v1", "v2", "v3", "v4", "v5", "v6", "v7", "v8", "v9", "v10", "v11", "v12", "v13", "v14", "v15", "v16", "v17", "v18", "v19", "v20"

template <int kind> DI void conv_wave(const Params& P, lptr sm, int ch, int xcd, int ii, u32 labw) {
    const int cg = cg_of(xcd, ii);
    const u32 m0a = (u32)(LD_TB + ch * 512 + (kind & 1) * 256);
    u32 epoch;
    if (kind == 0) {
        asm volatile("; claim\n" CONV_A_ASM : : [m0a] "s"(m0a) : "memory", "vcc", "scc", "s90", CONV_A_CLOB);
    } else if (kind == 1) {
        v4u sg;
        asm volatile("; claim\n" CONV_B_ASM : [ep] "=&s"(epoch), "=&{v[108:111]}"(sg) : [m0a] "s"(m0a), [sg] "s"(P.ws + WS_SEL + xcd * 128) : "memory", "vcc", "scc", "s90", "s92", CONV_B_CLOB);
        if (ch == 0) conv_final(P, sm, cg, xcd, tag_of(epoch), sg);
    } else {
        const Lab lb = lab_of(labw);
        const u32 nw = xcd < 2 ? 96u : 64u, u0 = (u32)(ch * (xcd < 2 ? 24 : 16) + ii);
        const char* pw = (const char*)P.wd + wd_unit(lb, HWREV ? 9 * lb.n - 1 - umin(u0, 9 * lb.n - 1) : umin(u0, 9 * lb.n - 1));
        const char* pw2 = (const char*)P.wd + wd_unit(lb, u0 + nw < 9 * lb.n ? u0 + nw : u0);
        const u32 ug = (u32)(2 * ii + (ch >> 1)), blk = (ug * lb.mg) >> 16, col = ug - blk * lb.n;
        const char* pg = (const char*)((ch & 1) ? P.wu : P.wg) + (umin(PG0 + PGN * blk, D_ - PGN) * (F_ * 2) + (lb.m0 + col) * 128);
        asm volatile("; claim\n" CONV_C_ASM : [ep] "=&s"(epoch)
                     : [pg] "s"(pg), [pgn] "n"(PGN - 1), [xb] "s"(P.x), [pm] "s"((xcd < 2 ? ii < 16 : ch < 2) ? ~0ull : 1ull), [x0] "s"((const char*)P.x + cg * 8), [wd] "s"(pw), [wd2] "s"(pw2), [o0] "s"((char*)P.out + cg * 8), [ws] "s"(P.ws), [ak] "s"(WS_ACK + (u32)(8 * ii + xcd) * 128u),
                       [bs] "s"((const char*)&conv_tab + 61440 + ch * 1024), [bl] "s"(LD_BD + ch * 1024), [tg] "s"(11 + (ch >> 1)), [am] "s"(1u >> ch), [pp] "s"(P.ws + cg * (72 * 128))
                     : "memory", "vcc", "scc", "s88", "s89", "s90", "s91", "s92", "s93", "s94", "s95", "s96", "s97", "s98", "s99", CONV_C_CLOB);
    }
}

DI u32 select8(const Params& P, int lane, int xcd, u32 tag, u32 t_in, u32 moe) {
    const __amdgpu_buffer_rsrc_t rws = ws_rsrc(P);
    const v2u t0 = ld2_sc1(rws, TREF && xcd >= 4 ? WS_T0 + (u32)xcd * 128u : OOB);
    const u32 co = WS_CAND + (u32)xcd * 16384u + (u32)lane * 128u, fi = (u32)(63 - lane) << 4;
    const u32 xk = 2047u * (D_ * 2) + (u32)(((lane & 7) ^ ((lane >> 3) & 6)) * 16);
    const u32 d = rfl(t0[0]) == tag ? t_in - rfl(t0[1]) : 0u, at = t_in + SEL_AT - (d < (u32)SEL_AT ? d : 0u);
    u32 g8, tc, ps;
    asm volatile(SEL_ASM
                 : [g8] "=&v"(g8), [tc] "=&v"(tc), [ps] "=&s"(ps)
                 : [co] "v"(co), [co2] "v"(co + 8192u), [fi] "v"(fi), [f0] "v"(fi | 1024u), [c4] "v"((lane & 4) ? 0u : ~0u), [c2] "v"((lane & 2) ? 0u : ~0u), [c1] "v"((lane & 1) ? 0u : ~0u),
                   [mk] "v"(0xffff000fu), [xk] "v"(xk), [pm] "s"(0x05010d0du), [ws] "s"(P.ws), [x] "s"(P.x), [tag] "s"(tag), [at] "s"(at), [moe] "s"(moe), [xs] "n"(LD_XS), [gap] "n"(SEL_GAP)
                 : "memory", "vcc", "scc", "s92", "s93", SEL_CLOB);
    u32 lo = umin(g8, dpp0<0xB1, 0xf>(g8));
    lo = umin(lo, dpp0<0x4E, 0xf>(lo)); lo = umin(lo, dpp0<0x141, 0xf>(lo));
    if (__builtin_expect(__any(tc > lo), 0)) {
        u32 all[32];
#pragma unroll 1
        for (int spin = 0; spin < SPIN_CAP; ++spin) {
            MEMFENCE();
            bool ok = true;
#pragma unroll
            for (int q = 0; q < 16; ++q) {
                const v4u e = ld_sc1(rws, WS_KEYS + (u32)lane * 16u + q * 1024);
                ok &= tag_ok(e, tag);
                all[2 * q] = (e[0] & 0xffff0000u) | (u32)(2047 - 128 * q - 2 * lane); all[2 * q + 1] = (e[2] & 0xffff0000u) | (u32)(2046 - 128 * q - 2 * lane);
            }
            if (__all(ok)) break;
            __builtin_amdgcn_s_sleep(8);
        }
        u32 bound = 0xffffffffu;
#pragma unroll 1
        for (int rd = 0; rd < 8; ++rd) {
            u32 m = 0;
#pragma unroll
            for (int q = 0; q < 32; ++q) { m = umax(m, all[q] <= bound ? all[q] : 0u); PIN(m); }
            const u32 M = wave_max(m);
            if ((lane & 7) == rd) g8 = M;
            bound = M - 1u;
        }
        VMWAIT(0);
        if (moe) dma_rows<1024>(P.x, xk - (from_lane(g8, lane >> 3) & 0x7ffu) * (D_ * 2), LD_XS);
    }
    return g8;
}

DI void moe_weights(const Params& P, lptr sm, int wave, int midx, u32 nit) {
    v4u wb[24];
    u64 base;
    asm volatile("; claim4" : "={v[96:99]}"(wb[0]), "={v[100:103]}"(wb[1]), "={v[104:107]}"(wb[2]), "={v[108:111]}"(wb[3]), "={v[12:13]}"(base));
    const int lane = fresh_lane();
    const u32 wo = (u32)(((lane & 7) ^ ((lane >> 3) & 6)) * 16);
    const int c = lane & 15, kg = lane >> 4;
    auto ldw = [&](int k) {
        asm volatile("global_load_dwordx4 %0, %1, off" : "=v"(wb[k]) : "v"(base) : "memory");
        base += (u64)(((k & 7) == 7 ? 173 : ((k & 7) == 3 ? 13 : 1)) * (F_ * 2));
        SB();
    };
    base += (u64)(12 * (F_ * 2));
#pragma unroll
    for (int k = 4; k < WIN0; ++k) ldw(k);
    const int w3 = wave >= 3 ? wave - 3 : wave, r8 = wave >= 3;
    u32 src = (u32)((midx * 64 + 8 * r8 + (lane >> 3)) * (D_ * 2) + w3 * 384) + wo, dst = LD_WD + w3 * 24576 + r8 * 1024;
    lptr rdw = sm + LD_RED + wave * 4096 + lane * 16;
    clptr rdr = sm + LD_RED + (wave & 3) * 1024 + (lane & 55) * 16 + (c >> 3) * 8;
    lptr hbw = sm + LD_HB + (4 * (kg & 1) + 2 * (c >> 3)) * HB_ROW + 2 * (8 * (c & 7) + (wave & 3) + 4 * (kg >> 1));
    clptr hs = sm + LD_HB + (c & 7) * HB_ROW + kg * 16;
    u32 po[3];
#pragma unroll
    for (int t = 0; t < 3; ++t) { po[t] = WS_PART + (u32)(((4 * (6 * wave + t + 3 * (kg >> 1)) + (c >> 2)) * 72 + midx) * 8 + 4 * (kg & 1) + (c & 3)) * 16u; PIN(po[t]); }
    const int kq = 4 * wave + kg;
    clptr xa = sm + LD_XS + (kq >> 3) * 1024 + (c & 7) * 128 + (((kq & 7) ^ (c & 6)) * 16);
    u32 idx = c < 8 ? 0x4444u : 0xeeeeu;
    clptr db[3][2];
#pragma unroll
    for (int t = 0; t < 3; ++t) {
        const int r = (lane >> 2) & 3, p = lane & 3, tile = 6 * wave + t + 3 * (r >> 1);
        db[t][0] = sm + LD_WD + (tile >> 2) * 8192 + (4 * kg + (r & 1)) * 128 + (((2 * (tile & 3) + (p >> 1)) ^ (4 * (kg & 1))) * 16) + 8 * (p & 1);
        db[t][1] = (clptr)((u32)(uintptr_t)db[t][0] ^ 32u);
        PIN(db[t][0]); PIN(db[t][1]);
    }
    PIN(rdw); PIN(rdr); PIN(hbw); PIN(hs); PIN(xa); PIN(idx); PIN(src);
    SB();
    v8u gf[3][4];
    {
        if (WIN0 < WIN && nit) wait_ge<1>(sm + FL_IT + 4 * umin(lane, nit - 1), 1);
#pragma unroll
        for (int k = WIN0; k < WIN; ++k) ldw(k);
        auto done = [&](int d) {
            if (d & 1) {
                asm volatile("" : "+v"(wb[d - 1]), "+v"(wb[d]));
                const v4u lo = wb[d - 1], hi = wb[d];
#pragma unroll
                for (int m = 0; m < 4; ++m) {
                    const int e = (d & 6) + (m >> 1);
                    gf[d >> 3][2 * (m & 1)][e] = perm_lo(hi[m], lo[m]); gf[d >> 3][2 * (m & 1) + 1][e] = perm_hi(hi[m], lo[m]);
                    PIN(gf[d >> 3][2 * (m & 1)][e]); PIN(gf[d >> 3][2 * (m & 1) + 1][e]);
                }
                SB();
            }
        };
#pragma unroll
        for (int k = WIN; k < 24; ++k) {
            VMWAIT(WIN - 1);
            ldw(k);
            done(k - WIN);
        }
        int nw = 0;
#pragma unroll
        for (int k = 24 - WIN; k < 24; ++k) {
            VMWAIT(23 - k + nw);
#pragma unroll
            for (; nw < (k - 23 + WIN) * NWE / WIN; ++nw) {
                asm volatile("s_mov_b32 m0, %2\n\ts_nop 0\n\tglobal_load_lds_dwordx4 %0, %1 offset:%3" ::"v"(src), "s"(P.wd), "s"(dst + (u32)((nw % 3) * 8064 + nw / 3 * 2048)), "n"((nw % 3) * 128) : "memory");
                if (nw % 3 == 2) src += 16 * D_ * 2;
            }
            done(k);
        }
    }

    asm volatile(FFN_ASM
                 : "+{v[0:7]}"(gf[0][0]), "+{v[8:15]}"(gf[0][1]), "+{v[16:23]}"(gf[0][2]), "+{v[24:31]}"(gf[0][3]), "+{v[32:39]}"(gf[1][0]), "+{v[40:47]}"(gf[1][1]),
                   "+{v[48:55]}"(gf[1][2]), "+{v[56:63]}"(gf[1][3]), "+{v[64:71]}"(gf[2][0]), "+{v[72:79]}"(gf[2][1]), "+{v[80:87]}"(gf[2][2]), "+{v[88:95]}"(gf[2][3])
                 : [xa] "v"(xa), [rdw] "v"(rdw), [rdr] "v"(rdr), [hbw] "v"(hbw), [hs] "v"(hs), [idx] "v"(idx), [d00] "v"(db[0][0]), [d01] "v"(db[0][1]), [d10] "v"(db[1][0]), [d11] "v"(db[1][1]),
                   [d20] "v"(db[2][0]), [d21] "v"(db[2][1]), [po0] "v"(po[0]), [po1] "v"(po[1]), [po2] "v"(po[2]), [ws] "s"(P.ws), [wv] "s"(wave), [tg] "n"(LD_TAG)
                 : "memory", "scc", FFN_CLOB);
}

DI void helper_prefetch(const Params& P, int lane, u32 idx, u32 cnt, Lab lb, u32 hw0) {
    const u32 c3 = (3u - (u32)((u64)P.wg >> 7) - lb.m0) & 7u, c3u = (3u - (u32)((u64)P.wu >> 7) - lb.m0) & 7u;
    const u32 n3 = (c3 < lb.n ? 1u : 0u) + (c3 + 8u < lb.n ? 1u : 0u), nl = lb.n - n3;
    const u32 mg2 = (u32)(65536.f * __builtin_amdgcn_rcpf((float)nl)) + 1u;
    const u32 rpu = (64u * mg2) >> 16, r = umin(((u32)lane * mg2) >> 16, rpu - 1u);
    u32 l = umin((u32)lane - nl * r, nl - 1u);
    l += l >= c3 ? 1u : 0u; l += l >= c3 + 8u ? 1u : 0u;
    const u32 go = r * (F_ * 2) + (lb.m0 + l) * 128u;
    constexpr u32 NSG = (D_ - SLOW0) / 64;
    u32 u = idx;
#pragma unroll 1
    for (; u < 2u * NSG * n3; u += cnt) {
        VMWAIT(HWIN - 1);
        const u32 h = n3 == 2u ? u >> 2 : u >> 1;
        touch((u & 1) ? P.wu : P.wg, (SLOW0 + h * 64u + (u32)lane) * (F_ * 2) + umin(lb.m0 + (n3 == 2u && (u & 2u) ? 8u : 0u) + ((u & 1) ? c3u : c3), 71u) * 128u);
    }
#pragma unroll 1
    for (u -= 2u * NSG * n3; HR0 + (u >> 1) * rpu < D_; u += cnt) {
        VMWAIT(HWIN - 1);
        touch((u & 1) ? P.wu : P.wg, go + umin(HR0 + (u >> 1) * rpu, D_ - rpu) * (F_ * 2));
    }
#pragma unroll 1
    for (u = hw0 + cnt - 1u - idx; u < 9u * lb.n; u += cnt) {
        VMWAIT(HWIN - 1);
        touch(P.wd, wd_unit(lb, HWREV ? (HWREV == 2 ? u - hw0 : 9u * lb.n - 1u - u) : u) + (u32)lane * 128u);
    }
}

#define TAB_CLOB "v148", "v149", "v150", "v151", "v152", "v153", "v154", "v155", "v156", "v157", "v158", "v159", "v160", "v161", "v162", "v163", "v164", "v165", "v166", "v167", "v32", "v33", "v34", "v35", "v36", "v37", "v38", "v39", "v40", "v41", "v42", "v43", "v44", "v45", "v46", "v47", "v48", "v49", "v50", "v51"
#define ENTRY_CLOB TAB_CLOB, "v62", "v63", "v137", "v138", "v139", "v140", "v141", "v142", "v143", "v144", "v145", "v146", "v147", "v64", "v65", "v66", "v67", "v68", "v69", "v70", "v71", "v72", "v73", "v74", "v75", "v76", "v77", "v78", "v79", "v80", "v81", "v82", "v83", "v84", "v85", "v86", "v87", "v88", "v89", "v90", "v91", "v92", "v93", "v94", "v95", "v96", "v97", "v98", "v99", "v100", "v101", "v102", "v103", "v104", "v105", "v106", "v107", "v108", "v109", "v110", "v111", "v112", "v113", "v114", "v115", "v116", "v117", "v118", "v119", "v120", "v121", "v122", "v123", "v124", "v125", "v126", "v127", "v128", "v129", "v130", "v131", "v132", "v133", "v134", "v135", "v136"
__global__ void __launch_bounds__(NTHR, 3) fused_kernel(Args) {
    __shared__ __attribute__((aligned(16))) unsigned char smem[LDS_BYTES];
    lptr sm = (lptr)smem;
    const int b = blockIdx.x;
    v16u ka0, ka1;
    int wave, item;
    u32 role, labw, tid = threadIdx.x;
#define LX(q, a, o) "global_load_dwordx4 v[96+4*(" #q "):99+4*(" #q ")], " a ", s[54:55] offset:" #o "\n\t"
#define LXS(s) LX(s, "v7", -2304+256*s) LX(4+s, "v7", 2304+256*s) LX(8+s, "v9", -2304+256*s) LX(12+s, "v9", 2304+256*s)
#define LG(q) "global_load_dwordx4 v[96+4*" #q ":99+4*" #q "], v[12:13], off\n\tv_lshl_add_u64 v[12:13], v[12:13], 0, s[54:55]\n\t"
#define LWS(p) "global_load_lds_dwordx4 v2, " p "\n\ts_mov_b64 exec, 0xff\n\tglobal_load_lds_dwordx4 v2, " p " offset:1024\n\ts_mov_b64 exec, -1\n\t"
    asm volatile("s_load_dwordx16 s[36:51], %[kap], 0x0\n\ts_load_dwordx16 s[16:31], %[kap], 0x40\n\t"
                 "v_readfirstlane_b32 %[w], %[tid]\n\ts_lshr_b32 s53, %[b], 3\n\tv_and_b32 v1, 63, %[tid]\n\ts_sub_u32 s56, s53, 24\n\t"
                 "s_cmpk_eq_u32 %[w], 640\n\ts_cselect_b32 s56, s56, -1\n\ts_cmp_lt_u32 s56, 8\n\ts_cbranch_scc0 Lb%=\n\t"
                 "s_getpc_b64 s[54:55]\n\ts_lshl_b32 s56, s56, 12\n\tv_min_u32 v3, 31, v1\n\tv_lshl_add_u32 v3, v3, 7, s56\n\tv_min_u32 v3, %[ce], v3\n\t"
                 "global_load_dword v3, v3, s[54:55]\n\ts_barrier\n\ts_waitcnt vmcnt(0)\n"
                 "Lx%=:\n\ts_waitcnt lgkmcnt(0)\n\ts_endpgm\n"
                 "Lb%=:\n\ts_and_b32 s52, %[b], 7\n\ts_lshl_b32 s57, s52, 7\n\ts_lshl2_add_u32 s55, s53, s57\n\t"
                 "s_load_dword %[rl], %[kap], s55 offset:0x80\n\ts_load_dword %[lb], %[kap], s57 offset:0x80\n\t"
                 "v_mov_b32 v0, 0\n\ts_cmp_lt_u32 s52, 2\n\ts_cselect_b32 s54, 24, 16\n\t"
                 "s_lshr_b32 %[w], %[w], 6\n\tv_lshlrev_b32 v2, 4, v1\n\ts_cmp_ge_u32 s53, s54\n\ts_cbranch_scc1 Ln%=\n\t"
                 "s_mul_i32 s54, %[w], 5120\n\tv_add_u32 v3, s54, v2\n\ts_and_b32 s55, %[w], 3\n\t"
                 "s_cmp_lt_u32 %[w], 8\n\ts_cbranch_scc1 Lt%=\n\ts_setprio 3\n\tv_add_u32 v4, 0x1000, v3\n\t"
                 "global_load_dwordx4 v[44:47], v3, %[tab] offset:3072\n\tglobal_load_dwordx4 v[48:51], v4, %[tab]\n\tglobal_load_dwordx4 v[40:43], v3, %[tab] offset:2048\n\t"
                 "s_and_b32 s56, s55, 1\n\ts_lshl1_add_u32 s56, s53, s56\n\ts_and_b32 s56, s56, 31\n\ts_cmp_lt_u32 s55, 2\n\ts_cselect_b32 s58, s57, 1024\n\t"
                 "v_lshl_add_u32 v62, s56, 6, v1\n\tv_mul_u32_u24 v62, %[rb], v62\n\tv_add_u32 v62, s58, v62\n"
                 "Lt%=:\n\ts_lshl4_add_u32 s54, s52, s53\n\ts_lshl3_add_u32 s56, s52, s53\n\ts_addk_i32 s56, 112\n\ts_cmp_lt_u32 s53, 16\n\ts_cselect_b32 s54, s54, s56\n\t"
                 "s_lshl2_add_u32 s54, s54, s55\n\t"
                 "s_bfe_u32 s56, %[w], 0x10002\n\ts_lshl1_add_u32 s55, s55, s56\n\ts_lshl_b32 m0, s55, 8\n\t"
                 "s_lshl1_add_u32 s56, s54, s56\n\ts_lshl_b32 s56, s56, 7\n\ts_lshl_b32 s54, s54, 1\n\t"
                 "v_lshl_add_u32 v4, v1, 1, s56\n\tv_mov_b32 v5, s54\n\tv_mov_b32 v6, 0x3f80\n\t"
                 "ds_write_addtid_b32 v0 offset:%[fl]\n\tds_write_addtid_b32 v0 offset:%[fu]\n\tds_write_addtid_b32 v6\n\t"
                 "v_mov_b64 v[64:65], 0\n\tv_mov_b64 v[66:67], 0\n\tv_mov_b64 v[68:69], 0\n\tv_mov_b64 v[70:71], 0\n\ts_waitcnt lgkmcnt(0)\n\t"
                 "s_cmp_ge_u32 %[w], 8\n\ts_cbranch_scc1 Lc%=\n\t"
                 "global_load_short_d16_hi v64, v4, s[38:39]\n\tglobal_load_short_d16_hi v65, v4, s[40:41]\n\tglobal_load_short_d16_hi v70, v5, s[50:51]\n\t"
                 "global_load_short_d16_hi v66, v4, s[42:43]\n\tglobal_load_short_d16_hi v67, v4, s[44:45]\n\tglobal_load_short_d16_hi v68, v4, s[46:47]\n\t"
                 "global_load_short_d16_hi v69, v4, s[48:49]\n"
                 "Lc%=:\n\ts_barrier\n\tglobal_load_short_d16_hi v71, v5, s[16:17]\n\tglobal_load_dword v72, v0, s[30:31] sc1\n\t"
                 "global_load_dwordx4 v[32:35], v3, %[tab]\n\tglobal_load_dwordx4 v[36:39], v3, %[tab] offset:1024\n\ts_branch Lw%=\n"
                 "Ln%=:\n\ts_mov_b32 m0, 0\n\ts_bitcmp1_b32 0x302, %[w]\n\tds_write_addtid_b32 v0\n\ts_cbranch_scc0 Lo%=\n\t"
                 "v_and_b32 v9, 0x330, v2\n\tv_bfe_u32 v7, v1, 2, 2\n\tv_lshrrev_b32 v8, 2, v9\n\tv_and_b32 v9, 48, v9\n\tv_and_or_b32 v9, v8, %[c0c], v9\n\t"
                 "v_mad_u32_u24 v7, v7, %[rb], v9\n\tv_lshrrev_b32 v8, 5, v1\n\tv_add_u32 v7, 2304, v7\n\ts_sub_i32 s56, %[w], 7\n\ts_max_i32 s56, s56, 0\n\t"
                 "v_mul_u32_u24 v8, 4480, v8\n\tv_add_u32 v9, 9216, v7\n\tv_add_u32 v8, v8, v7\n\tv_add_u32 v11, 9216, v8\n\ts_mov_b32 m0, %[wl]\n\t"
                 "s_waitcnt lgkmcnt(0)\n\ts_lshr_b32 s54, %[rl], 16\n\ts_cmp_ge_u32 s56, s54\n\ts_cbranch_scc1 Lx%=\n\t"
                 "s_bfe_u32 s54, %[rl], 0x80008\n\ts_add_u32 %[it], s54, s56\n\ts_mul_i32 s54, %[it], 18432\n\ts_add_u32 s54, s54, s36\n\ts_addc_u32 s55, s37, 0\n\t"
                 "global_load_dword v72, v0, s[30:31] sc1\n\t"
                 LWS("s[18:19]") "s_addk_i32 m0, 1152\n\ts_nop 0\n\t" LWS("s[26:27]")
                 LXS(0) LXS(1) LXS(2) LXS(3) LX(16, "v8", -1280) LX(17, "v11", -1280) "s_barrier\n\ts_branch Lw%=\n"
                 "Lo%=:\n\ts_cmp_lt_u32 %[w], 8\n\ts_cbranch_scc0 Lx%=\n\t"
                 "v_lshrrev_b32 v7, 4, v1\n\ts_lshl_b32 s56, %[w], 3\n\ts_sub_u32 s56, s56, 16\n\tv_add_u32 v7, s56, v7\n\tv_and_b32 v8, 0x70, v2\n\tv_mul_u32_u24 v7, 36864, v7\n\t"
                 "v_add_u32 v7, v7, v8\n\ts_mov_b32 s58, 0xff00ff00\n\ts_mov_b32 s59, s58\n\ts_mov_b64 s[54:55], 9216\n\ts_cmp_lg_u32 %[w], 0\n\t"
                 "s_waitcnt lgkmcnt(0)\n\ts_cbranch_scc1 Lp%=\n\tglobal_load_dword v136, v0, s[30:31] sc1\n\ts_barrier\n\ts_branch Lw%=\n"
                 "Lp%=:\n\ts_lshl_b32 s56, %[rl], 7\n\ts_and_b32 s56, s56, 0x7f80\n\ts_cbranch_scc0 Lv%=\n\ts_sub_u32 s56, s56, 128\n\t"
                 "v_add_u32 v8, s56, v7\n\tv_mov_b32 v9, 0\n\tv_lshl_add_u64 v[12:13], v[8:9], 0, s[20:21]\n\ts_mov_b64 exec, s[58:59]\n\tv_lshl_add_u64 v[12:13], v[8:9], 0, s[22:23]\n\ts_mov_b64 exec, -1\n\t"
                 "s_barrier\n\t" LG(0) LG(1) LG(2) LG(3) "s_branch Lw%=\n"
                 "Lv%=:\n\ts_barrier\n"
                 "Lw%=:\n\ts_waitcnt lgkmcnt(0)"
                 : "=&{s[36:51]}"(ka0), "=&{s[16:31]}"(ka1), [w] "=&s"(wave), [it] "=&s"(item), [rl] "=&s"(role), [lb] "=&s"(labw), [tid] "+{v0}"(tid)
                 : [b] "s"(b), [kap] "s"(__builtin_amdgcn_kernarg_segment_ptr()), [tab] "s"(&conv_tab), [rb] "s"(D_ * 2), [c0c] "s"(0xc0),
                   [fl] "n"(ROW_FL * TR), [fu] "n"(ROW_FU * TR), [ce] "n"(CODE_KB * 1024 - 128), [wl] "n"(LD_KA)
                 : "memory", "scc", "s52", "s53", "s54", "s55", "s56", "s57", "s58", "s59", "v1", "v2", "v3", "v4", "v5", "v6", "v7", "v8", "v9", "v10", "v11", "v12", "v13", ENTRY_CLOB);
    Params P;
#define KARG(T, v, i) (T)(((u64)v[2 * (i) + 1] << 32) | v[2 * (i)])
    P.x = KARG(const u16*, ka0, 0); P.wg = KARG(const u16*, ka1, 2); P.wu = KARG(const u16*, ka1, 3); P.wd = KARG(const u16*, ka1, 4);
    P.out = KARG(u16*, ka1, 6); P.ws = KARG(unsigned char*, ka1, 7);
    const int xcd = b & 7, ii = b >> 3, nconv = xcd < 2 ? 24 : 16;
    if (ii < nconv) {
        if (wave >= 8) { conv_wave<2>(P, sm, wave - 8, xcd, ii, labw); EXIT(); }
        if (wave >= 4) { conv_wave<1>(P, sm, wave - 4, xcd, ii, labw); EXIT(); }
        conv_wave<0>(P, sm, wave, xcd, ii, labw);
        EXIT();
    }
    if (wave == 1 || wave >= 8) {
        asm volatile("; claim\n\ts_setprio 3\n" ITEM_ASM : : [ws] "s"(P.ws), [it] "s"(item), [fl] "s"(FL_IT + 4 * (wave == 1 ? 0 : wave - 7)), [kb] "n"(WS_KEYS), [cb] "n"(WS_CAND), [wl] "n"(LD_KA)
                     : "memory", "vcc", "scc", "s88", "s89", "s92", "s93", "s98", ITEM_CLOB);
        EXIT();
    }
    const bool moe = (role & 255u) != 0u;
    if (moe && wave >= 2) { moe_weights(P, sm, wave - 2, (int)(role & 255u) - 1, role >> 16); EXIT(); }
    const int lane = fresh_lane();
    u32 tag0 = 0;
    if (!moe && wave >= 8 - HWAVES) {
        const Lab lb = lab_of(labw);
        if (RBQ && b == RB) EXIT();
        const u32 nh = (u32)(32 - nconv) - lb.n - (RBQ && xcd == (RB & 7) ? 1u : 0u), nit = role >> 16;
        if (nit && 7 - wave >= HT0) wait_ge<1>(sm + FL_IT + 4 * umin(lane, nit - 1), 1);
        helper_prefetch(P, lane, (u32)(7 - wave) * nh + (u32)(ii - nconv) - lb.n, HWAVES * nh, lb, HWALL ? 0u : HWC * (u32)nconv);
        EXIT();
    }
    if (wave) EXIT();
    const u32 t_in = (u32)__builtin_amdgcn_s_memrealtime();
    {
        u32 epoch;
        asm volatile("s_waitcnt vmcnt(0) ; epoch" : "={v136}"(epoch)::"memory");
        tag0 = tag_of(rfl(epoch));
    }
    st2_sc1(ws_rsrc(P), lane == 0 ? WS_ACK + (u32)b * 128u : (b == TB && lane >= 12 && lane < 16 ? WS_T0 + (u32)(lane - 8) * 128u : OOB), (v2u){tag0, t_in});
    if (!moe && b != RB) EXIT();
    flag1(sm + LD_TAG, tag0);
    const u32 k8 = select8(P, lane, xcd, tag0, t_in, role & 255u);
    if (moe) {
        u32 src[4];
#pragma unroll
        for (int j = 0; j < 4; ++j) { src[j] = (((role & 255u) - 1u) * 64u + (u32)(32 + 8 * j + (lane >> 3))) * (D_ * 2) + (u32)(((lane & 7) ^ ((lane >> 3) & 6)) * 16); PIN(src[j]); }
        VMWAIT(0);
        wg_barrier();
#pragma unroll
        for (int j = 0; j < 4; ++j) dma_rows<8192>(P.wd, src[j], LD_WD + 4096 + j * 1024);
        wg_barrier();
        VMWAIT(0);
        wg_barrier();
        EXIT();
    }
    {
        const __amdgpu_buffer_rsrc_t rws = ws_rsrc(P);
        v2u kg = ld2_sc1(rws, WS_KEYS + (2047u - (k8 & 0x7ffu)) * 8u);
        const v2u a0 = ld2_sc1(rws, (lane & 6) ? WS_ACK + (u32)(128 + lane) * 128u : OOB), a1 = ld2_sc1(rws, WS_ACK + (u32)(192 + lane) * 128u);
        const u32 late = wave_max(umax(a0[0] == tag0 ? umin(a0[1] - t_in + 64u, 255u) : 0u, a1[0] == tag0 ? umin(a1[1] - t_in + 64u, 255u) : 0u));
#pragma unroll 1
        for (int spin = 0; spin < SPIN_CAP && !__all(kg[1] == tag0); ++spin) {
            __builtin_amdgcn_s_sleep(8);
            MEMFENCE();
            kg = ld2_sc1(rws, WS_KEYS + (2047u - (k8 & 0x7ffu)) * 8u);
        }
        st_sc1(rws, WS_SEL + (u32)(lane >> 3) * 128u + (u32)(lane & 7) * 16u, (v4u){k8, tag0, (kg[0] & 0xffffu) | (t_in + PART_AT + (late * PART_K >> 3)) << 16, tag0});
        __builtin_amdgcn_s_sleep(BUMP_DELAY);
#pragma unroll 1
        for (int spin = 0; spin < SPIN_CAP; ++spin) {
            MEMFENCE();
            u32 bad = 0;
#pragma unroll
            for (int k = 0; k < 4; ++k) bad |= ld2_sc1(rws, WS_ACK + (u32)(k * 64 + lane) * 128u)[0] ^ tag0;
            if (__all(bad == 0u)) break;
            __builtin_amdgcn_s_sleep(10);
        }
        if (lane == 0) __hip_atomic_store((GAS u32*)(P.ws + WS_EPOCH), tag0, RLX_AGENT);
    }
    VMWAIT(0);
}

extern "C" void kernel_launch(void* const* d_in, const int* in_sizes, int n_in, void* d_out, int out_size,
                              void* d_ws, size_t ws_size, hipStream_t stream) {
    Args A = mk_roles();
    Params& P = A.p;
    P.x = (const u16*)d_in[0];
    P.Are = (const u16*)d_in[1]; P.Aim = (const u16*)d_in[2];
    P.Bre = (const u16*)d_in[3]; P.Bim = (const u16*)d_in[4];
    P.Cre = (const u16*)d_in[5]; P.Cim = (const u16*)d_in[6];
    P.logdt = (const u16*)d_in[7]; P.dskip = (const u16*)d_in[8];
    P.wr = (const u16*)d_in[10]; P.wg = (const u16*)d_in[11]; P.wu = (const u16*)d_in[12]; P.wd = (const u16*)d_in[13];
    P.gw = (const u16*)d_in[14];
    P.out = (u16*)d_out;
    P.ws = (unsigned char*)d_ws;
    fused_kernel<<<256, NTHR, 0, stream>>>(A);
}
```

```cpp
#include <hip/hip_runtime.h>
#include <stdint.h>

typedef unsigned int u32;
typedef unsigned long long u64;
typedef unsigned short u16;
typedef u32 v4u __attribute__((ext_vector_type(4)));
typedef u32 v2u __attribute__((ext_vector_type(2)));
typedef u32 v8u __attribute__((ext_vector_type(8)));
typedef u32 v16u __attribute__((ext_vector_type(16)));
typedef float v4f __attribute__((ext_vector_type(4)));
typedef float v2f __attribute__((ext_vector_type(2)));
typedef short v8s __attribute__((ext_vector_type(8)));
typedef __bf16 v2bf __attribute__((ext_vector_type(2)));
#define GAS __attribute__((address_space(1)))
#define LAS __attribute__((address_space(3)))
#define DI __device__ __forceinline__
#define RLX_AGENT __ATOMIC_RELAXED, __HIP_MEMORY_SCOPE_AGENT
#define SB() __builtin_amdgcn_sched_barrier(0)
#define MEMFENCE() asm volatile("" ::: "memory")
#define PIN(x) asm volatile("" : "+v"(x))
#define VMWAIT(n) asm volatile("s_waitcnt vmcnt(%0)" ::"n"(n) : "memory")

#define CODE_KB 24
#define WIN 5
#define WIN0 5
#define PART_AT 547
#define PART_K 4
#define NWE 6
#define PART_SEL 270
#define W2A 2
#define W2S 0
#define HWAVES 5
#define HT0 1
#define HWIN 2
#define HR0 320
#define PART_GAP 2
#define SLOW0 128
#define SEL_AT 205
#define TREF 1
#define SEL_GAP 3
#define BUMP_DELAY 16
#define NM_LIST 4, 4, 10, 11, 11, 10, 11, 11
#define NH_LIST 2, 2, 2, 2, 1, 0, 1, 1
#define NI_LIST 16, 16, 28, 21, 16, 0, 16, 15
constexpr int TB = 248;
constexpr int RB = 250;

constexpr int L_ = 2048, D_ = 576, N_ = 128, F_ = 4608;
constexpr int NTHR = 768;
constexpr int SPIN_CAP = 1 << 17;
constexpr u32 OOB = 0x7ffffff0u;
__device__ __forceinline__ unsigned tag_of(unsigned epoch) { return epoch == 0xffffffffu ? 1u : epoch + 1u; }

constexpr u32 WS_EPOCH = 0;
constexpr u32 WS_SEL = 1024;
constexpr u32 WS_T0 = 2048;
constexpr u32 WS_KEYS = 4096;
constexpr u32 WS_CAND = WS_KEYS + 16384;
constexpr u32 WS_ACK = WS_CAND + 8 * 128 * 128;
constexpr u32 WS_PART = WS_ACK + 256 * 128;
constexpr u32 WS_BYTES = WS_PART + 144 * 72 * 128;

constexpr int TR = 2080;
constexpr int LD_TB = 0;
constexpr int ROW_T1 = 16, ROW_FL = 26, ROW_FU = 27, ROW_T0 = 28, ROW_T2 = 44;
constexpr int U_PAD = 256, U_CH = 2 * (U_PAD + 2048);
constexpr int LD_U = LD_TB + 60 * TR;
constexpr int PA = 550, PB = 545, KA_CP = 1216, KA_CH = 2 * KA_CP;
constexpr int LD_KA = LD_U + 4 * U_CH;
constexpr int G_CH = 896;
constexpr int LD_G = LD_KA + 4 * KA_CH;
constexpr int LD_BD = LD_G + 4 * G_CH;
constexpr int LD_CONV_END = LD_BD + 5120;
constexpr int FL_IT = 64;
constexpr int LD_TAG = 48;
constexpr int FL_LATE = 56;
constexpr int LD_SCR = 512;
constexpr int LD_XS = 1024 + 2 * 9216;
constexpr int LD_WD = LD_XS + 9216;
constexpr int LD_RED = LD_WD + 9 * 8192;
constexpr int HB_ROW = 144;
constexpr int LD_HB = LD_RED + 6 * 4096;
constexpr int LD_MOE_END = LD_HB + 8 * HB_ROW;
constexpr int LDS_BYTES = LD_CONV_END > LD_MOE_END ? LD_CONV_END : LD_MOE_END;
static_assert(LDS_BYTES > 81920 && LDS_BYTES <= 163840, "lds: one workgroup per CU");
static_assert(LD_BD % 128 == 0 && LD_MOE_END <= LD_KA && LD_U % 16 == 0 && LD_KA % 16 == 0 && LD_G % 16 == 0 && LD_WD % 1024 == 0, "align");

struct Params {
    const u16 *x, *Are, *Aim, *Bre, *Bim, *Cre, *Cim, *logdt, *dskip, *wr, *wg, *wu, *wd, *gw;
    u16* out;
    unsigned char* ws;
};
struct Args { Params p; unsigned role[256]; };
constexpr int nconv_of(int label) { return label < 2 ? 24 : 16; }
constexpr Args mk_roles() {
    Args A{};
    const int nm[8] = {NM_LIST}, nh[8] = {NH_LIST}, ni[8] = {NI_LIST};
    int m0 = 0, i0 = 0;
    bool ok = true;
    for (int x = 0; x < 8; ++x) {
        const int nc = nconv_of(x), first = x * 32, w = 32 - nc;
        for (int ii = 0; ii < nc; ++ii) A.role[first + ii] = (unsigned)(m0 | nm[x] << 8 | (65536 / nm[x] + 1) << 16);
        const int rest = ni[x] - (w - nm[x]) * nh[x];
        ok = ok && rest >= 0;
        for (int ii = 31; ii >= nc; --ii) {
            const int cnt = ii - nc >= nm[x] ? nh[x] : rest / nm[x] + (ii - nc < rest % nm[x] ? 1 : 0);
            A.role[first + ii] = (unsigned)((ii - nc < nm[x] ? m0 + ii - nc + 1 : 0) | (cnt ? i0 : 0) << 8 | cnt << 16);
            i0 += cnt; ok = ok && cnt <= 3;
        }
        m0 += nm[x];
    }
    A.role[255] |= (m0 != 72 || i0 != 128 || !ok) ? 0x80000000u : 0u;
    return A;
}
static_assert(!(mk_roles().role[255] >> 31) && (mk_roles().role[(RB & 7) * 32 + (RB >> 3)] & 255) == 0, "roles");

#define EXIT() do { VMWAIT(0); __builtin_amdgcn_endpgm(); } while (0)

typedef LAS unsigned char* lptr;
typedef const LAS unsigned char* clptr;
DI float bf_lo(u32 v) { return __uint_as_float(v << 16); }
DI float bf_hi(u32 v) { return __uint_as_float(v & 0xffff0000u); }
DI u32 pk_bf16(float a, float b) { v2f f = {a, b}; return __builtin_bit_cast(u32, __builtin_convertvector(f, v2bf)); }
DI float dot2(u32 a, u32 b, float c) { return __builtin_amdgcn_fdot2_f32_bf16(__builtin_bit_cast(v2bf, a), __builtin_bit_cast(v2bf, b), c, false); }
DI float sigmoidf_(float s) { return __builtin_amdgcn_rcpf(1.f + __builtin_amdgcn_exp2f(-1.44269504f * s)); }
template <int CTRL, int RM> DI u32 dpp0(u32 v) { return (u32)__builtin_amdgcn_update_dpp(0, (int)v, CTRL, RM, 0xf, true); }
template <int CTRL, int RM> DI float dppf(float v) { return __uint_as_float(dpp0<CTRL, RM>(__float_as_uint(v))); }
DI u32 umax(u32 a, u32 b) { return a > b ? a : b; }
DI u32 umin(u32 a, u32 b) { return a < b ? a : b; }
DI int fresh_lane() { int l; asm volatile("v_mbcnt_lo_u32_b32 %0, -1, 0\n\tv_mbcnt_hi_u32_b32 %0, -1, %0" : "=v"(l)); return l; }
DI u32 rfl(u32 v) { return (u32)__builtin_amdgcn_readfirstlane((int)v); }
DI u32 rdl(u32 v, int l) { return (u32)__builtin_amdgcn_readlane((int)v, l); }
DI u32 wave_max(u32 v) {
    v = umax(v, dpp0<0x111, 0xf>(v)); v = umax(v, dpp0<0x112, 0xf>(v)); v = umax(v, dpp0<0x114, 0xf>(v)); v = umax(v, dpp0<0x118, 0xf>(v));
    v = umax(v, dpp0<0x142, 0xa>(v)); v = umax(v, dpp0<0x143, 0xc>(v));
    return rdl(v, 63);
}
DI u32 med3u(u32 a, u32 b, u32 c) { return umax(umin(a, b), umin(umax(a, b), c)); }
DI u32 perm_lo(u32 hi, u32 lo) { return __builtin_amdgcn_perm(hi, lo, 0x05040100u); }
DI u32 perm_hi(u32 hi, u32 lo) { return __builtin_amdgcn_perm(hi, lo, 0x07060302u); }
typedef __bf16 v8bf __attribute__((ext_vector_type(8)));
typedef __bf16 v16bf __attribute__((ext_vector_type(16)));
DI v4f smfmac(v4u a, v8u b, v4f c, u32 idx) { return __builtin_amdgcn_smfmac_f32_16x16x64_bf16(__builtin_bit_cast(v8bf, a), __builtin_bit_cast(v16bf, b), c, (int)idx, 0, 0); }
DI __amdgpu_buffer_rsrc_t mk_rsrc(const void* p, u32 n) { return __builtin_amdgcn_make_buffer_rsrc((void*)p, 0, n, 0x00020000); }
DI __amdgpu_buffer_rsrc_t ws_rsrc(const Params& P) { return mk_rsrc(P.ws, WS_BYTES); }
DI v4u ld_sc1(__amdgpu_buffer_rsrc_t r, u32 off) { return __builtin_amdgcn_raw_buffer_load_b128(r, off, 0, 16); }
DI v2u ld2_sc1(__amdgpu_buffer_rsrc_t r, u32 off) { return __builtin_amdgcn_raw_buffer_load_b64(r, off, 0, 16); }
DI void st_sc1(__amdgpu_buffer_rsrc_t r, u32 off, v4u v) { __builtin_amdgcn_raw_buffer_store_b128(v, r, off, 0, 16); }
DI void st2_sc1(__amdgpu_buffer_rsrc_t r, u32 off, v2u v) { __builtin_amdgcn_raw_buffer_store_b64(v, r, off, 0, 16); }
DI float g_of(u32 p) { return (float)__builtin_bit_cast(_Float16, (u16)p); }
DI bool tag_ok(v4u v, u32 tag) { return (v[1] == tag) & (v[3] == tag); }
DI v2u tr_read(clptr p) {
    typedef short v4s __attribute__((ext_vector_type(4)));
    return __builtin_bit_cast(v2u, __builtin_amdgcn_ds_read_tr16_b64_v4i16((LAS v4s*)p));
}
DI void fmac_(float& acc, float a, float b) { asm("v_fmac_f32 %0, %1, %2" : "+v"(acc) : "v"(a), "v"(b)); }
DI float mul_(float a, float b) { float r; asm("v_mul_f32 %0, %1, %2" : "=v"(r) : "v"(a), "v"(b)); return r; }
#define W16(p) (*(volatile LAS u16*)(p))
#define W32(p) (*(volatile LAS u32*)(p))
#define W64(p) (*(volatile LAS v2u*)(p))
#define W128(p) (*(volatile LAS v4u*)(p))

template <int SLEEP> DI void wait_ge(clptr p, u32 target) {
    u32 v, n;
    asm volatile("s_mov_b32 %1, 0x100000\n"
                 "Lw%=:\n\tds_read_b32 %0, %2\n\ts_waitcnt lgkmcnt(0)\n\tv_cmp_gt_u32 vcc, %3, %0\n\ts_cbranch_vccz Ld%=\n\t"
                 "s_sleep %4\n\ts_sub_u32 %1, %1, 1\n\ts_cbranch_scc0 Lw%=\n"
                 "Ld%=:"
                 : "=&v"(v), "=&s"(n) : "v"(p), "s"(target), "n"(SLEEP) : "memory", "vcc", "scc");
}
DI void flag1(lptr p, u32 v) {
    asm volatile("s_mov_b64 exec, 1\n\tds_write_b32 %0, %1\n\ts_mov_b64 exec, -1" ::"v"(p), "v"(v) : "memory");
}
template <int OFF> DI void put_row(u32 v) { asm volatile("ds_write_addtid_b32 %0 offset:%1" ::"v"(v), "n"(OFF) : "memory"); }
DI void wg_barrier() { asm volatile("s_waitcnt lgkmcnt(0)\n\ts_barrier" ::: "memory"); }

DI void touch(const void* base, u32 off) {
    asm volatile("s_movk_i32 m0, %2\n\ts_nop 0\n\tglobal_load_lds_dword %0, %1" ::"v"(off), "s"(base), "n"(LD_SCR) : "memory");
}
template <int STEP> DI void dma_rows(const void* base, u32 off, u32 dst) {
#define DR(q) "global_load_lds_dwordx4 %0, %1 offset:128*" #q "\n\ts_addk_i32 m0, %3\n\ts_nop 0\n\t"
    asm volatile("s_mov_b32 m0, %2\n\ts_nop 0\n\t" DR(0) DR(1) DR(2) DR(3) DR(4) DR(5) DR(6) DR(7) "global_load_lds_dwordx4 %0, %1 offset:1024" ::"v"(off), "s"(base), "s"(dst), "n"(STEP - 128) : "memory", "scc");
}
DI int cg_of(int xcd, int ii) { return ii < 16 ? 16 * xcd + ii : 112 + 8 * xcd + ii; }
struct Lab { u32 m0, n, mg; };
DI Lab lab_of(u32 w) { return {w & 255u, (w >> 8) & 255u, w >> 16}; }

DI u32 from_lane(u32 v, int l) { return (u32)__builtin_amdgcn_ds_bpermute(4 * l, (int)v); }

DI void conv_final(const Params& P, lptr sm, int cg, int xcd, u32 tag, v4u sg) {
    const int lane = fresh_lane();
    const __amdgpu_buffer_rsrc_t rws = ws_rsrc(P), ro = mk_rsrc(P.out, L_ * D_ * 2);
    const int j = (lane & 4) + (lane >> 4), cc = lane & 3;
    u32 po = WS_PART + (u32)cg * (72u * 128u) + (u32)lane * 16u;
    PIN(po);
    clptr fw = sm + LD_TB + ROW_FU * TR + (lane & 3) * 516 + 256;
#pragma unroll 1
    for (int spin = 0; spin < SPIN_CAP && !__all(tag_ok(sg, tag)); ++spin) {
        __builtin_amdgcn_s_sleep(4);
        MEMFENCE();
        sg = ld_sc1(rws, WS_SEL + (u32)xcd * 128u + (u32)(lane & 7) * 16u);
    }
    const u32 sel = from_lane(sg[0], j), tok = 2047u - (sel & 0x7ffu);
    wait_ge<1>(fw, 9);
    wait_ge<1>(sm + LD_TB + ROW_FL * TR + (lane & 3) * 516 + (lane & 4) * 64, 18);
    const float rs = sigmoidf_(bf_hi(sel ^ ((~(u32)((int)sel >> 31)) | 0x80000000u)));
    const float x = bf_lo((u32)W16(sm + LD_U + cc * U_CH + 2 * U_PAD + tok * 2u));
    const u32 c = (tok >> 4) & 15u;
    clptr ap = sm + LD_TB + (tok >> 7) * TR + cc * 512 + (c & 7u) * 64u + (((tok >> 2) ^ (c >> 1)) & 3u) * 16u + (tok & 3u) * 4u;
    const float a2 = *(const volatile LAS float*)(ap + (ROW_T0 - 4) * TR);
    const float a = *(const volatile LAS float*)ap + (tok >= 512u ? a2 : 0.f);
    const float g = g_of((u32)W16(sm + LD_TB + ROW_T1 * TR + tok * 2u));
    float gs = (1.f - g) * rs, base = x + g * (a - x);
    u32 oo = (lane & 9) ? OOB : (tok * D_ + (u32)(4 * cg + cc)) * 2u;
    PIN(base); PIN(gs); PIN(oo); PIN(fw);
    u32 po2 = po + 5120u;
    const u32 at = rfl(sg[2]);
    PIN(po2);
#pragma unroll 1
    for (int w = 0; w < 4096; ++w) {
        if ((int)((u32)__builtin_amdgcn_s_memrealtime() - at) >= 0) break;
        __builtin_amdgcn_s_sleep(1);
    }
    float s0, s1, s2, s3;
    u32 n;
#define PLD(k, a, o) "global_load_dwordx4 v[64+4*" #k ":67+4*" #k "], " a ", %[ws] offset:" #o " sc1\n\t"
#define PAD(k, w) "s_waitcnt vmcnt(" #w ")\n\tv_dot2c_f32_bf16 %[s0], %[lo], v[64+4*" #k "]\n\tv_dot2c_f32_bf16 %[s1], %[hi], v[64+4*" #k "]\n\t" \
                  "v_dot2c_f32_bf16 %[s2], %[lo], v[66+4*" #k "]\n\tv_dot2c_f32_bf16 %[s3], %[hi], v[66+4*" #k "]\n\t" \
                  "v_bitop3_b32 v[65+4*" #k "], v[65+4*" #k "], v[67+4*" #k "], %[tag] bitop3:0x7e\n\t"
    asm volatile("s_mov_b32 %[n], 0x20000\n\tv_mov_b32 v100, 1\n"
                 "Lr%=:\n\tv_cmp_ne_u32 vcc, 0, v100\n\tv_mov_b32 %[s0], 0\n\tv_mov_b32 %[s1], 0\n\tv_mov_b32 %[s2], 0\n\tv_mov_b32 %[s3], 0\n\ts_mov_b64 exec, vcc\n\t"
                 PLD(0, "%[p0]", 0) PLD(1, "%[p0]", 1024) PLD(2, "%[p0]", 2048) PLD(3, "%[p0]", 3072) PLD(4, "%[p1]", -1024)
                 PLD(5, "%[p1]", 0) PLD(6, "%[p1]", 1024) PLD(7, "%[p1]", 2048) PLD(8, "%[p1]", 3072) "s_mov_b64 exec, -1\n\t"
                 PAD(0, 8) PAD(1, 7) PAD(2, 6) "v_or3_b32 v100, v65, v69, v73\n\t" PAD(3, 5) PAD(4, 4) "v_or3_b32 v100, v100, v77, v81\n\t"
                 PAD(5, 3) PAD(6, 2) "v_or3_b32 v100, v100, v85, v89\n\t" PAD(7, 1) PAD(8, 0) "v_or3_b32 v100, v100, v93, v97\n\t"
                 "v_cmp_ne_u32 vcc, 0, v100\n\ts_cbranch_vccz Le%=\n\t"
                 "v_mov_b32 v65, %[tag]\n\tv_mov_b32 v69, %[tag]\n\tv_mov_b32 v73, %[tag]\n\tv_mov_b32 v77, %[tag]\n\tv_mov_b32 v81, %[tag]\n\t"
                 "v_mov_b32 v85, %[tag]\n\tv_mov_b32 v89, %[tag]\n\tv_mov_b32 v93, %[tag]\n\tv_mov_b32 v97, %[tag]\n\t"
                 "s_sleep %[gap]\n\ts_sub_u32 %[n], %[n], 1\n\ts_cbranch_scc0 Lr%=\n"
                 "Le%=:\n\ts_nop 1"
                 : [s0] "=&v"(s0), [s1] "=&v"(s1), [s2] "=&v"(s2), [s3] "=&v"(s3), [n] "=&s"(n)
                 : [p0] "v"(po), [p1] "v"(po2), [ws] "s"(P.ws), [tag] "s"(tag), [lo] "s"(0x00003f80u), [hi] "s"(0x3f800000u), [gap] "n"(PART_GAP)
                 : "memory", "vcc", "scc", "v64", "v65", "v66", "v67", "v68", "v69", "v70", "v71", "v72", "v73", "v74", "v75", "v76", "v77", "v78", "v79", "v80", "v81", "v82", "v83",
                   "v84", "v85", "v86", "v87", "v88", "v89", "v90", "v91", "v92", "v93", "v94", "v95", "v96", "v97", "v98", "v99", "v100");
    wait_ge<0>(fw, 20);
    const auto a3 = __builtin_amdgcn_permlane32_swap(__float_as_uint(s0), __float_as_uint(s2), false, false);
    const auto b2 = __builtin_amdgcn_permlane32_swap(__float_as_uint(s1), __float_as_uint(s3), false, false);
    const auto e = __builtin_amdgcn_permlane16_swap(__float_as_uint(__uint_as_float(a3[0]) + __uint_as_float(a3[1])), __float_as_uint(__uint_as_float(b2[0]) + __uint_as_float(b2[1])), false, false);
    float y = __uint_as_float(e[0]) + __uint_as_float(e[1]);
    y += dppf<0x128, 0xf>(y);
    const float o = base + gs * y;
    __builtin_amdgcn_raw_buffer_store_b32(pk_bf16(o, dppf<0xB1, 0xf>(o)), ro, oo, 0, 0);
}

struct ConvTab { u32 v[(12 * 5 + 5) * 256]; };
constexpr ConvTab mk_tab() {
    ConvTab t{};
    for (int kind = 0; kind < 3; ++kind) for (int ch = 0; ch < 4; ++ch) for (int lane = 0; lane < 64; ++lane) {
        const int c = lane & 15, kg = lane >> 4;
        const int ka = LD_KA + ch * KA_CH, gq = LD_G + ch * G_CH;
        const int kb = ka + ((c & 1) ? KA_CP + 2 * PB : 2 * PA) - 2 * (c + 16 - 8 * kg) - 64 * 15;
        u32 ad[20] = {};
        if (kind < 2) {
            ad[0] = LD_TB + ROW_FU * TR + ch * 512;
            ad[1] = LD_U + ch * U_CH + 2 * (U_PAD - 16 + 16 * c + 8 * kg) - 7 * 64;
            ad[2] = LD_TB + ROW_FU * TR + (lane & 3) * 516 + 256;
            ad[3] = LD_TB + (ROW_T0 + (c >> 3)) * TR + ch * 512 + 64 * (c & 7) + 16 * (kg ^ ((c >> 1) & 3));
            ad[4] = kind ? gq + 4 * (15 - c + 4 * kg) : kb;
            ad[5] = kind ? kb - 64 : LD_TB + (c >> 3) * TR + ch * 512 + 64 * (c & 7) + 16 * (kg ^ ((c >> 1) & 3));
            ad[6] = kind ? LD_BD + lane * 16 : LD_TB + ROW_FL * TR + ch * 512 + 256;
            ad[7] = gq;
        } else {
            ad[0] = LD_TB + ROW_FL * TR + ch * 512 + (lane & 1) * 260;
            ad[1] = LD_TB + c * TR + ch * 512 + kg * 16;
            ad[2] = LD_TB + (ROW_T1 + (c & 3) + (c >> 3) + 5 * ((c >> 2) & 1)) * TR + ch * 512 + kg * 16;
            ad[3] = ka + 2 * (PA - 3 - (4 * (kg & 1) + 128 * (kg >> 1) + 8 * c));
            ad[4] = ka + (kg < 2 ? 2 * (PA - 6) : KA_CP + 2 * (PB - 9)) - 2 * (256 + 128 * (kg & 1) + 8 * c);
            ad[5] = gq + (kg < 2 ? (c & 1) * 256 + 4 * (47 - 24) + 2 * kg : 512 + 4 * (63 - 24) + 2 * (c & 1) - 128 * (kg - 2)) - 4 * (c >> 1);
            ad[6] = kg < 2 ? 0x01000302u : 0x03020302u;
            ad[7] = kg < 2 ? 0x01000100u : 0x01000302u;
            ad[8] = lane == 0 ? 0x05040100u : ad[7];
            ad[9] = (128 * ch + 2 * lane) * (D_ * 2);
            ad[10] = LD_U + 2 * (U_PAD + 128 * ch + 2 * lane);
            ad[11] = LD_TB + ch * TR + 64 * (lane >> 3) + 16 * (((lane >> 1) ^ (lane >> 4)) & 3) + 8 * (lane & 1);
            ad[12] = WS_KEYS + (64 * ch + lane) * 16;
            ad[13] = LD_TB + ROW_FL * TR + (lane & 3) * 516;
            ad[14] = WS_PART + (18 * ch + (lane < 17 ? lane : 17)) * 128;
            ad[15] = LD_TB + ROW_FL * TR + (lane & 3) * 516 + (lane & 4) * 64;
            ad[16] = lane * 128;
            ad[17] = lane < 32 ? ka + (lane >> 4) * KA_CP + (c < 6 ? c * 16 : 1088 + (c & 3) * 16) : LD_U + ch * U_CH + (lane - 32) * 16;
            ad[18] = gq + (lane < 55 ? lane : 55) * 16;
            ad[19] = ka;
        }
        for (int k = 0; k < 20; ++k) t.v[((kind * 4 + ch) * 5 + (k >> 2)) * 256 + lane * 4 + (k & 3)] = ad[k];
    }
    const int lo[5] = {3, 6, 5, 4, 3}, hi[5] = {5, 10, 11, 12, 4};
    for (int k = 0; k < 5; ++k) for (int lane = 0; lane < 64; ++lane) for (int e = 0; e < 8; ++e) {
        const int l = (lane & 15) + 16 - 8 * (lane >> 4) - e;
        if (l >= lo[k] && l <= hi[k]) t.v[(60 + k) * 256 + lane * 4 + (e >> 1)] |= (k < 4 ? 0x3f80u : 0xffffu) << (16 * (e & 1));
    }
    return t;
}
static __device__ const ConvTab conv_tab = mk_tab();
#define CONV_A_ASM \
    "s_setprio 1\n" \
    "s_waitcnt vmcnt(4)\n" \
    "v_mul_f32_e32 v0, 0x3fb8aa3b, v70\n" \
    "v_exp_f32_e32 v0, v0\n" \
    "v_mul_f32_e32 v1, 0x3fb8aa3b, v64\n" \
    "v_cvt_pk_bf16_f32 v0, 0, v0\n" \
    "v_mul_f32_e32 v2, 0.15915494, v65\n" \
    "v_mul_f32_e32 v1, v0, v1\n" \
    "v_mul_f32_e32 v2, v0, v2\n" \
    "v_mul_f32_e32 v4, 0x41000000, v2\n" \
    "v_mul_f32_e32 v3, 0x41000000, v1\n" \
    "v_fract_f32_e32 v4, v4\n" \
    "v_exp_f32_e32 v3, v3\n" \
    "v_cos_f32_e32 v8, v4\n" \
    "v_sin_f32_e32 v9, v4\n" \
    "v_mul_f32_e32 v66, v0, v66\n" \
    "v_mul_f32_e64 v8, v3, v8\n" \
    "v_mul_f32_e64 v9, v3, v9\n" \
    "v_pk_mul_f32 v[18:19], v[8:9], v[8:9] op_sel_hi:[0,1]\n" \
    "v_cvt_pk_bf16_f32 v30, v8, v9\n" \
    "v_pk_fma_f32 v[14:15], v[8:9], v[8:9], v[18:19] op_sel:[1,1,0] op_sel_hi:[0,1,1] neg_lo:[0,1,0]\n" \
    "ds_write_addtid_b32 v30 offset:2080\n" \
    "v_pk_mul_f32 v[18:19], v[8:9], v[14:15] op_sel_hi:[0,1]\n" \
    "v_cvt_pk_bf16_f32 v31, v14, v15\n" \
    "v_pk_fma_f32 v[14:15], v[14:15], v[8:9], v[18:19] op_sel:[1,1,0] op_sel_hi:[0,1,1] neg_lo:[0,1,0]\n" \
    "ds_write_addtid_b32 v31 offset:4160\n" \
    "v_pk_mul_f32 v[18:19], v[8:9], v[14:15] op_sel_hi:[0,1]\n" \
    "v_cvt_pk_bf16_f32 v30, v14, v15\n" \
    "v_pk_fma_f32 v[14:15], v[14:15], v[8:9], v[18:19] op_sel:[1,1,0] op_sel_hi:[0,1,1] neg_lo:[0,1,0]\n" \
    "ds_write_addtid_b32 v30 offset:6240\n" \
    "v_pk_mul_f32 v[18:19], v[8:9], v[14:15] op_sel_hi:[0,1]\n" \
    "v_cvt_pk_bf16_f32 v31, v14, v15\n" \
    "v_pk_fma_f32 v[14:15], v[14:15], v[8:9], v[18:19] op_sel:[1,1,0] op_sel_hi:[0,1,1] neg_lo:[0,1,0]\n" \
    "ds_write_addtid_b32 v31 offset:8320\n" \
    "v_mul_f32_e32 v4, 0x43000000, v2\n" \
    "v_mul_f32_e32 v3, 0x43000000, v1\n" \
    "v_fract_f32_e32 v4, v4\n" \
    "v_exp_f32_e32 v3, v3\n" \
    "v_cos_f32_e32 v12, v4\n" \
    "v_sin_f32_e32 v13, v4\n" \
    "v_pk_mul_f32 v[18:19], v[8:9], v[14:15] op_sel_hi:[0,1]\n" \
    "v_cvt_pk_bf16_f32 v30, v14, v15\n" \
    "v_pk_fma_f32 v[14:15], v[14:15], v[8:9], v[18:19] op_sel:[1,1,0] op_sel_hi:[0,1,1] neg_lo:[0,1,0]\n" \
    "ds_write_addtid_b32 v30 offset:10400\n" \
    "v_pk_mul_f32 v[18:19], v[8:9], v[14:15] op_sel_hi:[0,1]\n" \
    "v_cvt_pk_bf16_f32 v31, v14, v15\n" \
    "v_pk_fma_f32 v[14:15], v[14:15], v[8:9], v[18:19] op_sel:[1,1,0] op_sel_hi:[0,1,1] neg_lo:[0,1,0]\n" \
    "ds_write_addtid_b32 v31 offset:12480\n" \
    "v_mul_f32_e64 v12, v3, v12\n" \
    "v_mul_f32_e64 v13, -v3, v13\n" \
    "v_pk_mul_f32 v[18:19], v[8:9], v[14:15] op_sel_hi:[0,1]\n" \
    "v_cvt_pk_bf16_f32 v30, v14, v15\n" \
    "v_pk_fma_f32 v[14:15], v[14:15], v[8:9], v[18:19] op_sel:[1,1,0] op_sel_hi:[0,1,1] neg_lo:[0,1,0]\n" \
    "ds_write_addtid_b32 v30 offset:14560\n" \
    "v_pk_mul_f32 v[18:19], v[8:9], v[14:15] op_sel_hi:[0,1]\n" \
    "v_cvt_pk_bf16_f32 v31, v14, v15\n" \
    "v_pk_fma_f32 v[14:15], v[14:15], v[8:9], v[18:19] op_sel:[1,1,0] op_sel_hi:[0,1,1] neg_lo:[0,1,0]\n" \
    "ds_write_addtid_b32 v31 offset:16640\n" \
    "v_fract_f32_e32 v6, v2\n" \
    "v_exp_f32_e32 v5, v1\n" \
    "v_cos_f32_e32 v10, v6\n" \
    "v_sin_f32_e32 v11, v6\n" \
    "v_pk_mul_f32 v[18:19], v[8:9], v[14:15] op_sel_hi:[0,1]\n" \
    "v_cvt_pk_bf16_f32 v30, v14, v15\n" \
    "v_pk_fma_f32 v[14:15], v[14:15], v[8:9], v[18:19] op_sel:[1,1,0] op_sel_hi:[0,1,1] neg_lo:[0,1,0]\n" \
    "ds_write_addtid_b32 v30 offset:18720\n" \
    "v_pk_mul_f32 v[18:19], v[8:9], v[14:15] op_sel_hi:[0,1]\n" \
    "v_cvt_pk_bf16_f32 v31, v14, v15\n" \
    "v_pk_fma_f32 v[14:15], v[14:15], v[8:9], v[18:19] op_sel:[1,1,0] op_sel_hi:[0,1,1] neg_lo:[0,1,0]\n" \
    "ds_write_addtid_b32 v31 offset:20800\n" \
    "v_mul_f32_e64 v10, v5, v10\n" \
    "v_mul_f32_e64 v11, -v5, v11\n" \
    "v_pk_mul_f32 v[18:19], v[8:9], v[14:15] op_sel_hi:[0,1]\n" \
    "v_cvt_pk_bf16_f32 v30, v14, v15\n" \
    "v_pk_fma_f32 v[14:15], v[14:15], v[8:9], v[18:19] op_sel:[1,1,0] op_sel_hi:[0,1,1] neg_lo:[0,1,0]\n" \
    "ds_write_addtid_b32 v30 offset:22880\n" \
    "v_mul_f32_e64 v67, v0, v67\n" \
    "v_mul_f32_e64 v16, v68, v66\n" \
    "v_mul_f32_e64 v17, -v68, v67\n" \
    "v_fma_f32 v16, -v69, v67, v16\n" \
    "v_fma_f32 v17, -v69, v66, v17\n" \
    "v_pk_mul_f32 v[18:19], v[8:9], v[14:15] op_sel_hi:[0,1]\n" \
    "v_cvt_pk_bf16_f32 v31, v14, v15\n" \
    "v_pk_fma_f32 v[14:15], v[14:15], v[8:9], v[18:19] op_sel:[1,1,0] op_sel_hi:[0,1,1] neg_lo:[0,1,0]\n" \
    "ds_write_addtid_b32 v31 offset:24960\n" \
    "v_pk_mul_f32 v[18:19], v[8:9], v[14:15] op_sel_hi:[0,1]\n" \
    "v_cvt_pk_bf16_f32 v30, v14, v15\n" \
    "v_pk_fma_f32 v[14:15], v[14:15], v[8:9], v[18:19] op_sel:[1,1,0] op_sel_hi:[0,1,1] neg_lo:[0,1,0]\n" \
    "ds_write_addtid_b32 v30 offset:27040\n" \
    "v_pk_mul_f32 v[18:19], v[8:9], v[14:15] op_sel_hi:[0,1]\n" \
    "v_cvt_pk_bf16_f32 v31, v14, v15\n" \
    "v_pk_fma_f32 v[14:15], v[14:15], v[8:9], v[18:19] op_sel:[1,1,0] op_sel_hi:[0,1,1] neg_lo:[0,1,0]\n" \
    "ds_write_addtid_b32 v31 offset:29120\n" \
    "v_pk_mul_f32 v[18:19], v[12:13], v[16:17] op_sel_hi:[0,1]\n" \
    "v_cvt_pk_bf16_f32 v30, v14, v15\n" \
    "v_pk_fma_f32 v[20:21], v[16:17], v[12:13], v[18:19] op_sel:[1,1,0] op_sel_hi:[0,1,1] neg_lo:[0,1,0]\n" \
    "ds_write_addtid_b32 v30 offset:31200\n" \
    "v_mov_b32_e32 v61, 1\n" \
    "ds_write_addtid_b32 v61 offset:54080\n" \
    "s_add_u32 m0, %[m0a], 54080\n" \
    "v_cvt_pk_bf16_f32 v31, v16, v17\n" \
    "ds_write_addtid_b32 v31 offset:4160\n" \
    "v_pk_mul_f32 v[18:19], v[10:11], v[16:17] op_sel_hi:[0,1]\n" \
    "v_cvt_pk_bf16_f32 v30, v20, v21\n" \
    "v_pk_fma_f32 v[16:17], v[16:17], v[10:11], v[18:19] op_sel:[1,1,0] op_sel_hi:[0,1,1] neg_lo:[0,1,0]\n" \
    "ds_write_addtid_b32 v30 offset:20800\n" \
    "v_pk_mul_f32 v[18:19], v[12:13], v[16:17] op_sel_hi:[0,1]\n" \
    "v_cvt_pk_bf16_f32 v31, v16, v17\n" \
    "v_pk_fma_f32 v[14:15], v[16:17], v[12:13], v[18:19] op_sel:[1,1,0] op_sel_hi:[0,1,1] neg_lo:[0,1,0]\n" \
    "ds_write_addtid_b32 v31 offset:6240\n" \
    "v_pk_mul_f32 v[18:19], v[10:11], v[16:17] op_sel_hi:[0,1]\n" \
    "v_cvt_pk_bf16_f32 v30, v14, v15\n" \
    "v_pk_fma_f32 v[16:17], v[16:17], v[10:11], v[18:19] op_sel:[1,1,0] op_sel_hi:[0,1,1] neg_lo:[0,1,0]\n" \
    "ds_write_addtid_b32 v30 offset:22880\n" \
    "v_pk_mul_f32 v[18:19], v[12:13], v[16:17] op_sel_hi:[0,1]\n" \
    "v_cvt_pk_bf16_f32 v31, v16, v17\n" \
    "v_pk_fma_f32 v[22:23], v[16:17], v[12:13], v[18:19] op_sel:[1,1,0] op_sel_hi:[0,1,1] neg_lo:[0,1,0]\n" \
    "ds_write_addtid_b32 v31 offset:8320\n" \
    "v_pk_mul_f32 v[18:19], v[10:11], v[16:17] op_sel_hi:[0,1]\n" \
    "v_cvt_pk_bf16_f32 v30, v22, v23\n" \
    "v_pk_fma_f32 v[16:17], v[16:17], v[10:11], v[18:19] op_sel:[1,1,0] op_sel_hi:[0,1,1] neg_lo:[0,1,0]\n" \
    "ds_write_addtid_b32 v30 offset:24960\n" \
    "v_pk_mul_f32 v[18:19], v[12:13], v[16:17] op_sel_hi:[0,1]\n" \
    "v_cvt_pk_bf16_f32 v31, v16, v17\n" \
    "v_pk_fma_f32 v[14:15], v[16:17], v[12:13], v[18:19] op_sel:[1,1,0] op_sel_hi:[0,1,1] neg_lo:[0,1,0]\n" \
    "ds_write_addtid_b32 v31 offset:10400\n" \
    "v_pk_mul_f32 v[18:19], v[10:11], v[16:17] op_sel_hi:[0,1]\n" \
    "v_cvt_pk_bf16_f32 v30, v14, v15\n" \
    "v_pk_fma_f32 v[16:17], v[16:17], v[10:11], v[18:19] op_sel:[1,1,0] op_sel_hi:[0,1,1] neg_lo:[0,1,0]\n" \
    "ds_write_addtid_b32 v30 offset:27040\n" \
    "v_pk_mul_f32 v[18:19], v[12:13], v[16:17] op_sel_hi:[0,1]\n" \
    "v_cvt_pk_bf16_f32 v31, v16, v17\n" \
    "v_pk_fma_f32 v[24:25], v[16:17], v[12:13], v[18:19] op_sel:[1,1,0] op_sel_hi:[0,1,1] neg_lo:[0,1,0]\n" \
    "ds_write_addtid_b32 v31 offset:12480\n" \
    "v_pk_mul_f32 v[18:19], v[10:11], v[16:17] op_sel_hi:[0,1]\n" \
    "v_cvt_pk_bf16_f32 v30, v24, v25\n" \
    "v_pk_fma_f32 v[16:17], v[16:17], v[10:11], v[18:19] op_sel:[1,1,0] op_sel_hi:[0,1,1] neg_lo:[0,1,0]\n" \
    "ds_write_addtid_b32 v30 offset:29120\n" \
    "v_pk_mul_f32 v[18:19], v[12:13], v[16:17] op_sel_hi:[0,1]\n" \
    "v_cvt_pk_bf16_f32 v31, v16, v17\n" \
    "v_pk_fma_f32 v[14:15], v[16:17], v[12:13], v[18:19] op_sel:[1,1,0] op_sel_hi:[0,1,1] neg_lo:[0,1,0]\n" \
    "ds_write_addtid_b32 v31 offset:14560\n" \
    "v_pk_mul_f32 v[18:19], v[10:11], v[16:17] op_sel_hi:[0,1]\n" \
    "v_cvt_pk_bf16_f32 v30, v14, v15\n" \
    "v_pk_fma_f32 v[16:17], v[16:17], v[10:11], v[18:19] op_sel:[1,1,0] op_sel_hi:[0,1,1] neg_lo:[0,1,0]\n" \
    "ds_write_addtid_b32 v30 offset:31200\n" \
    "v_pk_mul_f32 v[18:19], v[12:13], v[16:17] op_sel_hi:[0,1]\n" \
    "v_cvt_pk_bf16_f32 v31, v16, v17\n" \
    "v_pk_fma_f32 v[26:27], v[16:17], v[12:13], v[18:19] op_sel:[1,1,0] op_sel_hi:[0,1,1] neg_lo:[0,1,0]\n" \
    "ds_write_addtid_b32 v31 offset:16640\n" \
    "v_pk_mul_f32 v[18:19], v[10:11], v[16:17] op_sel_hi:[0,1]\n" \
    "v_cvt_pk_bf16_f32 v30, v26, v27\n" \
    "v_pk_fma_f32 v[16:17], v[16:17], v[10:11], v[18:19] op_sel:[1,1,0] op_sel_hi:[0,1,1] neg_lo:[0,1,0]\n" \
    "ds_write_addtid_b32 v30 offset:33280\n" \
    "v_pk_mul_f32 v[18:19], v[12:13], v[16:17] op_sel_hi:[0,1]\n" \
    "v_cvt_pk_bf16_f32 v31, v16, v17\n" \
    "v_pk_fma_f32 v[14:15], v[16:17], v[12:13], v[18:19] op_sel:[1,1,0] op_sel_hi:[0,1,1] neg_lo:[0,1,0]\n" \
    "ds_write_addtid_b32 v31 offset:18720\n" \
    "s_waitcnt vmcnt(0)\n" \
    "v_pk_mul_f32 v[18:19], v[12:13], v[20:21] op_sel_hi:[0,1]\n" \
    "v_cvt_pk_bf16_f32 v30, v14, v15\n" \
    "v_pk_fma_f32 v[20:21], v[20:21], v[12:13], v[18:19] op_sel:[1,1,0] op_sel_hi:[0,1,1] neg_lo:[0,1,0]\n" \
    "ds_write_addtid_b32 v30 offset:35360\n" \
    "v_mov_b32_e32 v61, 2\n" \
    "ds_write_addtid_b32 v61 offset:0\n" \
    "s_mov_b32 m0, %[m0a]\n" \
    "v_pk_mul_f32 v[18:19], v[12:13], v[22:23] op_sel_hi:[0,1]\n" \
    "v_cvt_pk_bf16_f32 v31, v20, v21\n" \
    "v_pk_fma_f32 v[22:23], v[22:23], v[12:13], v[18:19] op_sel:[1,1,0] op_sel_hi:[0,1,1] neg_lo:[0,1,0]\n" \
    "ds_write_addtid_b32 v31 offset:33280\n" \
    "v_pk_mul_f32 v[18:19], v[12:13], v[24:25] op_sel_hi:[0,1]\n" \
    "v_cvt_pk_bf16_f32 v30, v22, v23\n" \
    "v_pk_fma_f32 v[24:25], v[24:25], v[12:13], v[18:19] op_sel:[1,1,0] op_sel_hi:[0,1,1] neg_lo:[0,1,0]\n" \
    "ds_write_addtid_b32 v30 offset:35360\n" \
    "v_pk_mul_f32 v[18:19], v[12:13], v[26:27] op_sel_hi:[0,1]\n" \
    "v_cvt_pk_bf16_f32 v31, v24, v25\n" \
    "v_pk_fma_f32 v[26:27], v[26:27], v[12:13], v[18:19] op_sel:[1,1,0] op_sel_hi:[0,1,1] neg_lo:[0,1,0]\n" \
    "ds_write_addtid_b32 v31 offset:37440\n" \
    "v_pk_mul_f32 v[18:19], v[10:11], v[14:15] op_sel_hi:[0,1]\n" \
    "v_cvt_pk_bf16_f32 v30, v26, v27\n" \
    "v_pk_fma_f32 v[28:29], v[14:15], v[10:11], v[18:19] op_sel:[1,1,0] op_sel_hi:[0,1,1] neg_lo:[0,1,0]\n" \
    "ds_write_addtid_b32 v30 offset:39520\n" \
    "v_pk_mul_f32 v[18:19], v[12:13], v[28:29] op_sel_hi:[0,1]\n" \
    "v_pk_fma_f32 v[28:29], v[28:29], v[12:13], v[18:19] op_sel:[1,1,0] op_sel_hi:[0,1,1] neg_lo:[0,1,0]\n" \
    "v_pk_mul_f32 v[18:19], v[12:13], v[20:21] op_sel_hi:[0,1]\n" \
    "v_cvt_pk_bf16_f32 v31, v28, v29\n" \
    "v_pk_fma_f32 v[20:21], v[20:21], v[12:13], v[18:19] op_sel:[1,1,0] op_sel_hi:[0,1,1] neg_lo:[0,1,0]\n" \
    "ds_write_addtid_b32 v31 offset:41600\n" \
    "v_pk_mul_f32 v[18:19], v[12:13], v[22:23] op_sel_hi:[0,1]\n" \
    "v_cvt_pk_bf16_f32 v30, v20, v21\n" \
    "v_pk_fma_f32 v[22:23], v[22:23], v[12:13], v[18:19] op_sel:[1,1,0] op_sel_hi:[0,1,1] neg_lo:[0,1,0]\n" \
    "ds_write_addtid_b32 v30 offset:43680\n" \
    "v_pk_mul_f32 v[18:19], v[12:13], v[24:25] op_sel_hi:[0,1]\n" \
    "v_cvt_pk_bf16_f32 v31, v22, v23\n" \
    "v_pk_fma_f32 v[24:25], v[24:25], v[12:13], v[18:19] op_sel:[1,1,0] op_sel_hi:[0,1,1] neg_lo:[0,1,0]\n" \
    "ds_write_addtid_b32 v31 offset:45760\n" \
    "v_pk_mul_f32 v[18:19], v[12:13], v[26:27] op_sel_hi:[0,1]\n" \
    "v_cvt_pk_bf16_f32 v30, v24, v25\n" \
    "v_pk_fma_f32 v[26:27], v[26:27], v[12:13], v[18:19] op_sel:[1,1,0] op_sel_hi:[0,1,1] neg_lo:[0,1,0]\n" \
    "ds_write_addtid_b32 v30 offset:47840\n" \
    "v_pk_mul_f32 v[18:19], v[12:13], v[28:29] op_sel_hi:[0,1]\n" \
    "v_cvt_pk_bf16_f32 v31, v26, v27\n" \
    "v_pk_fma_f32 v[28:29], v[28:29], v[12:13], v[18:19] op_sel:[1,1,0] op_sel_hi:[0,1,1] neg_lo:[0,1,0]\n" \
    "ds_write_addtid_b32 v31 offset:49920\n" \
    "v_pk_mul_f32 v[18:19], v[12:13], v[24:25] op_sel_hi:[0,1]\n" \
    "v_cvt_pk_bf16_f32 v30, v28, v29\n" \
    "v_pk_fma_f32 v[24:25], v[24:25], v[12:13], v[18:19] op_sel:[1,1,0] op_sel_hi:[0,1,1] neg_lo:[0,1,0]\n" \
    "ds_write_addtid_b32 v30 offset:52000\n" \
    "v_mov_b32_e32 v61, 3\n" \
    "ds_write_addtid_b32 v61 offset:54080\n" \
    "s_add_u32 m0, %[m0a], 62400\n" \
    "v_pk_mul_f32 v[18:19], v[12:13], v[28:29] op_sel_hi:[0,1]\n" \
    "v_cvt_pk_bf16_f32 v31, v24, v25\n" \
    "v_pk_fma_f32 v[28:29], v[28:29], v[12:13], v[18:19] op_sel:[1,1,0] op_sel_hi:[0,1,1] neg_lo:[0,1,0]\n" \
    "ds_write_addtid_b32 v31 offset:29120\n" \
    "v_pk_mul_f32 v[18:19], v[12:13], v[24:25] op_sel_hi:[0,1]\n" \
    "v_cvt_pk_bf16_f32 v30, v28, v29\n" \
    "v_pk_fma_f32 v[24:25], v[24:25], v[12:13], v[18:19] op_sel:[1,1,0] op_sel_hi:[0,1,1] neg_lo:[0,1,0]\n" \
    "ds_write_addtid_b32 v30 offset:37440\n" \
    "v_pk_mul_f32 v[18:19], v[12:13], v[28:29] op_sel_hi:[0,1]\n" \
    "v_cvt_pk_bf16_f32 v31, v24, v25\n" \
    "v_pk_fma_f32 v[28:29], v[28:29], v[12:13], v[18:19] op_sel:[1,1,0] op_sel_hi:[0,1,1] neg_lo:[0,1,0]\n" \
    "ds_write_addtid_b32 v31 offset:31200\n" \
    "v_pk_mul_f32 v[18:19], v[12:13], v[24:25] op_sel_hi:[0,1]\n" \
    "v_cvt_pk_bf16_f32 v30, v28, v29\n" \
    "v_pk_fma_f32 v[24:25], v[24:25], v[12:13], v[18:19] op_sel:[1,1,0] op_sel_hi:[0,1,1] neg_lo:[0,1,0]\n" \
    "ds_write_addtid_b32 v30 offset:39520\n" \
    "v_pk_mul_f32 v[18:19], v[12:13], v[28:29] op_sel_hi:[0,1]\n" \
    "v_cvt_pk_bf16_f32 v31, v24, v25\n" \
    "v_pk_fma_f32 v[28:29], v[28:29], v[12:13], v[18:19] op_sel:[1,1,0] op_sel_hi:[0,1,1] neg_lo:[0,1,0]\n" \
    "ds_write_addtid_b32 v31 offset:33280\n" \
    "v_pk_mul_f32 v[18:19], v[12:13], v[24:25] op_sel_hi:[0,1]\n" \
    "v_cvt_pk_bf16_f32 v30, v28, v29\n" \
    "v_pk_fma_f32 v[24:25], v[24:25], v[12:13], v[18:19] op_sel:[1,1,0] op_sel_hi:[0,1,1] neg_lo:[0,1,0]\n" \
    "ds_write_addtid_b32 v30 offset:41600\n" \
    "v_pk_mul_f32 v[18:19], v[12:13], v[28:29] op_sel_hi:[0,1]\n" \
    "v_cvt_pk_bf16_f32 v31, v24, v25\n" \
    "v_pk_fma_f32 v[28:29], v[28:29], v[12:13], v[18:19] op_sel:[1,1,0] op_sel_hi:[0,1,1] neg_lo:[0,1,0]\n" \
    "ds_write_addtid_b32 v31 offset:35360\n" \
    "v_pk_mul_f32 v[18:19], v[12:13], v[28:29] op_sel_hi:[0,1]\n" \
    "v_cvt_pk_bf16_f32 v30, v28, v29\n" \
    "v_pk_fma_f32 v[28:29], v[28:29], v[12:13], v[18:19] op_sel:[1,1,0] op_sel_hi:[0,1,1] neg_lo:[0,1,0]\n" \
    "ds_write_addtid_b32 v30 offset:43680\n" \
    "v_pk_mul_f32 v[18:19], v[12:13], v[28:29] op_sel_hi:[0,1]\n" \
    "v_cvt_pk_bf16_f32 v31, v28, v29\n" \
    "v_pk_fma_f32 v[28:29], v[28:29], v[12:13], v[18:19] op_sel:[1,1,0] op_sel_hi:[0,1,1] neg_lo:[0,1,0]\n" \
    "ds_write_addtid_b32 v31 offset:45760\n" \
    "v_pk_mul_f32 v[18:19], v[12:13], v[28:29] op_sel_hi:[0,1]\n" \
    "v_cvt_pk_bf16_f32 v30, v28, v29\n" \
    "v_pk_fma_f32 v[28:29], v[28:29], v[12:13], v[18:19] op_sel:[1,1,0] op_sel_hi:[0,1,1] neg_lo:[0,1,0]\n" \
    "ds_write_addtid_b32 v30 offset:47840\n" \
    "ds_read_b32 v60, v32\n" \
    "v_pk_mul_f32 v[18:19], v[12:13], v[28:29] op_sel_hi:[0,1]\n" \
    "v_cvt_pk_bf16_f32 v31, v28, v29\n" \
    "v_pk_fma_f32 v[28:29], v[28:29], v[12:13], v[18:19] op_sel:[1,1,0] op_sel_hi:[0,1,1] neg_lo:[0,1,0]\n" \
    "ds_write_addtid_b32 v31 offset:49920\n" \
    "v_pk_mul_f32 v[18:19], v[12:13], v[28:29] op_sel_hi:[0,1]\n" \
    "v_cvt_pk_bf16_f32 v30, v28, v29\n" \
    "v_pk_fma_f32 v[28:29], v[28:29], v[12:13], v[18:19] op_sel:[1,1,0] op_sel_hi:[0,1,1] neg_lo:[0,1,0]\n" \
    "ds_write_addtid_b32 v30 offset:52000\n" \
    "v_pk_mul_f32 v[18:19], v[12:13], v[28:29] op_sel_hi:[0,1]\n" \
    "v_cvt_pk_bf16_f32 v31, v28, v29\n" \
    "v_pk_fma_f32 v[28:29], v[28:29], v[12:13], v[18:19] op_sel:[1,1,0] op_sel_hi:[0,1,1] neg_lo:[0,1,0]\n" \
    "ds_write_addtid_b32 v31 offset:54080\n" \
    "s_waitcnt lgkmcnt(3)\n" \
    "v_cmp_gt_u32_e32 vcc, 1, v60\n" \
    "s_cbranch_vccz Lae0_%=\n" \
    "s_mov_b32 s90, 0x100000\n" \
    "Lal0_%=:\n" \
    "s_sleep 1\n" \
    "ds_read_b32 v60, v32 offset:0\n" \
    "s_waitcnt lgkmcnt(0)\n" \
    "v_cmp_gt_u32_e32 vcc, 1, v60\n" \
    "s_cbranch_vccz Lae0_%=\n" \
    "s_sub_u32 s90, s90, 1\n" \
    "s_cbranch_scc0 Lal0_%=\n" \
    "Lae0_%=:\n" \
    "ds_read2_b32 v[64:65], v36 offset0:240 offset1:241\n" \
    "ds_read2_b32 v[66:67], v36 offset0:242 offset1:243\n" \
    "ds_read2_b32 v[68:69], v36 offset0:224 offset1:225\n" \
    "ds_read2_b32 v[70:71], v36 offset0:226 offset1:227\n" \
    "v_pk_mul_f32 v[18:19], v[12:13], v[28:29] op_sel_hi:[0,1]\n" \
    "v_cvt_pk_bf16_f32 v30, v28, v29\n" \
    "v_pk_fma_f32 v[28:29], v[28:29], v[12:13], v[18:19] op_sel:[1,1,0] op_sel_hi:[0,1,1] neg_lo:[0,1,0]\n" \
    "ds_write_addtid_b32 v30 offset:56160\n" \
    "ds_read2_b32 v[72:73], v36 offset0:208 offset1:209\n" \
    "ds_read2_b32 v[74:75], v36 offset0:210 offset1:211\n" \
    "ds_read2_b32 v[76:77], v36 offset0:192 offset1:193\n" \
    "ds_read2_b32 v[78:79], v36 offset0:194 offset1:195\n" \
    "v_pk_mul_f32 v[18:19], v[12:13], v[28:29] op_sel_hi:[0,1]\n" \
    "v_cvt_pk_bf16_f32 v31, v28, v29\n" \
    "v_pk_fma_f32 v[28:29], v[28:29], v[12:13], v[18:19] op_sel:[1,1,0] op_sel_hi:[0,1,1] neg_lo:[0,1,0]\n" \
    "ds_write_addtid_b32 v31 offset:58240\n" \
    "ds_read2_b32 v[80:81], v36 offset0:176 offset1:177\n" \
    "ds_read2_b32 v[82:83], v36 offset0:178 offset1:179\n" \
    "ds_read2_b32 v[84:85], v36 offset0:160 offset1:161\n" \
    "ds_read2_b32 v[86:87], v36 offset0:162 offset1:163\n" \
    "v_cvt_pk_bf16_f32 v30, v28, v29\n" \
    "ds_write_addtid_b32 v30 offset:60320\n" \
    "s_mov_b32 m0, %[m0a]\n" \
    "v_mov_b32_e32 v61, 4\n" \
    "ds_write_addtid_b32 v61 offset:54080\n" \
    "ds_read2_b32 v[88:89], v36 offset0:144 offset1:145\n" \
    "ds_read2_b32 v[90:91], v36 offset0:146 offset1:147\n" \
    "ds_read2_b32 v[92:93], v36 offset0:128 offset1:129\n" \
    "ds_read2_b32 v[94:95], v36 offset0:130 offset1:131\n" \
    "s_setprio 2\n" \
    "s_mov_b32 s90, 0x100000\n" \
    "Lal1_%=:\n" \
    "ds_read_b32 v62, v34 offset:0\n" \
    "s_waitcnt lgkmcnt(0)\n" \
    "v_cmp_gt_u32_e32 vcc, 1, v62\n" \
    "s_cbranch_vccz Lae1_%=\n" \
    "s_sleep 1\n" \
    "s_sub_u32 s90, s90, 1\n" \
    "s_cbranch_scc0 Lal1_%=\n" \
    "Lae1_%=:\n" \
    "ds_read_b128 v[156:159], v33 offset:448\n" \
    "ds_read_b128 v[152:155], v33 offset:384\n" \
    "ds_read_b128 v[148:151], v33 offset:320\n" \
    "ds_read_b128 v[144:147], v33 offset:256\n" \
    "ds_read_b128 v[140:143], v33 offset:192\n" \
    "ds_read_b128 v[136:139], v33 offset:128\n" \
    "ds_read_b128 v[132:135], v33 offset:64\n" \
    "ds_read_b128 v[128:131], v33 offset:0\n" \
    "ds_read_b32 v60, v32\n" \
    "s_waitcnt lgkmcnt(8)\n" \
    "v_mfma_f32_16x16x32_bf16 v[0:3], v[64:67], v[156:159], 0\n" \
    "s_waitcnt lgkmcnt(7)\n" \
    "v_mfma_f32_16x16x32_bf16 v[0:3], v[68:71], v[152:155], v[0:3]\n" \
    "s_waitcnt lgkmcnt(6)\n" \
    "v_mfma_f32_16x16x32_bf16 v[0:3], v[72:75], v[148:151], v[0:3]\n" \
    "s_waitcnt lgkmcnt(0)\n" \
    "v_cmp_gt_u32_e32 vcc, 2, v60\n" \
    "s_cbranch_vccz Lae2_%=\n" \
    "s_mov_b32 s90, 0x100000\n" \
    "Lal2_%=:\n" \
    "s_sleep 1\n" \
    "ds_read_b32 v60, v32 offset:0\n" \
    "s_waitcnt lgkmcnt(0)\n" \
    "v_cmp_gt_u32_e32 vcc, 2, v60\n" \
    "s_cbranch_vccz Lae2_%=\n" \
    "s_sub_u32 s90, s90, 1\n" \
    "s_cbranch_scc0 Lal2_%=\n" \
    "Lae2_%=:\n" \
    "v_mfma_f32_16x16x32_bf16 v[0:3], v[76:79], v[144:147], v[0:3]\n" \
    "ds_read2_b32 v[96:97], v36 offset0:112 offset1:113\n" \
    "ds_read2_b32 v[98:99], v36 offset0:114 offset1:115\n" \
    "ds_read2_b32 v[100:101], v36 offset0:96 offset1:97\n" \
    "ds_read2_b32 v[102:103], v36 offset0:98 offset1:99\n" \
    "v_mfma_f32_16x16x32_bf16 v[0:3], v[80:83], v[140:143], v[0:3]\n" \
    "ds_read2_b32 v[104:105], v36 offset0:80 offset1:81\n" \
    "ds_read2_b32 v[106:107], v36 offset0:82 offset1:83\n" \
    "ds_read2_b32 v[108:109], v36 offset0:64 offset1:65\n" \
    "ds_read2_b32 v[110:111], v36 offset0:66 offset1:67\n" \
    "v_mfma_f32_16x16x32_bf16 v[0:3], v[84:87], v[136:139], v[0:3]\n" \
    "ds_read2_b32 v[112:113], v36 offset0:48 offset1:49\n" \
    "ds_read2_b32 v[114:115], v36 offset0:50 offset1:51\n" \
    "ds_read2_b32 v[116:117], v36 offset0:32 offset1:33\n" \
    "ds_read2_b32 v[118:119], v36 offset0:34 offset1:35\n" \
    "v_mfma_f32_16x16x32_bf16 v[0:3], v[88:91], v[132:135], v[0:3]\n" \
    "ds_read2_b32 v[120:121], v36 offset0:16 offset1:17\n" \
    "ds_read2_b32 v[122:123], v36 offset0:18 offset1:19\n" \
    "ds_read2_b32 v[124:125], v36 offset0:0 offset1:1\n" \
    "ds_read2_b32 v[126:127], v36 offset0:2 offset1:3\n" \
    "v_mfma_f32_16x16x32_bf16 v[0:3], v[92:95], v[128:131], v[0:3]\n" \
    "s_waitcnt lgkmcnt(14)\n" \
    "v_mfma_f32_16x16x32_bf16 v[4:7], v[96:99], v[156:159], 0\n" \
    "ds_read_b128 v[156:159], v33 offset:960\n" \
    "s_waitcnt lgkmcnt(13)\n" \
    "v_mfma_f32_16x16x32_bf16 v[4:7], v[100:103], v[152:155], v[4:7]\n" \
    "ds_read_b128 v[152:155], v33 offset:896\n" \
    "ds_read_b32 v62, v34\n" \
    "s_waitcnt lgkmcnt(13)\n" \
    "v_mfma_f32_16x16x32_bf16 v[4:7], v[104:107], v[148:151], v[4:7]\n" \
    "ds_read_b128 v[148:151], v33 offset:832\n" \
    "s_waitcnt lgkmcnt(12)\n" \
    "v_mfma_f32_16x16x32_bf16 v[4:7], v[108:111], v[144:147], v[4:7]\n" \
    "ds_read_b128 v[144:147], v33 offset:768\n" \
    "s_waitcnt lgkmcnt(11)\n" \
    "v_mfma_f32_16x16x32_bf16 v[4:7], v[112:115], v[140:143], v[4:7]\n" \
    "ds_read_b128 v[140:143], v33 offset:704\n" \
    "s_waitcnt lgkmcnt(10)\n" \
    "v_mfma_f32_16x16x32_bf16 v[4:7], v[116:119], v[136:139], v[4:7]\n" \
    "ds_read_b128 v[136:139], v33 offset:640\n" \
    "s_waitcnt lgkmcnt(9)\n" \
    "v_mfma_f32_16x16x32_bf16 v[4:7], v[120:123], v[132:135], v[4:7]\n" \
    "ds_read_b128 v[132:135], v33 offset:576\n" \
    "s_waitcnt lgkmcnt(8)\n" \
    "v_mfma_f32_16x16x32_bf16 v[4:7], v[124:127], v[128:131], v[4:7]\n" \
    "ds_read_b128 v[128:131], v33 offset:512\n" \
    "s_waitcnt lgkmcnt(6)\n" \
    "v_cmp_gt_u32_e32 vcc, 3, v62\n" \
    "s_cbranch_vccnz Las3_%=\n" \
    "Lae3_%=:\n" \
    "s_waitcnt lgkmcnt(4)\n" \
    "v_mfma_f32_16x16x32_bf16 v[4:7], v[64:67], v[156:159], v[4:7]\n" \
    "v_mfma_f32_16x16x32_bf16 v[8:11], v[96:99], v[156:159], 0\n" \
    "ds_read_b128 v[156:159], v33 offset:1472\n" \
    "v_mfma_f32_16x16x32_bf16 v[4:7], v[68:71], v[152:155], v[4:7]\n" \
    "v_mfma_f32_16x16x32_bf16 v[8:11], v[100:103], v[152:155], v[8:11]\n" \
    "ds_read_b128 v[152:155], v33 offset:1408\n" \
    "v_mov_b32_e64 v61, 11\n" \
    "v_mfma_f32_16x16x32_bf16 v[4:7], v[72:75], v[148:151], v[4:7]\n" \
    "ds_write_b128 v37, v[0:3] offset:0\n" \
    "ds_write_addtid_b32 v61 offset:54080\n" \
    "v_mfma_f32_16x16x32_bf16 v[8:11], v[104:107], v[148:151], v[8:11]\n" \
    "ds_read_b128 v[148:151], v33 offset:1344\n" \
    "v_mfma_f32_16x16x32_bf16 v[4:7], v[76:79], v[144:147], v[4:7]\n" \
    "v_mfma_f32_16x16x32_bf16 v[8:11], v[108:111], v[144:147], v[8:11]\n" \
    "ds_read_b128 v[144:147], v33 offset:1280\n" \
    "s_waitcnt lgkmcnt(6)\n" \
    "v_mfma_f32_16x16x32_bf16 v[4:7], v[80:83], v[140:143], v[4:7]\n" \
    "v_mfma_f32_16x16x32_bf16 v[8:11], v[112:115], v[140:143], v[8:11]\n" \
    "ds_read_b128 v[140:143], v33 offset:1216\n" \
    "v_mfma_f32_16x16x32_bf16 v[4:7], v[84:87], v[136:139], v[4:7]\n" \
    "v_mfma_f32_16x16x32_bf16 v[8:11], v[116:119], v[136:139], v[8:11]\n" \
    "ds_read_b128 v[136:139], v33 offset:1152\n" \
    "v_mfma_f32_16x16x32_bf16 v[4:7], v[88:91], v[132:135], v[4:7]\n" \
    "v_mfma_f32_16x16x32_bf16 v[8:11], v[120:123], v[132:135], v[8:11]\n" \
    "ds_read_b128 v[132:135], v33 offset:1088\n" \
    "v_mfma_f32_16x16x32_bf16 v[4:7], v[92:95], v[128:131], v[4:7]\n" \
    "v_mfma_f32_16x16x32_bf16 v[8:11], v[124:127], v[128:131], v[8:11]\n" \
    "ds_read_b128 v[128:131], v33 offset:1024\n" \
    "s_waitcnt lgkmcnt(4)\n" \
    "v_mfma_f32_16x16x32_bf16 v[8:11], v[64:67], v[156:159], v[8:11]\n" \
    "v_mfma_f32_16x16x32_bf16 v[12:15], v[96:99], v[156:159], 0\n" \
    "ds_read_b128 v[156:159], v33 offset:1984\n" \
    "v_mfma_f32_16x16x32_bf16 v[8:11], v[68:71], v[152:155], v[8:11]\n" \
    "v_mfma_f32_16x16x32_bf16 v[12:15], v[100:103], v[152:155], v[12:15]\n" \
    "ds_read_b128 v[152:155], v33 offset:1920\n" \
    "v_mov_b32_e64 v61, 12\n" \
    "v_mfma_f32_16x16x32_bf16 v[8:11], v[72:75], v[148:151], v[8:11]\n" \
    "ds_write_b128 v37, v[4:7] offset:4160\n" \
    "ds_write_addtid_b32 v61 offset:54080\n" \
    "v_mfma_f32_16x16x32_bf16 v[12:15], v[104:107], v[148:151], v[12:15]\n" \
    "ds_read_b128 v[148:151], v33 offset:1856\n" \
    "v_mfma_f32_16x16x32_bf16 v[8:11], v[76:79], v[144:147], v[8:11]\n" \
    "v_mfma_f32_16x16x32_bf16 v[12:15], v[108:111], v[144:147], v[12:15]\n" \
    "ds_read_b128 v[144:147], v33 offset:1792\n" \
    "s_waitcnt lgkmcnt(6)\n" \
    "v_mfma_f32_16x16x32_bf16 v[8:11], v[80:83], v[140:143], v[8:11]\n" \
    "v_mfma_f32_16x16x32_bf16 v[12:15], v[112:115], v[140:143], v[12:15]\n" \
    "ds_read_b128 v[140:143], v33 offset:1728\n" \
    "v_mfma_f32_16x16x32_bf16 v[8:11], v[84:87], v[136:139], v[8:11]\n" \
    "v_mfma_f32_16x16x32_bf16 v[12:15], v[116:119], v[136:139], v[12:15]\n" \
    "ds_read_b128 v[136:139], v33 offset:1664\n" \
    "v_mfma_f32_16x16x32_bf16 v[8:11], v[88:91], v[132:135], v[8:11]\n" \
    "ds_read_b32 v62, v34\n" \
    "v_mfma_f32_16x16x32_bf16 v[12:15], v[120:123], v[132:135], v[12:15]\n" \
    "ds_read_b128 v[132:135], v33 offset:1600\n" \
    "v_mfma_f32_16x16x32_bf16 v[8:11], v[92:95], v[128:131], v[8:11]\n" \
    "v_mfma_f32_16x16x32_bf16 v[12:15], v[124:127], v[128:131], v[12:15]\n" \
    "ds_read_b128 v[128:131], v33 offset:1536\n" \
    "s_waitcnt lgkmcnt(2)\n" \
    "v_cmp_gt_u32_e32 vcc, 5, v62\n" \
    "s_cbranch_vccnz Las4_%=\n" \
    "Lae4_%=:\n" \
    "v_mfma_f32_16x16x32_bf16 v[12:15], v[64:67], v[156:159], v[12:15]\n" \
    "v_mfma_f32_16x16x32_bf16 v[16:19], v[96:99], v[156:159], 0\n" \
    "ds_read_b128 v[156:159], v33 offset:2496\n" \
    "v_mfma_f32_16x16x32_bf16 v[12:15], v[68:71], v[152:155], v[12:15]\n" \
    "v_mfma_f32_16x16x32_bf16 v[16:19], v[100:103], v[152:155], v[16:19]\n" \
    "ds_read_b128 v[152:155], v33 offset:2432\n" \
    "v_mov_b32_e64 v61, 13\n" \
    "v_mfma_f32_16x16x32_bf16 v[12:15], v[72:75], v[148:151], v[12:15]\n" \
    "ds_write_b128 v37, v[8:11] offset:8320\n" \
    "ds_write_addtid_b32 v61 offset:54080\n" \
    "v_mfma_f32_16x16x32_bf16 v[16:19], v[104:107], v[148:151], v[16:19]\n" \
    "ds_read_b128 v[148:151], v33 offset:2368\n" \
    "v_mfma_f32_16x16x32_bf16 v[12:15], v[76:79], v[144:147], v[12:15]\n" \
    "v_mfma_f32_16x16x32_bf16 v[16:19], v[108:111], v[144:147], v[16:19]\n" \
    "ds_read_b128 v[144:147], v33 offset:2304\n" \
    "s_waitcnt lgkmcnt(6)\n" \
    "v_mfma_f32_16x16x32_bf16 v[12:15], v[80:83], v[140:143], v[12:15]\n" \
    "v_mfma_f32_16x16x32_bf16 v[16:19], v[112:115], v[140:143], v[16:19]\n" \
    "ds_read_b128 v[140:143], v33 offset:2240\n" \
    "v_mfma_f32_16x16x32_bf16 v[12:15], v[84:87], v[136:139], v[12:15]\n" \
    "v_mfma_f32_16x16x32_bf16 v[16:19], v[116:119], v[136:139], v[16:19]\n" \
    "ds_read_b128 v[136:139], v33 offset:2176\n" \
    "v_mfma_f32_16x16x32_bf16 v[12:15], v[88:91], v[132:135], v[12:15]\n" \
    "v_mfma_f32_16x16x32_bf16 v[16:19], v[120:123], v[132:135], v[16:19]\n" \
    "ds_read_b128 v[132:135], v33 offset:2112\n" \
    "v_mfma_f32_16x16x32_bf16 v[12:15], v[92:95], v[128:131], v[12:15]\n" \
    "v_mfma_f32_16x16x32_bf16 v[16:19], v[124:127], v[128:131], v[16:19]\n" \
    "ds_read_b128 v[128:131], v33 offset:2048\n" \
    "s_waitcnt lgkmcnt(4)\n" \
    "v_mfma_f32_16x16x32_bf16 v[16:19], v[64:67], v[156:159], v[16:19]\n" \
    "v_mfma_f32_16x16x32_bf16 v[20:23], v[96:99], v[156:159], 0\n" \
    "ds_read_b128 v[156:159], v33 offset:3008\n" \
    "v_mfma_f32_16x16x32_bf16 v[16:19], v[68:71], v[152:155], v[16:19]\n" \
    "v_mfma_f32_16x16x32_bf16 v[20:23], v[100:103], v[152:155], v[20:23]\n" \
    "ds_read_b128 v[152:155], v33 offset:2944\n" \
    "v_mov_b32_e64 v61, 14\n" \
    "v_mfma_f32_16x16x32_bf16 v[16:19], v[72:75], v[148:151], v[16:19]\n" \
    "ds_write_b128 v37, v[12:15] offset:12480\n" \
    "ds_write_addtid_b32 v61 offset:54080\n" \
    "v_mfma_f32_16x16x32_bf16 v[20:23], v[104:107], v[148:151], v[20:23]\n" \
    "ds_read_b128 v[148:151], v33 offset:2880\n" \
    "v_mfma_f32_16x16x32_bf16 v[16:19], v[76:79], v[144:147], v[16:19]\n" \
    "v_mfma_f32_16x16x32_bf16 v[20:23], v[108:111], v[144:147], v[20:23]\n" \
    "ds_read_b128 v[144:147], v33 offset:2816\n" \
    "s_waitcnt lgkmcnt(6)\n" \
    "v_mfma_f32_16x16x32_bf16 v[16:19], v[80:83], v[140:143], v[16:19]\n" \
    "v_mfma_f32_16x16x32_bf16 v[20:23], v[112:115], v[140:143], v[20:23]\n" \
    "ds_read_b128 v[140:143], v33 offset:2752\n" \
    "v_mfma_f32_16x16x32_bf16 v[16:19], v[84:87], v[136:139], v[16:19]\n" \
    "v_mfma_f32_16x16x32_bf16 v[20:23], v[116:119], v[136:139], v[20:23]\n" \
    "ds_read_b128 v[136:139], v33 offset:2688\n" \
    "v_mfma_f32_16x16x32_bf16 v[16:19], v[88:91], v[132:135], v[16:19]\n" \
    "ds_read_b32 v62, v34\n" \
    "v_mfma_f32_16x16x32_bf16 v[20:23], v[120:123], v[132:135], v[20:23]\n" \
    "ds_read_b128 v[132:135], v33 offset:2624\n" \
    "v_mfma_f32_16x16x32_bf16 v[16:19], v[92:95], v[128:131], v[16:19]\n" \
    "v_mfma_f32_16x16x32_bf16 v[20:23], v[124:127], v[128:131], v[20:23]\n" \
    "ds_read_b128 v[128:131], v33 offset:2560\n" \
    "s_waitcnt lgkmcnt(2)\n" \
    "v_cmp_gt_u32_e32 vcc, 7, v62\n" \
    "s_cbranch_vccnz Las5_%=\n" \
    "Lae5_%=:\n" \
    "v_mfma_f32_16x16x32_bf16 v[20:23], v[64:67], v[156:159], v[20:23]\n" \
    "v_mfma_f32_16x16x32_bf16 v[24:27], v[96:99], v[156:159], 0\n" \
    "ds_read_b128 v[156:159], v33 offset:3520\n" \
    "v_mfma_f32_16x16x32_bf16 v[20:23], v[68:71], v[152:155], v[20:23]\n" \
    "v_mfma_f32_16x16x32_bf16 v[24:27], v[100:103], v[152:155], v[24:27]\n" \
    "ds_read_b128 v[152:155], v33 offset:3456\n" \
    "v_mov_b32_e64 v61, 15\n" \
    "v_mfma_f32_16x16x32_bf16 v[20:23], v[72:75], v[148:151], v[20:23]\n" \
    "ds_write_b128 v37, v[16:19] offset:16640\n" \
    "ds_write_addtid_b32 v61 offset:54080\n" \
    "v_mfma_f32_16x16x32_bf16 v[24:27], v[104:107], v[148:151], v[24:27]\n" \
    "ds_read_b128 v[148:151], v33 offset:3392\n" \
    "v_mfma_f32_16x16x32_bf16 v[20:23], v[76:79], v[144:147], v[20:23]\n" \
    "v_mfma_f32_16x16x32_bf16 v[24:27], v[108:111], v[144:147], v[24:27]\n" \
    "ds_read_b128 v[144:147], v33 offset:3328\n" \
    "s_waitcnt lgkmcnt(6)\n" \
    "v_mfma_f32_16x16x32_bf16 v[20:23], v[80:83], v[140:143], v[20:23]\n" \
    "v_mfma_f32_16x16x32_bf16 v[24:27], v[112:115], v[140:143], v[24:27]\n" \
    "ds_read_b128 v[140:143], v33 offset:3264\n" \
    "v_mfma_f32_16x16x32_bf16 v[20:23], v[84:87], v[136:139], v[20:23]\n" \
    "v_mfma_f32_16x16x32_bf16 v[24:27], v[116:119], v[136:139], v[24:27]\n" \
    "ds_read_b128 v[136:139], v33 offset:3200\n" \
    "v_mfma_f32_16x16x32_bf16 v[20:23], v[88:91], v[132:135], v[20:23]\n" \
    "v_mfma_f32_16x16x32_bf16 v[24:27], v[120:123], v[132:135], v[24:27]\n" \
    "ds_read_b128 v[132:135], v33 offset:3136\n" \
    "v_mfma_f32_16x16x32_bf16 v[20:23], v[92:95], v[128:131], v[20:23]\n" \
    "v_mfma_f32_16x16x32_bf16 v[24:27], v[124:127], v[128:131], v[24:27]\n" \
    "ds_read_b128 v[128:131], v33 offset:3072\n" \
    "s_waitcnt lgkmcnt(4)\n" \
    "v_mfma_f32_16x16x32_bf16 v[24:27], v[64:67], v[156:159], v[24:27]\n" \
    "v_mfma_f32_16x16x32_bf16 v[28:31], v[96:99], v[156:159], 0\n" \
    "ds_read_b128 v[156:159], v33 offset:4032\n" \
    "v_mfma_f32_16x16x32_bf16 v[24:27], v[68:71], v[152:155], v[24:27]\n" \
    "v_mfma_f32_16x16x32_bf16 v[28:31], v[100:103], v[152:155], v[28:31]\n" \
    "ds_read_b128 v[152:155], v33 offset:3968\n" \
    "v_mov_b32_e64 v61, 16\n" \
    "v_mfma_f32_16x16x32_bf16 v[24:27], v[72:75], v[148:151], v[24:27]\n" \
    "ds_write_b128 v37, v[20:23] offset:20800\n" \
    "ds_write_addtid_b32 v61 offset:54080\n" \
    "v_mfma_f32_16x16x32_bf16 v[28:31], v[104:107], v[148:151], v[28:31]\n" \
    "ds_read_b128 v[148:151], v33 offset:3904\n" \
    "v_mfma_f32_16x16x32_bf16 v[24:27], v[76:79], v[144:147], v[24:27]\n" \
    "v_mfma_f32_16x16x32_bf16 v[28:31], v[108:111], v[144:147], v[28:31]\n" \
    "ds_read_b128 v[144:147], v33 offset:3840\n" \
    "s_waitcnt lgkmcnt(6)\n" \
    "v_mfma_f32_16x16x32_bf16 v[24:27], v[80:83], v[140:143], v[24:27]\n" \
    "v_mfma_f32_16x16x32_bf16 v[28:31], v[112:115], v[140:143], v[28:31]\n" \
    "ds_read_b128 v[140:143], v33 offset:3776\n" \
    "v_mfma_f32_16x16x32_bf16 v[24:27], v[84:87], v[136:139], v[24:27]\n" \
    "v_mfma_f32_16x16x32_bf16 v[28:31], v[116:119], v[136:139], v[28:31]\n" \
    "ds_read_b128 v[136:139], v33 offset:3712\n" \
    "v_mfma_f32_16x16x32_bf16 v[24:27], v[88:91], v[132:135], v[24:27]\n" \
    "v_mfma_f32_16x16x32_bf16 v[28:31], v[120:123], v[132:135], v[28:31]\n" \
    "ds_read_b128 v[132:135], v33 offset:3648\n" \
    "v_mfma_f32_16x16x32_bf16 v[24:27], v[92:95], v[128:131], v[24:27]\n" \
    "v_mfma_f32_16x16x32_bf16 v[28:31], v[124:127], v[128:131], v[28:31]\n" \
    "ds_read_b128 v[128:131], v33 offset:3584\n" \
    "s_waitcnt lgkmcnt(4)\n" \
    "v_mfma_f32_16x16x32_bf16 v[28:31], v[64:67], v[156:159], v[28:31]\n" \
    "v_mfma_f32_16x16x32_bf16 v[28:31], v[68:71], v[152:155], v[28:31]\n" \
    "v_mfma_f32_16x16x32_bf16 v[28:31], v[72:75], v[148:151], v[28:31]\n" \
    "v_mov_b32_e64 v61, 17\n" \
    "v_mfma_f32_16x16x32_bf16 v[28:31], v[76:79], v[144:147], v[28:31]\n" \
    "ds_write_b128 v37, v[24:27] offset:24960\n" \
    "ds_write_addtid_b32 v61 offset:54080\n" \
    "s_waitcnt lgkmcnt(2)\n" \
    "v_mfma_f32_16x16x32_bf16 v[28:31], v[80:83], v[140:143], v[28:31]\n" \
    "v_mfma_f32_16x16x32_bf16 v[28:31], v[84:87], v[136:139], v[28:31]\n" \
    "v_mfma_f32_16x16x32_bf16 v[28:31], v[88:91], v[132:135], v[28:31]\n" \
    "v_mfma_f32_16x16x32_bf16 v[28:31], v[92:95], v[128:131], v[28:31]\n" \
    "v_mov_b32_e64 v61, 18\n" \
    "s_nop 6\n" \
    "ds_write_b128 v37, v[28:31] offset:29120\n" \
    "ds_write_addtid_b32 v61 offset:54080\n" \
    "s_branch Lax_%=\n" \
    "Las3_%=:\n" \
    "s_mov_b32 s90, 0x100000\n" \
    "Lal3_%=:\n" \
    "ds_read_b32 v62, v34 offset:0\n" \
    "s_waitcnt lgkmcnt(0)\n" \
    "v_cmp_gt_u32_e32 vcc, 3, v62\n" \
    "s_cbranch_vccz Lae3_%=\n" \
    "s_sub_u32 s90, s90, 1\n" \
    "s_cbranch_scc0 Lal3_%=\n" \
    "s_branch Lae3_%=\n" \
    "Las4_%=:\n" \
    "s_mov_b32 s90, 0x100000\n" \
    "Lal4_%=:\n" \
    "ds_read_b32 v62, v34 offset:0\n" \
    "s_waitcnt lgkmcnt(0)\n" \
    "v_cmp_gt_u32_e32 vcc, 5, v62\n" \
    "s_cbranch_vccz Lae4_%=\n" \
    "s_sub_u32 s90, s90, 1\n" \
    "s_cbranch_scc0 Lal4_%=\n" \
    "s_branch Lae4_%=\n" \
    "Las5_%=:\n" \
    "s_mov_b32 s90, 0x100000\n" \
    "Lal5_%=:\n" \
    "ds_read_b32 v62, v34 offset:0\n" \
    "s_waitcnt lgkmcnt(0)\n" \
    "v_cmp_gt_u32_e32 vcc, 7, v62\n" \
    "s_cbranch_vccz Lae5_%=\n" \
    "s_sub_u32 s90, s90, 1\n" \
    "s_cbranch_scc0 Lal5_%=\n" \
    "s_branch Lae5_%=\n" \
    "Lax_%=:\n"
#define CONV_A_CLOB "v0", "v1", "v2", "v3", "v4", "v5", "v6", "v7", "v8", "v9", "v10", "v11", "v12", "v13", "v14", "v15", "v16", "v17", "v18", "v19", "v20", "v21", "v22", "v23", "v24", "v25", "v26", "v27", "v28", "v29", "v30", "v31", "v32", "v33", "v34", "v36", "v37", "v60", "v61", "v62", "v64", "v65", "v66", "v67", "v68", "v69", "v70", "v71", "v72", "v73", "v74", "v75", "v76", "v77", "v78", "v79", "v80", "v81", "v82", "v83", "v84", "v85", "v86", "v87", "v88", "v89", "v90", "v91", "v92", "v93", "v94", "v95", "v96", "v97", "v98", "v99", "v100", "v101", "v102", "v103", "v104", "v105", "v106", "v107", "v108", "v109", "v110", "v111", "v112", "v113", "v114", "v115", "v116", "v117", "v118", "v119", "v120", "v121", "v122", "v123", "v124", "v125", "v126", "v127", "v128", "v129", "v130", "v131", "v132", "v133", "v134", "v135", "v136", "v137", "v138", "v139", "v140", "v141", "v142", "v143", "v144", "v145", "v146", "v147", "v148", "v149", "v150", "v151", "v152", "v153", "v154", "v155", "v156", "v157", "v158", "v159"
#define CONV_B_ASM \
    "s_mov_b32 s92, 0x07060302\n" \
    "s_setprio 2\n" \
    "s_waitcnt vmcnt(4)\n" \
    "v_mul_f32_e32 v0, 0x3fb8aa3b, v70\n" \
    "v_exp_f32_e32 v0, v0\n" \
    "v_mul_f32_e32 v1, 0x3fb8aa3b, v64\n" \
    "v_cvt_pk_bf16_f32 v0, 0, v0\n" \
    "v_mul_f32_e32 v2, 0.15915494, v65\n" \
    "v_mul_f32_e32 v1, v0, v1\n" \
    "v_mul_f32_e32 v2, v0, v2\n" \
    "v_mul_f32_e32 v4, 0x41000000, v2\n" \
    "v_mul_f32_e32 v3, 0x41000000, v1\n" \
    "v_fract_f32_e32 v4, v4\n" \
    "v_exp_f32_e32 v3, v3\n" \
    "v_cos_f32_e32 v8, v4\n" \
    "v_sin_f32_e32 v9, v4\n" \
    "v_mul_f32_e32 v66, v0, v66\n" \
    "v_mul_f32_e64 v8, v3, v8\n" \
    "v_mul_f32_e64 v9, v3, v9\n" \
    "v_pk_mul_f32 v[18:19], v[8:9], v[8:9] op_sel_hi:[0,1]\n" \
    "v_cvt_pk_bf16_f32 v30, v8, v9\n" \
    "v_pk_fma_f32 v[14:15], v[8:9], v[8:9], v[18:19] op_sel:[1,1,0] op_sel_hi:[0,1,1] neg_lo:[0,1,0]\n" \
    "ds_write_addtid_b32 v30 offset:2080\n" \
    "v_pk_mul_f32 v[18:19], v[8:9], v[14:15] op_sel_hi:[0,1]\n" \
    "v_cvt_pk_bf16_f32 v31, v14, v15\n" \
    "v_pk_fma_f32 v[14:15], v[14:15], v[8:9], v[18:19] op_sel:[1,1,0] op_sel_hi:[0,1,1] neg_lo:[0,1,0]\n" \
    "ds_write_addtid_b32 v31 offset:4160\n" \
    "v_pk_mul_f32 v[18:19], v[8:9], v[14:15] op_sel_hi:[0,1]\n" \
    "v_cvt_pk_bf16_f32 v30, v14, v15\n" \
    "v_pk_fma_f32 v[14:15], v[14:15], v[8:9], v[18:19] op_sel:[1,1,0] op_sel_hi:[0,1,1] neg_lo:[0,1,0]\n" \
    "ds_write_addtid_b32 v30 offset:6240\n" \
    "v_pk_mul_f32 v[18:19], v[8:9], v[14:15] op_sel_hi:[0,1]\n" \
    "v_cvt_pk_bf16_f32 v31, v14, v15\n" \
    "v_pk_fma_f32 v[14:15], v[14:15], v[8:9], v[18:19] op_sel:[1,1,0] op_sel_hi:[0,1,1] neg_lo:[0,1,0]\n" \
    "ds_write_addtid_b32 v31 offset:8320\n" \
    "v_mul_f32_e32 v4, 0x43000000, v2\n" \
    "v_mul_f32_e32 v3, 0x43000000, v1\n" \
    "v_fract_f32_e32 v4, v4\n" \
    "v_exp_f32_e32 v3, v3\n" \
    "v_cos_f32_e32 v12, v4\n" \
    "v_sin_f32_e32 v13, v4\n" \
    "v_pk_mul_f32 v[18:19], v[8:9], v[14:15] op_sel_hi:[0,1]\n" \
    "v_cvt_pk_bf16_f32 v30, v14, v15\n" \
    "v_pk_fma_f32 v[14:15], v[14:15], v[8:9], v[18:19] op_sel:[1,1,0] op_sel_hi:[0,1,1] neg_lo:[0,1,0]\n" \
    "ds_write_addtid_b32 v30 offset:10400\n" \
    "v_pk_mul_f32 v[18:19], v[8:9], v[14:15] op_sel_hi:[0,1]\n" \
    "v_cvt_pk_bf16_f32 v31, v14, v15\n" \
    "v_pk_fma_f32 v[14:15], v[14:15], v[8:9], v[18:19] op_sel:[1,1,0] op_sel_hi:[0,1,1] neg_lo:[0,1,0]\n" \
    "ds_write_addtid_b32 v31 offset:12480\n" \
    "v_mul_f32_e64 v12, v3, v12\n" \
    "v_mul_f32_e64 v13, -v3, v13\n" \
    "v_pk_mul_f32 v[18:19], v[8:9], v[14:15] op_sel_hi:[0,1]\n" \
    "v_cvt_pk_bf16_f32 v30, v14, v15\n" \
    "v_pk_fma_f32 v[14:15], v[14:15], v[8:9], v[18:19] op_sel:[1,1,0] op_sel_hi:[0,1,1] neg_lo:[0,1,0]\n" \
    "ds_write_addtid_b32 v30 offset:14560\n" \
    "v_pk_mul_f32 v[18:19], v[8:9], v[14:15] op_sel_hi:[0,1]\n" \
    "v_cvt_pk_bf16_f32 v31, v14, v15\n" \
    "v_pk_fma_f32 v[14:15], v[14:15], v[8:9], v[18:19] op_sel:[1,1,0] op_sel_hi:[0,1,1] neg_lo:[0,1,0]\n" \
    "ds_write_addtid_b32 v31 offset:16640\n" \
    "v_fract_f32_e32 v6, v2\n" \
    "v_exp_f32_e32 v5, v1\n" \
    "v_cos_f32_e32 v10, v6\n" \
    "v_sin_f32_e32 v11, v6\n" \
    "v_pk_mul_f32 v[18:19], v[8:9], v[14:15] op_sel_hi:[0,1]\n" \
    "v_cvt_pk_bf16_f32 v30, v14, v15\n" \
    "v_pk_fma_f32 v[14:15], v[14:15], v[8:9], v[18:19] op_sel:[1,1,0] op_sel_hi:[0,1,1] neg_lo:[0,1,0]\n" \
    "ds_write_addtid_b32 v30 offset:18720\n" \
    "v_pk_mul_f32 v[18:19], v[8:9], v[14:15] op_sel_hi:[0,1]\n" \
    "v_cvt_pk_bf16_f32 v31, v14, v15\n" \
    "v_pk_fma_f32 v[14:15], v[14:15], v[8:9], v[18:19] op_sel:[1,1,0] op_sel_hi:[0,1,1] neg_lo:[0,1,0]\n" \
    "ds_write_addtid_b32 v31 offset:20800\n" \
    "v_mul_f32_e64 v10, v5, v10\n" \
    "v_mul_f32_e64 v11, -v5, v11\n" \
    "v_pk_mul_f32 v[18:19], v[8:9], v[14:15] op_sel_hi:[0,1]\n" \
    "v_cvt_pk_bf16_f32 v30, v14, v15\n" \
    "v_pk_fma_f32 v[14:15], v[14:15], v[8:9], v[18:19] op_sel:[1,1,0] op_sel_hi:[0,1,1] neg_lo:[0,1,0]\n" \
    "ds_write_addtid_b32 v30 offset:22880\n" \
    "v_mul_f32_e64 v67, v0, v67\n" \
    "v_mul_f32_e64 v16, v68, v66\n" \
    "v_mul_f32_e64 v17, -v68, v67\n" \
    "v_fma_f32 v16, -v69, v67, v16\n" \
    "v_fma_f32 v17, -v69, v66, v17\n" \
    "v_pk_mul_f32 v[18:19], v[8:9], v[14:15] op_sel_hi:[0,1]\n" \
    "v_cvt_pk_bf16_f32 v31, v14, v15\n" \
    "v_pk_fma_f32 v[14:15], v[14:15], v[8:9], v[18:19] op_sel:[1,1,0] op_sel_hi:[0,1,1] neg_lo:[0,1,0]\n" \
    "ds_write_addtid_b32 v31 offset:24960\n" \
    "v_pk_mul_f32 v[18:19], v[8:9], v[14:15] op_sel_hi:[0,1]\n" \
    "v_cvt_pk_bf16_f32 v30, v14, v15\n" \
    "v_pk_fma_f32 v[14:15], v[14:15], v[8:9], v[18:19] op_sel:[1,1,0] op_sel_hi:[0,1,1] neg_lo:[0,1,0]\n" \
    "ds_write_addtid_b32 v30 offset:27040\n" \
    "v_pk_mul_f32 v[18:19], v[8:9], v[14:15] op_sel_hi:[0,1]\n" \
    "v_cvt_pk_bf16_f32 v31, v14, v15\n" \
    "v_pk_fma_f32 v[14:15], v[14:15], v[8:9], v[18:19] op_sel:[1,1,0] op_sel_hi:[0,1,1] neg_lo:[0,1,0]\n" \
    "ds_write_addtid_b32 v31 offset:29120\n" \
    "v_pk_mul_f32 v[18:19], v[12:13], v[16:17] op_sel_hi:[0,1]\n" \
    "v_cvt_pk_bf16_f32 v30, v14, v15\n" \
    "v_pk_fma_f32 v[20:21], v[16:17], v[12:13], v[18:19] op_sel:[1,1,0] op_sel_hi:[0,1,1] neg_lo:[0,1,0]\n" \
    "ds_write_addtid_b32 v30 offset:31200\n" \
    "v_mov_b32_e32 v61, 1\n" \
    "ds_write_addtid_b32 v61 offset:54080\n" \
    "s_add_u32 m0, %[m0a], 54080\n" \
    "v_cvt_pk_bf16_f32 v31, v16, v17\n" \
    "ds_write_addtid_b32 v31 offset:4160\n" \
    "v_pk_mul_f32 v[18:19], v[10:11], v[16:17] op_sel_hi:[0,1]\n" \
    "v_cvt_pk_bf16_f32 v30, v20, v21\n" \
    "v_pk_fma_f32 v[16:17], v[16:17], v[10:11], v[18:19] op_sel:[1,1,0] op_sel_hi:[0,1,1] neg_lo:[0,1,0]\n" \
    "ds_write_addtid_b32 v30 offset:20800\n" \
    "v_pk_mul_f32 v[18:19], v[12:13], v[16:17] op_sel_hi:[0,1]\n" \
    "v_cvt_pk_bf16_f32 v31, v16, v17\n" \
    "v_pk_fma_f32 v[14:15], v[16:17], v[12:13], v[18:19] op_sel:[1,1,0] op_sel_hi:[0,1,1] neg_lo:[0,1,0]\n" \
    "ds_write_addtid_b32 v31 offset:6240\n" \
    "v_pk_mul_f32 v[18:19], v[10:11], v[16:17] op_sel_hi:[0,1]\n" \
    "v_cvt_pk_bf16_f32 v30, v14, v15\n" \
    "v_pk_fma_f32 v[16:17], v[16:17], v[10:11], v[18:19] op_sel:[1,1,0] op_sel_hi:[0,1,1] neg_lo:[0,1,0]\n" \
    "ds_write_addtid_b32 v30 offset:22880\n" \
    "v_pk_mul_f32 v[18:19], v[12:13], v[16:17] op_sel_hi:[0,1]\n" \
    "v_cvt_pk_bf16_f32 v31, v16, v17\n" \
    "v_pk_fma_f32 v[22:23], v[16:17], v[12:13], v[18:19] op_sel:[1,1,0] op_sel_hi:[0,1,1] neg_lo:[0,1,0]\n" \
    "ds_write_addtid_b32 v31 offset:8320\n" \
    "v_pk_mul_f32 v[18:19], v[10:11], v[16:17] op_sel_hi:[0,1]\n" \
    "v_cvt_pk_bf16_f32 v30, v22, v23\n" \
    "v_pk_fma_f32 v[16:17], v[16:17], v[10:11], v[18:19] op_sel:[1,1,0] op_sel_hi:[0,1,1] neg_lo:[0,1,0]\n" \
    "ds_write_addtid_b32 v30 offset:24960\n" \
    "v_pk_mul_f32 v[18:19], v[12:13], v[16:17] op_sel_hi:[0,1]\n" \
    "v_cvt_pk_bf16_f32 v31, v16, v17\n" \
    "v_pk_fma_f32 v[14:15], v[16:17], v[12:13], v[18:19] op_sel:[1,1,0] op_sel_hi:[0,1,1] neg_lo:[0,1,0]\n" \
    "ds_write_addtid_b32 v31 offset:10400\n" \
    "v_pk_mul_f32 v[18:19], v[10:11], v[16:17] op_sel_hi:[0,1]\n" \
    "v_cvt_pk_bf16_f32 v30, v14, v15\n" \
    "v_pk_fma_f32 v[16:17], v[16:17], v[10:11], v[18:19] op_sel:[1,1,0] op_sel_hi:[0,1,1] neg_lo:[0,1,0]\n" \
    "ds_write_addtid_b32 v30 offset:27040\n" \
    "v_pk_mul_f32 v[18:19], v[12:13], v[16:17] op_sel_hi:[0,1]\n" \
    "v_cvt_pk_bf16_f32 v31, v16, v17\n" \
    "v_pk_fma_f32 v[24:25], v[16:17], v[12:13], v[18:19] op_sel:[1,1,0] op_sel_hi:[0,1,1] neg_lo:[0,1,0]\n" \
    "ds_write_addtid_b32 v31 offset:12480\n" \
    "v_pk_mul_f32 v[18:19], v[10:11], v[16:17] op_sel_hi:[0,1]\n" \
    "v_cvt_pk_bf16_f32 v30, v24, v25\n" \
    "v_pk_fma_f32 v[16:17], v[16:17], v[10:11], v[18:19] op_sel:[1,1,0] op_sel_hi:[0,1,1] neg_lo:[0,1,0]\n" \
    "ds_write_addtid_b32 v30 offset:29120\n" \
    "v_pk_mul_f32 v[18:19], v[12:13], v[16:17] op_sel_hi:[0,1]\n" \
    "v_cvt_pk_bf16_f32 v31, v16, v17\n" \
    "v_pk_fma_f32 v[14:15], v[16:17], v[12:13], v[18:19] op_sel:[1,1,0] op_sel_hi:[0,1,1] neg_lo:[0,1,0]\n" \
    "ds_write_addtid_b32 v31 offset:14560\n" \
    "v_pk_mul_f32 v[18:19], v[10:11], v[16:17] op_sel_hi:[0,1]\n" \
    "v_cvt_pk_bf16_f32 v30, v14, v15\n" \
    "v_pk_fma_f32 v[16:17], v[16:17], v[10:11], v[18:19] op_sel:[1,1,0] op_sel_hi:[0,1,1] neg_lo:[0,1,0]\n" \
    "ds_write_addtid_b32 v30 offset:31200\n" \
    "v_pk_mul_f32 v[18:19], v[12:13], v[16:17] op_sel_hi:[0,1]\n" \
    "v_cvt_pk_bf16_f32 v31, v16, v17\n" \
    "v_pk_fma_f32 v[26:27], v[16:17], v[12:13], v[18:19] op_sel:[1,1,0] op_sel_hi:[0,1,1] neg_lo:[0,1,0]\n" \
    "ds_write_addtid_b32 v31 offset:16640\n" \
    "v_pk_mul_f32 v[18:19], v[10:11], v[16:17] op_sel_hi:[0,1]\n" \
    "v_cvt_pk_bf16_f32 v30, v26, v27\n" \
    "v_pk_fma_f32 v[16:17], v[16:17], v[10:11], v[18:19] op_sel:[1,1,0] op_sel_hi:[0,1,1] neg_lo:[0,1,0]\n" \
    "ds_write_addtid_b32 v30 offset:33280\n" \
    "v_pk_mul_f32 v[18:19], v[12:13], v[16:17] op_sel_hi:[0,1]\n" \
    "v_cvt_pk_bf16_f32 v31, v16, v17\n" \
    "v_pk_fma_f32 v[14:15], v[16:17], v[12:13], v[18:19] op_sel:[1,1,0] op_sel_hi:[0,1,1] neg_lo:[0,1,0]\n" \
    "ds_write_addtid_b32 v31 offset:18720\n" \
    "s_waitcnt vmcnt(0)\n" \
    "v_readfirstlane_b32 %[ep], v72\n" \
    "v_pk_mul_f32 v[18:19], v[12:13], v[20:21] op_sel_hi:[0,1]\n" \
    "v_cvt_pk_bf16_f32 v30, v14, v15\n" \
    "v_pk_fma_f32 v[20:21], v[20:21], v[12:13], v[18:19] op_sel:[1,1,0] op_sel_hi:[0,1,1] neg_lo:[0,1,0]\n" \
    "ds_write_addtid_b32 v30 offset:35360\n" \
    "v_mov_b32_e32 v61, 2\n" \
    "ds_write_addtid_b32 v61 offset:0\n" \
    "s_mov_b32 m0, %[m0a]\n" \
    "v_pk_mul_f32 v[18:19], v[12:13], v[22:23] op_sel_hi:[0,1]\n" \
    "v_cvt_pk_bf16_f32 v31, v20, v21\n" \
    "v_pk_fma_f32 v[22:23], v[22:23], v[12:13], v[18:19] op_sel:[1,1,0] op_sel_hi:[0,1,1] neg_lo:[0,1,0]\n" \
    "ds_write_addtid_b32 v31 offset:33280\n" \
    "v_pk_mul_f32 v[18:19], v[12:13], v[24:25] op_sel_hi:[0,1]\n" \
    "v_cvt_pk_bf16_f32 v30, v22, v23\n" \
    "v_pk_fma_f32 v[24:25], v[24:25], v[12:13], v[18:19] op_sel:[1,1,0] op_sel_hi:[0,1,1] neg_lo:[0,1,0]\n" \
    "ds_write_addtid_b32 v30 offset:35360\n" \
    "v_pk_mul_f32 v[18:19], v[12:13], v[26:27] op_sel_hi:[0,1]\n" \
    "v_cvt_pk_bf16_f32 v31, v24, v25\n" \
    "v_pk_fma_f32 v[26:27], v[26:27], v[12:13], v[18:19] op_sel:[1,1,0] op_sel_hi:[0,1,1] neg_lo:[0,1,0]\n" \
    "ds_write_addtid_b32 v31 offset:37440\n" \
    "v_pk_mul_f32 v[18:19], v[10:11], v[14:15] op_sel_hi:[0,1]\n" \
    "v_cvt_pk_bf16_f32 v30, v26, v27\n" \
    "v_pk_fma_f32 v[28:29], v[14:15], v[10:11], v[18:19] op_sel:[1,1,0] op_sel_hi:[0,1,1] neg_lo:[0,1,0]\n" \
    "ds_write_addtid_b32 v30 offset:39520\n" \
    "v_pk_mul_f32 v[18:19], v[12:13], v[28:29] op_sel_hi:[0,1]\n" \
    "v_pk_fma_f32 v[28:29], v[28:29], v[12:13], v[18:19] op_sel:[1,1,0] op_sel_hi:[0,1,1] neg_lo:[0,1,0]\n" \
    "v_pk_mul_f32 v[18:19], v[12:13], v[20:21] op_sel_hi:[0,1]\n" \
    "v_cvt_pk_bf16_f32 v31, v28, v29\n" \
    "v_pk_fma_f32 v[20:21], v[20:21], v[12:13], v[18:19] op_sel:[1,1,0] op_sel_hi:[0,1,1] neg_lo:[0,1,0]\n" \
    "ds_write_addtid_b32 v31 offset:41600\n" \
    "v_pk_mul_f32 v[18:19], v[12:13], v[22:23] op_sel_hi:[0,1]\n" \
    "v_cvt_pk_bf16_f32 v30, v20, v21\n" \
    "v_pk_fma_f32 v[22:23], v[22:23], v[12:13], v[18:19] op_sel:[1,1,0] op_sel_hi:[0,1,1] neg_lo:[0,1,0]\n" \
    "ds_write_addtid_b32 v30 offset:43680\n" \
    "v_pk_mul_f32 v[18:19], v[12:13], v[24:25] op_sel_hi:[0,1]\n" \
    "v_cvt_pk_bf16_f32 v31, v22, v23\n" \
    "v_pk_fma_f32 v[24:25], v[24:25], v[12:13], v[18:19] op_sel:[1,1,0] op_sel_hi:[0,1,1] neg_lo:[0,1,0]\n" \
    "ds_write_addtid_b32 v31 offset:45760\n" \
    "v_pk_mul_f32 v[18:19], v[12:13], v[26:27] op_sel_hi:[0,1]\n" \
    "v_cvt_pk_bf16_f32 v30, v24, v25\n" \
    "v_pk_fma_f32 v[26:27], v[26:27], v[12:13], v[18:19] op_sel:[1,1,0] op_sel_hi:[0,1,1] neg_lo:[0,1,0]\n" \
    "ds_write_addtid_b32 v30 offset:47840\n" \
    "v_pk_mul_f32 v[18:19], v[12:13], v[28:29] op_sel_hi:[0,1]\n" \
    "v_cvt_pk_bf16_f32 v31, v26, v27\n" \
    "v_pk_fma_f32 v[28:29], v[28:29], v[12:13], v[18:19] op_sel:[1,1,0] op_sel_hi:[0,1,1] neg_lo:[0,1,0]\n" \
    "ds_write_addtid_b32 v31 offset:49920\n" \
    "v_pk_mul_f32 v[18:19], v[12:13], v[24:25] op_sel_hi:[0,1]\n" \
    "v_cvt_pk_bf16_f32 v30, v28, v29\n" \
    "v_pk_fma_f32 v[24:25], v[24:25], v[12:13], v[18:19] op_sel:[1,1,0] op_sel_hi:[0,1,1] neg_lo:[0,1,0]\n" \
    "ds_write_addtid_b32 v30 offset:52000\n" \
    "v_mov_b32_e32 v61, 3\n" \
    "ds_write_addtid_b32 v61 offset:54080\n" \
    "s_add_u32 m0, %[m0a], 62400\n" \
    "v_pk_mul_f32 v[18:19], v[12:13], v[28:29] op_sel_hi:[0,1]\n" \
    "v_cvt_pk_bf16_f32 v31, v24, v25\n" \
    "v_pk_fma_f32 v[28:29], v[28:29], v[12:13], v[18:19] op_sel:[1,1,0] op_sel_hi:[0,1,1] neg_lo:[0,1,0]\n" \
    "ds_write_addtid_b32 v31 offset:29120\n" \
    "v_pk_mul_f32 v[18:19], v[12:13], v[24:25] op_sel_hi:[0,1]\n" \
    "v_cvt_pk_bf16_f32 v30, v28, v29\n" \
    "v_pk_fma_f32 v[24:25], v[24:25], v[12:13], v[18:19] op_sel:[1,1,0] op_sel_hi:[0,1,1] neg_lo:[0,1,0]\n" \
    "ds_write_addtid_b32 v30 offset:37440\n" \
    "v_pk_mul_f32 v[18:19], v[12:13], v[28:29] op_sel_hi:[0,1]\n" \
    "v_cvt_pk_bf16_f32 v31, v24, v25\n" \
    "v_pk_fma_f32 v[28:29], v[28:29], v[12:13], v[18:19] op_sel:[1,1,0] op_sel_hi:[0,1,1] neg_lo:[0,1,0]\n" \
    "ds_write_addtid_b32 v31 offset:31200\n" \
    "v_pk_mul_f32 v[18:19], v[12:13], v[24:25] op_sel_hi:[0,1]\n" \
    "v_cvt_pk_bf16_f32 v30, v28, v29\n" \
    "v_pk_fma_f32 v[24:25], v[24:25], v[12:13], v[18:19] op_sel:[1,1,0] op_sel_hi:[0,1,1] neg_lo:[0,1,0]\n" \
    "ds_write_addtid_b32 v30 offset:39520\n" \
    "v_pk_mul_f32 v[18:19], v[12:13], v[28:29] op_sel_hi:[0,1]\n" \
    "v_cvt_pk_bf16_f32 v31, v24, v25\n" \
    "v_pk_fma_f32 v[28:29], v[28:29], v[12:13], v[18:19] op_sel:[1,1,0] op_sel_hi:[0,1,1] neg_lo:[0,1,0]\n" \
    "ds_write_addtid_b32 v31 offset:33280\n" \
    "v_pk_mul_f32 v[18:19], v[12:13], v[24:25] op_sel_hi:[0,1]\n" \
    "v_cvt_pk_bf16_f32 v30, v28, v29\n" \
    "v_pk_fma_f32 v[24:25], v[24:25], v[12:13], v[18:19] op_sel:[1,1,0] op_sel_hi:[0,1,1] neg_lo:[0,1,0]\n" \
    "ds_write_addtid_b32 v30 offset:41600\n" \
    "v_pk_mul_f32 v[18:19], v[12:13], v[28:29] op_sel_hi:[0,1]\n" \
    "v_cvt_pk_bf16_f32 v31, v24, v25\n" \
    "v_pk_fma_f32 v[28:29], v[28:29], v[12:13], v[18:19] op_sel:[1,1,0] op_sel_hi:[0,1,1] neg_lo:[0,1,0]\n" \
    "ds_write_addtid_b32 v31 offset:35360\n" \
    "v_pk_mul_f32 v[18:19], v[12:13], v[28:29] op_sel_hi:[0,1]\n" \
    "v_cvt_pk_bf16_f32 v30, v28, v29\n" \
    "v_pk_fma_f32 v[28:29], v[28:29], v[12:13], v[18:19] op_sel:[1,1,0] op_sel_hi:[0,1,1] neg_lo:[0,1,0]\n" \
    "ds_write_addtid_b32 v30 offset:43680\n" \
    "v_pk_mul_f32 v[18:19], v[12:13], v[28:29] op_sel_hi:[0,1]\n" \
    "v_cvt_pk_bf16_f32 v31, v28, v29\n" \
    "v_pk_fma_f32 v[28:29], v[28:29], v[12:13], v[18:19] op_sel:[1,1,0] op_sel_hi:[0,1,1] neg_lo:[0,1,0]\n" \
    "ds_write_addtid_b32 v31 offset:45760\n" \
    "v_pk_mul_f32 v[18:19], v[12:13], v[28:29] op_sel_hi:[0,1]\n" \
    "v_cvt_pk_bf16_f32 v30, v28, v29\n" \
    "v_pk_fma_f32 v[28:29], v[28:29], v[12:13], v[18:19] op_sel:[1,1,0] op_sel_hi:[0,1,1] neg_lo:[0,1,0]\n" \
    "ds_write_addtid_b32 v30 offset:47840\n" \
    "v_pk_mul_f32 v[18:19], v[12:13], v[28:29] op_sel_hi:[0,1]\n" \
    "v_cvt_pk_bf16_f32 v31, v28, v29\n" \
    "v_pk_fma_f32 v[28:29], v[28:29], v[12:13], v[18:19] op_sel:[1,1,0] op_sel_hi:[0,1,1] neg_lo:[0,1,0]\n" \
    "ds_write_addtid_b32 v31 offset:49920\n" \
    "v_pk_mul_f32 v[18:19], v[12:13], v[28:29] op_sel_hi:[0,1]\n" \
    "v_cvt_pk_bf16_f32 v30, v28, v29\n" \
    "v_pk_fma_f32 v[28:29], v[28:29], v[12:13], v[18:19] op_sel:[1,1,0] op_sel_hi:[0,1,1] neg_lo:[0,1,0]\n" \
    "ds_write_addtid_b32 v30 offset:52000\n" \
    "v_pk_mul_f32 v[18:19], v[12:13], v[28:29] op_sel_hi:[0,1]\n" \
    "v_cvt_pk_bf16_f32 v31, v28, v29\n" \
    "v_pk_fma_f32 v[28:29], v[28:29], v[12:13], v[18:19] op_sel:[1,1,0] op_sel_hi:[0,1,1] neg_lo:[0,1,0]\n" \
    "ds_write_addtid_b32 v31 offset:54080\n" \
    "v_pk_mul_f32 v[18:19], v[12:13], v[28:29] op_sel_hi:[0,1]\n" \
    "v_cvt_pk_bf16_f32 v30, v28, v29\n" \
    "v_pk_fma_f32 v[28:29], v[28:29], v[12:13], v[18:19] op_sel:[1,1,0] op_sel_hi:[0,1,1] neg_lo:[0,1,0]\n" \
    "ds_write_addtid_b32 v30 offset:56160\n" \
    "v_pk_mul_f32 v[18:19], v[12:13], v[28:29] op_sel_hi:[0,1]\n" \
    "v_cvt_pk_bf16_f32 v31, v28, v29\n" \
    "v_pk_fma_f32 v[28:29], v[28:29], v[12:13], v[18:19] op_sel:[1,1,0] op_sel_hi:[0,1,1] neg_lo:[0,1,0]\n" \
    "ds_write_addtid_b32 v31 offset:58240\n" \
    "v_cvt_pk_bf16_f32 v30, v28, v29\n" \
    "ds_write_addtid_b32 v30 offset:60320\n" \
    "s_mov_b32 m0, %[m0a]\n" \
    "v_mov_b32_e32 v61, 4\n" \
    "ds_write_addtid_b32 v61 offset:54080\n" \
    "s_setprio 1\n" \
    "ds_read_b32 v63, v34\n" \
    "ds_read_b128 v[152:155], v38 offset:1024\n" \
    "ds_read_b128 v[160:163], v38 offset:3072\n" \
    "ds_read_b128 v[148:151], v38 offset:0\n" \
    "ds_read_b128 v[156:159], v38 offset:2048\n" \
    "ds_read_b128 v[164:167], v38 offset:4096\n" \
    "s_waitcnt lgkmcnt(5)\n" \
    "v_cmp_gt_u32_e32 vcc, 2, v63\n" \
    "s_cbranch_vccz Lbe0_%=\n" \
    "s_mov_b32 s90, 0x100000\n" \
    "Lbl0_%=:\n" \
    "s_sleep 1\n" \
    "ds_read_b32 v63, v34 offset:0\n" \
    "ds_read_b128 v[152:155], v38 offset:1024\n" \
    "ds_read_b128 v[160:163], v38 offset:3072\n" \
    "ds_read_b128 v[148:151], v38 offset:0\n" \
    "ds_read_b128 v[156:159], v38 offset:2048\n" \
    "ds_read_b128 v[164:167], v38 offset:4096\n" \
    "s_waitcnt lgkmcnt(0)\n" \
    "v_cmp_gt_u32_e32 vcc, 2, v63\n" \
    "s_cbranch_vccz Lbe0_%=\n" \
    "s_sub_u32 s90, s90, 1\n" \
    "s_cbranch_scc0 Lbl0_%=\n" \
    "Lbe0_%=:\n" \
    "ds_read_b128 v[108:111], v33 offset:448\n" \
    "ds_read_b128 v[112:115], v33 offset:432\n" \
    "ds_read_b128 v[116:119], v33 offset:960\n" \
    "ds_read_b128 v[120:123], v33 offset:944\n" \
    "s_waitcnt lgkmcnt(3)\n" \
    "v_mfma_f32_16x16x32_bf16 v[48:51], v[108:111], v[152:155], 0\n" \
    "ds_read_b32 v60, v32\n" \
    "s_waitcnt lgkmcnt(3)\n" \
    "v_mfma_f32_16x16x32_bf16 v[52:55], v[112:115], v[152:155], 0\n" \
    "v_mfma_f32_16x16x32_bf16 v[56:59], v[112:115], v[160:163], 0\n" \
    "v_mfma_f32_16x16x32_bf16 v[0:3], v[108:111], v[148:151], 0\n" \
    "v_mfma_f32_16x16x32_bf16 v[4:7], v[112:115], v[148:151], 0\n" \
    "v_mfma_f32_16x16x32_bf16 v[40:43], v[108:111], v[156:159], 0\n" \
    "s_waitcnt lgkmcnt(2)\n" \
    "v_mfma_f32_16x16x32_bf16 v[8:11], v[116:119], v[148:151], 0\n" \
    "s_nop 3\n" \
    "v_cvt_pk_bf16_f32 v0, v0, v48\n" \
    "v_cvt_pk_bf16_f32 v1, v1, v49\n" \
    "v_cvt_pk_bf16_f32 v2, v2, v50\n" \
    "v_cvt_pk_bf16_f32 v3, v3, v51\n" \
    "s_waitcnt lgkmcnt(1)\n" \
    "v_mfma_f32_16x16x32_bf16 v[12:15], v[120:123], v[148:151], 0\n" \
    "v_cvt_pk_bf16_f32 v4, v4, v52\n" \
    "v_cvt_pk_bf16_f32 v5, v5, v53\n" \
    "v_cvt_pk_bf16_f32 v6, v6, v54\n" \
    "v_cvt_pk_bf16_f32 v7, v7, v55\n" \
    "v_mfma_f32_16x16x32_bf16 v[44:47], v[116:119], v[156:159], 0\n" \
    "v_cvt_pk_bf16_f32 v40, v40, v56\n" \
    "v_cvt_pk_bf16_f32 v41, v41, v57\n" \
    "v_cvt_pk_bf16_f32 v42, v42, v58\n" \
    "v_cvt_pk_bf16_f32 v43, v43, v59\n" \
    "v_mfma_f32_16x16x32_bf16 v[48:51], v[116:119], v[152:155], 0\n" \
    "v_mfma_f32_16x16x32_bf16 v[52:55], v[120:123], v[152:155], 0\n" \
    "v_mfma_f32_16x16x32_bf16 v[56:59], v[120:123], v[160:163], 0\n" \
    "s_waitcnt lgkmcnt(0)\n" \
    "v_cmp_gt_u32_e32 vcc, 2, v60\n" \
    "s_cbranch_vccz Lbe1_%=\n" \
    "s_mov_b32 s90, 0x100000\n" \
    "Lbl1_%=:\n" \
    "s_sleep 1\n" \
    "ds_read_b32 v60, v32 offset:0\n" \
    "s_waitcnt lgkmcnt(0)\n" \
    "v_cmp_gt_u32_e32 vcc, 2, v60\n" \
    "s_cbranch_vccz Lbe1_%=\n" \
    "s_sub_u32 s90, s90, 1\n" \
    "s_cbranch_scc0 Lbl1_%=\n" \
    "Lbe1_%=:\n" \
    "ds_read2_b32 v[104:105], v37 offset0:0 offset1:1\n" \
    "ds_read2_b32 v[106:107], v37 offset0:2 offset1:3\n" \
    "s_waitcnt lgkmcnt(0)\n" \
    "v_mfma_f32_16x16x32_bf16 v[132:135], v[104:107], v[108:111], 0\n" \
    "v_mfma_f32_16x16x32_bf16 v[136:139], v[104:107], v[116:119], 0\n" \
    "ds_read_b32 v60, v32\n" \
    "s_waitcnt lgkmcnt(0)\n" \
    "v_cmp_gt_u32_e32 vcc, 3, v60\n" \
    "s_cbranch_vccz Lbe2_%=\n" \
    "s_mov_b32 s90, 0x100000\n" \
    "Lbl2_%=:\n" \
    "s_sleep 1\n" \
    "ds_read_b32 v60, v32 offset:0\n" \
    "s_waitcnt lgkmcnt(0)\n" \
    "v_cmp_gt_u32_e32 vcc, 3, v60\n" \
    "s_cbranch_vccz Lbe2_%=\n" \
    "s_sub_u32 s90, s90, 1\n" \
    "s_cbranch_scc0 Lbl2_%=\n" \
    "Lbe2_%=:\n" \
    "ds_read_b32 v62, v39 offset:320\n" \
    "ds_read_b32 v63, v34\n" \
    "ds_read2_b32 v[64:65], v36 offset0:32 offset1:33\n" \
    "ds_read2_b32 v[66:67], v36 offset0:34 offset1:35\n" \
    "ds_read2_b32 v[76:77], v36 offset0:96 offset1:97\n" \
    "ds_read2_b32 v[78:79], v36 offset0:98 offset1:99\n" \
    "s_waitcnt lgkmcnt(5)\n" \
    "v_perm_b32 v62, v62, v62, s92\n" \
    "v_and_b32_e64 v164, v62, v164\n" \
    "v_and_b32_e64 v165, v62, v165\n" \
    "v_and_b32_e64 v166, v62, v166\n" \
    "v_and_b32_e64 v167, v62, v167\n" \
    "s_nop 0\n" \
    "s_nop 0\n" \
    "v_mfma_f32_16x16x32_bf16 v[140:143], v[164:167], v[108:111], 0\n" \
    "v_mfma_f32_16x16x32_bf16 v[124:127], v[164:167], v[116:119], 0\n" \
    "s_waitcnt lgkmcnt(4)\n" \
    "v_cmp_gt_u32_e32 vcc, 3, v63\n" \
    "s_cbranch_vccnz Lbs3_%=\n" \
    "Lbe3_%=:\n" \
    "ds_read_b128 v[108:111], v33 offset:1472\n" \
    "ds_read_b128 v[112:115], v33 offset:1456\n" \
    "s_setprio 3\n" \
    "s_waitcnt lgkmcnt(4)\n" \
    "v_mfma_f32_16x16x32_bf16 v[132:135], v[0:3], v[64:67], v[132:135]\n" \
    "v_cvt_pk_bf16_f32 v8, v8, v48\n" \
    "v_cvt_pk_bf16_f32 v9, v9, v49\n" \
    "v_cvt_pk_bf16_f32 v10, v10, v50\n" \
    "v_cvt_pk_bf16_f32 v11, v11, v51\n" \
    "v_cvt_pk_bf16_f32 v12, v12, v52\n" \
    "v_cvt_pk_bf16_f32 v13, v13, v53\n" \
    "v_cvt_pk_bf16_f32 v14, v14, v54\n" \
    "v_cvt_pk_bf16_f32 v15, v15, v55\n" \
    "v_cvt_pk_bf16_f32 v44, v44, v56\n" \
    "v_cvt_pk_bf16_f32 v45, v45, v57\n" \
    "v_cvt_pk_bf16_f32 v46, v46, v58\n" \
    "v_cvt_pk_bf16_f32 v47, v47, v59\n" \
    "ds_read_b32 v63, v34\n" \
    "s_waitcnt lgkmcnt(3)\n" \
    "v_mfma_f32_16x16x32_bf16 v[132:135], v[4:7], v[76:79], v[132:135]\n" \
    "s_waitcnt lgkmcnt(2)\n" \
    "v_mfma_f32_16x16x32_bf16 v[128:131], v[164:167], v[108:111], 0\n" \
    "ds_read2_b32 v[68:69], v36 offset0:16 offset1:17\n" \
    "ds_read2_b32 v[70:71], v36 offset0:18 offset1:19\n" \
    "v_mfma_f32_16x16x32_bf16 v[140:143], v[104:107], v[108:111], v[140:143]\n" \
    "ds_read2_b32 v[80:81], v36 offset0:80 offset1:81\n" \
    "ds_read2_b32 v[82:83], v36 offset0:82 offset1:83\n" \
    "v_mfma_f32_16x16x32_bf16 v[16:19], v[108:111], v[148:151], 0\n" \
    "s_waitcnt lgkmcnt(5)\n" \
    "v_mfma_f32_16x16x32_bf16 v[20:23], v[112:115], v[148:151], 0\n" \
    "v_mfma_f32_16x16x32_bf16 v[144:147], v[108:111], v[156:159], 0\n" \
    "v_mfma_f32_16x16x32_bf16 v[48:51], v[108:111], v[152:155], 0\n" \
    "v_mfma_f32_16x16x32_bf16 v[52:55], v[112:115], v[152:155], 0\n" \
    "v_mfma_f32_16x16x32_bf16 v[56:59], v[112:115], v[160:163], 0\n" \
    "s_waitcnt lgkmcnt(4)\n" \
    "v_cmp_gt_u32_e32 vcc, 4, v63\n" \
    "s_cbranch_vccnz Lbs4_%=\n" \
    "Lbe4_%=:\n" \
    "ds_read_b128 v[116:119], v33 offset:1984\n" \
    "ds_read_b128 v[120:123], v33 offset:1968\n" \
    "v_mfma_f32_16x16x32_bf16 v[136:139], v[8:11], v[64:67], v[136:139]\n" \
    "v_mov_b32_e64 v61, 13\n" \
    "ds_write_b128 v35, v[132:135] offset:0\n" \
    "ds_write_addtid_b32 v61 offset:54080\n" \
    "v_cvt_pk_bf16_f32 v16, v16, v48\n" \
    "v_cvt_pk_bf16_f32 v17, v17, v49\n" \
    "v_cvt_pk_bf16_f32 v18, v18, v50\n" \
    "v_cvt_pk_bf16_f32 v19, v19, v51\n" \
    "v_cvt_pk_bf16_f32 v20, v20, v52\n" \
    "v_cvt_pk_bf16_f32 v21, v21, v53\n" \
    "v_cvt_pk_bf16_f32 v22, v22, v54\n" \
    "v_cvt_pk_bf16_f32 v23, v23, v55\n" \
    "v_cvt_pk_bf16_f32 v144, v144, v56\n" \
    "v_cvt_pk_bf16_f32 v145, v145, v57\n" \
    "v_cvt_pk_bf16_f32 v146, v146, v58\n" \
    "v_cvt_pk_bf16_f32 v147, v147, v59\n" \
    "ds_read_b32 v63, v34\n" \
    "v_mfma_f32_16x16x32_bf16 v[136:139], v[12:15], v[76:79], v[136:139]\n" \
    "s_waitcnt lgkmcnt(7)\n" \
    "v_mfma_f32_16x16x32_bf16 v[136:139], v[0:3], v[68:71], v[136:139]\n" \
    "s_waitcnt lgkmcnt(5)\n" \
    "v_mfma_f32_16x16x32_bf16 v[136:139], v[4:7], v[80:83], v[136:139]\n" \
    "s_waitcnt lgkmcnt(4)\n" \
    "v_mfma_f32_16x16x32_bf16 v[132:135], v[164:167], v[116:119], 0\n" \
    "ds_read2_b32 v[72:73], v36 offset0:0 offset1:1\n" \
    "ds_read2_b32 v[74:75], v36 offset0:2 offset1:3\n" \
    "v_mfma_f32_16x16x32_bf16 v[124:127], v[104:107], v[116:119], v[124:127]\n" \
    "ds_read2_b32 v[84:85], v36 offset0:64 offset1:65\n" \
    "ds_read2_b32 v[86:87], v36 offset0:66 offset1:67\n" \
    "v_mfma_f32_16x16x32_bf16 v[24:27], v[116:119], v[148:151], 0\n" \
    "ds_read2_b32 v[88:89], v36 offset0:176 offset1:177\n" \
    "ds_read2_b32 v[90:91], v36 offset0:178 offset1:179\n" \
    "s_waitcnt lgkmcnt(9)\n" \
    "v_mfma_f32_16x16x32_bf16 v[28:31], v[120:123], v[148:151], 0\n" \
    "v_mfma_f32_16x16x32_bf16 v[164:167], v[116:119], v[156:159], 0\n" \
    "v_mfma_f32_16x16x32_bf16 v[48:51], v[116:119], v[152:155], 0\n" \
    "v_mfma_f32_16x16x32_bf16 v[52:55], v[120:123], v[152:155], 0\n" \
    "v_mfma_f32_16x16x32_bf16 v[56:59], v[120:123], v[160:163], 0\n" \
    "s_waitcnt lgkmcnt(6)\n" \
    "v_cmp_gt_u32_e32 vcc, 5, v63\n" \
    "s_cbranch_vccnz Lbs5_%=\n" \
    "Lbe5_%=:\n" \
    "ds_read_b128 v[108:111], v33 offset:2496\n" \
    "ds_read_b128 v[112:115], v33 offset:2480\n" \
    "v_mfma_f32_16x16x32_bf16 v[140:143], v[16:19], v[64:67], v[140:143]\n" \
    "v_mov_b32_e64 v61, 14\n" \
    "ds_write_b128 v35, v[136:139] offset:4160\n" \
    "ds_write_addtid_b32 v61 offset:54080\n" \
    "v_mfma_f32_16x16x32_bf16 v[140:143], v[20:23], v[76:79], v[140:143]\n" \
    "v_cvt_pk_bf16_f32 v24, v24, v48\n" \
    "v_cvt_pk_bf16_f32 v25, v25, v49\n" \
    "v_cvt_pk_bf16_f32 v26, v26, v50\n" \
    "v_mfma_f32_16x16x32_bf16 v[140:143], v[8:11], v[68:71], v[140:143]\n" \
    "v_cvt_pk_bf16_f32 v27, v27, v51\n" \
    "v_cvt_pk_bf16_f32 v28, v28, v52\n" \
    "v_cvt_pk_bf16_f32 v29, v29, v53\n" \
    "v_mfma_f32_16x16x32_bf16 v[140:143], v[12:15], v[80:83], v[140:143]\n" \
    "v_cvt_pk_bf16_f32 v30, v30, v54\n" \
    "v_cvt_pk_bf16_f32 v31, v31, v55\n" \
    "v_cvt_pk_bf16_f32 v164, v164, v56\n" \
    "s_waitcnt lgkmcnt(8)\n" \
    "v_mfma_f32_16x16x32_bf16 v[140:143], v[0:3], v[72:75], v[140:143]\n" \
    "v_cvt_pk_bf16_f32 v165, v165, v57\n" \
    "v_cvt_pk_bf16_f32 v166, v166, v58\n" \
    "v_cvt_pk_bf16_f32 v167, v167, v59\n" \
    "s_waitcnt lgkmcnt(6)\n" \
    "v_mfma_f32_16x16x32_bf16 v[140:143], v[4:7], v[84:87], v[140:143]\n" \
    "ds_read_b32 v63, v34\n" \
    "s_waitcnt lgkmcnt(5)\n" \
    "v_mfma_f32_16x16x32_bf16 v[140:143], v[40:43], v[88:91], v[140:143]\n" \
    "s_waitcnt lgkmcnt(4)\n" \
    "v_mfma_f32_16x16x32_bf16 v[128:131], v[104:107], v[108:111], v[128:131]\n" \
    "ds_read2_b32 v[92:93], v36 offset0:160 offset1:161\n" \
    "ds_read2_b32 v[94:95], v36 offset0:162 offset1:163\n" \
    "v_mfma_f32_16x16x32_bf16 v[0:3], v[108:111], v[148:151], 0\n" \
    "s_waitcnt lgkmcnt(5)\n" \
    "v_mfma_f32_16x16x32_bf16 v[4:7], v[112:115], v[148:151], 0\n" \
    "v_mfma_f32_16x16x32_bf16 v[48:51], v[108:111], v[152:155], 0\n" \
    "v_mfma_f32_16x16x32_bf16 v[52:55], v[112:115], v[152:155], 0\n" \
    "s_waitcnt lgkmcnt(2)\n" \
    "v_cmp_gt_u32_e32 vcc, 6, v63\n" \
    "s_cbranch_vccnz Lbs6_%=\n" \
    "Lbe6_%=:\n" \
    "ds_read_b128 v[116:119], v33 offset:3008\n" \
    "ds_read_b128 v[120:123], v33 offset:2992\n" \
    "v_mfma_f32_16x16x32_bf16 v[124:127], v[24:27], v[64:67], v[124:127]\n" \
    "v_mov_b32_e64 v61, 15\n" \
    "ds_write_b128 v35, v[140:143] offset:8320\n" \
    "v_mfma_f32_16x16x32_bf16 v[124:127], v[28:31], v[76:79], v[124:127]\n" \
    "ds_write_addtid_b32 v61 offset:54080\n" \
    "v_cvt_pk_bf16_f32 v0, v0, v48\n" \
    "v_mfma_f32_16x16x32_bf16 v[124:127], v[16:19], v[68:71], v[124:127]\n" \
    "v_cvt_pk_bf16_f32 v1, v1, v49\n" \
    "v_cvt_pk_bf16_f32 v2, v2, v50\n" \
    "v_mfma_f32_16x16x32_bf16 v[124:127], v[20:23], v[80:83], v[124:127]\n" \
    "v_cvt_pk_bf16_f32 v3, v3, v51\n" \
    "v_cvt_pk_bf16_f32 v4, v4, v52\n" \
    "v_mfma_f32_16x16x32_bf16 v[124:127], v[8:11], v[72:75], v[124:127]\n" \
    "v_cvt_pk_bf16_f32 v5, v5, v53\n" \
    "v_cvt_pk_bf16_f32 v6, v6, v54\n" \
    "v_mfma_f32_16x16x32_bf16 v[124:127], v[12:15], v[84:87], v[124:127]\n" \
    "v_cvt_pk_bf16_f32 v7, v7, v55\n" \
    "v_mfma_f32_16x16x32_bf16 v[124:127], v[44:47], v[88:91], v[124:127]\n" \
    "s_waitcnt lgkmcnt(4)\n" \
    "v_mfma_f32_16x16x32_bf16 v[124:127], v[40:43], v[92:95], v[124:127]\n" \
    "s_waitcnt lgkmcnt(3)\n" \
    "v_mfma_f32_16x16x32_bf16 v[132:135], v[104:107], v[116:119], v[132:135]\n" \
    "ds_read2_b32 v[96:97], v36 offset0:144 offset1:145\n" \
    "ds_read2_b32 v[98:99], v36 offset0:146 offset1:147\n" \
    "v_mfma_f32_16x16x32_bf16 v[8:11], v[116:119], v[148:151], 0\n" \
    "ds_read2_b32 v[100:101], v36 offset0:128 offset1:129\n" \
    "ds_read2_b32 v[102:103], v36 offset0:130 offset1:131\n" \
    "s_waitcnt lgkmcnt(6)\n" \
    "v_mfma_f32_16x16x32_bf16 v[12:15], v[120:123], v[148:151], 0\n" \
    "v_mfma_f32_16x16x32_bf16 v[48:51], v[116:119], v[152:155], 0\n" \
    "v_mfma_f32_16x16x32_bf16 v[52:55], v[120:123], v[152:155], 0\n" \
    "v_and_b32_e32 v112, 0x70, v38\n" \
    "global_load_dwordx4 v[108:111], v112, %[sg] sc1\n" \
    "v_mfma_f32_16x16x32_bf16 v[128:131], v[0:3], v[64:67], v[128:131]\n" \
    "v_mov_b32_e64 v61, 16\n" \
    "ds_write_b128 v35, v[124:127] offset:12480\n" \
    "v_mfma_f32_16x16x32_bf16 v[128:131], v[4:7], v[76:79], v[128:131]\n" \
    "ds_write_addtid_b32 v61 offset:54080\n" \
    "v_cvt_pk_bf16_f32 v8, v8, v48\n" \
    "v_mfma_f32_16x16x32_bf16 v[128:131], v[24:27], v[68:71], v[128:131]\n" \
    "v_cvt_pk_bf16_f32 v9, v9, v49\n" \
    "v_cvt_pk_bf16_f32 v10, v10, v50\n" \
    "v_mfma_f32_16x16x32_bf16 v[128:131], v[28:31], v[80:83], v[128:131]\n" \
    "v_cvt_pk_bf16_f32 v11, v11, v51\n" \
    "v_cvt_pk_bf16_f32 v12, v12, v52\n" \
    "v_mfma_f32_16x16x32_bf16 v[128:131], v[16:19], v[72:75], v[128:131]\n" \
    "v_cvt_pk_bf16_f32 v13, v13, v53\n" \
    "v_cvt_pk_bf16_f32 v14, v14, v54\n" \
    "v_mfma_f32_16x16x32_bf16 v[128:131], v[20:23], v[84:87], v[128:131]\n" \
    "v_cvt_pk_bf16_f32 v15, v15, v55\n" \
    "v_mfma_f32_16x16x32_bf16 v[128:131], v[144:147], v[88:91], v[128:131]\n" \
    "v_mfma_f32_16x16x32_bf16 v[128:131], v[44:47], v[92:95], v[128:131]\n" \
    "s_waitcnt lgkmcnt(4)\n" \
    "v_mfma_f32_16x16x32_bf16 v[128:131], v[40:43], v[96:99], v[128:131]\n" \
    "v_mfma_f32_16x16x32_bf16 v[132:135], v[8:11], v[64:67], v[132:135]\n" \
    "v_mov_b32_e64 v61, 17\n" \
    "s_nop 5\n" \
    "ds_write_b128 v35, v[128:131] offset:16640\n" \
    "v_mfma_f32_16x16x32_bf16 v[132:135], v[12:15], v[76:79], v[132:135]\n" \
    "ds_write_addtid_b32 v61 offset:54080\n" \
    "v_mfma_f32_16x16x32_bf16 v[132:135], v[0:3], v[68:71], v[132:135]\n" \
    "v_mfma_f32_16x16x32_bf16 v[132:135], v[4:7], v[80:83], v[132:135]\n" \
    "v_mfma_f32_16x16x32_bf16 v[132:135], v[24:27], v[72:75], v[132:135]\n" \
    "v_mfma_f32_16x16x32_bf16 v[132:135], v[28:31], v[84:87], v[132:135]\n" \
    "v_mfma_f32_16x16x32_bf16 v[132:135], v[164:167], v[88:91], v[132:135]\n" \
    "v_mfma_f32_16x16x32_bf16 v[132:135], v[144:147], v[92:95], v[132:135]\n" \
    "v_mfma_f32_16x16x32_bf16 v[132:135], v[44:47], v[96:99], v[132:135]\n" \
    "s_waitcnt lgkmcnt(4)\n" \
    "v_mfma_f32_16x16x32_bf16 v[132:135], v[40:43], v[100:103], v[132:135]\n" \
    "v_mov_b32_e64 v61, 18\n" \
    "s_nop 6\n" \
    "ds_write_b128 v35, v[132:135] offset:20800\n" \
    "ds_write_addtid_b32 v61 offset:54080\n" \
    "s_setprio 0\n" \
    "s_waitcnt vmcnt(0)\n" \
    "s_branch Lbx_%=\n" \
    "Lbs3_%=:\n" \
    "s_mov_b32 s90, 0x100000\n" \
    "Lbl3_%=:\n" \
    "ds_read_b32 v63, v34 offset:0\n" \
    "s_waitcnt lgkmcnt(0)\n" \
    "v_cmp_gt_u32_e32 vcc, 3, v63\n" \
    "s_cbranch_vccz Lbe3_%=\n" \
    "s_sub_u32 s90, s90, 1\n" \
    "s_cbranch_scc0 Lbl3_%=\n" \
    "s_branch Lbe3_%=\n" \
    "Lbs4_%=:\n" \
    "s_mov_b32 s90, 0x100000\n" \
    "Lbl4_%=:\n" \
    "ds_read_b32 v63, v34 offset:0\n" \
    "s_waitcnt lgkmcnt(0)\n" \
    "v_cmp_gt_u32_e32 vcc, 4, v63\n" \
    "s_cbranch_vccz Lbe4_%=\n" \
    "s_sub_u32 s90, s90, 1\n" \
    "s_cbranch_scc0 Lbl4_%=\n" \
    "s_branch Lbe4_%=\n" \
    "Lbs5_%=:\n" \
    "s_mov_b32 s90, 0x100000\n" \
    "Lbl5_%=:\n" \
    "ds_read_b32 v63, v34 offset:0\n" \
    "s_waitcnt lgkmcnt(0)\n" \
    "v_cmp_gt_u32_e32 vcc, 5, v63\n" \
    "s_cbranch_vccz Lbe5_%=\n" \
    "s_sub_u32 s90, s90, 1\n" \
    "s_cbranch_scc0 Lbl5_%=\n" \
    "s_branch Lbe5_%=\n" \
    "Lbs6_%=:\n" \
    "s_mov_b32 s90, 0x100000\n" \
    "Lbl6_%=:\n" \
    "ds_read_b32 v63, v34 offset:0\n" \
    "s_waitcnt lgkmcnt(0)\n" \
    "v_cmp_gt_u32_e32 vcc, 6, v63\n" \
    "s_cbranch_vccz Lbe6_%=\n" \
    "s_sub_u32 s90, s90, 1\n" \
    "s_cbranch_scc0 Lbl6_%=\n" \
    "s_branch Lbe6_%=\n" \
    "Lbx_%=:\n"
#define CONV_B_CLOB "v0", "v1", "v2", "v3", "v4", "v5", "v6", "v7", "v8", "v9", "v10", "v11", "v12", "v13", "v14", "v15", "v16", "v17", "v18", "v19", "v20", "v21", "v22", "v23", "v24", "v25", "v26", "v27", "v28", "v29", "v30", "v31", "v32", "v33", "v34", "v35", "v36", "v37", "v38", "v39", "v40", "v41", "v42", "v43", "v44", "v45", "v46", "v47", "v48", "v49", "v50", "v51", "v52", "v53", "v54", "v55", "v56", "v57", "v58", "v59", "v60", "v61", "v62", "v63", "v64", "v65", "v66", "v67", "v68", "v69", "v70", "v71", "v72", "v73", "v74", "v75", "v76", "v77", "v78", "v79", "v80", "v81", "v82", "v83", "v84", "v85", "v86", "v87", "v88", "v89", "v90", "v91", "v92", "v93", "v94", "v95", "v96", "v97", "v98", "v99", "v100", "v101", "v102", "v103", "v104", "v105", "v106", "v107", "v112", "v113", "v114", "v115", "v116", "v117", "v118", "v119", "v120", "v121", "v122", "v123", "v124", "v125", "v126", "v127", "v128", "v129", "v130", "v131", "v132", "v133", "v134", "v135", "v136", "v137", "v138", "v139", "v140", "v141", "v142", "v143", "v144", "v145", "v146", "v147", "v148", "v149", "v150", "v151", "v152", "v153", "v154", "v155", "v156", "v157", "v158", "v159", "v160", "v161", "v162", "v163", "v164", "v165", "v166", "v167"
#define CONV_C_ASM \
    "s_setprio 3\n" \
    "s_mov_b64 exec, %[pm]\n" \
    "global_load_dword v63, v62, %[xb]\n" \
    "s_mov_b64 exec, -1\n" \
    "s_mov_b32 s91, 0x05040100\n" \
    "s_mov_b32 s92, 0x07060302\n" \
    "s_mov_b64 s[96:97], %[o0]\n" \
    "s_mov_b64 s[94:95], %[x0]\n" \
    "v_mov_b64 v[160:161], 0\n" \
    "v_mov_b64 v[162:163], 0\n" \
    "s_waitcnt vmcnt(6)\n" \
    "s_mov_b32 s93, m0\n" \
    "v_lshrrev_b32_e32 v0, 3, v48\n" \
    "s_mov_b32 m0, %[bl]\n" \
    "s_sub_u32 s88, %[bl], s93\n" \
    "s_sub_u32 s88, s88, s93\n" \
    "global_load_lds_dwordx4 v0, %[bs]\n" \
    "s_add_u32 m0, s88, 4096\n" \
    "s_lshl_b32 s88, s93, 1\n" \
    "s_sub_u32 s88, 4096, s88\n" \
    "v_add_u32_e32 v0, s88, v0\n" \
    "global_load_lds_dwordx4 v0, %[bs]\n" \
    "s_mov_b32 m0, s93\n" \
    "ds_write_b128 v49, v[160:163]\n" \
    "ds_write_b128 v50, v[160:163]\n" \
    "s_waitcnt vmcnt(3)\n" \
    "v_add_u32_e32 v56, 91520, v33\n" \
    "s_mov_b64 exec, 1\n" \
    "v_mov_b32_e32 v160, v71\n" \
    "s_mov_b64 exec, -1\n" \
    "v_readfirstlane_b32 %[ep], v72\n" \
    "s_nop 2\n" \
    "s_add_u32 s98, %[ep], 1\n" \
    "s_max_u32 s98, s98, 1\n" \
    "s_mov_b32 s99, %[tg]\n" \
    "global_load_dwordx2 v[12:13], v41, s[94:95] offset:0\n" \
    "s_mov_b32 s90, 0x100000\n" \
    "Lcl0_%=:\n" \
    "ds_read_b32 v60, v32 offset:0\n" \
    "s_waitcnt lgkmcnt(0)\n" \
    "v_cmp_gt_u32_e32 vcc, 1, v60\n" \
    "s_cbranch_vccz Lce0_%=\n" \
    "s_sleep 2\n" \
    "s_sub_u32 s90, s90, 1\n" \
    "s_cbranch_scc0 Lcl0_%=\n" \
    "Lce0_%=:\n" \
    "ds_read_b128 v[96:99], v33 offset:0\n" \
    "ds_read_b128 v[100:103], v33 offset:64\n" \
    "ds_read_b128 v[104:107], v33 offset:128\n" \
    "ds_read_b128 v[108:111], v33 offset:192\n" \
    "ds_read_b128 v[112:115], v33 offset:256\n" \
    "ds_read_b128 v[116:119], v33 offset:320\n" \
    "ds_read_b128 v[120:123], v33 offset:384\n" \
    "ds_read_b128 v[124:127], v33 offset:448\n" \
    "global_load_dwordx2 v[14:15], v41, s[94:95] offset:1152\n" \
    "s_add_u32 s94, s94, 589824\n" \
    "s_addc_u32 s95, s95, 0\n" \
    "global_load_dwordx2 v[16:17], v41, s[94:95] offset:0\n" \
    "global_load_dwordx2 v[18:19], v41, s[94:95] offset:1152\n" \
    "s_add_u32 s94, s94, 589824\n" \
    "s_addc_u32 s95, s95, 0\n" \
    "s_mov_b32 s90, 0x100000\n" \
    "Lcl1_%=:\n" \
    "ds_read_b32 v60, v32 offset:0\n" \
    "s_waitcnt lgkmcnt(0)\n" \
    "v_cmp_gt_u32_e32 vcc, 2, v60\n" \
    "s_cbranch_vccz Lce1_%=\n" \
    "s_sleep 1\n" \
    "s_sub_u32 s90, s90, 1\n" \
    "s_cbranch_scc0 Lcl1_%=\n" \
    "Lce1_%=:\n" \
    "ds_read_b128 v[128:131], v33 offset:58240\n" \
    "ds_read_b128 v[132:135], v33 offset:58304\n" \
    "ds_read_b128 v[136:139], v33 offset:58368\n" \
    "ds_read_b128 v[140:143], v33 offset:58432\n" \
    "ds_read_b128 v[144:147], v33 offset:58496\n" \
    "ds_read_b128 v[148:151], v33 offset:58560\n" \
    "ds_read_b128 v[152:155], v33 offset:58624\n" \
    "ds_read_b128 v[156:159], v33 offset:58688\n" \
    "s_waitcnt lgkmcnt(6)\n" \
    "v_mfma_f32_16x16x32_bf16 v[160:163], v[128:131], v[96:99], v[160:163]\n" \
    "v_mfma_f32_16x16x32_bf16 v[160:163], v[132:135], v[100:103], v[160:163]\n" \
    "s_waitcnt lgkmcnt(4)\n" \
    "v_mfma_f32_16x16x32_bf16 v[160:163], v[136:139], v[104:107], v[160:163]\n" \
    "v_mfma_f32_16x16x32_bf16 v[160:163], v[140:143], v[108:111], v[160:163]\n" \
    "s_waitcnt lgkmcnt(2)\n" \
    "v_mfma_f32_16x16x32_bf16 v[160:163], v[144:147], v[112:115], v[160:163]\n" \
    "v_mfma_f32_16x16x32_bf16 v[160:163], v[148:151], v[116:119], v[160:163]\n" \
    "s_waitcnt lgkmcnt(0)\n" \
    "v_mfma_f32_16x16x32_bf16 v[160:163], v[152:155], v[120:123], v[160:163]\n" \
    "v_mfma_f32_16x16x32_bf16 v[160:163], v[156:159], v[124:127], v[160:163]\n" \
    "s_nop 7\n" \
    "v_cvt_pk_bf16_f32 v0, v163, v162\n" \
    "v_cvt_pk_bf16_f32 v1, v161, v160\n" \
    "v_cvt_pk_bf16_f32 v2, v162, v161\n" \
    "ds_write_b16 v35, v0\n" \
    "ds_write_b32 v35, v2 offset:2\n" \
    "ds_write_b16_d16_hi v35, v1 offset:6\n" \
    "ds_write_b32 v35, v0 offset:1206\n" \
    "ds_write_b32 v35, v1 offset:1210\n" \
    "v_readlane_b32 s93, v0, 63\n" \
    "v_mov_b32_e32 v61, 1\n" \
    "ds_write_addtid_b32 v61 offset:56160\n" \
    "global_load_dwordx2 v[20:21], v41, s[94:95] offset:0\n" \
    "global_load_dwordx2 v[22:23], v41, s[94:95] offset:1152\n" \
    "s_add_u32 s94, s94, 589824\n" \
    "s_addc_u32 s95, s95, 0\n" \
    "s_mov_b32 s90, 0x100000\n" \
    "Lcl2_%=:\n" \
    "ds_read_b32 v60, v32 offset:0\n" \
    "s_waitcnt lgkmcnt(0)\n" \
    "v_cmp_gt_u32_e32 vcc, 3, v60\n" \
    "s_cbranch_vccz Lce2_%=\n" \
    "s_sleep 1\n" \
    "s_sub_u32 s90, s90, 1\n" \
    "s_cbranch_scc0 Lcl2_%=\n" \
    "Lce2_%=:\n" \
    "ds_read_b128 v[128:131], v34 offset:0\n" \
    "ds_read_b128 v[132:135], v34 offset:64\n" \
    "ds_read_b128 v[136:139], v34 offset:128\n" \
    "ds_read_b128 v[140:143], v34 offset:192\n" \
    "ds_read_b128 v[144:147], v34 offset:256\n" \
    "ds_read_b128 v[148:151], v34 offset:320\n" \
    "ds_read_b128 v[152:155], v34 offset:384\n" \
    "ds_read_b128 v[156:159], v34 offset:448\n" \
    "s_waitcnt lgkmcnt(6)\n" \
    "v_mfma_f32_16x16x32_bf16 v[160:163], v[128:131], v[96:99], 0\n" \
    "v_mfma_f32_16x16x32_bf16 v[160:163], v[132:135], v[100:103], v[160:163]\n" \
    "s_waitcnt lgkmcnt(4)\n" \
    "v_mfma_f32_16x16x32_bf16 v[160:163], v[136:139], v[104:107], v[160:163]\n" \
    "v_mfma_f32_16x16x32_bf16 v[160:163], v[140:143], v[108:111], v[160:163]\n" \
    "s_waitcnt lgkmcnt(2)\n" \
    "v_mfma_f32_16x16x32_bf16 v[160:163], v[144:147], v[112:115], v[160:163]\n" \
    "v_mfma_f32_16x16x32_bf16 v[160:163], v[148:151], v[116:119], v[160:163]\n" \
    "s_waitcnt lgkmcnt(0)\n" \
    "v_mfma_f32_16x16x32_bf16 v[160:163], v[152:155], v[120:123], v[160:163]\n" \
    "v_mfma_f32_16x16x32_bf16 v[160:163], v[156:159], v[124:127], v[160:163]\n" \
    "ds_read_b32 v60, v32\n" \
    "s_nop 6\n" \
    "v_cvt_pk_bf16_f32 v1, v160, v161\n" \
    "v_cvt_pk_bf16_f32 v2, v162, v163\n" \
    "v_perm_b32 v6, v1, v1, v38\n" \
    "v_perm_b32 v7, s93, v1, v40\n" \
    "v_perm_b32 v4, v2, v2, v38\n" \
    "v_perm_b32 v5, v2, v2, v39\n" \
    "ds_write_b128 v36, v[4:7]\n" \
    "s_mov_b64 exec, 1\n" \
    "ds_write_b16 v51, v1 offset:1792\n" \
    "ds_write_b16 v51, v1 offset:1794\n" \
    "s_brev_b64 exec, 1\n" \
    "ds_write2_b32 v51, v4, v4 offset0:18 offset1:19\n" \
    "ds_write_b16 v51, v4 offset:1278\n" \
    "s_mov_b64 exec, -1\n" \
    "v_mov_b32_e32 v61, 2\n" \
    "ds_write_addtid_b32 v61 offset:56160\n" \
    "global_load_dwordx2 v[24:25], v41, s[94:95] offset:0\n" \
    "global_load_dwordx2 v[26:27], v41, s[94:95] offset:1152\n" \
    "v_mov_b32_e32 v61, 2\n" \
    "s_waitcnt vmcnt(6)\n" \
    "v_perm_b32 v8, v14, v12, s91\n" \
    "v_perm_b32 v9, v14, v12, s92\n" \
    "v_perm_b32 v10, v15, v13, s91\n" \
    "v_perm_b32 v11, v15, v13, s92\n" \
    "ds_write2st64_b32 v42, v8, v9 offset0:0 offset1:18\n" \
    "ds_write2st64_b32 v42, v10, v11 offset0:36 offset1:54\n" \
    "ds_write_addtid_b32 v61 offset:56416\n" \
    "v_mov_b32_e32 v61, 4\n" \
    "s_waitcnt vmcnt(4)\n" \
    "v_perm_b32 v8, v18, v16, s91\n" \
    "v_perm_b32 v9, v18, v16, s92\n" \
    "v_perm_b32 v10, v19, v17, s91\n" \
    "v_perm_b32 v11, v19, v17, s92\n" \
    "ds_write2st64_b32 v42, v8, v9 offset0:4 offset1:22\n" \
    "ds_write2st64_b32 v42, v10, v11 offset0:40 offset1:58\n" \
    "ds_write_addtid_b32 v61 offset:56416\n" \
    "s_waitcnt lgkmcnt(12)\n" \
    "v_cmp_gt_u32_e32 vcc, 4, v60\n" \
    "s_cbranch_vccz Lce3_%=\n" \
    "s_mov_b32 s90, 0x100000\n" \
    "Lcl3_%=:\n" \
    "s_sleep 1\n" \
    "ds_read_b32 v60, v32 offset:0\n" \
    "s_waitcnt lgkmcnt(0)\n" \
    "v_cmp_gt_u32_e32 vcc, 4, v60\n" \
    "s_cbranch_vccz Lce3_%=\n" \
    "s_sub_u32 s90, s90, 1\n" \
    "s_cbranch_scc0 Lcl3_%=\n" \
    "Lce3_%=:\n" \
    "ds_read_b128 v[128:131], v56 offset:0\n" \
    "ds_read_b128 v[132:135], v56 offset:64\n" \
    "ds_read_b128 v[136:139], v56 offset:128\n" \
    "ds_read_b128 v[140:143], v56 offset:192\n" \
    "ds_read_b128 v[144:147], v56 offset:256\n" \
    "ds_read_b128 v[148:151], v56 offset:320\n" \
    "ds_read_b128 v[152:155], v56 offset:384\n" \
    "ds_read_b128 v[156:159], v56 offset:448\n" \
    "s_waitcnt lgkmcnt(6)\n" \
    "v_mfma_f32_16x16x32_bf16 v[160:163], v[128:131], v[96:99], 0\n" \
    "v_mfma_f32_16x16x32_bf16 v[160:163], v[132:135], v[100:103], v[160:163]\n" \
    "s_waitcnt lgkmcnt(4)\n" \
    "v_mfma_f32_16x16x32_bf16 v[160:163], v[136:139], v[104:107], v[160:163]\n" \
    "v_mfma_f32_16x16x32_bf16 v[160:163], v[140:143], v[108:111], v[160:163]\n" \
    "s_waitcnt lgkmcnt(2)\n" \
    "v_mfma_f32_16x16x32_bf16 v[160:163], v[144:147], v[112:115], v[160:163]\n" \
    "v_mfma_f32_16x16x32_bf16 v[160:163], v[148:151], v[116:119], v[160:163]\n" \
    "s_waitcnt lgkmcnt(0)\n" \
    "v_mfma_f32_16x16x32_bf16 v[160:163], v[152:155], v[120:123], v[160:163]\n" \
    "v_mfma_f32_16x16x32_bf16 v[160:163], v[156:159], v[124:127], v[160:163]\n" \
    "s_nop 7\n" \
    "v_cvt_pk_bf16_f32 v0, v160, v161\n" \
    "v_cvt_pk_bf16_f32 v1, v162, v163\n" \
    "ds_write_b16 v37, v0 offset:96\n" \
    "ds_write_b16_d16_hi v37, v0 offset:64\n" \
    "ds_write_b16 v37, v1 offset:32\n" \
    "ds_write_b16_d16_hi v37, v1\n" \
    "v_mov_b32_e32 v61, 3\n" \
    "ds_write_addtid_b32 v61 offset:56160\n" \
    "global_load_dwordx4 v[80:83], v44, %[ws] sc1\n" \
    "v_add_u32_e32 v44, 0x1000, v44\n" \
    "global_load_dwordx4 v[84:87], v44, %[ws] sc1\n" \
    "v_add_u32_e32 v44, 0x1000, v44\n" \
    "global_load_dwordx4 v[88:91], v44, %[ws] sc1\n" \
    "v_add_u32_e32 v44, 0x1000, v44\n" \
    "global_load_dwordx4 v[92:95], v44, %[ws] sc1\n" \
    "v_add_u32_e32 v44, 0xffffd000, v44\n" \
    "v_mov_b32_e32 v61, 6\n" \
    "s_waitcnt vmcnt(6)\n" \
    "v_perm_b32 v8, v22, v20, s91\n" \
    "v_perm_b32 v9, v22, v20, s92\n" \
    "v_perm_b32 v10, v23, v21, s91\n" \
    "v_perm_b32 v11, v23, v21, s92\n" \
    "ds_write2st64_b32 v42, v8, v9 offset0:8 offset1:26\n" \
    "ds_write2st64_b32 v42, v10, v11 offset0:44 offset1:62\n" \
    "ds_write_addtid_b32 v61 offset:56416\n" \
    "v_mov_b32_e32 v61, 8\n" \
    "s_waitcnt vmcnt(4)\n" \
    "v_perm_b32 v8, v26, v24, s91\n" \
    "v_perm_b32 v9, v26, v24, s92\n" \
    "v_perm_b32 v10, v27, v25, s91\n" \
    "v_perm_b32 v11, v27, v25, s92\n" \
    "ds_write2st64_b32 v42, v8, v9 offset0:12 offset1:30\n" \
    "ds_write2st64_b32 v42, v10, v11 offset0:48 offset1:66\n" \
    "ds_write_addtid_b32 v61 offset:56416\n" \
    "s_mov_b32 s90, 0x100000\n" \
    "Lcl4_%=:\n" \
    "ds_read_b32 v60, v45 offset:2336\n" \
    "s_waitcnt lgkmcnt(0)\n" \
    "v_cmp_gt_u32_e32 vcc, 2, v60\n" \
    "s_cbranch_vccz Lce4_%=\n" \
    "s_sleep 1\n" \
    "s_sub_u32 s90, s90, 1\n" \
    "s_cbranch_scc0 Lcl4_%=\n" \
    "Lce4_%=:\n" \
    "v_mov_b32_e32 v0, %[ak]\n" \
    "v_mov_b32_e32 v1, s98\n" \
    "s_mov_b32 exec_lo, %[am]\n" \
    "s_mov_b32 exec_hi, 0\n" \
    "global_store_dword v0, v1, %[ws] sc1\n" \
    "s_mov_b64 exec, -1\n" \
    "v_min_u32_e32 v57, %[wl], v48\n" \
    "global_load_dword v63, v57, %[wd]\n" \
    "v_add_u32_e32 v57, 0x2000, v48\n" \
    "v_min_u32_e32 v57, %[wl], v57\n" \
    "global_load_dword v63, v57, %[wd]\n" \
    "s_mov_b32 s93, 0xffff0000\n" \
    "v_add_u32_e32 v58, 58240, v43\n" \
    "s_waitcnt vmcnt(6)\n" \
    "v_cmp_ne_u32_e32 vcc, s98, v81\n" \
    "v_cmp_ne_u32_e64 s[88:89], s98, v83\n" \
    "s_or_b64 vcc, vcc, s[88:89]\n" \
    "s_cbranch_scc1 Lcs5_%=\n" \
    "Lce5_%=:\n" \
    "v_cvt_f32_f16_e32 v104, v80\n" \
    "v_cvt_f32_f16_e32 v105, v82\n" \
    "v_lshlrev_b32_e32 v96, 16, v12\n" \
    "v_and_b32_e32 v97, s93, v12\n" \
    "v_lshlrev_b32_e32 v98, 16, v13\n" \
    "v_and_b32_e32 v99, s93, v13\n" \
    "v_lshlrev_b32_e32 v100, 16, v14\n" \
    "v_and_b32_e32 v101, s93, v14\n" \
    "v_lshlrev_b32_e32 v102, 16, v15\n" \
    "v_and_b32_e32 v103, s93, v15\n" \
    "v_sub_f32_e32 v106, 1.0, v104\n" \
    "v_sub_f32_e32 v107, 1.0, v105\n" \
    "v_mul_f32_e32 v96, v106, v96\n" \
    "v_mul_f32_e32 v97, v106, v97\n" \
    "v_mul_f32_e32 v98, v106, v98\n" \
    "v_mul_f32_e32 v99, v106, v99\n" \
    "v_mul_f32_e32 v100, v107, v100\n" \
    "v_mul_f32_e32 v101, v107, v101\n" \
    "v_mul_f32_e32 v102, v107, v102\n" \
    "v_mul_f32_e32 v103, v107, v103\n" \
    "s_mov_b32 s90, 0x100000\n" \
    "Lcl6_%=:\n" \
    "ds_read_b32 v60, v45 offset:0\n" \
    "ds_read_b64 v[64:65], v43 offset:0\n" \
    "ds_read_b64 v[66:67], v43 offset:512\n" \
    "ds_read_b64 v[68:69], v43 offset:1024\n" \
    "ds_read_b64 v[70:71], v43 offset:1536\n" \
    "s_waitcnt lgkmcnt(0)\n" \
    "v_cmp_gt_u32_e32 vcc, s99, v60\n" \
    "s_cbranch_vccz Lce6_%=\n" \
    "s_sleep 2\n" \
    "s_sub_u32 s90, s90, 1\n" \
    "s_cbranch_scc0 Lcl6_%=\n" \
    "Lce6_%=:\n" \
    "v_fmac_f32_e32 v96, v104, v64\n" \
    "v_fmac_f32_e32 v97, v104, v66\n" \
    "v_fmac_f32_e32 v98, v104, v68\n" \
    "v_fmac_f32_e32 v99, v104, v70\n" \
    "v_cvt_pk_bf16_f32 v108, v96, v97\n" \
    "v_cvt_pk_bf16_f32 v109, v98, v99\n" \
    "global_store_dwordx2 v41, v[108:109], s[96:97] offset:0\n" \
    "v_fmac_f32_e32 v100, v105, v65\n" \
    "v_fmac_f32_e32 v101, v105, v67\n" \
    "v_fmac_f32_e32 v102, v105, v69\n" \
    "v_fmac_f32_e32 v103, v105, v71\n" \
    "v_cvt_pk_bf16_f32 v110, v100, v101\n" \
    "v_cvt_pk_bf16_f32 v111, v102, v103\n" \
    "global_store_dwordx2 v41, v[110:111], s[96:97] offset:1152\n" \
    "s_add_u32 s96, s96, 589824\n" \
    "s_addc_u32 s97, s97, 0\n" \
    "s_add_u32 s99, s99, 2\n" \
    "v_lshrrev_b32_e32 v1, 2, v44\n" \
    "v_add_u32_e32 v1, %[gt], v1\n" \
    "v_perm_b32 v2, v82, v80, s91\n" \
    "ds_write_b32 v1, v2 offset:0\n" \
    "s_waitcnt vmcnt(7)\n" \
    "v_cmp_ne_u32_e32 vcc, s98, v85\n" \
    "v_cmp_ne_u32_e64 s[88:89], s98, v87\n" \
    "s_or_b64 vcc, vcc, s[88:89]\n" \
    "s_cbranch_scc1 Lcs7_%=\n" \
    "Lce7_%=:\n" \
    "v_perm_b32 v2, v86, v84, s91\n" \
    "ds_write_b32 v1, v2 offset:1024\n" \
    "s_waitcnt vmcnt(6)\n" \
    "v_cmp_ne_u32_e32 vcc, s98, v89\n" \
    "v_cmp_ne_u32_e64 s[88:89], s98, v91\n" \
    "s_or_b64 vcc, vcc, s[88:89]\n" \
    "s_cbranch_scc1 Lcs8_%=\n" \
    "Lce8_%=:\n" \
    "v_perm_b32 v2, v90, v88, s91\n" \
    "ds_write_b32 v1, v2 offset:2048\n" \
    "s_waitcnt vmcnt(5)\n" \
    "v_cmp_ne_u32_e32 vcc, s98, v93\n" \
    "v_cmp_ne_u32_e64 s[88:89], s98, v95\n" \
    "s_or_b64 vcc, vcc, s[88:89]\n" \
    "s_cbranch_scc1 Lcs9_%=\n" \
    "Lce9_%=:\n" \
    "v_perm_b32 v2, v94, v92, s91\n" \
    "ds_write_b32 v1, v2 offset:3072\n" \
    "v_mov_b32_e32 v61, 9\n" \
    "ds_write_addtid_b32 v61 offset:56416\n" \
    "v_cvt_f32_f16_e32 v104, v84\n" \
    "v_cvt_f32_f16_e32 v105, v86\n" \
    "v_lshlrev_b32_e32 v96, 16, v16\n" \
    "v_and_b32_e32 v97, s93, v16\n" \
    "v_lshlrev_b32_e32 v98, 16, v17\n" \
    "v_and_b32_e32 v99, s93, v17\n" \
    "v_lshlrev_b32_e32 v100, 16, v18\n" \
    "v_and_b32_e32 v101, s93, v18\n" \
    "v_lshlrev_b32_e32 v102, 16, v19\n" \
    "v_and_b32_e32 v103, s93, v19\n" \
    "v_sub_f32_e32 v106, 1.0, v104\n" \
    "v_sub_f32_e32 v107, 1.0, v105\n" \
    "v_mul_f32_e32 v96, v106, v96\n" \
    "v_mul_f32_e32 v97, v106, v97\n" \
    "v_mul_f32_e32 v98, v106, v98\n" \
    "v_mul_f32_e32 v99, v106, v99\n" \
    "v_mul_f32_e32 v100, v107, v100\n" \
    "v_mul_f32_e32 v101, v107, v101\n" \
    "v_mul_f32_e32 v102, v107, v102\n" \
    "v_mul_f32_e32 v103, v107, v103\n" \
    "s_mov_b32 s90, 0x100000\n" \
    "Lcl10_%=:\n" \
    "ds_read_b32 v60, v47 offset:0\n" \
    "ds_read_b64 v[64:65], v43 offset:8320\n" \
    "ds_read_b64 v[66:67], v43 offset:8832\n" \
    "ds_read_b64 v[68:69], v43 offset:9344\n" \
    "ds_read_b64 v[70:71], v43 offset:9856\n" \
    "ds_read_b64 v[72:73], v58 offset:0\n" \
    "ds_read_b64 v[74:75], v58 offset:512\n" \
    "ds_read_b64 v[76:77], v58 offset:1024\n" \
    "ds_read_b64 v[78:79], v58 offset:1536\n" \
    "s_waitcnt lgkmcnt(0)\n" \
    "v_cmp_gt_u32_e32 vcc, s99, v60\n" \
    "s_cbranch_vccz Lce10_%=\n" \
    "s_sleep 2\n" \
    "s_sub_u32 s90, s90, 1\n" \
    "s_cbranch_scc0 Lcl10_%=\n" \
    "Lce10_%=:\n" \
    "v_fmac_f32_e32 v96, v104, v64\n" \
    "v_fmac_f32_e32 v97, v104, v66\n" \
    "v_fmac_f32_e32 v98, v104, v68\n" \
    "v_fmac_f32_e32 v99, v104, v70\n" \
    "v_fmac_f32_e32 v96, v104, v72\n" \
    "v_fmac_f32_e32 v97, v104, v74\n" \
    "v_fmac_f32_e32 v98, v104, v76\n" \
    "v_fmac_f32_e32 v99, v104, v78\n" \
    "v_cvt_pk_bf16_f32 v112, v96, v97\n" \
    "v_cvt_pk_bf16_f32 v113, v98, v99\n" \
    "global_store_dwordx2 v41, v[112:113], s[96:97] offset:0\n" \
    "v_fmac_f32_e32 v100, v105, v65\n" \
    "v_fmac_f32_e32 v101, v105, v67\n" \
    "v_fmac_f32_e32 v102, v105, v69\n" \
    "v_fmac_f32_e32 v103, v105, v71\n" \
    "v_fmac_f32_e32 v100, v105, v73\n" \
    "v_fmac_f32_e32 v101, v105, v75\n" \
    "v_fmac_f32_e32 v102, v105, v77\n" \
    "v_fmac_f32_e32 v103, v105, v79\n" \
    "v_cvt_pk_bf16_f32 v114, v100, v101\n" \
    "v_cvt_pk_bf16_f32 v115, v102, v103\n" \
    "global_store_dwordx2 v41, v[114:115], s[96:97] offset:1152\n" \
    "s_add_u32 s96, s96, 589824\n" \
    "s_addc_u32 s97, s97, 0\n" \
    "s_add_u32 s99, s99, 2\n" \
    "v_cvt_f32_f16_e32 v104, v88\n" \
    "v_cvt_f32_f16_e32 v105, v90\n" \
    "v_lshlrev_b32_e32 v96, 16, v20\n" \
    "v_and_b32_e32 v97, s93, v20\n" \
    "v_lshlrev_b32_e32 v98, 16, v21\n" \
    "v_and_b32_e32 v99, s93, v21\n" \
    "v_lshlrev_b32_e32 v100, 16, v22\n" \
    "v_and_b32_e32 v101, s93, v22\n" \
    "v_lshlrev_b32_e32 v102, 16, v23\n" \
    "v_and_b32_e32 v103, s93, v23\n" \
    "v_sub_f32_e32 v106, 1.0, v104\n" \
    "v_sub_f32_e32 v107, 1.0, v105\n" \
    "v_mul_f32_e32 v96, v106, v96\n" \
    "v_mul_f32_e32 v97, v106, v97\n" \
    "v_mul_f32_e32 v98, v106, v98\n" \
    "v_mul_f32_e32 v99, v106, v99\n" \
    "v_mul_f32_e32 v100, v107, v100\n" \
    "v_mul_f32_e32 v101, v107, v101\n" \
    "v_mul_f32_e32 v102, v107, v102\n" \
    "v_mul_f32_e32 v103, v107, v103\n" \
    "s_mov_b32 s90, 0x100000\n" \
    "Lcl11_%=:\n" \
    "ds_read_b32 v60, v47 offset:0\n" \
    "ds_read_b64 v[64:65], v43 offset:16640\n" \
    "ds_read_b64 v[66:67], v43 offset:17152\n" \
    "ds_read_b64 v[68:69], v43 offset:17664\n" \
    "ds_read_b64 v[70:71], v43 offset:18176\n" \
    "ds_read_b64 v[72:73], v58 offset:8320\n" \
    "ds_read_b64 v[74:75], v58 offset:8832\n" \
    "ds_read_b64 v[76:77], v58 offset:9344\n" \
    "ds_read_b64 v[78:79], v58 offset:9856\n" \
    "s_waitcnt lgkmcnt(0)\n" \
    "v_cmp_gt_u32_e32 vcc, s99, v60\n" \
    "s_cbranch_vccz Lce11_%=\n" \
    "s_sleep 2\n" \
    "s_sub_u32 s90, s90, 1\n" \
    "s_cbranch_scc0 Lcl11_%=\n" \
    "Lce11_%=:\n" \
    "v_fmac_f32_e32 v96, v104, v64\n" \
    "v_fmac_f32_e32 v97, v104, v66\n" \
    "v_fmac_f32_e32 v98, v104, v68\n" \
    "v_fmac_f32_e32 v99, v104, v70\n" \
    "v_fmac_f32_e32 v96, v104, v72\n" \
    "v_fmac_f32_e32 v97, v104, v74\n" \
    "v_fmac_f32_e32 v98, v104, v76\n" \
    "v_fmac_f32_e32 v99, v104, v78\n" \
    "v_cvt_pk_bf16_f32 v108, v96, v97\n" \
    "v_cvt_pk_bf16_f32 v109, v98, v99\n" \
    "global_store_dwordx2 v41, v[108:109], s[96:97] offset:0\n" \
    "v_fmac_f32_e32 v100, v105, v65\n" \
    "v_fmac_f32_e32 v101, v105, v67\n" \
    "v_fmac_f32_e32 v102, v105, v69\n" \
    "v_fmac_f32_e32 v103, v105, v71\n" \
    "v_fmac_f32_e32 v100, v105, v73\n" \
    "v_fmac_f32_e32 v101, v105, v75\n" \
    "v_fmac_f32_e32 v102, v105, v77\n" \
    "v_fmac_f32_e32 v103, v105, v79\n" \
    "v_cvt_pk_bf16_f32 v110, v100, v101\n" \
    "v_cvt_pk_bf16_f32 v111, v102, v103\n" \
    "global_store_dwordx2 v41, v[110:111], s[96:97] offset:1152\n" \
    "s_add_u32 s96, s96, 589824\n" \
    "s_addc_u32 s97, s97, 0\n" \
    "s_add_u32 s99, s99, 2\n" \
    "v_cvt_f32_f16_e32 v104, v92\n" \
    "v_cvt_f32_f16_e32 v105, v94\n" \
    "v_lshlrev_b32_e32 v96, 16, v24\n" \
    "v_and_b32_e32 v97, s93, v24\n" \
    "v_lshlrev_b32_e32 v98, 16, v25\n" \
    "v_and_b32_e32 v99, s93, v25\n" \
    "v_lshlrev_b32_e32 v100, 16, v26\n" \
    "v_and_b32_e32 v101, s93, v26\n" \
    "v_lshlrev_b32_e32 v102, 16, v27\n" \
    "v_and_b32_e32 v103, s93, v27\n" \
    "v_sub_f32_e32 v106, 1.0, v104\n" \
    "v_sub_f32_e32 v107, 1.0, v105\n" \
    "v_mul_f32_e32 v96, v106, v96\n" \
    "v_mul_f32_e32 v97, v106, v97\n" \
    "v_mul_f32_e32 v98, v106, v98\n" \
    "v_mul_f32_e32 v99, v106, v99\n" \
    "v_mul_f32_e32 v100, v107, v100\n" \
    "v_mul_f32_e32 v101, v107, v101\n" \
    "v_mul_f32_e32 v102, v107, v102\n" \
    "v_mul_f32_e32 v103, v107, v103\n" \
    "s_mov_b32 s90, 0x100000\n" \
    "Lcl12_%=:\n" \
    "ds_read_b32 v60, v47 offset:0\n" \
    "ds_read_b64 v[64:65], v43 offset:24960\n" \
    "ds_read_b64 v[66:67], v43 offset:25472\n" \
    "ds_read_b64 v[68:69], v43 offset:25984\n" \
    "ds_read_b64 v[70:71], v43 offset:26496\n" \
    "ds_read_b64 v[72:73], v58 offset:16640\n" \
    "ds_read_b64 v[74:75], v58 offset:17152\n" \
    "ds_read_b64 v[76:77], v58 offset:17664\n" \
    "ds_read_b64 v[78:79], v58 offset:18176\n" \
    "s_waitcnt lgkmcnt(0)\n" \
    "v_cmp_gt_u32_e32 vcc, s99, v60\n" \
    "s_cbranch_vccz Lce12_%=\n" \
    "s_sub_u32 s90, s90, 1\n" \
    "s_cbranch_scc0 Lcl12_%=\n" \
    "Lce12_%=:\n" \
    "v_fmac_f32_e32 v96, v104, v64\n" \
    "v_fmac_f32_e32 v97, v104, v66\n" \
    "v_fmac_f32_e32 v98, v104, v68\n" \
    "v_fmac_f32_e32 v99, v104, v70\n" \
    "v_fmac_f32_e32 v96, v104, v72\n" \
    "v_fmac_f32_e32 v97, v104, v74\n" \
    "v_fmac_f32_e32 v98, v104, v76\n" \
    "v_fmac_f32_e32 v99, v104, v78\n" \
    "v_cvt_pk_bf16_f32 v112, v96, v97\n" \
    "v_cvt_pk_bf16_f32 v113, v98, v99\n" \
    "global_store_dwordx2 v41, v[112:113], s[96:97] offset:0\n" \
    "v_fmac_f32_e32 v100, v105, v65\n" \
    "v_fmac_f32_e32 v101, v105, v67\n" \
    "v_fmac_f32_e32 v102, v105, v69\n" \
    "v_fmac_f32_e32 v103, v105, v71\n" \
    "v_fmac_f32_e32 v100, v105, v73\n" \
    "v_fmac_f32_e32 v101, v105, v75\n" \
    "v_fmac_f32_e32 v102, v105, v77\n" \
    "v_fmac_f32_e32 v103, v105, v79\n" \
    "v_cvt_pk_bf16_f32 v114, v100, v101\n" \
    "v_cvt_pk_bf16_f32 v115, v102, v103\n" \
    "global_store_dwordx2 v41, v[114:115], s[96:97] offset:1152\n" \
    "v_mov_b32_e32 v61, 20\n" \
    "s_waitcnt vmcnt(0)\n" \
    "ds_write_addtid_b32 v61 offset:56416\n" \
    "s_branch Lcx_%=\n" \
    "Lcs5_%=:\n" \
    "s_mov_b32 s90, 0x20000\n" \
    "v_add_u32_e32 v0, 0, v44\n" \
    "Lcl5_%=:\n" \
    "s_sleep 4\n" \
    "global_load_dwordx4 v[80:83], v0, %[ws] sc1\n" \
    "s_waitcnt vmcnt(0)\n" \
    "v_cmp_ne_u32_e32 vcc, s98, v81\n" \
    "v_cmp_ne_u32_e64 s[88:89], s98, v83\n" \
    "s_nop 2\n" \
    "s_or_b64 vcc, vcc, s[88:89]\n" \
    "s_cbranch_scc0 Lce5_%=\n" \
    "s_sub_u32 s90, s90, 1\n" \
    "s_cbranch_scc0 Lcl5_%=\n" \
    "s_branch Lce5_%=\n" \
    "Lcs7_%=:\n" \
    "s_mov_b32 s90, 0x20000\n" \
    "v_add_u32_e32 v0, 4096, v44\n" \
    "Lcl7_%=:\n" \
    "s_sleep 4\n" \
    "global_load_dwordx4 v[84:87], v0, %[ws] sc1\n" \
    "s_waitcnt vmcnt(0)\n" \
    "v_cmp_ne_u32_e32 vcc, s98, v85\n" \
    "v_cmp_ne_u32_e64 s[88:89], s98, v87\n" \
    "s_nop 2\n" \
    "s_or_b64 vcc, vcc, s[88:89]\n" \
    "s_cbranch_scc0 Lce7_%=\n" \
    "s_sub_u32 s90, s90, 1\n" \
    "s_cbranch_scc0 Lcl7_%=\n" \
    "s_branch Lce7_%=\n" \
    "Lcs8_%=:\n" \
    "s_mov_b32 s90, 0x20000\n" \
    "v_add_u32_e32 v0, 8192, v44\n" \
    "Lcl8_%=:\n" \
    "s_sleep 4\n" \
    "global_load_dwordx4 v[88:91], v0, %[ws] sc1\n" \
    "s_waitcnt vmcnt(0)\n" \
    "v_cmp_ne_u32_e32 vcc, s98, v89\n" \
    "v_cmp_ne_u32_e64 s[88:89], s98, v91\n" \
    "s_nop 2\n" \
    "s_or_b64 vcc, vcc, s[88:89]\n" \
    "s_cbranch_scc0 Lce8_%=\n" \
    "s_sub_u32 s90, s90, 1\n" \
    "s_cbranch_scc0 Lcl8_%=\n" \
    "s_branch Lce8_%=\n" \
    "Lcs9_%=:\n" \
    "s_mov_b32 s90, 0x20000\n" \
    "v_add_u32_e32 v0, 12288, v44\n" \
    "Lcl9_%=:\n" \
    "s_sleep 4\n" \
    "global_load_dwordx4 v[92:95], v0, %[ws] sc1\n" \
    "s_waitcnt vmcnt(0)\n" \
    "v_cmp_ne_u32_e32 vcc, s98, v93\n" \
    "v_cmp_ne_u32_e64 s[88:89], s98, v95\n" \
    "s_nop 2\n" \
    "s_or_b64 vcc, vcc, s[88:89]\n" \
    "s_cbranch_scc0 Lce9_%=\n" \
    "s_sub_u32 s90, s90, 1\n" \
    "s_cbranch_scc0 Lcl9_%=\n" \
    "s_branch Lce9_%=\n" \
    "Lcx_%=:\n"
#define CONV_C_CLOB "v0", "v1", "v2", "v4", "v5", "v6", "v7", "v8", "v9", "v10", "v11", "v12", "v13", "v14", "v15", "v16", "v17", "v18", "v19", "v20", "v21", "v22", "v23", "v24", "v25", "v26", "v27", "v32", "v33", "v34", "v35", "v36", "v37", "v38", "v39", "v40", "v41", "v42", "v43", "v44", "v45", "v47", "v48", "v49", "v50", "v51", "v56", "v57", "v58", "v60", "v61", "v62", "v63", "v64", "v65", "v66", "v67", "v68", "v69", "v70", "v71", "v72", "v73", "v74", "v75", "v76", "v77", "v78", "v79", "v80", "v81", "v82", "v83", "v84", "v85", "v86", "v87", "v88", "v89", "v90", "v91", "v92", "v93", "v94", "v95", "v96", "v97", "v98", "v99", "v100", "v101", "v102", "v103", "v104", "v105", "v106", "v107", "v108", "v109", "v110", "v111", "v112", "v113", "v114", "v115", "v116", "v117", "v118", "v119", "v120", "v121", "v122", "v123", "v124", "v125", "v126", "v127", "v128", "v129", "v130", "v131", "v132", "v133", "v134", "v135", "v136", "v137", "v138", "v139", "v140", "v141", "v142", "v143", "v144", "v145", "v146", "v147", "v148", "v149", "v150", "v151", "v152", "v153", "v154", "v155", "v156", "v157", "v158", "v159", "v160", "v161", "v162", "v163"
#define ITEM_ASM \
    "v_mbcnt_lo_u32_b32 v53, -1, 0\n" \
    "v_mbcnt_hi_u32_b32 v53, -1, v53\n" \
    "s_lshl_b32 s88, %[it], 7\n" \
    "s_add_u32 s89, s88, %[kb]\n" \
    "s_add_u32 s88, s88, %[cb]\n" \
    "v_and_b32_e32 v28, 3, v53\n" \
    "v_bfe_u32 v31, v53, 2, 2\n" \
    "v_lshrrev_b32_e32 v52, 4, v53\n" \
    "v_lshlrev_b32_e32 v30, 3, v31\n" \
    "v_lshl_or_b32 v30, v28, 5, v30\n" \
    "v_add_u32_e32 v30, s89, v30\n" \
    "v_lshlrev_b32_e32 v28, 4, v28\n" \
    "v_lshl_or_b32 v28, v52, 6, v28\n" \
    "v_and_b32_e32 v52, 1, v31\n" \
    "v_mul_u32_u24_e32 v52, 1152, v52\n" \
    "s_mov_b32 s89, %[wl]\n" \
    "v_add3_u32 v28, v28, v52, s89\n" \
    "v_lshlrev_b32_e32 v29, 14, v53\n" \
    "v_add_u32_e32 v29, s88, v29\n" \
    "v_sub_u32_e32 v31, 15, v31\n" \
    "v_mov_b64 v[16:17], 0\n" \
    "v_mov_b64 v[18:19], 0\n" \
    "v_mov_b64 v[20:21], 0\n" \
    "v_mov_b64 v[22:23], 0\n" \
    "s_mov_b32 vcc_lo, 0xaaaaaaaa\n" \
    "v_mov_b32_e32 v52, 0x07060302\n" \
    "s_mov_b32 s88, 0x05040100\n" \
    "v_mov_b32_e32 v53, s88\n" \
    "v_cndmask_b32_e32 v52, v53, v52, vcc\n" \
    "s_mov_b32 s88, 0x80008000\n" \
    "s_mov_b32 s89, 0xffff0000\n" \
    "s_mov_b32 s92, 0x07060304\n" \
    "s_mov_b32 s93, 0x07060204\n" \
    "s_mov_b32 vcc_lo, 0xcccccccc\n" \
    "v_mov_b32_e32 v91, %[fl]\n" \
    "v_mov_b32_e32 v92, 1\n" \
    "s_waitcnt vmcnt(18)\n" \
    "ds_read_b128 v[0:3], v28 offset:0\n" \
    "ds_read_b128 v[4:7], v28 offset:256\n" \
    "ds_read_b128 v[8:11], v28 offset:512\n" \
    "ds_read_b128 v[12:15], v28 offset:768\n" \
    "s_mov_b32 exec_hi, 0\n" \
    "ds_read_b128 v[16:19], v28 offset:1024\n" \
    "s_mov_b64 exec, -1\n" \
    "s_mov_b32 exec_lo, 0\n" \
    "ds_read_b128 v[20:23], v28 offset:896\n" \
    "s_mov_b32 exec_lo, -1\n" \
    "v_readfirstlane_b32 s98, v72\n" \
    "v_add_u32_e32 v54, -4, v31\n" \
    "v_add_u32_e32 v55, -8, v31\n" \
    "v_add_u32_e32 v90, -12, v31\n" \
    "s_add_u32 s98, s98, 1\n" \
    "s_max_u32 s98, s98, 1\n" \
    "v_mov_b32_e32 v25, s98\n" \
    "v_mov_b32_e32 v27, s98\n" \
    "s_waitcnt vmcnt(17)\n" \
    "s_waitcnt lgkmcnt(5)\n" \
    "v_mfma_f32_16x16x32_bf16 v[74:77], v[0:3], v[96:99], 0\n" \
    "s_waitcnt vmcnt(16)\n" \
    "v_mfma_f32_16x16x32_bf16 v[78:81], v[0:3], v[112:115], 0\n" \
    "s_waitcnt vmcnt(15)\n" \
    "v_mfma_f32_16x16x32_bf16 v[82:85], v[0:3], v[128:131], 0\n" \
    "s_waitcnt vmcnt(14)\n" \
    "v_mfma_f32_16x16x32_bf16 v[86:89], v[0:3], v[144:147], 0\n" \
    "s_waitcnt vmcnt(13)\n" \
    "s_waitcnt lgkmcnt(4)\n" \
    "v_mfma_f32_16x16x32_bf16 v[74:77], v[4:7], v[100:103], v[74:77]\n" \
    "s_waitcnt vmcnt(12)\n" \
    "v_mfma_f32_16x16x32_bf16 v[78:81], v[4:7], v[116:119], v[78:81]\n" \
    "s_waitcnt vmcnt(11)\n" \
    "v_mfma_f32_16x16x32_bf16 v[82:85], v[4:7], v[132:135], v[82:85]\n" \
    "s_waitcnt vmcnt(10)\n" \
    "v_mfma_f32_16x16x32_bf16 v[86:89], v[4:7], v[148:151], v[86:89]\n" \
    "s_waitcnt vmcnt(9)\n" \
    "s_waitcnt lgkmcnt(3)\n" \
    "v_mfma_f32_16x16x32_bf16 v[74:77], v[8:11], v[104:107], v[74:77]\n" \
    "s_waitcnt vmcnt(8)\n" \
    "v_mfma_f32_16x16x32_bf16 v[78:81], v[8:11], v[120:123], v[78:81]\n" \
    "s_waitcnt vmcnt(7)\n" \
    "v_mfma_f32_16x16x32_bf16 v[82:85], v[8:11], v[136:139], v[82:85]\n" \
    "s_waitcnt vmcnt(6)\n" \
    "v_mfma_f32_16x16x32_bf16 v[86:89], v[8:11], v[152:155], v[86:89]\n" \
    "s_waitcnt vmcnt(5)\n" \
    "s_waitcnt lgkmcnt(2)\n" \
    "v_mfma_f32_16x16x32_bf16 v[74:77], v[12:15], v[108:111], v[74:77]\n" \
    "s_waitcnt vmcnt(4)\n" \
    "v_mfma_f32_16x16x32_bf16 v[78:81], v[12:15], v[124:127], v[78:81]\n" \
    "s_waitcnt vmcnt(3)\n" \
    "v_mfma_f32_16x16x32_bf16 v[82:85], v[12:15], v[140:143], v[82:85]\n" \
    "s_waitcnt vmcnt(2)\n" \
    "v_mfma_f32_16x16x32_bf16 v[86:89], v[12:15], v[156:159], v[86:89]\n" \
    "s_waitcnt vmcnt(1)\n" \
    "s_waitcnt lgkmcnt(1)\n" \
    "v_mfma_f32_16x16x32_bf16 v[74:77], v[16:19], v[160:163], v[74:77]\n" \
    "s_waitcnt lgkmcnt(0)\n" \
    "v_mfma_f32_16x16x32_bf16 v[78:81], v[20:23], v[160:163], v[78:81]\n" \
    "s_waitcnt vmcnt(0)\n" \
    "v_mfma_f32_16x16x32_bf16 v[82:85], v[16:19], v[164:167], v[82:85]\n" \
    "v_mfma_f32_16x16x32_bf16 v[86:89], v[20:23], v[164:167], v[86:89]\n" \
    "s_nop 2\n" \
    "v_mov_b32_dpp v0, v74 quad_perm:[0,0,0,0] row_mask:0xf bank_mask:0xf\n" \
    "s_nop 0\n" \
    "v_mov_b32_dpp v1, v78 quad_perm:[0,0,0,0] row_mask:0xf bank_mask:0xf\n" \
    "s_nop 0\n" \
    "v_mov_b32_dpp v2, v82 quad_perm:[0,0,0,0] row_mask:0xf bank_mask:0xf\n" \
    "v_mov_b32_dpp v3, v86 quad_perm:[0,0,0,0] row_mask:0xf bank_mask:0xf\n" \
    "v_add_f32_dpp v0, v75, v0 quad_perm:[1,1,1,1] row_mask:0xf bank_mask:0xf\n" \
    "v_add_f32_dpp v1, v79, v1 quad_perm:[1,1,1,1] row_mask:0xf bank_mask:0xf\n" \
    "v_add_f32_dpp v2, v83, v2 quad_perm:[1,1,1,1] row_mask:0xf bank_mask:0xf\n" \
    "v_add_f32_dpp v3, v87, v3 quad_perm:[1,1,1,1] row_mask:0xf bank_mask:0xf\n" \
    "v_add_f32_dpp v0, v76, v0 quad_perm:[2,2,2,2] row_mask:0xf bank_mask:0xf\n" \
    "v_add_f32_dpp v1, v80, v1 quad_perm:[2,2,2,2] row_mask:0xf bank_mask:0xf\n" \
    "v_add_f32_dpp v2, v84, v2 quad_perm:[2,2,2,2] row_mask:0xf bank_mask:0xf\n" \
    "v_add_f32_dpp v3, v88, v3 quad_perm:[2,2,2,2] row_mask:0xf bank_mask:0xf\n" \
    "v_add_f32_dpp v0, v77, v0 quad_perm:[3,3,3,3] row_mask:0xf bank_mask:0xf\n" \
    "v_add_f32_dpp v1, v81, v1 quad_perm:[3,3,3,3] row_mask:0xf bank_mask:0xf\n" \
    "v_add_f32_dpp v2, v85, v2 quad_perm:[3,3,3,3] row_mask:0xf bank_mask:0xf\n" \
    "v_add_f32_dpp v3, v89, v3 quad_perm:[3,3,3,3] row_mask:0xf bank_mask:0xf\n" \
    "v_max_f32_e32 v4, 0xff800000, v0\n" \
    "v_max_f32_e32 v5, 0xff800000, v1\n" \
    "v_max_f32_e32 v6, 0xff800000, v2\n" \
    "v_max_f32_e32 v7, 0xff800000, v3\n" \
    "v_cvt_pk_bf16_f32 v8, v4, v5\n" \
    "v_cvt_pk_bf16_f32 v9, v6, v7\n" \
    "v_pk_ashrrev_i16 v4, 15, v8 op_sel_hi:[0,1]\n" \
    "v_pk_ashrrev_i16 v5, 15, v9 op_sel_hi:[0,1]\n" \
    "v_bitop3_b32 v8, v8, v4, s88 bitop3:0x1e\n" \
    "v_bitop3_b32 v9, v9, v5, s88 bitop3:0x1e\n" \
    "v_lshl_or_b32 v4, v8, 16, v31\n" \
    "v_and_or_b32 v5, v8, s89, v54\n" \
    "v_lshl_or_b32 v6, v9, 16, v55\n" \
    "v_and_or_b32 v7, v9, s89, v90\n" \
    "v_max_u32_e32 v10, v4, v5\n" \
    "v_max_u32_e32 v12, v6, v7\n" \
    "v_min_u32_e32 v11, v4, v5\n" \
    "v_min_u32_e32 v13, v6, v7\n" \
    "v_max_u32_e32 v14, v10, v12\n" \
    "v_min_u32_e32 v17, v10, v12\n" \
    "v_min_u32_e32 v18, v10, v13\n" \
    "v_max3_u32 v15, v17, v11, v13\n" \
    "v_min_u32_e32 v16, v11, v12\n" \
    "v_max_u32_e32 v16, v16, v18\n" \
    "v_min_u32_dpp v4, v14, v14 row_ror:4 row_mask:0xf bank_mask:0xf\n" \
    "v_mov_b32_dpp v5, v15 row_ror:4 row_mask:0xf bank_mask:0xf\n" \
    "v_max_u32_dpp v10, v14, v14 row_ror:4 row_mask:0xf bank_mask:0xf\n" \
    "v_min_u32_dpp v17, v14, v15 row_ror:4 row_mask:0xf bank_mask:0xf\n" \
    "v_max_u32_dpp v6, v16, v16 row_ror:4 row_mask:0xf bank_mask:0xf\n" \
    "v_max3_u32 v11, v4, v15, v5\n" \
    "v_min_u32_e32 v18, v14, v5\n" \
    "v_max3_u32 v12, v17, v18, v6\n" \
    "v_min_u32_dpp v4, v10, v10 row_ror:8 row_mask:0xf bank_mask:0xf\n" \
    "v_mov_b32_dpp v5, v11 row_ror:8 row_mask:0xf bank_mask:0xf\n" \
    "v_max_u32_dpp v14, v10, v10 row_ror:8 row_mask:0xf bank_mask:0xf\n" \
    "v_min_u32_dpp v17, v10, v11 row_ror:8 row_mask:0xf bank_mask:0xf\n" \
    "v_max_u32_dpp v6, v12, v12 row_ror:8 row_mask:0xf bank_mask:0xf\n" \
    "v_max3_u32 v15, v4, v11, v5\n" \
    "v_min_u32_e32 v18, v10, v5\n" \
    "v_max3_u32 v16, v17, v18, v6\n" \
    "s_mov_b64 exec, 0xff\n" \
    "v_perm_b32 v24, v14, v16, s92\n" \
    "v_perm_b32 v26, v15, v16, s93\n" \
    "global_store_dwordx4 v29, v[24:27], %[ws] sc1\n" \
    "ds_write_b32 v91, v92\n" \
    "s_mov_b64 exec, -1\n" \
    "v_mul_f32_e32 v0, 0xbfb8aa3b, v0\n" \
    "v_mul_f32_e32 v1, 0xbfb8aa3b, v1\n" \
    "v_mul_f32_e32 v2, 0xbfb8aa3b, v2\n" \
    "v_mul_f32_e32 v3, 0xbfb8aa3b, v3\n" \
    "v_exp_f32_e32 v0, v0\n" \
    "v_exp_f32_e32 v1, v1\n" \
    "v_exp_f32_e32 v2, v2\n" \
    "v_exp_f32_e32 v3, v3\n" \
    "v_add_f32_e32 v0, 1.0, v0\n" \
    "v_add_f32_e32 v1, 1.0, v1\n" \
    "v_add_f32_e32 v2, 1.0, v2\n" \
    "v_add_f32_e32 v3, 1.0, v3\n" \
    "v_rcp_f32_e32 v0, v0\n" \
    "v_rcp_f32_e32 v1, v1\n" \
    "v_rcp_f32_e32 v2, v2\n" \
    "v_rcp_f32_e32 v3, v3\n" \
    "v_cvt_f16_f32_e32 v0, v0\n" \
    "v_cvt_f16_f32_e32 v1, v1\n" \
    "v_cvt_f16_f32_e32 v2, v2\n" \
    "v_cvt_f16_f32_e32 v3, v3\n" \
    "v_pack_b32_f16 v0, v0, v1\n" \
    "v_pack_b32_f16 v2, v2, v3\n" \
    "v_cndmask_b32_e32 v8, v8, v9, vcc\n" \
    "v_cndmask_b32_e32 v0, v0, v2, vcc\n" \
    "v_mov_b32_e32 v11, s98\n" \
    "s_nop 0\n" \
    "v_permlane16_swap_b32 v0, v1\n" \
    "v_perm_b32 v10, v8, v1, v52\n" \
    "s_mov_b64 exec, 0xffff\n" \
    "global_store_dwordx2 v30, v[10:11], %[ws] sc1\n" \
    "s_mov_b64 exec, -1\n"
#define ITEM_CLOB "v0", "v1", "v2", "v3", "v4", "v5", "v6", "v7", "v8", "v9", "v10", "v11", "v12", "v13", "v14", "v15", "v16", "v17", "v18", "v19", "v20", "v21", "v22", "v23", "v24", "v25", "v26", "v27", "v28", "v29", "v30", "v31", "v52", "v53", "v54", "v55", "v72", "v74", "v75", "v76", "v77", "v78", "v79", "v80", "v81", "v82", "v83", "v84", "v85", "v86", "v87", "v88", "v89", "v90", "v91", "v92", "v96", "v97", "v98", "v99", "v100", "v101", "v102", "v103", "v104", "v105", "v106", "v107", "v108", "v109", "v110", "v111", "v112", "v113", "v114", "v115", "v116", "v117", "v118", "v119", "v120", "v121", "v122", "v123", "v124", "v125", "v126", "v127", "v128", "v129", "v130", "v131", "v132", "v133", "v134", "v135", "v136", "v137", "v138", "v139", "v140", "v141", "v142", "v143", "v144", "v145", "v146", "v147", "v148", "v149", "v150", "v151", "v152", "v153", "v154", "v155", "v156", "v157", "v158", "v159", "v160", "v161", "v162", "v163", "v164", "v165", "v166", "v167"
#define FFN_ASM \
    "v_mov_b64 v[96:97], 0\n" \
    "v_mov_b64 v[98:99], 0\n" \
    "v_mov_b64 v[100:101], 0\n" \
    "v_mov_b64 v[102:103], 0\n" \
    "v_mov_b64 v[104:105], 0\n" \
    "v_mov_b64 v[106:107], 0\n" \
    "v_mov_b64 v[108:109], 0\n" \
    "v_mov_b64 v[110:111], 0\n" \
    "v_mov_b64 v[112:113], 0\n" \
    "v_mov_b64 v[114:115], 0\n" \
    "v_mov_b64 v[116:117], 0\n" \
    "v_mov_b64 v[118:119], 0\n" \
    "v_mov_b64 v[120:121], 0\n" \
    "v_mov_b64 v[122:123], 0\n" \
    "s_waitcnt lgkmcnt(0)\n" \
    "s_barrier\n" \
    "ds_read_b128 v[124:127], %[xa] offset:0\n" \
    "ds_read_b128 v[128:131], %[xa] offset:3072\n" \
    "ds_read_b128 v[132:135], %[xa] offset:6144\n" \
    "ds_read_b32 v136, v112 offset:%[tg]\n" \
    "s_waitcnt lgkmcnt(3)\n" \
    "v_smfmac_f32_16x16x64_bf16 v[96:99], v[124:127], v[0:7], %[idx]\n" \
    "v_smfmac_f32_16x16x64_bf16 v[100:103], v[124:127], v[8:15], %[idx]\n" \
    "v_smfmac_f32_16x16x64_bf16 v[104:107], v[124:127], v[16:23], %[idx]\n" \
    "v_smfmac_f32_16x16x64_bf16 v[108:111], v[124:127], v[24:31], %[idx]\n" \
    "s_waitcnt lgkmcnt(2)\n" \
    "v_smfmac_f32_16x16x64_bf16 v[96:99], v[128:131], v[32:39], %[idx]\n" \
    "v_smfmac_f32_16x16x64_bf16 v[100:103], v[128:131], v[40:47], %[idx]\n" \
    "v_smfmac_f32_16x16x64_bf16 v[104:107], v[128:131], v[48:55], %[idx]\n" \
    "v_smfmac_f32_16x16x64_bf16 v[108:111], v[128:131], v[56:63], %[idx]\n" \
    "s_waitcnt lgkmcnt(1)\n" \
    "v_smfmac_f32_16x16x64_bf16 v[96:99], v[132:135], v[64:71], %[idx]\n" \
    "v_smfmac_f32_16x16x64_bf16 v[100:103], v[132:135], v[72:79], %[idx]\n" \
    "v_smfmac_f32_16x16x64_bf16 v[104:107], v[132:135], v[80:87], %[idx]\n" \
    "v_smfmac_f32_16x16x64_bf16 v[108:111], v[132:135], v[88:95], %[idx]\n" \
    "s_nop 4\n" \
    "ds_write_b128 %[rdw], v[96:99] offset:0\n" \
    "ds_write_b128 %[rdw], v[100:103] offset:1024\n" \
    "ds_write_b128 %[rdw], v[104:107] offset:2048\n" \
    "ds_write_b128 %[rdw], v[108:111] offset:3072\n" \
    "s_cmp_gt_u32 %[wv], 3\n" \
    "s_waitcnt lgkmcnt(4)\n" \
    "v_mov_b32_e32 v73, v136\n" \
    "v_mov_b32_e32 v75, v136\n" \
    "v_mov_b32_e32 v77, v136\n" \
    "v_mov_b32_e32 v79, v136\n" \
    "v_mov_b32_e32 v81, v136\n" \
    "v_mov_b32_e32 v83, v136\n" \
    "s_waitcnt vmcnt(0) lgkmcnt(0)\n" \
    "s_barrier\n" \
    "s_cbranch_scc1 Lfs_%=\n" \
    "ds_read_b64 v[48:49], %[rdr] offset:0\n" \
    "ds_read_b64 v[50:51], %[rdr] offset:4096\n" \
    "ds_read_b64 v[52:53], %[rdr] offset:8192\n" \
    "ds_read_b64 v[54:55], %[rdr] offset:12288\n" \
    "ds_read_b64 v[56:57], %[rdr] offset:16384\n" \
    "ds_read_b64 v[58:59], %[rdr] offset:20480\n" \
    "ds_read_b64 v[60:61], %[rdr] offset:128\n" \
    "ds_read_b64 v[62:63], %[rdr] offset:4224\n" \
    "ds_read_b64 v[64:65], %[rdr] offset:8320\n" \
    "ds_read_b64 v[66:67], %[rdr] offset:12416\n" \
    "ds_read_b64 v[68:69], %[rdr] offset:16512\n" \
    "ds_read_b64 v[70:71], %[rdr] offset:20608\n" \
    "ds_read_b64_tr_b16 v[0:1], %[d00] offset:0\n" \
    "ds_read_b64_tr_b16 v[2:3], %[d01] offset:256\n" \
    "ds_read_b64_tr_b16 v[4:5], %[d00] offset:2048\n" \
    "ds_read_b64_tr_b16 v[6:7], %[d01] offset:2304\n" \
    "ds_read_b64_tr_b16 v[8:9], %[d10] offset:0\n" \
    "ds_read_b64_tr_b16 v[10:11], %[d11] offset:256\n" \
    "ds_read_b64_tr_b16 v[12:13], %[d10] offset:2048\n" \
    "ds_read_b64_tr_b16 v[14:15], %[d11] offset:2304\n" \
    "ds_read_b64_tr_b16 v[16:17], %[d20] offset:0\n" \
    "ds_read_b64_tr_b16 v[18:19], %[d21] offset:256\n" \
    "ds_read_b64_tr_b16 v[20:21], %[d20] offset:2048\n" \
    "ds_read_b64_tr_b16 v[22:23], %[d21] offset:2304\n" \
    "s_waitcnt lgkmcnt(14)\n" \
    "v_pk_add_f32 v[48:49], v[48:49], v[50:51]\n" \
    "v_pk_add_f32 v[52:53], v[52:53], v[54:55]\n" \
    "v_pk_add_f32 v[56:57], v[56:57], v[58:59]\n" \
    "v_pk_add_f32 v[48:49], v[48:49], v[52:53]\n" \
    "v_pk_add_f32 v[60:61], v[60:61], v[62:63]\n" \
    "v_pk_add_f32 v[48:49], v[48:49], v[56:57]\n" \
    "v_pk_add_f32 v[64:65], v[64:65], v[66:67]\n" \
    "v_mul_f32_e32 v50, 0xbfb8aa3b, v48\n" \
    "v_mul_f32_e32 v51, 0xbfb8aa3b, v49\n" \
    "v_exp_f32_e32 v50, v50\n" \
    "v_exp_f32_e32 v51, v51\n" \
    "s_waitcnt lgkmcnt(12)\n" \
    "v_pk_add_f32 v[68:69], v[68:69], v[70:71]\n" \
    "v_add_f32_e32 v50, 1.0, v50\n" \
    "v_add_f32_e32 v51, 1.0, v51\n" \
    "v_rcp_f32_e32 v50, v50\n" \
    "v_rcp_f32_e32 v51, v51\n" \
    "v_pk_add_f32 v[60:61], v[60:61], v[64:65]\n" \
    "v_pk_add_f32 v[60:61], v[60:61], v[68:69]\n" \
    "v_pk_mul_f32 v[48:49], v[48:49], v[50:51]\n" \
    "v_pk_mul_f32 v[48:49], v[48:49], v[60:61]\n" \
    "v_cvt_pk_bf16_f32 v48, v48, v49\n" \
    "ds_write_b16 %[hbw], v48\n" \
    "ds_write_b16_d16_hi %[hbw], v48 offset:144\n" \
    "s_branch Lfj_%=\n" \
    "Lfs_%=:\n" \
    "ds_read_b64_tr_b16 v[0:1], %[d00] offset:0\n" \
    "ds_read_b64_tr_b16 v[2:3], %[d01] offset:256\n" \
    "ds_read_b64_tr_b16 v[4:5], %[d00] offset:2048\n" \
    "ds_read_b64_tr_b16 v[6:7], %[d01] offset:2304\n" \
    "ds_read_b64_tr_b16 v[8:9], %[d10] offset:0\n" \
    "ds_read_b64_tr_b16 v[10:11], %[d11] offset:256\n" \
    "ds_read_b64_tr_b16 v[12:13], %[d10] offset:2048\n" \
    "ds_read_b64_tr_b16 v[14:15], %[d11] offset:2304\n" \
    "ds_read_b64_tr_b16 v[16:17], %[d20] offset:0\n" \
    "ds_read_b64_tr_b16 v[18:19], %[d21] offset:256\n" \
    "ds_read_b64_tr_b16 v[20:21], %[d20] offset:2048\n" \
    "ds_read_b64_tr_b16 v[22:23], %[d21] offset:2304\n" \
    "Lfj_%=:\n" \
    "s_waitcnt vmcnt(0) lgkmcnt(0)\n" \
    "s_barrier\n" \
    "ds_read_b128 v[84:87], %[hs]\n" \
    "ds_read_b128 v[88:91], %[hs] offset:64\n" \
    "ds_read_b64_tr_b16 v[24:25], %[d00] offset:4096\n" \
    "ds_read_b64_tr_b16 v[26:27], %[d01] offset:4352\n" \
    "ds_read_b64_tr_b16 v[28:29], %[d00] offset:6144\n" \
    "ds_read_b64_tr_b16 v[30:31], %[d01] offset:6400\n" \
    "ds_read_b64_tr_b16 v[32:33], %[d10] offset:4096\n" \
    "ds_read_b64_tr_b16 v[34:35], %[d11] offset:4352\n" \
    "ds_read_b64_tr_b16 v[36:37], %[d10] offset:6144\n" \
    "ds_read_b64_tr_b16 v[38:39], %[d11] offset:6400\n" \
    "ds_read_b64_tr_b16 v[40:41], %[d20] offset:4096\n" \
    "ds_read_b64_tr_b16 v[42:43], %[d21] offset:4352\n" \
    "ds_read_b64_tr_b16 v[44:45], %[d20] offset:6144\n" \
    "ds_read_b64_tr_b16 v[46:47], %[d21] offset:6400\n" \
    "s_waitcnt lgkmcnt(13)\n" \
    "v_smfmac_f32_16x16x64_bf16 v[112:115], v[84:87], v[0:7], %[idx]\n" \
    "v_smfmac_f32_16x16x64_bf16 v[116:119], v[84:87], v[8:15], %[idx]\n" \
    "v_smfmac_f32_16x16x64_bf16 v[120:123], v[84:87], v[16:23], %[idx]\n" \
    "s_waitcnt lgkmcnt(8)\n" \
    "v_smfmac_f32_16x16x64_bf16 v[112:115], v[88:91], v[24:31], %[idx]\n" \
    "s_waitcnt lgkmcnt(4)\n" \
    "v_smfmac_f32_16x16x64_bf16 v[116:119], v[88:91], v[32:39], %[idx]\n" \
    "s_waitcnt lgkmcnt(0)\n" \
    "v_smfmac_f32_16x16x64_bf16 v[120:123], v[88:91], v[40:47], %[idx]\n" \
    "s_nop 3\n" \
    "v_cvt_pk_bf16_f32 v72, v112, v113\n" \
    "v_cvt_pk_bf16_f32 v74, v114, v115\n" \
    "global_store_dwordx4 %[po0], v[72:75], %[ws] sc1\n" \
    "v_cvt_pk_bf16_f32 v76, v116, v117\n" \
    "v_cvt_pk_bf16_f32 v78, v118, v119\n" \
    "global_store_dwordx4 %[po1], v[76:79], %[ws] sc1\n" \
    "v_cvt_pk_bf16_f32 v80, v120, v121\n" \
    "v_cvt_pk_bf16_f32 v82, v122, v123\n" \
    "global_store_dwordx4 %[po2], v[80:83], %[ws] sc1\n"
#define FFN_CLOB "v96", "v97", "v98", "v99", "v100", "v101", "v102", "v103", "v104", "v105", "v106", "v107", "v108", "v109", "v110", "v111", "v112", "v113", "v114", "v115", "v116", "v117", "v118", "v119", "v120", "v121", "v122", "v123", "v124", "v125", "v126", "v127", "v128", "v129", "v130", "v131", "v132", "v133", "v134", "v135", "v136"
#define SEL_ASM \
    "s_mov_b32 %[ps], 0\n" \
    "s_branch Lsn_%=\n" \
    "Lsg_%=:\n" \
    "s_mov_b32 %[ps], 4096\n" \
    "Lsw_%=:\n" \
    "s_memrealtime s[92:93]\n" \
    "s_waitcnt lgkmcnt(0)\n" \
    "s_sub_u32 s92, s92, %[at]\n" \
    "s_cmp_lt_i32 s92, 0\n" \
    "s_cbranch_scc0 Lsr_%=\n" \
    "s_sleep 1\n" \
    "s_sub_u32 %[ps], %[ps], 1\n" \
    "s_cbranch_scc0 Lsw_%=\n" \
    "Lsr_%=:\n" \
    "s_mov_b32 %[ps], 0x20000\n" \
    "Lsp_%=:\n" \
    "global_load_dwordx4 v[0:3], %[co], %[ws] sc1\n" \
    "global_load_dwordx4 v[4:7], %[co2], %[ws] sc1\n" \
    "s_mov_b64 exec, -1\n" \
    "s_waitcnt vmcnt(1)\n" \
    "v_bitop3_b32 v10, v1, v3, %[tag] bitop3:0x7e\n" \
    "s_waitcnt vmcnt(0)\n" \
    "v_bitop3_b32 v11, v5, v7, %[tag] bitop3:0x7e\n" \
    "v_or_b32_e32 v10, v10, v11\n" \
    "v_cmp_ne_u32_e32 vcc, 0, v10\n" \
    "s_cbranch_vccz Lsn_%=\n" \
    "s_sleep %[gap]\n" \
    "s_mov_b64 exec, vcc\n" \
    "s_sub_u32 %[ps], %[ps], 1\n" \
    "s_cbranch_scc0 Lsp_%=\n" \
    "s_mov_b64 exec, -1\n" \
    "s_mov_b32 %[ps], 1\n" \
    "Lsn_%=:\n" \
    "v_and_or_b32 v15, v0, %[mk], %[f0]\n" \
    "v_and_or_b32 v17, v4, %[mk], %[fi]\n" \
    "v_and_or_b32 v16, v2, %[mk], %[f0]\n" \
    "v_and_or_b32 v18, v6, %[mk], %[fi]\n" \
    "v_max_u32_e32 v8, v15, v17\n" \
    "v_min_u32_e32 v9, v15, v17\n" \
    "v_max3_u32 v9, v9, v16, v18\n" \
    "v_mov_b32_dpp v10, v8 quad_perm:[1,0,3,2] row_mask:0xf bank_mask:0xf\n" \
    "v_med3_u32 v8, v8, v10, %[c1]\n" \
    "v_mov_b32_dpp v11, v9 quad_perm:[1,0,3,2] row_mask:0xf bank_mask:0xf\n" \
    "v_med3_u32 v9, v9, v11, %[c1]\n" \
    "v_mov_b32_dpp v10, v8 quad_perm:[3,2,1,0] row_mask:0xf bank_mask:0xf\n" \
    "v_med3_u32 v8, v8, v10, %[c2]\n" \
    "v_mov_b32_dpp v11, v9 quad_perm:[3,2,1,0] row_mask:0xf bank_mask:0xf\n" \
    "v_med3_u32 v9, v9, v11, %[c2]\n" \
    "v_mov_b32_dpp v10, v8 quad_perm:[1,0,3,2] row_mask:0xf bank_mask:0xf\n" \
    "v_med3_u32 v8, v8, v10, %[c1]\n" \
    "v_mov_b32_dpp v11, v9 quad_perm:[1,0,3,2] row_mask:0xf bank_mask:0xf\n" \
    "v_med3_u32 v9, v9, v11, %[c1]\n" \
    "v_mov_b32_dpp v10, v8 row_half_mirror row_mask:0xf bank_mask:0xf\n" \
    "v_med3_u32 v8, v8, v10, %[c4]\n" \
    "v_max_u32_dpp v9, v9, v9 row_half_mirror row_mask:0xf bank_mask:0xf\n" \
    "v_perm_b32 v19, v0, v2, %[pm]\n" \
    "v_mov_b32_dpp v10, v8 quad_perm:[2,3,0,1] row_mask:0xf bank_mask:0xf\n" \
    "v_med3_u32 v8, v8, v10, %[c2]\n" \
    "v_mov_b32_dpp v11, v9 quad_perm:[2,3,0,1] row_mask:0xf bank_mask:0xf\n" \
    "v_med3_u32 v9, v9, v11, %[c2]\n" \
    "v_mov_b32_dpp v10, v8 quad_perm:[1,0,3,2] row_mask:0xf bank_mask:0xf\n" \
    "v_med3_u32 v8, v8, v10, %[c1]\n" \
    "v_mov_b32_dpp v11, v9 quad_perm:[1,0,3,2] row_mask:0xf bank_mask:0xf\n" \
    "v_med3_u32 v9, v9, v11, %[c1]\n" \
    "v_max_u32_dpp v8, v8, v8 row_mirror row_mask:0xf bank_mask:0xf\n" \
    "s_nop 0\n" \
    "v_max_u32_dpp v9, v9, v9 row_mirror row_mask:0xf bank_mask:0xf\n" \
    "v_max_u32_dpp v10, v8, v8 row_shl:4 row_mask:0xf bank_mask:0x5\n" \
    "v_min_u32_dpp v10, v8, v8 row_shr:4 row_mask:0xf bank_mask:0xa\n" \
    "v_mov_b32_dpp v11, v9 quad_perm:[2,3,0,1] row_mask:0xf bank_mask:0xf\n" \
    "v_med3_u32 v9, v9, v11, %[c2]\n" \
    "v_mov_b32_dpp v8, v10 quad_perm:[2,3,0,1] row_mask:0xf bank_mask:0xf\n" \
    "v_med3_u32 v10, v10, v8, %[c2]\n" \
    "v_mov_b32_dpp v11, v9 quad_perm:[1,0,3,2] row_mask:0xf bank_mask:0xf\n" \
    "v_med3_u32 v9, v9, v11, %[c1]\n" \
    "v_mov_b32_dpp v8, v10 quad_perm:[1,0,3,2] row_mask:0xf bank_mask:0xf\n" \
    "v_med3_u32 v10, v10, v8, %[c1]\n" \
    "v_mov_b32_e32 v13, v9\n" \
    "v_perm_b32 v20, v4, v6, %[pm]\n" \
    "v_mov_b32_e32 v12, v10\n" \
    "v_permlane16_swap_b32 v9, v13\n" \
    "v_min_u32_e32 v15, v15, v18\n" \
    "v_permlane16_swap_b32 v10, v12\n" \
    "v_max_u32_dpp v9, v13, v9 quad_perm:[3,2,1,0] row_mask:0xf bank_mask:0xf\n" \
    "v_min_u32_e32 v16, v16, v17\n" \
    "v_max_u32_dpp v10, v12, v10 row_half_mirror row_mask:0xf bank_mask:0xf\n" \
    "v_mov_b32_dpp v11, v9 quad_perm:[2,3,0,1] row_mask:0xf bank_mask:0xf\n" \
    "v_med3_u32 v9, v9, v11, %[c2]\n" \
    "v_mov_b32_dpp v8, v10 row_ror:4 row_mask:0xf bank_mask:0xf\n" \
    "v_med3_u32 v10, v10, v8, %[c4]\n" \
    "v_mov_b32_dpp v11, v9 quad_perm:[1,0,3,2] row_mask:0xf bank_mask:0xf\n" \
    "v_med3_u32 v9, v9, v11, %[c1]\n" \
    "v_mov_b32_dpp v8, v10 quad_perm:[2,3,0,1] row_mask:0xf bank_mask:0xf\n" \
    "v_med3_u32 v10, v10, v8, %[c2]\n" \
    "v_mov_b32_e32 v13, v9\n" \
    "v_max3_u32 v19, v19, v20, v15\n" \
    "v_mov_b32_dpp v8, v10 quad_perm:[1,0,3,2] row_mask:0xf bank_mask:0xf\n" \
    "v_med3_u32 v10, v10, v8, %[c1]\n" \
    "v_permlane32_swap_b32 v9, v13\n" \
    "v_max_u32_e32 %[tc], v19, v16\n" \
    "v_mov_b32_e32 v12, v10\n" \
    "v_max_u32_dpp v9, v13, v9 quad_perm:[3,2,1,0] row_mask:0xf bank_mask:0xf\n" \
    "s_nop 0\n" \
    "v_permlane32_swap_b32 v10, v12\n" \
    "v_mov_b32_dpp v11, v9 quad_perm:[2,3,0,1] row_mask:0xf bank_mask:0xf\n" \
    "v_med3_u32 v9, v9, v11, %[c2]\n" \
    "v_max_u32_dpp v10, v12, v10 row_half_mirror row_mask:0xf bank_mask:0xf\n" \
    "s_nop 0\n" \
    "v_mov_b32_dpp v11, v9 quad_perm:[1,0,3,2] row_mask:0xf bank_mask:0xf\n" \
    "v_med3_u32 v9, v9, v11, %[c1]\n" \
    "v_mov_b32_dpp v8, v10 row_ror:4 row_mask:0xf bank_mask:0xf\n" \
    "v_med3_u32 v10, v10, v8, %[c4]\n" \
    "v_and_b32_e32 v9, v9, %[c4]\n" \
    "s_nop 0\n" \
    "v_mov_b32_dpp v8, v10 quad_perm:[2,3,0,1] row_mask:0xf bank_mask:0xf\n" \
    "v_med3_u32 v10, v10, v8, %[c2]\n" \
    "s_nop 1\n" \
    "v_mov_b32_dpp v8, v10 quad_perm:[1,0,3,2] row_mask:0xf bank_mask:0xf\n" \
    "v_med3_u32 v10, v10, v8, %[c1]\n" \
    "v_max_u32_dpp v10, v9, v10 row_half_mirror row_mask:0xf bank_mask:0xf\n" \
    "s_cmp_eq_u32 %[ps], 0\n" \
    "s_cbranch_scc1 Lsg_%=\n" \
    "v_mov_b32_dpp v14, v10 row_newbcast:0 row_mask:0x1 bank_mask:0x3\n" \
    "v_mov_b32_dpp v14, v10 row_newbcast:1 row_mask:0x1 bank_mask:0xc\n" \
    "v_mov_b32_dpp v14, v10 row_newbcast:2 row_mask:0x2 bank_mask:0x3\n" \
    "v_mov_b32_dpp v14, v10 row_newbcast:3 row_mask:0x2 bank_mask:0xc\n" \
    "v_mov_b32_dpp v14, v10 row_newbcast:4 row_mask:0x4 bank_mask:0x3\n" \
    "v_mov_b32_dpp v14, v10 row_newbcast:5 row_mask:0x4 bank_mask:0xc\n" \
    "v_mov_b32_dpp v14, v10 row_newbcast:6 row_mask:0x8 bank_mask:0x3\n" \
    "v_mov_b32_dpp v14, v10 row_newbcast:7 row_mask:0x8 bank_mask:0xc\n" \
    "s_mov_b32 m0, %[xs]\n" \
    "v_and_b32_e32 v14, 0x7ff, v14\n" \
    "v_mul_u32_u24_e32 v14, 1152, v14\n" \
    "v_sub_u32_e32 v14, %[xk], v14\n" \
    "s_cmp_eq_u32 %[moe], 0\n" \
    "s_cbranch_scc1 Lse_%=\n" \
    "global_load_lds_dwordx4 v14, %[x] offset:0\n" \
    "s_addk_i32 m0, 896\n" \
    "s_nop 0\n" \
    "global_load_lds_dwordx4 v14, %[x] offset:128\n" \
    "s_addk_i32 m0, 896\n" \
    "s_nop 0\n" \
    "global_load_lds_dwordx4 v14, %[x] offset:256\n" \
    "s_addk_i32 m0, 896\n" \
    "s_nop 0\n" \
    "global_load_lds_dwordx4 v14, %[x] offset:384\n" \
    "s_addk_i32 m0, 896\n" \
    "s_nop 0\n" \
    "global_load_lds_dwordx4 v14, %[x] offset:512\n" \
    "s_addk_i32 m0, 896\n" \
    "s_nop 0\n" \
    "global_load_lds_dwordx4 v14, %[x] offset:640\n" \
    "s_addk_i32 m0, 896\n" \
    "s_nop 0\n" \
    "global_load_lds_dwordx4 v14, %[x] offset:768\n" \
    "s_addk_i32 m0, 896\n" \
    "s_nop 0\n" \
    "global_load_lds_dwordx4 v14, %[x] offset:896\n" \
    "s_addk_i32 m0, 896\n" \
    "s_nop 0\n" \
    "global_load_lds_dwordx4 v14, %[x] offset:1024\n" \
    "Lse_%=:\n" \
    "v_mov_b32_e32 %[g8], v10\n"
#define SEL_CLOB "v0", "v1", "v2", "v3", "v4", "v5", "v6", "v7", "v8", "v9", "v10", "v11", "v12", "v13", "v14", "v15", "v16", "v17", "v18", "v19", "v20"

template <int kind> DI void conv_wave(const Params& P, lptr sm, int ch, int xcd, int ii, u32 labw) {
    const int cg = cg_of(xcd, ii);
    const u32 m0a = (u32)(LD_TB + ch * 512 + (kind & 1) * 256);
    u32 epoch;
    if (kind == 0) {
        asm volatile("; claim\n" CONV_A_ASM : : [m0a] "s"(m0a) : "memory", "vcc", "scc", "s90", CONV_A_CLOB);
    } else if (kind == 1) {
        v4u sg;
        asm volatile("; claim\n" CONV_B_ASM : [ep] "=&s"(epoch), "=&{v[108:111]}"(sg) : [m0a] "s"(m0a), [sg] "s"(P.ws + WS_SEL + xcd * 128) : "memory", "vcc", "scc", "s90", "s92", CONV_B_CLOB);
        if (ch == 0) conv_final(P, sm, cg, xcd, tag_of(epoch), sg);
    } else {
        const Lab lb = lab_of(labw);
        const u32 nl = xcd < 2 ? 6u * lb.n : 9u * lb.n, u0 = (u32)(4 * ii + ch);
        const char* pw = (const char*)P.wd + (lb.m0 * 576u + u0 * nl) * 128u;
        asm volatile("; claim\n" CONV_C_ASM : [ep] "=&s"(epoch)
                     : [xb] "s"(P.x), [pm] "s"((xcd < 2 ? ii < 16 : ch < 2) ? ~0ull : 1ull), [x0] "s"((const char*)P.x + cg * 8), [wd] "s"(pw), [wl] "s"((nl - 1u) * 128u), [o0] "s"((char*)P.out + cg * 8), [ws] "s"(P.ws), [ak] "s"(WS_ACK + (u32)(8 * ii + xcd) * 128u),
                       [bs] "s"((const char*)&conv_tab + 61440 + ch * 1024), [bl] "s"(LD_BD + ch * 1024), [tg] "s"(11 + (ch >> 1)), [am] "s"(1u >> ch), [gt] "n"(LD_TB + ROW_T1 * TR - (int)WS_KEYS / 4), [pp] "s"(P.ws + cg * (72 * 128))
                     : "memory", "vcc", "scc", "s88", "s89", "s90", "s91", "s92", "s93", "s94", "s95", "s96", "s97", "s98", "s99", CONV_C_CLOB);
    }
}

DI u32 select8(const Params& P, int lane, int xcd, u32 tag, u32 t_in, u32 moe) {
    const __amdgpu_buffer_rsrc_t rws = ws_rsrc(P);
    const v2u t0 = ld2_sc1(rws, TREF && xcd >= 4 ? WS_T0 + (u32)xcd * 128u : OOB);
    const u32 co = WS_CAND + (u32)xcd * 16384u + (u32)lane * 128u, fi = (u32)(63 - lane) << 4;
    const u32 xk = 2047u * (D_ * 2) + (u32)(((lane & 7) ^ ((lane >> 3) & 6)) * 16);
    const u32 d = rfl(t0[0]) == tag ? t_in - rfl(t0[1]) : 0u, at = t_in + SEL_AT - (d < (u32)SEL_AT ? d : 0u);
    u32 g8, tc, ps;
    asm volatile(SEL_ASM
                 : [g8] "=&v"(g8), [tc] "=&v"(tc), [ps] "=&s"(ps)
                 : [co] "v"(co), [co2] "v"(co + 8192u), [fi] "v"(fi), [f0] "v"(fi | 1024u), [c4] "v"((lane & 4) ? 0u : ~0u), [c2] "v"((lane & 2) ? 0u : ~0u), [c1] "v"((lane & 1) ? 0u : ~0u),
                   [mk] "v"(0xffff000fu), [xk] "v"(xk), [pm] "s"(0x05010d0du), [ws] "s"(P.ws), [x] "s"(P.x), [tag] "s"(tag), [at] "s"(at), [moe] "s"(moe), [xs] "n"(LD_XS), [gap] "n"(SEL_GAP)
                 : "memory", "vcc", "scc", "s92", "s93", SEL_CLOB);
    u32 lo = umin(g8, dpp0<0xB1, 0xf>(g8));
    lo = umin(lo, dpp0<0x4E, 0xf>(lo)); lo = umin(lo, dpp0<0x141, 0xf>(lo));
    if (__builtin_expect(__any(tc > lo), 0)) {
        u32 all[32];
#pragma unroll 1
        for (int spin = 0; spin < SPIN_CAP; ++spin) {
            MEMFENCE();
            bool ok = true;
#pragma unroll
            for (int q = 0; q < 16; ++q) {
                const v4u e = ld_sc1(rws, WS_KEYS + (u32)lane * 16u + q * 1024);
                ok &= tag_ok(e, tag);
                all[2 * q] = (e[0] & 0xffff0000u) | (u32)(2047 - 128 * q - 2 * lane); all[2 * q + 1] = (e[2] & 0xffff0000u) | (u32)(2046 - 128 * q - 2 * lane);
            }
            if (__all(ok)) break;
            __builtin_amdgcn_s_sleep(8);
        }
        u32 bound = 0xffffffffu;
#pragma unroll 1
        for (int rd = 0; rd < 8; ++rd) {
            u32 m = 0;
#pragma unroll
            for (int q = 0; q < 32; ++q) { m = umax(m, all[q] <= bound ? all[q] : 0u); PIN(m); }
            const u32 M = wave_max(m);
            if ((lane & 7) == rd) g8 = M;
            bound = M - 1u;
        }
        VMWAIT(0);
        if (moe) dma_rows<1024>(P.x, xk - (from_lane(g8, lane >> 3) & 0x7ffu) * (D_ * 2), LD_XS);
    }
    return g8;
}

DI void moe_weights(const Params& P, lptr sm, int wave, int midx, u32 nit) {
    v4u wb[24];
    u64 base;
    asm volatile("; claim4" : "={v[96:99]}"(wb[0]), "={v[100:103]}"(wb[1]), "={v[104:107]}"(wb[2]), "={v[108:111]}"(wb[3]), "={v[12:13]}"(base));
    const int lane = fresh_lane();
    const u32 wo = (u32)(((lane & 7) ^ ((lane >> 3) & 6)) * 16);
    const int c = lane & 15, kg = lane >> 4;
    auto ldw = [&](int k) {
        asm volatile("global_load_dwordx4 %0, %1, off" : "=v"(wb[k]) : "v"(base) : "memory");
        base += (u64)(((k & 7) == 7 ? 173 : ((k & 7) == 3 ? 13 : 1)) * (F_ * 2));
        SB();
    };
    base += (u64)(12 * (F_ * 2));
#pragma unroll
    for (int k = 4; k < WIN0; ++k) ldw(k);
    const int w3 = wave >= 3 ? wave - 3 : wave, r8 = wave >= 3;
    u32 src = (u32)((midx * 64 + 8 * r8 + (lane >> 3)) * (D_ * 2) + w3 * 384) + wo, dst = LD_WD + w3 * 24576 + r8 * 1024;
    lptr rdw = sm + LD_RED + wave * 4096 + lane * 16;
    clptr rdr = sm + LD_RED + (wave & 3) * 1024 + (lane & 55) * 16 + (c >> 3) * 8;
    lptr hbw = sm + LD_HB + (4 * (kg & 1) + 2 * (c >> 3)) * HB_ROW + 2 * (8 * (c & 7) + (wave & 3) + 4 * (kg >> 1));
    clptr hs = sm + LD_HB + (c & 7) * HB_ROW + kg * 16;
    u32 po[3];
#pragma unroll
    for (int t = 0; t < 3; ++t) { po[t] = WS_PART + (u32)(((4 * (6 * wave + t + 3 * (kg >> 1)) + (c >> 2)) * 72 + midx) * 8 + 4 * (kg & 1) + (c & 3)) * 16u; PIN(po[t]); }
    const int kq = 4 * wave + kg;
    clptr xa = sm + LD_XS + (kq >> 3) * 1024 + (c & 7) * 128 + (((kq & 7) ^ (c & 6)) * 16);
    u32 idx = c < 8 ? 0x4444u : 0xeeeeu;
    clptr db[3][2];
#pragma unroll
    for (int t = 0; t < 3; ++t) {
        const int r = (lane >> 2) & 3, p = lane & 3, tile = 6 * wave + t + 3 * (r >> 1);
        db[t][0] = sm + LD_WD + (tile >> 2) * 8192 + (4 * kg + (r & 1)) * 128 + (((2 * (tile & 3) + (p >> 1)) ^ (4 * (kg & 1))) * 16) + 8 * (p & 1);
        db[t][1] = (clptr)((u32)(uintptr_t)db[t][0] ^ 32u);
        PIN(db[t][0]); PIN(db[t][1]);
    }
    PIN(rdw); PIN(rdr); PIN(hbw); PIN(hs); PIN(xa); PIN(idx); PIN(src);
    SB();
    v8u gf[3][4];
    {
        if (WIN0 < WIN && nit) wait_ge<1>(sm + FL_IT + 4 * umin(lane, nit - 1), 1);
#pragma unroll
        for (int k = WIN0; k < WIN; ++k) ldw(k);
        auto done = [&](int d) {
            if (d & 1) {
                asm volatile("" : "+v"(wb[d - 1]), "+v"(wb[d]));
                const v4u lo = wb[d - 1], hi = wb[d];
#pragma unroll
                for (int m = 0; m < 4; ++m) {
                    const int e = (d & 6) + (m >> 1);
                    gf[d >> 3][2 * (m & 1)][e] = perm_lo(hi[m], lo[m]); gf[d >> 3][2 * (m & 1) + 1][e] = perm_hi(hi[m], lo[m]);
                    PIN(gf[d >> 3][2 * (m & 1)][e]); PIN(gf[d >> 3][2 * (m & 1) + 1][e]);
                }
                SB();
            }
        };
#pragma unroll
        for (int k = WIN; k < 24; ++k) {
            VMWAIT(WIN - 1);
            ldw(k);
            done(k - WIN);
        }
        int nw = 0;
#pragma unroll
        for (int k = 24 - WIN; k < 24; ++k) {
            VMWAIT(23 - k + nw);
#pragma unroll
            for (; nw < (k - 23 + WIN) * NWE / WIN; ++nw) {
                asm volatile("s_mov_b32 m0, %2\n\ts_nop 0\n\tglobal_load_lds_dwordx4 %0, %1 offset:%3" ::"v"(src), "s"(P.wd), "s"(dst + (u32)((nw % 3) * 8064 + nw / 3 * 2048)), "n"((nw % 3) * 128) : "memory");
                if (nw % 3 == 2) src += 16 * D_ * 2;
            }
            done(k);
        }
    }

    asm volatile(FFN_ASM
                 : "+{v[0:7]}"(gf[0][0]), "+{v[8:15]}"(gf[0][1]), "+{v[16:23]}"(gf[0][2]), "+{v[24:31]}"(gf[0][3]), "+{v[32:39]}"(gf[1][0]), "+{v[40:47]}"(gf[1][1]),
                   "+{v[48:55]}"(gf[1][2]), "+{v[56:63]}"(gf[1][3]), "+{v[64:71]}"(gf[2][0]), "+{v[72:79]}"(gf[2][1]), "+{v[80:87]}"(gf[2][2]), "+{v[88:95]}"(gf[2][3])
                 : [xa] "v"(xa), [rdw] "v"(rdw), [rdr] "v"(rdr), [hbw] "v"(hbw), [hs] "v"(hs), [idx] "v"(idx), [d00] "v"(db[0][0]), [d01] "v"(db[0][1]), [d10] "v"(db[1][0]), [d11] "v"(db[1][1]),
                   [d20] "v"(db[2][0]), [d21] "v"(db[2][1]), [po0] "v"(po[0]), [po1] "v"(po[1]), [po2] "v"(po[2]), [ws] "s"(P.ws), [wv] "s"(wave), [tg] "n"(LD_TAG)
                 : "memory", "scc", FFN_CLOB);
}

DI void helper_prefetch(const Params& P, int lane, u32 idx, u32 cnt, Lab lb) {
    const u32 c3 = (3u - (u32)((u64)P.wg >> 7) - lb.m0) & 7u, c3u = (3u - (u32)((u64)P.wu >> 7) - lb.m0) & 7u;
    const u32 n3 = (c3 < lb.n ? 1u : 0u) + (c3 + 8u < lb.n ? 1u : 0u), nl = lb.n - n3;
    const u32 mg2 = (u32)(65536.f * __builtin_amdgcn_rcpf((float)nl)) + 1u;
    const u32 rpu = (64u * mg2) >> 16, r = umin(((u32)lane * mg2) >> 16, rpu - 1u);
    u32 l = umin((u32)lane - nl * r, nl - 1u);
    l += l >= c3 ? 1u : 0u; l += l >= c3 + 8u ? 1u : 0u;
    const u32 go = r * (F_ * 2) + (lb.m0 + l) * 128u;
    constexpr u32 NSG = (D_ - SLOW0) / 64;
    u32 u = idx;
#pragma unroll 1
    for (; u < 2u * NSG * n3; u += cnt) {
        VMWAIT(HWIN - 1);
        const u32 h = n3 == 2u ? u >> 2 : u >> 1;
        touch((u & 1) ? P.wu : P.wg, (SLOW0 + h * 64u + (u32)lane) * (F_ * 2) + umin(lb.m0 + (n3 == 2u && (u & 2u) ? 8u : 0u) + ((u & 1) ? c3u : c3), 71u) * 128u);
    }
#pragma unroll 1
    for (u -= 2u * NSG * n3; HR0 + (u >> 1) * rpu < D_; u += cnt) {
        VMWAIT(HWIN - 1);
        touch((u & 1) ? P.wu : P.wg, go + umin(HR0 + (u >> 1) * rpu, D_ - rpu) * (F_ * 2));
    }
}

#define TAB_CLOB "v148", "v149", "v150", "v151", "v152", "v153", "v154", "v155", "v156", "v157", "v158", "v159", "v160", "v161", "v162", "v163", "v164", "v165", "v166", "v167", "v32", "v33", "v34", "v35", "v36", "v37", "v38", "v39", "v40", "v41", "v42", "v43", "v44", "v45", "v46", "v47", "v48", "v49", "v50", "v51"
#define ENTRY_CLOB TAB_CLOB, "v62", "v63", "v137", "v138", "v139", "v140", "v141", "v142", "v143", "v144", "v145", "v146", "v147", "v64", "v65", "v66", "v67", "v68", "v69", "v70", "v71", "v72", "v73", "v74", "v75", "v76", "v77", "v78", "v79", "v80", "v81", "v82", "v83", "v84", "v85", "v86", "v87", "v88", "v89", "v90", "v91", "v92", "v93", "v94", "v95", "v96", "v97", "v98", "v99", "v100", "v101", "v102", "v103", "v104", "v105", "v106", "v107", "v108", "v109", "v110", "v111", "v112", "v113", "v114", "v115", "v116", "v117", "v118", "v119", "v120", "v121", "v122", "v123", "v124", "v125", "v126", "v127", "v128", "v129", "v130", "v131", "v132", "v133", "v134", "v135", "v136"
__global__ void __launch_bounds__(NTHR, 3) fused_kernel(Args) {
    __shared__ __attribute__((aligned(16))) unsigned char smem[LDS_BYTES];
    lptr sm = (lptr)smem;
    const int b = blockIdx.x;
    v16u ka0, ka1;
    int wave, item;
    u32 role, labw, tid = threadIdx.x;
#define LX(q, a, o) "global_load_dwordx4 v[96+4*(" #q "):99+4*(" #q ")], " a ", s[54:55] offset:" #o "\n\t"
#define LXS(s) LX(s, "v7", -2304+256*s) LX(4+s, "v7", 2304+256*s) LX(8+s, "v9", -2304+256*s) LX(12+s, "v9", 2304+256*s)
#define LG(q) "global_load_dwordx4 v[96+4*" #q ":99+4*" #q "], v[12:13], off\n\tv_lshl_add_u64 v[12:13], v[12:13], 0, s[54:55]\n\t"
#define NPRE 0
#define EARLY_LOADS "s_barrier\n\t" LG(0) LG(1) LG(2) LG(3)
#define LWS(p) "global_load_lds_dwordx4 v2, " p "\n\ts_mov_b64 exec, 0xff\n\tglobal_load_lds_dwordx4 v2, " p " offset:1024\n\ts_mov_b64 exec, -1\n\t"
    asm volatile("s_load_dwordx16 s[36:51], %[kap], 0x0\n\ts_load_dwordx16 s[16:31], %[kap], 0x40\n\t"
                 "v_readfirstlane_b32 %[w], %[tid]\n\ts_lshr_b32 s53, %[b], 3\n\tv_and_b32 v1, 63, %[tid]\n\ts_sub_u32 s56, s53, 24\n\t"
                 "s_cmpk_eq_u32 %[w], 640\n\ts_cselect_b32 s56, s56, -1\n\ts_cmp_lt_u32 s56, 8\n\ts_cbranch_scc0 Lb%=\n\t"
                 "s_getpc_b64 s[54:55]\n\ts_lshl_b32 s56, s56, 12\n\tv_min_u32 v3, 31, v1\n\tv_lshl_add_u32 v3, v3, 7, s56\n\tv_min_u32 v3, %[ce], v3\n\t"
                 "global_load_dword v3, v3, s[54:55]\n\ts_barrier\n\ts_waitcnt vmcnt(0)\n"
                 "Lx%=:\n\ts_waitcnt lgkmcnt(0)\n\ts_endpgm\n"
                 "Lb%=:\n\ts_and_b32 s52, %[b], 7\n\ts_lshl_b32 s57, s52, 7\n\ts_lshl2_add_u32 s55, s53, s57\n\t"
                 "s_load_dword %[rl], %[kap], s55 offset:0x80\n\ts_load_dword %[lb], %[kap], s57 offset:0x80\n\t"
                 "v_mov_b32 v0, 0\n\ts_cmp_lt_u32 s52, 2\n\ts_cselect_b32 s54, 24, 16\n\t"
                 "s_lshr_b32 %[w], %[w], 6\n\tv_lshlrev_b32 v2, 4, v1\n\ts_cmp_ge_u32 s53, s54\n\ts_cbranch_scc1 Ln%=\n\t"
                 "s_mul_i32 s54, %[w], 5120\n\tv_add_u32 v3, s54, v2\n\ts_and_b32 s55, %[w], 3\n\t"
                 "s_cmp_lt_u32 %[w], 8\n\ts_cbranch_scc1 Lt%=\n\ts_setprio 3\n\tv_add_u32 v4, 0x1000, v3\n\t"
                 "global_load_dwordx4 v[44:47], v3, %[tab] offset:3072\n\tglobal_load_dwordx4 v[48:51], v4, %[tab]\n\tglobal_load_dwordx4 v[40:43], v3, %[tab] offset:2048\n\t"
                 "s_and_b32 s56, s55, 1\n\ts_lshl1_add_u32 s56, s53, s56\n\ts_and_b32 s56, s56, 31\n\ts_cmp_lt_u32 s55, 2\n\ts_cselect_b32 s58, s57, 1024\n\t"
                 "v_lshl_add_u32 v62, s56, 6, v1\n\tv_mul_u32_u24 v62, %[rb], v62\n\tv_add_u32 v62, s58, v62\n"
                 "Lt%=:\n\ts_lshl4_add_u32 s54, s52, s53\n\ts_lshl3_add_u32 s56, s52, s53\n\ts_addk_i32 s56, 112\n\ts_cmp_lt_u32 s53, 16\n\ts_cselect_b32 s54, s54, s56\n\t"
                 "s_lshl2_add_u32 s54, s54, s55\n\t"
                 "s_bfe_u32 s56, %[w], 0x10002\n\ts_lshl1_add_u32 s55, s55, s56\n\ts_lshl_b32 m0, s55, 8\n\t"
                 "s_lshl1_add_u32 s56, s54, s56\n\ts_lshl_b32 s56, s56, 7\n\ts_lshl_b32 s54, s54, 1\n\t"
                 "v_lshl_add_u32 v4, v1, 1, s56\n\tv_mov_b32 v5, s54\n\tv_mov_b32 v6, 0x3f80\n\t"
                 "ds_write_addtid_b32 v0 offset:%[fl]\n\tds_write_addtid_b32 v0 offset:%[fu]\n\tds_write_addtid_b32 v6\n\t"
                 "v_mov_b64 v[64:65], 0\n\tv_mov_b64 v[66:67], 0\n\tv_mov_b64 v[68:69], 0\n\tv_mov_b64 v[70:71], 0\n\ts_waitcnt lgkmcnt(0)\n\t"
                 "s_cmp_ge_u32 %[w], 8\n\ts_cbranch_scc1 Lc%=\n\t"
                 "global_load_short_d16_hi v64, v4, s[38:39]\n\tglobal_load_short_d16_hi v65, v4, s[40:41]\n\tglobal_load_short_d16_hi v70, v5, s[50:51]\n\t"
                 "global_load_short_d16_hi v66, v4, s[42:43]\n\tglobal_load_short_d16_hi v67, v4, s[44:45]\n\tglobal_load_short_d16_hi v68, v4, s[46:47]\n\t"
                 "global_load_short_d16_hi v69, v4, s[48:49]\n"
                 "Lc%=:\n\ts_barrier\n\tglobal_load_short_d16_hi v71, v5, s[16:17]\n\tglobal_load_dword v72, v0, s[30:31] sc1\n\t"
                 "global_load_dwordx4 v[32:35], v3, %[tab]\n\tglobal_load_dwordx4 v[36:39], v3, %[tab] offset:1024\n\ts_branch Lw%=\n"
                 "Ln%=:\n\ts_mov_b32 m0, 0\n\ts_bitcmp1_b32 0x302, %[w]\n\tds_write_addtid_b32 v0\n\ts_cbranch_scc0 Lo%=\n\t"
                 "v_and_b32 v9, 0x330, v2\n\tv_bfe_u32 v7, v1, 2, 2\n\tv_lshrrev_b32 v8, 2, v9\n\tv_and_b32 v9, 48, v9\n\tv_and_or_b32 v9, v8, %[c0c], v9\n\t"
                 "v_mad_u32_u24 v7, v7, %[rb], v9\n\tv_lshrrev_b32 v8, 5, v1\n\tv_add_u32 v7, 2304, v7\n\ts_sub_i32 s56, %[w], 7\n\ts_max_i32 s56, s56, 0\n\t"
                 "v_mul_u32_u24 v8, 4480, v8\n\tv_add_u32 v9, 9216, v7\n\tv_add_u32 v8, v8, v7\n\tv_add_u32 v11, 9216, v8\n\ts_mov_b32 m0, %[wl]\n\t"
                 "s_waitcnt lgkmcnt(0)\n\ts_lshr_b32 s54, %[rl], 16\n\ts_cmp_ge_u32 s56, s54\n\ts_cbranch_scc1 Lx%=\n\t"
                 "s_bfe_u32 s54, %[rl], 0x80008\n\ts_add_u32 %[it], s54, s56\n\ts_mul_i32 s54, %[it], 18432\n\ts_add_u32 s54, s54, s36\n\ts_addc_u32 s55, s37, 0\n\t"
                 "global_load_dword v72, v0, s[30:31] sc1\n\t"
                 LWS("s[18:19]") "s_addk_i32 m0, 1152\n\ts_nop 0\n\t" LWS("s[26:27]")
                 LXS(0) LXS(1) LXS(2) LXS(3) LX(16, "v8", -1280) LX(17, "v11", -1280) "s_barrier\n\ts_branch Lw%=\n"
                 "Lo%=:\n\ts_cmp_lt_u32 %[w], 8\n\ts_cbranch_scc0 Lx%=\n\t"
                 "v_lshrrev_b32 v7, 4, v1\n\ts_lshl_b32 s56, %[w], 3\n\ts_sub_u32 s56, s56, 16\n\tv_add_u32 v7, s56, v7\n\tv_and_b32 v8, 0x70, v2\n\tv_mul_u32_u24 v7, 36864, v7\n\t"
                 "v_add_u32 v7, v7, v8\n\ts_mov_b32 s58, 0xff00ff00\n\ts_mov_b32 s59, s58\n\ts_mov_b64 s[54:55], 9216\n\ts_cmp_lg_u32 %[w], 0\n\t"
                 "s_waitcnt lgkmcnt(0)\n\ts_cbranch_scc1 Lp%=\n\tglobal_load_dword v136, v0, s[30:31] sc1\n\ts_barrier\n\ts_branch Lw%=\n"
                 "Lp%=:\n\ts_lshl_b32 s56, %[rl], 7\n\ts_and_b32 s56, s56, 0x7f80\n\ts_cbranch_scc0 Lv%=\n\ts_sub_u32 s56, s56, 128\n\t"
                 "v_add_u32 v8, s56, v7\n\tv_mov_b32 v9, 0\n\tv_lshl_add_u64 v[12:13], v[8:9], 0, s[20:21]\n\ts_mov_b64 exec, s[58:59]\n\tv_lshl_add_u64 v[12:13], v[8:9], 0, s[22:23]\n\ts_mov_b64 exec, -1\n\t"
                 EARLY_LOADS "s_branch Lw%=\n"
                 "Lv%=:\n\ts_barrier\n"
                 "Lw%=:\n\ts_waitcnt lgkmcnt(0)"
                 : "=&{s[36:51]}"(ka0), "=&{s[16:31]}"(ka1), [w] "=&s"(wave), [it] "=&s"(item), [rl] "=&s"(role), [lb] "=&s"(labw), [tid] "+{v0}"(tid)
                 : [b] "s"(b), [kap] "s"(__builtin_amdgcn_kernarg_segment_ptr()), [tab] "s"(&conv_tab), [rb] "s"(D_ * 2), [c0c] "s"(0xc0),
                   [fl] "n"(ROW_FL * TR), [fu] "n"(ROW_FU * TR), [ce] "n"(CODE_KB * 1024 - 128), [wl] "n"(LD_KA)
                 : "memory", "scc", "s52", "s53", "s54", "s55", "s56", "s57", "s58", "s59", "v1", "v2", "v3", "v4", "v5", "v6", "v7", "v8", "v9", "v10", "v11", "v12", "v13", ENTRY_CLOB);
    Params P;
#define KARG(T, v, i) (T)(((u64)v[2 * (i) + 1] << 32) | v[2 * (i)])
    P.x = KARG(const u16*, ka0, 0); P.wg = KARG(const u16*, ka1, 2); P.wu = KARG(const u16*, ka1, 3); P.wd = KARG(const u16*, ka1, 4);
    P.out = KARG(u16*, ka1, 6); P.ws = KARG(unsigned char*, ka1, 7);
    const int xcd = b & 7, ii = b >> 3, nconv = xcd < 2 ? 24 : 16;
    if (ii < nconv) {
        if (wave >= 8) { conv_wave<2>(P, sm, wave - 8, xcd, ii, labw); EXIT(); }
        if (wave >= 4) { conv_wave<1>(P, sm, wave - 4, xcd, ii, labw); EXIT(); }
        conv_wave<0>(P, sm, wave, xcd, ii, labw);
        EXIT();
    }
    if (wave == 1 || wave >= 8) {
        asm volatile("; claim\n\ts_setprio 3\n" ITEM_ASM : : [ws] "s"(P.ws), [it] "s"(item), [fl] "s"(FL_IT + 4 * (wave == 1 ? 0 : wave - 7)), [kb] "n"(WS_KEYS), [cb] "n"(WS_CAND), [wl] "n"(LD_KA)
                     : "memory", "vcc", "scc", "s88", "s89", "s92", "s93", "s98", ITEM_CLOB);
        EXIT();
    }
    const bool moe = (role & 255u) != 0u;
    if (moe && wave >= 2) { moe_weights(P, sm, wave - 2, (int)(role & 255u) - 1, role >> 16); EXIT(); }
    const int lane = fresh_lane();
    u32 tag0 = 0;
    if (!moe && wave >= 8 - HWAVES) {
        const Lab lb = lab_of(labw);
        if (b == RB) {
            if (wave == 7) {
                const u32 t_in = (u32)__builtin_amdgcn_s_memrealtime();
                __builtin_amdgcn_s_sleep(SEL_AT * 10 / 32);
                wait_ge<1>(sm + LD_TAG, 1);
                const u32 tag = W32(sm + LD_TAG);
                const __amdgpu_buffer_rsrc_t rws = ws_rsrc(P);
                const v2u a0 = ld2_sc1(rws, (lane & 6) ? WS_ACK + (u32)(128 + lane) * 128u : OOB), a1 = ld2_sc1(rws, WS_ACK + (u32)(192 + lane) * 128u);
                flag1(sm + FL_LATE, wave_max(umax(a0[0] == tag ? umin(a0[1] - t_in + 64u, 255u) : 1u, a1[0] == tag ? umin(a1[1] - t_in + 64u, 255u) : 1u)));
            }
            EXIT();
        }
        const u32 nh = (u32)(32 - nconv) - lb.n - (xcd == (RB & 7) ? 1u : 0u), nit = role >> 16;
        if (nit && 7 - wave >= HT0) wait_ge<1>(sm + FL_IT + 4 * umin(lane, nit - 1), 1);
        helper_prefetch(P, lane, (u32)(7 - wave) * nh + (u32)(ii - nconv) - lb.n, HWAVES * nh, lb);
        EXIT();
    }
    if (wave) EXIT();
    const u32 t_in = (u32)__builtin_amdgcn_s_memrealtime();
    {
        u32 epoch;
        asm volatile("s_waitcnt vmcnt(0) ; epoch" : "={v136}"(epoch)::"memory");
        tag0 = tag_of(rfl(epoch));
    }
    st2_sc1(ws_rsrc(P), lane == 0 ? WS_ACK + (u32)b * 128u : (b == TB && lane >= 12 && lane < 16 ? WS_T0 + (u32)(lane - 8) * 128u : OOB), (v2u){tag0, t_in});
    if (!moe && b != RB) EXIT();
    flag1(sm + LD_TAG, tag0);
    const u32 k8 = select8(P, lane, xcd, tag0, t_in, role & 255u);
    if (moe) {
        u32 src[4];
#pragma unroll
        for (int j = 0; j < 4; ++j) { src[j] = (((role & 255u) - 1u) * 64u + (u32)(32 + 8 * j + (lane >> 3))) * (D_ * 2) + (u32)(((lane & 7) ^ ((lane >> 3) & 6)) * 16); PIN(src[j]); }
        VMWAIT(0);
        wg_barrier();
        if (W2S) __builtin_amdgcn_s_sleep(W2S);
#pragma unroll
        for (int j = 0; j < W2A; ++j) dma_rows<8192>(P.wd, src[j], LD_WD + 4096 + j * 1024);
        wg_barrier();
#pragma unroll
        for (int j = W2A; j < 4; ++j) dma_rows<8192>(P.wd, src[j], LD_WD + 4096 + j * 1024);
        VMWAIT(0);
        wg_barrier();
        EXIT();
    }
    {
        const __amdgpu_buffer_rsrc_t rws = ws_rsrc(P);
        const u32 t_sel = (u32)__builtin_amdgcn_s_memrealtime() - t_in;
        wait_ge<0>(sm + FL_LATE, 1);
        st_sc1(rws, WS_SEL + (u32)(lane >> 3) * 128u + (u32)(lane & 7) * 16u, (v4u){k8, tag0, t_in + umax(PART_AT + (W32(sm + FL_LATE) * PART_K >> 3), t_sel + PART_SEL), tag0});
        __builtin_amdgcn_s_sleep(BUMP_DELAY);
#pragma unroll 1
        for (int spin = 0; spin < SPIN_CAP; ++spin) {
            MEMFENCE();
            u32 bad = 0;
#pragma unroll
            for (int k = 0; k < 4; ++k) bad |= ld2_sc1(rws, WS_ACK + (u32)(k * 64 + lane) * 128u)[0] ^ tag0;
            if (__all(bad == 0u)) break;
            __builtin_amdgcn_s_sleep(10);
        }
        if (lane == 0) __hip_atomic_store((GAS u32*)(P.ws + WS_EPOCH), tag0, RLX_AGENT);
    }
    VMWAIT(0);
}

extern "C" void kernel_launch(void* const* d_in, const int* in_sizes, int n_in, void* d_out, int out_size,
                              void* d_ws, size_t ws_size, hipStream_t stream) {
    Args A = mk_roles();
    Params& P = A.p;
    P.x = (const u16*)d_in[0];
    P.Are = (const u16*)d_in[1]; P.Aim = (const u16*)d_in[2];
    P.Bre = (const u16*)d_in[3]; P.Bim = (const u16*)d_in[4];
    P.Cre = (const u16*)d_in[5]; P.Cim = (const u16*)d_in[6];
    P.logdt = (const u16*)d_in[7]; P.dskip = (const u16*)d_in[8];
    P.wr = (const u16*)d_in[10]; P.wg = (const u16*)d_in[11]; P.wu = (const u16*)d_in[12]; P.wd = (const u16*)d_in[13];
    P.gw = (const u16*)d_in[14];
    P.out = (u16*)d_out;
    P.ws = (unsigned char*)d_ws;
    fused_kernel<<<256, NTHR, 0, stream>>>(A);
}
```

```cpp
#include <hip/hip_runtime.h>
#include <stdint.h>

typedef unsigned int u32;
typedef unsigned long long u64;
typedef unsigned short u16;
typedef u32 v4u __attribute__((ext_vector_type(4)));
typedef u32 v2u __attribute__((ext_vector_type(2)));
typedef u32 v8u __attribute__((ext_vector_type(8)));
typedef u32 v16u __attribute__((ext_vector_type(16)));
typedef float v4f __attribute__((ext_vector_type(4)));
typedef float v2f __attribute__((ext_vector_type(2)));
typedef short v8s __attribute__((ext_vector_type(8)));
typedef __bf16 v2bf __attribute__((ext_vector_type(2)));
#define GAS __attribute__((address_space(1)))
#define LAS __attribute__((address_space(3)))
#define DI __device__ __forceinline__
#define RLX_AGENT __ATOMIC_RELAXED, __HIP_MEMORY_SCOPE_AGENT
#define SB() __builtin_amdgcn_sched_barrier(0)
#define MEMFENCE() asm volatile("" ::: "memory")
#define PIN(x) asm volatile("" : "+v"(x))
#define VMWAIT(n) asm volatile("s_waitcnt vmcnt(%0)" ::"n"(n) : "memory")

#define CODE_KB 25
#define WIN 5
#define WIN0 5
#define PART_AT 610
#define NWE 6
#define W2A 2
#define W2S 0
#define FPRIO 2
#define PART_AT1 566
#define PART_K 4
#define PART_SEL 270
#define HWAVES 5
#define HT0 1
#define HWIN 2
#define HR0 320
#define PART_GAP 2
#define SLOW0 128
#define SEL_AT 240
#define TREF 1
#define SEL_GAP 3
#define BUMP_DELAY 36
#define NM_LIST 6, 6, 10, 10, 10, 10, 10, 10
#define NH_LIST 3, 3, 2, 2, 1, 0, 2, 2
#define NI_LIST 12, 12, 22, 22, 16, 0, 22, 22
constexpr int TB = 248;
constexpr int RB = 250;

constexpr int L_ = 2048, D_ = 576, N_ = 128, F_ = 4608;
constexpr int NTHR = 768;
constexpr int SPIN_CAP = 1 << 17;
constexpr u32 OOB = 0x7ffffff0u;
__device__ __forceinline__ unsigned tag_of(unsigned epoch) { return epoch == 0xffffffffu ? 1u : epoch + 1u; }

constexpr u32 WS_EPOCH = 0;
constexpr u32 WS_T1 = 1024;
constexpr u32 WS_T0 = 2048;
constexpr u32 WS_KEYS = 4096;
constexpr u32 WS_CAND = WS_KEYS + 16384;
constexpr u32 WS_ACK = WS_CAND + 8 * 128 * 128;
constexpr u32 WS_PART = WS_ACK + 256 * 128;
constexpr u32 WS_BYTES = WS_PART + 144 * 72 * 128;

constexpr int TR = 2080;
constexpr int LD_TB = 0;
constexpr int ROW_T1 = 16, ROW_FL = 26, ROW_FU = 27, ROW_T0 = 28, ROW_T2 = 44;
constexpr int LD_KT = LD_TB + ROW_T1 * TR;
constexpr int U_PAD = 256, U_CH = 2 * (U_PAD + 2048);
constexpr int LD_U = LD_TB + 60 * TR;
constexpr int PA = 550, PB = 545, KA_CP = 1216, KA_CH = 2 * KA_CP;
constexpr int LD_KA = LD_U + 4 * U_CH;
constexpr int G_CH = 896;
constexpr int LD_G = LD_KA + 4 * KA_CH;
constexpr int LD_BD = LD_G + 4 * G_CH;
constexpr int LD_CONV_END = LD_BD + 5120;
constexpr int FL_IT = 64;
constexpr int LD_TAG = 48;
constexpr int LD_SCR = 512;
constexpr int LD_XS = 1024 + 2 * 9216;
constexpr int LD_WD = LD_XS + 9216;
constexpr int LD_RED = LD_WD + 9 * 8192;
constexpr int HB_ROW = 144;
constexpr int LD_HB = LD_RED + 6 * 4096;
constexpr int LD_MOE_END = LD_HB + 8 * HB_ROW;
constexpr int LDS_BYTES = LD_CONV_END > LD_MOE_END ? LD_CONV_END : LD_MOE_END;
static_assert(LDS_BYTES > 81920 && LDS_BYTES <= 163840, "lds: one workgroup per CU");
static_assert(LD_BD % 128 == 0 && LD_MOE_END <= LD_KA && LD_U % 16 == 0 && LD_KA % 16 == 0 && LD_G % 16 == 0 && LD_WD % 1024 == 0, "align");

struct Params {
    const u16 *x, *Are, *Aim, *Bre, *Bim, *Cre, *Cim, *logdt, *dskip, *wr, *wg, *wu, *wd, *gw;
    u16* out;
    unsigned char* ws;
};
struct Args { Params p; unsigned role[256]; };
constexpr int nconv_of(int label) { return label < 2 ? 24 : 16; }
constexpr Args mk_roles() {
    Args A{};
    const int nm[8] = {NM_LIST}, nh[8] = {NH_LIST}, ni[8] = {NI_LIST};
    int m0 = 0, i0 = 0;
    bool ok = true;
    for (int x = 0; x < 8; ++x) {
        const int nc = nconv_of(x), first = x * 32, w = 32 - nc;
        for (int ii = 0; ii < nc; ++ii) A.role[first + ii] = (unsigned)(m0 | nm[x] << 8 | (65536 / nm[x] + 1) << 16);
        const int rest = ni[x] - (w - nm[x]) * nh[x];
        ok = ok && rest >= 0;
        for (int ii = 31; ii >= nc; --ii) {
            const int cnt = ii - nc >= nm[x] ? nh[x] : rest / nm[x] + (ii - nc < rest % nm[x] ? 1 : 0);
            A.role[first + ii] = (unsigned)((ii - nc < nm[x] ? m0 + ii - nc + 1 : 0) | (cnt ? i0 : 0) << 8 | cnt << 16);
            i0 += cnt; ok = ok && cnt <= 3;
        }
        m0 += nm[x];
    }
    A.role[255] |= (m0 != 72 || i0 != 128 || !ok) ? 0x80000000u : 0u;
    return A;
}
static_assert(!(mk_roles().role[255] >> 31) && (mk_roles().role[(RB & 7) * 32 + (RB >> 3)] & 255) == 0, "roles");

#define EXIT() do { VMWAIT(0); __builtin_amdgcn_endpgm(); } while (0)

typedef LAS unsigned char* lptr;
typedef const LAS unsigned char* clptr;
DI float bf_lo(u32 v) { return __uint_as_float(v << 16); }
DI float bf_hi(u32 v) { return __uint_as_float(v & 0xffff0000u); }
DI u32 pk_bf16(float a, float b) { v2f f = {a, b}; return __builtin_bit_cast(u32, __builtin_convertvector(f, v2bf)); }
DI float dot2(u32 a, u32 b, float c) { return __builtin_amdgcn_fdot2_f32_bf16(__builtin_bit_cast(v2bf, a), __builtin_bit_cast(v2bf, b), c, false); }
DI float sigmoidf_(float s) { return __builtin_amdgcn_rcpf(1.f + __builtin_amdgcn_exp2f(-1.44269504f * s)); }
template <int CTRL, int RM> DI u32 dpp0(u32 v) { return (u32)__builtin_amdgcn_update_dpp(0, (int)v, CTRL, RM, 0xf, true); }
template <int CTRL, int RM> DI float dppf(float v) { return __uint_as_float(dpp0<CTRL, RM>(__float_as_uint(v))); }
DI u32 umax(u32 a, u32 b) { return a > b ? a : b; }
DI u32 umin(u32 a, u32 b) { return a < b ? a : b; }
DI int fresh_lane() { int l; asm volatile("v_mbcnt_lo_u32_b32 %0, -1, 0\n\tv_mbcnt_hi_u32_b32 %0, -1, %0" : "=v"(l)); return l; }
DI u32 rfl(u32 v) { return (u32)__builtin_amdgcn_readfirstlane((int)v); }
DI u32 rdl(u32 v, int l) { return (u32)__builtin_amdgcn_readlane((int)v, l); }
DI u32 wave_max(u32 v) {
    v = umax(v, dpp0<0x111, 0xf>(v)); v = umax(v, dpp0<0x112, 0xf>(v)); v = umax(v, dpp0<0x114, 0xf>(v)); v = umax(v, dpp0<0x118, 0xf>(v));
    v = umax(v, dpp0<0x142, 0xa>(v)); v = umax(v, dpp0<0x143, 0xc>(v));
    return rdl(v, 63);
}
DI u32 perm_lo(u32 hi, u32 lo) { return __builtin_amdgcn_perm(hi, lo, 0x05040100u); }
DI u32 perm_hi(u32 hi, u32 lo) { return __builtin_amdgcn_perm(hi, lo, 0x07060302u); }
typedef __bf16 v8bf __attribute__((ext_vector_type(8)));
typedef __bf16 v16bf __attribute__((ext_vector_type(16)));
DI v4f smfmac(v4u a, v8u b, v4f c, u32 idx) { return __builtin_amdgcn_smfmac_f32_16x16x64_bf16(__builtin_bit_cast(v8bf, a), __builtin_bit_cast(v16bf, b), c, (int)idx, 0, 0); }
DI __amdgpu_buffer_rsrc_t mk_rsrc(const void* p, u32 n) { return __builtin_amdgcn_make_buffer_rsrc((void*)p, 0, n, 0x00020000); }
DI __amdgpu_buffer_rsrc_t ws_rsrc(const Params& P) { return mk_rsrc(P.ws, WS_BYTES); }
DI v4u ld_sc1(__amdgpu_buffer_rsrc_t r, u32 off) { return __builtin_amdgcn_raw_buffer_load_b128(r, off, 0, 16); }
DI v2u ld2_sc1(__amdgpu_buffer_rsrc_t r, u32 off) { return __builtin_amdgcn_raw_buffer_load_b64(r, off, 0, 16); }
DI void st_sc1(__amdgpu_buffer_rsrc_t r, u32 off, v4u v) { __builtin_amdgcn_raw_buffer_store_b128(v, r, off, 0, 16); }
DI void st2_sc1(__amdgpu_buffer_rsrc_t r, u32 off, v2u v) { __builtin_amdgcn_raw_buffer_store_b64(v, r, off, 0, 16); }
DI float g_of(u32 p) { return (float)__builtin_bit_cast(_Float16, (u16)p); }
DI bool tag_ok(v4u v, u32 tag) { return (v[1] == tag) & (v[3] == tag); }
DI v2u tr_read(clptr p) {
    typedef short v4s __attribute__((ext_vector_type(4)));
    return __builtin_bit_cast(v2u, __builtin_amdgcn_ds_read_tr16_b64_v4i16((LAS v4s*)p));
}
DI void fmac_(float& acc, float a, float b) { asm("v_fmac_f32 %0, %1, %2" : "+v"(acc) : "v"(a), "v"(b)); }
DI float mul_(float a, float b) { float r; asm("v_mul_f32 %0, %1, %2" : "=v"(r) : "v"(a), "v"(b)); return r; }
#define W16(p) (*(volatile LAS u16*)(p))
#define W32(p) (*(volatile LAS u32*)(p))
#define W64(p) (*(volatile LAS v2u*)(p))
#define W128(p) (*(volatile LAS v4u*)(p))

template <int SLEEP> DI void wait_ge(clptr p, u32 target) {
    u32 v, n;
    asm volatile("s_mov_b32 %1, 0x100000\n"
                 "Lw%=:\n\tds_read_b32 %0, %2\n\ts_waitcnt lgkmcnt(0)\n\tv_cmp_gt_u32 vcc, %3, %0\n\ts_cbranch_vccz Ld%=\n\t"
                 "s_sleep %4\n\ts_sub_u32 %1, %1, 1\n\ts_cbranch_scc0 Lw%=\n"
                 "Ld%=:"
                 : "=&v"(v), "=&s"(n) : "v"(p), "s"(target), "n"(SLEEP) : "memory", "vcc", "scc");
}
DI void flag1(lptr p, u32 v) {
    asm volatile("s_mov_b64 exec, 1\n\tds_write_b32 %0, %1\n\ts_mov_b64 exec, -1" ::"v"(p), "v"(v) : "memory");
}
template <int OFF> DI void put_row(u32 v) { asm volatile("ds_write_addtid_b32 %0 offset:%1" ::"v"(v), "n"(OFF) : "memory"); }
DI void wg_barrier() { asm volatile("s_waitcnt lgkmcnt(0)\n\ts_barrier" ::: "memory"); }

DI void touch(const void* base, u32 off) {
    asm volatile("s_movk_i32 m0, %2\n\ts_nop 0\n\tglobal_load_lds_dword %0, %1" ::"v"(off), "s"(base), "n"(LD_SCR) : "memory");
}
template <int STEP> DI void dma_rows(const void* base, u32 off, u32 dst) {
#define DR(q) "global_load_lds_dwordx4 %0, %1 offset:128*" #q "\n\ts_addk_i32 m0, %3\n\ts_nop 0\n\t"
    asm volatile("s_mov_b32 m0, %2\n\ts_nop 0\n\t" DR(0) DR(1) DR(2) DR(3) DR(4) DR(5) DR(6) DR(7) "global_load_lds_dwordx4 %0, %1 offset:1024" ::"v"(off), "s"(base), "s"(dst), "n"(STEP - 128) : "memory", "scc");
}
DI int cg_of(int xcd, int ii) { return ii < 16 ? 16 * xcd + ii : 112 + 8 * xcd + ii; }
struct Lab { u32 m0, n, mg; };
DI Lab lab_of(u32 w) { return {w & 255u, (w >> 8) & 255u, w >> 16}; }

DI u32 top8_slow(const u32 (&all)[32], int lane) {
    u32 g8 = 0, bound = 0xffffffffu;
#pragma unroll 1
    for (int rd = 0; rd < 8; ++rd) {
        u32 m = 0;
#pragma unroll
        for (int q = 0; q < 32; ++q) { m = umax(m, all[q] <= bound ? all[q] : 0u); PIN(m); }
        const u32 M = wave_max(m);
        if ((lane & 7) == rd) g8 = M;
        bound = M - 1u;
    }
    return g8;
}
DI u32 from_lane(u32 v, int l) { return (u32)__builtin_amdgcn_ds_bpermute(4 * l, (int)v); }

struct ConvTab { u32 v[(12 * 5 + 5) * 256]; };
constexpr ConvTab mk_tab() {
    ConvTab t{};
    for (int kind = 0; kind < 3; ++kind) for (int ch = 0; ch < 4; ++ch) for (int lane = 0; lane < 64; ++lane) {
        const int c = lane & 15, kg = lane >> 4;
        const int ka = LD_KA + ch * KA_CH, gq = LD_G + ch * G_CH;
        const int kb = ka + ((c & 1) ? KA_CP + 2 * PB : 2 * PA) - 2 * (c + 16 - 8 * kg) - 64 * 15;
        u32 ad[20] = {};
        if (kind < 2) {
            ad[0] = LD_TB + ROW_FU * TR + ch * 512;
            ad[1] = LD_U + ch * U_CH + 2 * (U_PAD - 16 + 16 * c + 8 * kg) - 7 * 64;
            ad[2] = LD_TB + ROW_FU * TR + (lane & 3) * 516 + 256;
            ad[3] = LD_TB + (ROW_T0 + (c >> 3)) * TR + ch * 512 + 64 * (c & 7) + 16 * (kg ^ ((c >> 1) & 3));
            ad[4] = kind ? gq + 4 * (15 - c + 4 * kg) : kb;
            ad[5] = kind ? kb - 64 : LD_TB + (c >> 3) * TR + ch * 512 + 64 * (c & 7) + 16 * (kg ^ ((c >> 1) & 3));
            ad[6] = kind ? LD_BD + lane * 16 : LD_TB + ROW_FL * TR + ch * 512 + 256;
            ad[7] = gq;
        } else {
            ad[0] = LD_TB + ROW_FL * TR + ch * 512 + (lane & 1) * 260;
            ad[1] = LD_TB + c * TR + ch * 512 + kg * 16;
            ad[2] = LD_TB + (ROW_T1 + (c & 3) + (c >> 3) + 5 * ((c >> 2) & 1)) * TR + ch * 512 + kg * 16;
            ad[3] = ka + 2 * (PA - 3 - (4 * (kg & 1) + 128 * (kg >> 1) + 8 * c));
            ad[4] = ka + (kg < 2 ? 2 * (PA - 6) : KA_CP + 2 * (PB - 9)) - 2 * (256 + 128 * (kg & 1) + 8 * c);
            ad[5] = gq + (kg < 2 ? (c & 1) * 256 + 4 * (47 - 24) + 2 * kg : 512 + 4 * (63 - 24) + 2 * (c & 1) - 128 * (kg - 2)) - 4 * (c >> 1);
            ad[6] = kg < 2 ? 0x01000302u : 0x03020302u;
            ad[7] = kg < 2 ? 0x01000100u : 0x01000302u;
            ad[8] = lane == 0 ? 0x05040100u : ad[7];
            ad[9] = (128 * ch + 2 * lane) * (D_ * 2);
            ad[10] = LD_U + 2 * (U_PAD + 128 * ch + 2 * lane);
            ad[11] = LD_TB + ch * TR + 64 * (lane >> 3) + 16 * (((lane >> 1) ^ (lane >> 4)) & 3) + 8 * (lane & 1);
            ad[12] = WS_KEYS + (64 * ch + lane) * 16;
            ad[13] = LD_TB + ROW_FL * TR + (lane & 3) * 516;
            ad[14] = WS_PART + (18 * ch + (lane < 17 ? lane : 17)) * 128;
            ad[15] = LD_TB + ROW_FL * TR + (lane & 3) * 516 + (lane & 4) * 64;
            ad[16] = lane * 128;
            ad[17] = lane < 32 ? ka + (lane >> 4) * KA_CP + (c < 6 ? c * 16 : 1088 + (c & 3) * 16) : LD_U + ch * U_CH + (lane - 32) * 16;
            ad[18] = gq + (lane < 55 ? lane : 55) * 16;
            ad[19] = ka;
        }
        for (int k = 0; k < 20; ++k) t.v[((kind * 4 + ch) * 5 + (k >> 2)) * 256 + lane * 4 + (k & 3)] = ad[k];
    }
    const int lo[5] = {3, 6, 5, 4, 3}, hi[5] = {5, 10, 11, 12, 4};
    for (int k = 0; k < 5; ++k) for (int lane = 0; lane < 64; ++lane) for (int e = 0; e < 8; ++e) {
        const int l = (lane & 15) + 16 - 8 * (lane >> 4) - e;
        if (l >= lo[k] && l <= hi[k]) t.v[(60 + k) * 256 + lane * 4 + (e >> 1)] |= (k < 4 ? 0x3f80u : 0xffffu) << (16 * (e & 1));
    }
    return t;
}
static __device__ const ConvTab conv_tab = mk_tab();
#define CONV_A_ASM \
    "s_setprio 1\n" \
    "s_waitcnt vmcnt(4)\n" \
    "v_mul_f32_e32 v0, 0x3fb8aa3b, v70\n" \
    "v_exp_f32_e32 v0, v0\n" \
    "v_mul_f32_e32 v1, 0x3fb8aa3b, v64\n" \
    "v_cvt_pk_bf16_f32 v0, 0, v0\n" \
    "v_mul_f32_e32 v2, 0.15915494, v65\n" \
    "v_mul_f32_e32 v1, v0, v1\n" \
    "v_mul_f32_e32 v2, v0, v2\n" \
    "v_mul_f32_e32 v4, 0x41000000, v2\n" \
    "v_mul_f32_e32 v3, 0x41000000, v1\n" \
    "v_fract_f32_e32 v4, v4\n" \
    "v_exp_f32_e32 v3, v3\n" \
    "v_cos_f32_e32 v8, v4\n" \
    "v_sin_f32_e32 v9, v4\n" \
    "v_mul_f32_e32 v66, v0, v66\n" \
    "v_mul_f32_e64 v8, v3, v8\n" \
    "v_mul_f32_e64 v9, v3, v9\n" \
    "v_pk_mul_f32 v[18:19], v[8:9], v[8:9] op_sel_hi:[0,1]\n" \
    "v_cvt_pk_bf16_f32 v30, v8, v9\n" \
    "v_pk_fma_f32 v[14:15], v[8:9], v[8:9], v[18:19] op_sel:[1,1,0] op_sel_hi:[0,1,1] neg_lo:[0,1,0]\n" \
    "ds_write_addtid_b32 v30 offset:2080\n" \
    "v_pk_mul_f32 v[18:19], v[8:9], v[14:15] op_sel_hi:[0,1]\n" \
    "v_cvt_pk_bf16_f32 v31, v14, v15\n" \
    "v_pk_fma_f32 v[14:15], v[14:15], v[8:9], v[18:19] op_sel:[1,1,0] op_sel_hi:[0,1,1] neg_lo:[0,1,0]\n" \
    "ds_write_addtid_b32 v31 offset:4160\n" \
    "v_pk_mul_f32 v[18:19], v[8:9], v[14:15] op_sel_hi:[0,1]\n" \
    "v_cvt_pk_bf16_f32 v30, v14, v15\n" \
    "v_pk_fma_f32 v[14:15], v[14:15], v[8:9], v[18:19] op_sel:[1,1,0] op_sel_hi:[0,1,1] neg_lo:[0,1,0]\n" \
    "ds_write_addtid_b32 v30 offset:6240\n" \
    "v_pk_mul_f32 v[18:19], v[8:9], v[14:15] op_sel_hi:[0,1]\n" \
    "v_cvt_pk_bf16_f32 v31, v14, v15\n" \
    "v_pk_fma_f32 v[14:15], v[14:15], v[8:9], v[18:19] op_sel:[1,1,0] op_sel_hi:[0,1,1] neg_lo:[0,1,0]\n" \
    "ds_write_addtid_b32 v31 offset:8320\n" \
    "v_mul_f32_e32 v4, 0x43000000, v2\n" \
    "v_mul_f32_e32 v3, 0x43000000, v1\n" \
    "v_fract_f32_e32 v4, v4\n" \
    "v_exp_f32_e32 v3, v3\n" \
    "v_cos_f32_e32 v12, v4\n" \
    "v_sin_f32_e32 v13, v4\n" \
    "v_pk_mul_f32 v[18:19], v[8:9], v[14:15] op_sel_hi:[0,1]\n" \
    "v_cvt_pk_bf16_f32 v30, v14, v15\n" \
    "v_pk_fma_f32 v[14:15], v[14:15], v[8:9], v[18:19] op_sel:[1,1,0] op_sel_hi:[0,1,1] neg_lo:[0,1,0]\n" \
    "ds_write_addtid_b32 v30 offset:10400\n" \
    "v_pk_mul_f32 v[18:19], v[8:9], v[14:15] op_sel_hi:[0,1]\n" \
    "v_cvt_pk_bf16_f32 v31, v14, v15\n" \
    "v_pk_fma_f32 v[14:15], v[14:15], v[8:9], v[18:19] op_sel:[1,1,0] op_sel_hi:[0,1,1] neg_lo:[0,1,0]\n" \
    "ds_write_addtid_b32 v31 offset:12480\n" \
    "v_mul_f32_e64 v12, v3, v12\n" \
    "v_mul_f32_e64 v13, -v3, v13\n" \
    "v_pk_mul_f32 v[18:19], v[8:9], v[14:15] op_sel_hi:[0,1]\n" \
    "v_cvt_pk_bf16_f32 v30, v14, v15\n" \
    "v_pk_fma_f32 v[14:15], v[14:15], v[8:9], v[18:19] op_sel:[1,1,0] op_sel_hi:[0,1,1] neg_lo:[0,1,0]\n" \
    "ds_write_addtid_b32 v30 offset:14560\n" \
    "v_pk_mul_f32 v[18:19], v[8:9], v[14:15] op_sel_hi:[0,1]\n" \
    "v_cvt_pk_bf16_f32 v31, v14, v15\n" \
    "v_pk_fma_f32 v[14:15], v[14:15], v[8:9], v[18:19] op_sel:[1,1,0] op_sel_hi:[0,1,1] neg_lo:[0,1,0]\n" \
    "ds_write_addtid_b32 v31 offset:16640\n" \
    "v_fract_f32_e32 v6, v2\n" \
    "v_exp_f32_e32 v5, v1\n" \
    "v_cos_f32_e32 v10, v6\n" \
    "v_sin_f32_e32 v11, v6\n" \
    "v_pk_mul_f32 v[18:19], v[8:9], v[14:15] op_sel_hi:[0,1]\n" \
    "v_cvt_pk_bf16_f32 v30, v14, v15\n" \
    "v_pk_fma_f32 v[14:15], v[14:15], v[8:9], v[18:19] op_sel:[1,1,0] op_sel_hi:[0,1,1] neg_lo:[0,1,0]\n" \
    "ds_write_addtid_b32 v30 offset:18720\n" \
    "v_pk_mul_f32 v[18:19], v[8:9], v[14:15] op_sel_hi:[0,1]\n" \
    "v_cvt_pk_bf16_f32 v31, v14, v15\n" \
    "v_pk_fma_f32 v[14:15], v[14:15], v[8:9], v[18:19] op_sel:[1,1,0] op_sel_hi:[0,1,1] neg_lo:[0,1,0]\n" \
    "ds_write_addtid_b32 v31 offset:20800\n" \
    "v_mul_f32_e64 v10, v5, v10\n" \
    "v_mul_f32_e64 v11, -v5, v11\n" \
    "v_pk_mul_f32 v[18:19], v[8:9], v[14:15] op_sel_hi:[0,1]\n" \
    "v_cvt_pk_bf16_f32 v30, v14, v15\n" \
    "v_pk_fma_f32 v[14:15], v[14:15], v[8:9], v[18:19] op_sel:[1,1,0] op_sel_hi:[0,1,1] neg_lo:[0,1,0]\n" \
    "ds_write_addtid_b32 v30 offset:22880\n" \
    "v_mul_f32_e64 v67, v0, v67\n" \
    "v_mul_f32_e64 v16, v68, v66\n" \
    "v_mul_f32_e64 v17, -v68, v67\n" \
    "v_fma_f32 v16, -v69, v67, v16\n" \
    "v_fma_f32 v17, -v69, v66, v17\n" \
    "v_pk_mul_f32 v[18:19], v[8:9], v[14:15] op_sel_hi:[0,1]\n" \
    "v_cvt_pk_bf16_f32 v31, v14, v15\n" \
    "v_pk_fma_f32 v[14:15], v[14:15], v[8:9], v[18:19] op_sel:[1,1,0] op_sel_hi:[0,1,1] neg_lo:[0,1,0]\n" \
    "ds_write_addtid_b32 v31 offset:24960\n" \
    "v_pk_mul_f32 v[18:19], v[8:9], v[14:15] op_sel_hi:[0,1]\n" \
    "v_cvt_pk_bf16_f32 v30, v14, v15\n" \
    "v_pk_fma_f32 v[14:15], v[14:15], v[8:9], v[18:19] op_sel:[1,1,0] op_sel_hi:[0,1,1] neg_lo:[0,1,0]\n" \
    "ds_write_addtid_b32 v30 offset:27040\n" \
    "v_pk_mul_f32 v[18:19], v[8:9], v[14:15] op_sel_hi:[0,1]\n" \
    "v_cvt_pk_bf16_f32 v31, v14, v15\n" \
    "v_pk_fma_f32 v[14:15], v[14:15], v[8:9], v[18:19] op_sel:[1,1,0] op_sel_hi:[0,1,1] neg_lo:[0,1,0]\n" \
    "ds_write_addtid_b32 v31 offset:29120\n" \
    "v_pk_mul_f32 v[18:19], v[12:13], v[16:17] op_sel_hi:[0,1]\n" \
    "v_cvt_pk_bf16_f32 v30, v14, v15\n" \
    "v_pk_fma_f32 v[20:21], v[16:17], v[12:13], v[18:19] op_sel:[1,1,0] op_sel_hi:[0,1,1] neg_lo:[0,1,0]\n" \
    "ds_write_addtid_b32 v30 offset:31200\n" \
    "v_mov_b32_e32 v61, 1\n" \
    "ds_write_addtid_b32 v61 offset:54080\n" \
    "s_add_u32 m0, %[m0a], 54080\n" \
    "v_cvt_pk_bf16_f32 v31, v16, v17\n" \
    "ds_write_addtid_b32 v31 offset:4160\n" \
    "v_pk_mul_f32 v[18:19], v[10:11], v[16:17] op_sel_hi:[0,1]\n" \
    "v_cvt_pk_bf16_f32 v30, v20, v21\n" \
    "v_pk_fma_f32 v[16:17], v[16:17], v[10:11], v[18:19] op_sel:[1,1,0] op_sel_hi:[0,1,1] neg_lo:[0,1,0]\n" \
    "ds_write_addtid_b32 v30 offset:20800\n" \
    "v_pk_mul_f32 v[18:19], v[12:13], v[16:17] op_sel_hi:[0,1]\n" \
    "v_cvt_pk_bf16_f32 v31, v16, v17\n" \
    "v_pk_fma_f32 v[14:15], v[16:17], v[12:13], v[18:19] op_sel:[1,1,0] op_sel_hi:[0,1,1] neg_lo:[0,1,0]\n" \
    "ds_write_addtid_b32 v31 offset:6240\n" \
    "v_pk_mul_f32 v[18:19], v[10:11], v[16:17] op_sel_hi:[0,1]\n" \
    "v_cvt_pk_bf16_f32 v30, v14, v15\n" \
    "v_pk_fma_f32 v[16:17], v[16:17], v[10:11], v[18:19] op_sel:[1,1,0] op_sel_hi:[0,1,1] neg_lo:[0,1,0]\n" \
    "ds_write_addtid_b32 v30 offset:22880\n" \
    "v_pk_mul_f32 v[18:19], v[12:13], v[16:17] op_sel_hi:[0,1]\n" \
    "v_cvt_pk_bf16_f32 v31, v16, v17\n" \
    "v_pk_fma_f32 v[22:23], v[16:17], v[12:13], v[18:19] op_sel:[1,1,0] op_sel_hi:[0,1,1] neg_lo:[0,1,0]\n" \
    "ds_write_addtid_b32 v31 offset:8320\n" \
    "v_pk_mul_f32 v[18:19], v[10:11], v[16:17] op_sel_hi:[0,1]\n" \
    "v_cvt_pk_bf16_f32 v30, v22, v23\n" \
    "v_pk_fma_f32 v[16:17], v[16:17], v[10:11], v[18:19] op_sel:[1,1,0] op_sel_hi:[0,1,1] neg_lo:[0,1,0]\n" \
    "ds_write_addtid_b32 v30 offset:24960\n" \
    "v_pk_mul_f32 v[18:19], v[12:13], v[16:17] op_sel_hi:[0,1]\n" \
    "v_cvt_pk_bf16_f32 v31, v16, v17\n" \
    "v_pk_fma_f32 v[14:15], v[16:17], v[12:13], v[18:19] op_sel:[1,1,0] op_sel_hi:[0,1,1] neg_lo:[0,1,0]\n" \
    "ds_write_addtid_b32 v31 offset:10400\n" \
    "v_pk_mul_f32 v[18:19], v[10:11], v[16:17] op_sel_hi:[0,1]\n" \
    "v_cvt_pk_bf16_f32 v30, v14, v15\n" \
    "v_pk_fma_f32 v[16:17], v[16:17], v[10:11], v[18:19] op_sel:[1,1,0] op_sel_hi:[0,1,1] neg_lo:[0,1,0]\n" \
    "ds_write_addtid_b32 v30 offset:27040\n" \
    "v_pk_mul_f32 v[18:19], v[12:13], v[16:17] op_sel_hi:[0,1]\n" \
    "v_cvt_pk_bf16_f32 v31, v16, v17\n" \
    "v_pk_fma_f32 v[24:25], v[16:17], v[12:13], v[18:19] op_sel:[1,1,0] op_sel_hi:[0,1,1] neg_lo:[0,1,0]\n" \
    "ds_write_addtid_b32 v31 offset:12480\n" \
    "v_pk_mul_f32 v[18:19], v[10:11], v[16:17] op_sel_hi:[0,1]\n" \
    "v_cvt_pk_bf16_f32 v30, v24, v25\n" \
    "v_pk_fma_f32 v[16:17], v[16:17], v[10:11], v[18:19] op_sel:[1,1,0] op_sel_hi:[0,1,1] neg_lo:[0,1,0]\n" \
    "ds_write_addtid_b32 v30 offset:29120\n" \
    "v_pk_mul_f32 v[18:19], v[12:13], v[16:17] op_sel_hi:[0,1]\n" \
    "v_cvt_pk_bf16_f32 v31, v16, v17\n" \
    "v_pk_fma_f32 v[14:15], v[16:17], v[12:13], v[18:19] op_sel:[1,1,0] op_sel_hi:[0,1,1] neg_lo:[0,1,0]\n" \
    "ds_write_addtid_b32 v31 offset:14560\n" \
    "v_pk_mul_f32 v[18:19], v[10:11], v[16:17] op_sel_hi:[0,1]\n" \
    "v_cvt_pk_bf16_f32 v30, v14, v15\n" \
    "v_pk_fma_f32 v[16:17], v[16:17], v[10:11], v[18:19] op_sel:[1,1,0] op_sel_hi:[0,1,1] neg_lo:[0,1,0]\n" \
    "ds_write_addtid_b32 v30 offset:31200\n" \
    "v_pk_mul_f32 v[18:19], v[12:13], v[16:17] op_sel_hi:[0,1]\n" \
    "v_cvt_pk_bf16_f32 v31, v16, v17\n" \
    "v_pk_fma_f32 v[26:27], v[16:17], v[12:13], v[18:19] op_sel:[1,1,0] op_sel_hi:[0,1,1] neg_lo:[0,1,0]\n" \
    "ds_write_addtid_b32 v31 offset:16640\n" \
    "v_pk_mul_f32 v[18:19], v[10:11], v[16:17] op_sel_hi:[0,1]\n" \
    "v_cvt_pk_bf16_f32 v30, v26, v27\n" \
    "v_pk_fma_f32 v[16:17], v[16:17], v[10:11], v[18:19] op_sel:[1,1,0] op_sel_hi:[0,1,1] neg_lo:[0,1,0]\n" \
    "ds_write_addtid_b32 v30 offset:33280\n" \
    "v_pk_mul_f32 v[18:19], v[12:13], v[16:17] op_sel_hi:[0,1]\n" \
    "v_cvt_pk_bf16_f32 v31, v16, v17\n" \
    "v_pk_fma_f32 v[14:15], v[16:17], v[12:13], v[18:19] op_sel:[1,1,0] op_sel_hi:[0,1,1] neg_lo:[0,1,0]\n" \
    "ds_write_addtid_b32 v31 offset:18720\n" \
    "s_waitcnt vmcnt(0)\n" \
    "v_pk_mul_f32 v[18:19], v[12:13], v[20:21] op_sel_hi:[0,1]\n" \
    "v_cvt_pk_bf16_f32 v30, v14, v15\n" \
    "v_pk_fma_f32 v[20:21], v[20:21], v[12:13], v[18:19] op_sel:[1,1,0] op_sel_hi:[0,1,1] neg_lo:[0,1,0]\n" \
    "ds_write_addtid_b32 v30 offset:35360\n" \
    "v_mov_b32_e32 v61, 2\n" \
    "ds_write_addtid_b32 v61 offset:0\n" \
    "s_mov_b32 m0, %[m0a]\n" \
    "v_pk_mul_f32 v[18:19], v[12:13], v[22:23] op_sel_hi:[0,1]\n" \
    "v_cvt_pk_bf16_f32 v31, v20, v21\n" \
    "v_pk_fma_f32 v[22:23], v[22:23], v[12:13], v[18:19] op_sel:[1,1,0] op_sel_hi:[0,1,1] neg_lo:[0,1,0]\n" \
    "ds_write_addtid_b32 v31 offset:33280\n" \
    "v_pk_mul_f32 v[18:19], v[12:13], v[24:25] op_sel_hi:[0,1]\n" \
    "v_cvt_pk_bf16_f32 v30, v22, v23\n" \
    "v_pk_fma_f32 v[24:25], v[24:25], v[12:13], v[18:19] op_sel:[1,1,0] op_sel_hi:[0,1,1] neg_lo:[0,1,0]\n" \
    "ds_write_addtid_b32 v30 offset:35360\n" \
    "v_pk_mul_f32 v[18:19], v[12:13], v[26:27] op_sel_hi:[0,1]\n" \
    "v_cvt_pk_bf16_f32 v31, v24, v25\n" \
    "v_pk_fma_f32 v[26:27], v[26:27], v[12:13], v[18:19] op_sel:[1,1,0] op_sel_hi:[0,1,1] neg_lo:[0,1,0]\n" \
    "ds_write_addtid_b32 v31 offset:37440\n" \
    "v_pk_mul_f32 v[18:19], v[10:11], v[14:15] op_sel_hi:[0,1]\n" \
    "v_cvt_pk_bf16_f32 v30, v26, v27\n" \
    "v_pk_fma_f32 v[28:29], v[14:15], v[10:11], v[18:19] op_sel:[1,1,0] op_sel_hi:[0,1,1] neg_lo:[0,1,0]\n" \
    "ds_write_addtid_b32 v30 offset:39520\n" \
    "v_pk_mul_f32 v[18:19], v[12:13], v[28:29] op_sel_hi:[0,1]\n" \
    "v_pk_fma_f32 v[28:29], v[28:29], v[12:13], v[18:19] op_sel:[1,1,0] op_sel_hi:[0,1,1] neg_lo:[0,1,0]\n" \
    "v_pk_mul_f32 v[18:19], v[12:13], v[20:21] op_sel_hi:[0,1]\n" \
    "v_cvt_pk_bf16_f32 v31, v28, v29\n" \
    "v_pk_fma_f32 v[20:21], v[20:21], v[12:13], v[18:19] op_sel:[1,1,0] op_sel_hi:[0,1,1] neg_lo:[0,1,0]\n" \
    "ds_write_addtid_b32 v31 offset:41600\n" \
    "v_pk_mul_f32 v[18:19], v[12:13], v[22:23] op_sel_hi:[0,1]\n" \
    "v_cvt_pk_bf16_f32 v30, v20, v21\n" \
    "v_pk_fma_f32 v[22:23], v[22:23], v[12:13], v[18:19] op_sel:[1,1,0] op_sel_hi:[0,1,1] neg_lo:[0,1,0]\n" \
    "ds_write_addtid_b32 v30 offset:43680\n" \
    "v_pk_mul_f32 v[18:19], v[12:13], v[24:25] op_sel_hi:[0,1]\n" \
    "v_cvt_pk_bf16_f32 v31, v22, v23\n" \
    "v_pk_fma_f32 v[24:25], v[24:25], v[12:13], v[18:19] op_sel:[1,1,0] op_sel_hi:[0,1,1] neg_lo:[0,1,0]\n" \
    "ds_write_addtid_b32 v31 offset:45760\n" \
    "v_pk_mul_f32 v[18:19], v[12:13], v[26:27] op_sel_hi:[0,1]\n" \
    "v_cvt_pk_bf16_f32 v30, v24, v25\n" \
    "v_pk_fma_f32 v[26:27], v[26:27], v[12:13], v[18:19] op_sel:[1,1,0] op_sel_hi:[0,1,1] neg_lo:[0,1,0]\n" \
    "ds_write_addtid_b32 v30 offset:47840\n" \
    "v_pk_mul_f32 v[18:19], v[12:13], v[28:29] op_sel_hi:[0,1]\n" \
    "v_cvt_pk_bf16_f32 v31, v26, v27\n" \
    "v_pk_fma_f32 v[28:29], v[28:29], v[12:13], v[18:19] op_sel:[1,1,0] op_sel_hi:[0,1,1] neg_lo:[0,1,0]\n" \
    "ds_write_addtid_b32 v31 offset:49920\n" \
    "v_pk_mul_f32 v[18:19], v[12:13], v[24:25] op_sel_hi:[0,1]\n" \
    "v_cvt_pk_bf16_f32 v30, v28, v29\n" \
    "v_pk_fma_f32 v[24:25], v[24:25], v[12:13], v[18:19] op_sel:[1,1,0] op_sel_hi:[0,1,1] neg_lo:[0,1,0]\n" \
    "ds_write_addtid_b32 v30 offset:52000\n" \
    "v_mov_b32_e32 v61, 3\n" \
    "ds_write_addtid_b32 v61 offset:54080\n" \
    "s_add_u32 m0, %[m0a], 62400\n" \
    "v_pk_mul_f32 v[18:19], v[12:13], v[28:29] op_sel_hi:[0,1]\n" \
    "v_cvt_pk_bf16_f32 v31, v24, v25\n" \
    "v_pk_fma_f32 v[28:29], v[28:29], v[12:13], v[18:19] op_sel:[1,1,0] op_sel_hi:[0,1,1] neg_lo:[0,1,0]\n" \
    "ds_write_addtid_b32 v31 offset:29120\n" \
    "v_pk_mul_f32 v[18:19], v[12:13], v[24:25] op_sel_hi:[0,1]\n" \
    "v_cvt_pk_bf16_f32 v30, v28, v29\n" \
    "v_pk_fma_f32 v[24:25], v[24:25], v[12:13], v[18:19] op_sel:[1,1,0] op_sel_hi:[0,1,1] neg_lo:[0,1,0]\n" \
    "ds_write_addtid_b32 v30 offset:37440\n" \
    "v_pk_mul_f32 v[18:19], v[12:13], v[28:29] op_sel_hi:[0,1]\n" \
    "v_cvt_pk_bf16_f32 v31, v24, v25\n" \
    "v_pk_fma_f32 v[28:29], v[28:29], v[12:13], v[18:19] op_sel:[1,1,0] op_sel_hi:[0,1,1] neg_lo:[0,1,0]\n" \
    "ds_write_addtid_b32 v31 offset:31200\n" \
    "v_pk_mul_f32 v[18:19], v[12:13], v[24:25] op_sel_hi:[0,1]\n" \
    "v_cvt_pk_bf16_f32 v30, v28, v29\n" \
    "v_pk_fma_f32 v[24:25], v[24:25], v[12:13], v[18:19] op_sel:[1,1,0] op_sel_hi:[0,1,1] neg_lo:[0,1,0]\n" \
    "ds_write_addtid_b32 v30 offset:39520\n" \
    "v_pk_mul_f32 v[18:19], v[12:13], v[28:29] op_sel_hi:[0,1]\n" \
    "v_cvt_pk_bf16_f32 v31, v24, v25\n" \
    "v_pk_fma_f32 v[28:29], v[28:29], v[12:13], v[18:19] op_sel:[1,1,0] op_sel_hi:[0,1,1] neg_lo:[0,1,0]\n" \
    "ds_write_addtid_b32 v31 offset:33280\n" \
    "v_pk_mul_f32 v[18:19], v[12:13], v[24:25] op_sel_hi:[0,1]\n" \
    "v_cvt_pk_bf16_f32 v30, v28, v29\n" \
    "v_pk_fma_f32 v[24:25], v[24:25], v[12:13], v[18:19] op_sel:[1,1,0] op_sel_hi:[0,1,1] neg_lo:[0,1,0]\n" \
    "ds_write_addtid_b32 v30 offset:41600\n" \
    "v_pk_mul_f32 v[18:19], v[12:13], v[28:29] op_sel_hi:[0,1]\n" \
    "v_cvt_pk_bf16_f32 v31, v24, v25\n" \
    "v_pk_fma_f32 v[28:29], v[28:29], v[12:13], v[18:19] op_sel:[1,1,0] op_sel_hi:[0,1,1] neg_lo:[0,1,0]\n" \
    "ds_write_addtid_b32 v31 offset:35360\n" \
    "v_pk_mul_f32 v[18:19], v[12:13], v[28:29] op_sel_hi:[0,1]\n" \
    "v_cvt_pk_bf16_f32 v30, v28, v29\n" \
    "v_pk_fma_f32 v[28:29], v[28:29], v[12:13], v[18:19] op_sel:[1,1,0] op_sel_hi:[0,1,1] neg_lo:[0,1,0]\n" \
    "ds_write_addtid_b32 v30 offset:43680\n" \
    "v_pk_mul_f32 v[18:19], v[12:13], v[28:29] op_sel_hi:[0,1]\n" \
    "v_cvt_pk_bf16_f32 v31, v28, v29\n" \
    "v_pk_fma_f32 v[28:29], v[28:29], v[12:13], v[18:19] op_sel:[1,1,0] op_sel_hi:[0,1,1] neg_lo:[0,1,0]\n" \
    "ds_write_addtid_b32 v31 offset:45760\n" \
    "v_pk_mul_f32 v[18:19], v[12:13], v[28:29] op_sel_hi:[0,1]\n" \
    "v_cvt_pk_bf16_f32 v30, v28, v29\n" \
    "v_pk_fma_f32 v[28:29], v[28:29], v[12:13], v[18:19] op_sel:[1,1,0] op_sel_hi:[0,1,1] neg_lo:[0,1,0]\n" \
    "ds_write_addtid_b32 v30 offset:47840\n" \
    "ds_read_b32 v60, v32\n" \
    "v_pk_mul_f32 v[18:19], v[12:13], v[28:29] op_sel_hi:[0,1]\n" \
    "v_cvt_pk_bf16_f32 v31, v28, v29\n" \
    "v_pk_fma_f32 v[28:29], v[28:29], v[12:13], v[18:19] op_sel:[1,1,0] op_sel_hi:[0,1,1] neg_lo:[0,1,0]\n" \
    "ds_write_addtid_b32 v31 offset:49920\n" \
    "v_pk_mul_f32 v[18:19], v[12:13], v[28:29] op_sel_hi:[0,1]\n" \
    "v_cvt_pk_bf16_f32 v30, v28, v29\n" \
    "v_pk_fma_f32 v[28:29], v[28:29], v[12:13], v[18:19] op_sel:[1,1,0] op_sel_hi:[0,1,1] neg_lo:[0,1,0]\n" \
    "ds_write_addtid_b32 v30 offset:52000\n" \
    "v_pk_mul_f32 v[18:19], v[12:13], v[28:29] op_sel_hi:[0,1]\n" \
    "v_cvt_pk_bf16_f32 v31, v28, v29\n" \
    "v_pk_fma_f32 v[28:29], v[28:29], v[12:13], v[18:19] op_sel:[1,1,0] op_sel_hi:[0,1,1] neg_lo:[0,1,0]\n" \
    "ds_write_addtid_b32 v31 offset:54080\n" \
    "s_waitcnt lgkmcnt(3)\n" \
    "v_cmp_gt_u32_e32 vcc, 1, v60\n" \
    "s_cbranch_vccz Lae0_%=\n" \
    "s_mov_b32 s90, 0x100000\n" \
    "Lal0_%=:\n" \
    "s_sleep 1\n" \
    "ds_read_b32 v60, v32 offset:0\n" \
    "s_waitcnt lgkmcnt(0)\n" \
    "v_cmp_gt_u32_e32 vcc, 1, v60\n" \
    "s_cbranch_vccz Lae0_%=\n" \
    "s_sub_u32 s90, s90, 1\n" \
    "s_cbranch_scc0 Lal0_%=\n" \
    "Lae0_%=:\n" \
    "ds_read2_b32 v[64:65], v36 offset0:240 offset1:241\n" \
    "ds_read2_b32 v[66:67], v36 offset0:242 offset1:243\n" \
    "ds_read2_b32 v[68:69], v36 offset0:224 offset1:225\n" \
    "ds_read2_b32 v[70:71], v36 offset0:226 offset1:227\n" \
    "v_pk_mul_f32 v[18:19], v[12:13], v[28:29] op_sel_hi:[0,1]\n" \
    "v_cvt_pk_bf16_f32 v30, v28, v29\n" \
    "v_pk_fma_f32 v[28:29], v[28:29], v[12:13], v[18:19] op_sel:[1,1,0] op_sel_hi:[0,1,1] neg_lo:[0,1,0]\n" \
    "ds_write_addtid_b32 v30 offset:56160\n" \
    "ds_read2_b32 v[72:73], v36 offset0:208 offset1:209\n" \
    "ds_read2_b32 v[74:75], v36 offset0:210 offset1:211\n" \
    "ds_read2_b32 v[76:77], v36 offset0:192 offset1:193\n" \
    "ds_read2_b32 v[78:79], v36 offset0:194 offset1:195\n" \
    "v_pk_mul_f32 v[18:19], v[12:13], v[28:29] op_sel_hi:[0,1]\n" \
    "v_cvt_pk_bf16_f32 v31, v28, v29\n" \
    "v_pk_fma_f32 v[28:29], v[28:29], v[12:13], v[18:19] op_sel:[1,1,0] op_sel_hi:[0,1,1] neg_lo:[0,1,0]\n" \
    "ds_write_addtid_b32 v31 offset:58240\n" \
    "ds_read2_b32 v[80:81], v36 offset0:176 offset1:177\n" \
    "ds_read2_b32 v[82:83], v36 offset0:178 offset1:179\n" \
    "ds_read2_b32 v[84:85], v36 offset0:160 offset1:161\n" \
    "ds_read2_b32 v[86:87], v36 offset0:162 offset1:163\n" \
    "v_cvt_pk_bf16_f32 v30, v28, v29\n" \
    "ds_write_addtid_b32 v30 offset:60320\n" \
    "s_mov_b32 m0, %[m0a]\n" \
    "v_mov_b32_e32 v61, 4\n" \
    "ds_write_addtid_b32 v61 offset:54080\n" \
    "ds_read2_b32 v[88:89], v36 offset0:144 offset1:145\n" \
    "ds_read2_b32 v[90:91], v36 offset0:146 offset1:147\n" \
    "ds_read2_b32 v[92:93], v36 offset0:128 offset1:129\n" \
    "ds_read2_b32 v[94:95], v36 offset0:130 offset1:131\n" \
    "s_setprio 2\n" \
    "s_mov_b32 s90, 0x100000\n" \
    "Lal1_%=:\n" \
    "ds_read_b32 v62, v34 offset:0\n" \
    "s_waitcnt lgkmcnt(0)\n" \
    "v_cmp_gt_u32_e32 vcc, 1, v62\n" \
    "s_cbranch_vccz Lae1_%=\n" \
    "s_sleep 1\n" \
    "s_sub_u32 s90, s90, 1\n" \
    "s_cbranch_scc0 Lal1_%=\n" \
    "Lae1_%=:\n" \
    "ds_read_b128 v[156:159], v33 offset:448\n" \
    "ds_read_b128 v[152:155], v33 offset:384\n" \
    "ds_read_b128 v[148:151], v33 offset:320\n" \
    "ds_read_b128 v[144:147], v33 offset:256\n" \
    "ds_read_b128 v[140:143], v33 offset:192\n" \
    "ds_read_b128 v[136:139], v33 offset:128\n" \
    "ds_read_b128 v[132:135], v33 offset:64\n" \
    "ds_read_b128 v[128:131], v33 offset:0\n" \
    "ds_read_b32 v60, v32\n" \
    "s_waitcnt lgkmcnt(8)\n" \
    "v_mfma_f32_16x16x32_bf16 v[0:3], v[64:67], v[156:159], 0\n" \
    "s_waitcnt lgkmcnt(7)\n" \
    "v_mfma_f32_16x16x32_bf16 v[0:3], v[68:71], v[152:155], v[0:3]\n" \
    "s_waitcnt lgkmcnt(6)\n" \
    "v_mfma_f32_16x16x32_bf16 v[0:3], v[72:75], v[148:151], v[0:3]\n" \
    "s_waitcnt lgkmcnt(0)\n" \
    "v_cmp_gt_u32_e32 vcc, 2, v60\n" \
    "s_cbranch_vccz Lae2_%=\n" \
    "s_mov_b32 s90, 0x100000\n" \
    "Lal2_%=:\n" \
    "s_sleep 1\n" \
    "ds_read_b32 v60, v32 offset:0\n" \
    "s_waitcnt lgkmcnt(0)\n" \
    "v_cmp_gt_u32_e32 vcc, 2, v60\n" \
    "s_cbranch_vccz Lae2_%=\n" \
    "s_sub_u32 s90, s90, 1\n" \
    "s_cbranch_scc0 Lal2_%=\n" \
    "Lae2_%=:\n" \
    "v_mfma_f32_16x16x32_bf16 v[0:3], v[76:79], v[144:147], v[0:3]\n" \
    "ds_read2_b32 v[96:97], v36 offset0:112 offset1:113\n" \
    "ds_read2_b32 v[98:99], v36 offset0:114 offset1:115\n" \
    "ds_read2_b32 v[100:101], v36 offset0:96 offset1:97\n" \
    "ds_read2_b32 v[102:103], v36 offset0:98 offset1:99\n" \
    "v_mfma_f32_16x16x32_bf16 v[0:3], v[80:83], v[140:143], v[0:3]\n" \
    "ds_read2_b32 v[104:105], v36 offset0:80 offset1:81\n" \
    "ds_read2_b32 v[106:107], v36 offset0:82 offset1:83\n" \
    "ds_read2_b32 v[108:109], v36 offset0:64 offset1:65\n" \
    "ds_read2_b32 v[110:111], v36 offset0:66 offset1:67\n" \
    "v_mfma_f32_16x16x32_bf16 v[0:3], v[84:87], v[136:139], v[0:3]\n" \
    "ds_read2_b32 v[112:113], v36 offset0:48 offset1:49\n" \
    "ds_read2_b32 v[114:115], v36 offset0:50 offset1:51\n" \
    "ds_read2_b32 v[116:117], v36 offset0:32 offset1:33\n" \
    "ds_read2_b32 v[118:119], v36 offset0:34 offset1:35\n" \
    "v_mfma_f32_16x16x32_bf16 v[0:3], v[88:91], v[132:135], v[0:3]\n" \
    "ds_read2_b32 v[120:121], v36 offset0:16 offset1:17\n" \
    "ds_read2_b32 v[122:123], v36 offset0:18 offset1:19\n" \
    "ds_read2_b32 v[124:125], v36 offset0:0 offset1:1\n" \
    "ds_read2_b32 v[126:127], v36 offset0:2 offset1:3\n" \
    "v_mfma_f32_16x16x32_bf16 v[0:3], v[92:95], v[128:131], v[0:3]\n" \
    "s_waitcnt lgkmcnt(14)\n" \
    "v_mfma_f32_16x16x32_bf16 v[4:7], v[96:99], v[156:159], 0\n" \
    "ds_read_b128 v[156:159], v33 offset:960\n" \
    "s_waitcnt lgkmcnt(13)\n" \
    "v_mfma_f32_16x16x32_bf16 v[4:7], v[100:103], v[152:155], v[4:7]\n" \
    "ds_read_b128 v[152:155], v33 offset:896\n" \
    "ds_read_b32 v62, v34\n" \
    "s_waitcnt lgkmcnt(13)\n" \
    "v_mfma_f32_16x16x32_bf16 v[4:7], v[104:107], v[148:151], v[4:7]\n" \
    "ds_read_b128 v[148:151], v33 offset:832\n" \
    "s_waitcnt lgkmcnt(12)\n" \
    "v_mfma_f32_16x16x32_bf16 v[4:7], v[108:111], v[144:147], v[4:7]\n" \
    "ds_read_b128 v[144:147], v33 offset:768\n" \
    "s_waitcnt lgkmcnt(11)\n" \
    "v_mfma_f32_16x16x32_bf16 v[4:7], v[112:115], v[140:143], v[4:7]\n" \
    "ds_read_b128 v[140:143], v33 offset:704\n" \
    "s_waitcnt lgkmcnt(10)\n" \
    "v_mfma_f32_16x16x32_bf16 v[4:7], v[116:119], v[136:139], v[4:7]\n" \
    "ds_read_b128 v[136:139], v33 offset:640\n" \
    "s_waitcnt lgkmcnt(9)\n" \
    "v_mfma_f32_16x16x32_bf16 v[4:7], v[120:123], v[132:135], v[4:7]\n" \
    "ds_read_b128 v[132:135], v33 offset:576\n" \
    "s_waitcnt lgkmcnt(8)\n" \
    "v_mfma_f32_16x16x32_bf16 v[4:7], v[124:127], v[128:131], v[4:7]\n" \
    "ds_read_b128 v[128:131], v33 offset:512\n" \
    "s_waitcnt lgkmcnt(6)\n" \
    "v_cmp_gt_u32_e32 vcc, 3, v62\n" \
    "s_cbranch_vccnz Las3_%=\n" \
    "Lae3_%=:\n" \
    "s_waitcnt lgkmcnt(4)\n" \
    "v_mfma_f32_16x16x32_bf16 v[4:7], v[64:67], v[156:159], v[4:7]\n" \
    "v_mfma_f32_16x16x32_bf16 v[8:11], v[96:99], v[156:159], 0\n" \
    "ds_read_b128 v[156:159], v33 offset:1472\n" \
    "v_mfma_f32_16x16x32_bf16 v[4:7], v[68:71], v[152:155], v[4:7]\n" \
    "v_mfma_f32_16x16x32_bf16 v[8:11], v[100:103], v[152:155], v[8:11]\n" \
    "ds_read_b128 v[152:155], v33 offset:1408\n" \
    "v_mov_b32_e64 v61, 11\n" \
    "v_mfma_f32_16x16x32_bf16 v[4:7], v[72:75], v[148:151], v[4:7]\n" \
    "ds_write_b128 v37, v[0:3] offset:0\n" \
    "ds_write_addtid_b32 v61 offset:54080\n" \
    "v_mfma_f32_16x16x32_bf16 v[8:11], v[104:107], v[148:151], v[8:11]\n" \
    "ds_read_b128 v[148:151], v33 offset:1344\n" \
    "v_mfma_f32_16x16x32_bf16 v[4:7], v[76:79], v[144:147], v[4:7]\n" \
    "v_mfma_f32_16x16x32_bf16 v[8:11], v[108:111], v[144:147], v[8:11]\n" \
    "ds_read_b128 v[144:147], v33 offset:1280\n" \
    "s_waitcnt lgkmcnt(6)\n" \
    "v_mfma_f32_16x16x32_bf16 v[4:7], v[80:83], v[140:143], v[4:7]\n" \
    "v_mfma_f32_16x16x32_bf16 v[8:11], v[112:115], v[140:143], v[8:11]\n" \
    "ds_read_b128 v[140:143], v33 offset:1216\n" \
    "v_mfma_f32_16x16x32_bf16 v[4:7], v[84:87], v[136:139], v[4:7]\n" \
    "v_mfma_f32_16x16x32_bf16 v[8:11], v[116:119], v[136:139], v[8:11]\n" \
    "ds_read_b128 v[136:139], v33 offset:1152\n" \
    "v_mfma_f32_16x16x32_bf16 v[4:7], v[88:91], v[132:135], v[4:7]\n" \
    "v_mfma_f32_16x16x32_bf16 v[8:11], v[120:123], v[132:135], v[8:11]\n" \
    "ds_read_b128 v[132:135], v33 offset:1088\n" \
    "v_mfma_f32_16x16x32_bf16 v[4:7], v[92:95], v[128:131], v[4:7]\n" \
    "v_mfma_f32_16x16x32_bf16 v[8:11], v[124:127], v[128:131], v[8:11]\n" \
    "ds_read_b128 v[128:131], v33 offset:1024\n" \
    "s_waitcnt lgkmcnt(4)\n" \
    "v_mfma_f32_16x16x32_bf16 v[8:11], v[64:67], v[156:159], v[8:11]\n" \
    "v_mfma_f32_16x16x32_bf16 v[12:15], v[96:99], v[156:159], 0\n" \
    "ds_read_b128 v[156:159], v33 offset:1984\n" \
    "v_mfma_f32_16x16x32_bf16 v[8:11], v[68:71], v[152:155], v[8:11]\n" \
    "v_mfma_f32_16x16x32_bf16 v[12:15], v[100:103], v[152:155], v[12:15]\n" \
    "ds_read_b128 v[152:155], v33 offset:1920\n" \
    "v_mov_b32_e64 v61, 12\n" \
    "v_mfma_f32_16x16x32_bf16 v[8:11], v[72:75], v[148:151], v[8:11]\n" \
    "ds_write_b128 v37, v[4:7] offset:4160\n" \
    "ds_write_addtid_b32 v61 offset:54080\n" \
    "v_mfma_f32_16x16x32_bf16 v[12:15], v[104:107], v[148:151], v[12:15]\n" \
    "ds_read_b128 v[148:151], v33 offset:1856\n" \
    "v_mfma_f32_16x16x32_bf16 v[8:11], v[76:79], v[144:147], v[8:11]\n" \
    "v_mfma_f32_16x16x32_bf16 v[12:15], v[108:111], v[144:147], v[12:15]\n" \
    "ds_read_b128 v[144:147], v33 offset:1792\n" \
    "s_waitcnt lgkmcnt(6)\n" \
    "v_mfma_f32_16x16x32_bf16 v[8:11], v[80:83], v[140:143], v[8:11]\n" \
    "v_mfma_f32_16x16x32_bf16 v[12:15], v[112:115], v[140:143], v[12:15]\n" \
    "ds_read_b128 v[140:143], v33 offset:1728\n" \
    "v_mfma_f32_16x16x32_bf16 v[8:11], v[84:87], v[136:139], v[8:11]\n" \
    "v_mfma_f32_16x16x32_bf16 v[12:15], v[116:119], v[136:139], v[12:15]\n" \
    "ds_read_b128 v[136:139], v33 offset:1664\n" \
    "v_mfma_f32_16x16x32_bf16 v[8:11], v[88:91], v[132:135], v[8:11]\n" \
    "ds_read_b32 v62, v34\n" \
    "v_mfma_f32_16x16x32_bf16 v[12:15], v[120:123], v[132:135], v[12:15]\n" \
    "ds_read_b128 v[132:135], v33 offset:1600\n" \
    "v_mfma_f32_16x16x32_bf16 v[8:11], v[92:95], v[128:131], v[8:11]\n" \
    "v_mfma_f32_16x16x32_bf16 v[12:15], v[124:127], v[128:131], v[12:15]\n" \
    "ds_read_b128 v[128:131], v33 offset:1536\n" \
    "s_waitcnt lgkmcnt(2)\n" \
    "v_cmp_gt_u32_e32 vcc, 5, v62\n" \
    "s_cbranch_vccnz Las4_%=\n" \
    "Lae4_%=:\n" \
    "v_mfma_f32_16x16x32_bf16 v[12:15], v[64:67], v[156:159], v[12:15]\n" \
    "v_mfma_f32_16x16x32_bf16 v[16:19], v[96:99], v[156:159], 0\n" \
    "ds_read_b128 v[156:159], v33 offset:2496\n" \
    "v_mfma_f32_16x16x32_bf16 v[12:15], v[68:71], v[152:155], v[12:15]\n" \
    "v_mfma_f32_16x16x32_bf16 v[16:19], v[100:103], v[152:155], v[16:19]\n" \
    "ds_read_b128 v[152:155], v33 offset:2432\n" \
    "v_mov_b32_e64 v61, 13\n" \
    "v_mfma_f32_16x16x32_bf16 v[12:15], v[72:75], v[148:151], v[12:15]\n" \
    "ds_write_b128 v37, v[8:11] offset:8320\n" \
    "ds_write_addtid_b32 v61 offset:54080\n" \
    "v_mfma_f32_16x16x32_bf16 v[16:19], v[104:107], v[148:151], v[16:19]\n" \
    "ds_read_b128 v[148:151], v33 offset:2368\n" \
    "v_mfma_f32_16x16x32_bf16 v[12:15], v[76:79], v[144:147], v[12:15]\n" \
    "v_mfma_f32_16x16x32_bf16 v[16:19], v[108:111], v[144:147], v[16:19]\n" \
    "ds_read_b128 v[144:147], v33 offset:2304\n" \
    "s_waitcnt lgkmcnt(6)\n" \
    "v_mfma_f32_16x16x32_bf16 v[12:15], v[80:83], v[140:143], v[12:15]\n" \
    "v_mfma_f32_16x16x32_bf16 v[16:19], v[112:115], v[140:143], v[16:19]\n" \
    "ds_read_b128 v[140:143], v33 offset:2240\n" \
    "v_mfma_f32_16x16x32_bf16 v[12:15], v[84:87], v[136:139], v[12:15]\n" \
    "v_mfma_f32_16x16x32_bf16 v[16:19], v[116:119], v[136:139], v[16:19]\n" \
    "ds_read_b128 v[136:139], v33 offset:2176\n" \
    "v_mfma_f32_16x16x32_bf16 v[12:15], v[88:91], v[132:135], v[12:15]\n" \
    "v_mfma_f32_16x16x32_bf16 v[16:19], v[120:123], v[132:135], v[16:19]\n" \
    "ds_read_b128 v[132:135], v33 offset:2112\n" \
    "v_mfma_f32_16x16x32_bf16 v[12:15], v[92:95], v[128:131], v[12:15]\n" \
    "v_mfma_f32_16x16x32_bf16 v[16:19], v[124:127], v[128:131], v[16:19]\n" \
    "ds_read_b128 v[128:131], v33 offset:2048\n" \
    "s_waitcnt lgkmcnt(4)\n" \
    "v_mfma_f32_16x16x32_bf16 v[16:19], v[64:67], v[156:159], v[16:19]\n" \
    "v_mfma_f32_16x16x32_bf16 v[20:23], v[96:99], v[156:159], 0\n" \
    "ds_read_b128 v[156:159], v33 offset:3008\n" \
    "v_mfma_f32_16x16x32_bf16 v[16:19], v[68:71], v[152:155], v[16:19]\n" \
    "v_mfma_f32_16x16x32_bf16 v[20:23], v[100:103], v[152:155], v[20:23]\n" \
    "ds_read_b128 v[152:155], v33 offset:2944\n" \
    "v_mov_b32_e64 v61, 14\n" \
    "v_mfma_f32_16x16x32_bf16 v[16:19], v[72:75], v[148:151], v[16:19]\n" \
    "ds_write_b128 v37, v[12:15] offset:12480\n" \
    "ds_write_addtid_b32 v61 offset:54080\n" \
    "v_mfma_f32_16x16x32_bf16 v[20:23], v[104:107], v[148:151], v[20:23]\n" \
    "ds_read_b128 v[148:151], v33 offset:2880\n" \
    "v_mfma_f32_16x16x32_bf16 v[16:19], v[76:79], v[144:147], v[16:19]\n" \
    "v_mfma_f32_16x16x32_bf16 v[20:23], v[108:111], v[144:147], v[20:23]\n" \
    "ds_read_b128 v[144:147], v33 offset:2816\n" \
    "s_waitcnt lgkmcnt(6)\n" \
    "v_mfma_f32_16x16x32_bf16 v[16:19], v[80:83], v[140:143], v[16:19]\n" \
    "v_mfma_f32_16x16x32_bf16 v[20:23], v[112:115], v[140:143], v[20:23]\n" \
    "ds_read_b128 v[140:143], v33 offset:2752\n" \
    "v_mfma_f32_16x16x32_bf16 v[16:19], v[84:87], v[136:139], v[16:19]\n" \
    "v_mfma_f32_16x16x32_bf16 v[20:23], v[116:119], v[136:139], v[20:23]\n" \
    "ds_read_b128 v[136:139], v33 offset:2688\n" \
    "v_mfma_f32_16x16x32_bf16 v[16:19], v[88:91], v[132:135], v[16:19]\n" \
    "ds_read_b32 v62, v34\n" \
    "v_mfma_f32_16x16x32_bf16 v[20:23], v[120:123], v[132:135], v[20:23]\n" \
    "ds_read_b128 v[132:135], v33 offset:2624\n" \
    "v_mfma_f32_16x16x32_bf16 v[16:19], v[92:95], v[128:131], v[16:19]\n" \
    "v_mfma_f32_16x16x32_bf16 v[20:23], v[124:127], v[128:131], v[20:23]\n" \
    "ds_read_b128 v[128:131], v33 offset:2560\n" \
    "s_waitcnt lgkmcnt(2)\n" \
    "v_cmp_gt_u32_e32 vcc, 7, v62\n" \
    "s_cbranch_vccnz Las5_%=\n" \
    "Lae5_%=:\n" \
    "v_mfma_f32_16x16x32_bf16 v[20:23], v[64:67], v[156:159], v[20:23]\n" \
    "v_mfma_f32_16x16x32_bf16 v[24:27], v[96:99], v[156:159], 0\n" \
    "ds_read_b128 v[156:159], v33 offset:3520\n" \
    "v_mfma_f32_16x16x32_bf16 v[20:23], v[68:71], v[152:155], v[20:23]\n" \
    "v_mfma_f32_16x16x32_bf16 v[24:27], v[100:103], v[152:155], v[24:27]\n" \
    "ds_read_b128 v[152:155], v33 offset:3456\n" \
    "v_mov_b32_e64 v61, 15\n" \
    "v_mfma_f32_16x16x32_bf16 v[20:23], v[72:75], v[148:151], v[20:23]\n" \
    "ds_write_b128 v37, v[16:19] offset:16640\n" \
    "ds_write_addtid_b32 v61 offset:54080\n" \
    "v_mfma_f32_16x16x32_bf16 v[24:27], v[104:107], v[148:151], v[24:27]\n" \
    "ds_read_b128 v[148:151], v33 offset:3392\n" \
    "v_mfma_f32_16x16x32_bf16 v[20:23], v[76:79], v[144:147], v[20:23]\n" \
    "v_mfma_f32_16x16x32_bf16 v[24:27], v[108:111], v[144:147], v[24:27]\n" \
    "ds_read_b128 v[144:147], v33 offset:3328\n" \
    "s_waitcnt lgkmcnt(6)\n" \
    "v_mfma_f32_16x16x32_bf16 v[20:23], v[80:83], v[140:143], v[20:23]\n" \
    "v_mfma_f32_16x16x32_bf16 v[24:27], v[112:115], v[140:143], v[24:27]\n" \
    "ds_read_b128 v[140:143], v33 offset:3264\n" \
    "v_mfma_f32_16x16x32_bf16 v[20:23], v[84:87], v[136:139], v[20:23]\n" \
    "v_mfma_f32_16x16x32_bf16 v[24:27], v[116:119], v[136:139], v[24:27]\n" \
    "ds_read_b128 v[136:139], v33 offset:3200\n" \
    "v_mfma_f32_16x16x32_bf16 v[20:23], v[88:91], v[132:135], v[20:23]\n" \
    "v_mfma_f32_16x16x32_bf16 v[24:27], v[120:123], v[132:135], v[24:27]\n" \
    "ds_read_b128 v[132:135], v33 offset:3136\n" \
    "v_mfma_f32_16x16x32_bf16 v[20:23], v[92:95], v[128:131], v[20:23]\n" \
    "v_mfma_f32_16x16x32_bf16 v[24:27], v[124:127], v[128:131], v[24:27]\n" \
    "ds_read_b128 v[128:131], v33 offset:3072\n" \
    "s_waitcnt lgkmcnt(4)\n" \
    "v_mfma_f32_16x16x32_bf16 v[24:27], v[64:67], v[156:159], v[24:27]\n" \
    "v_mfma_f32_16x16x32_bf16 v[28:31], v[96:99], v[156:159], 0\n" \
    "ds_read_b128 v[156:159], v33 offset:4032\n" \
    "v_mfma_f32_16x16x32_bf16 v[24:27], v[68:71], v[152:155], v[24:27]\n" \
    "v_mfma_f32_16x16x32_bf16 v[28:31], v[100:103], v[152:155], v[28:31]\n" \
    "ds_read_b128 v[152:155], v33 offset:3968\n" \
    "v_mov_b32_e64 v61, 16\n" \
    "v_mfma_f32_16x16x32_bf16 v[24:27], v[72:75], v[148:151], v[24:27]\n" \
    "ds_write_b128 v37, v[20:23] offset:20800\n" \
    "ds_write_addtid_b32 v61 offset:54080\n" \
    "v_mfma_f32_16x16x32_bf16 v[28:31], v[104:107], v[148:151], v[28:31]\n" \
    "ds_read_b128 v[148:151], v33 offset:3904\n" \
    "v_mfma_f32_16x16x32_bf16 v[24:27], v[76:79], v[144:147], v[24:27]\n" \
    "v_mfma_f32_16x16x32_bf16 v[28:31], v[108:111], v[144:147], v[28:31]\n" \
    "ds_read_b128 v[144:147], v33 offset:3840\n" \
    "s_waitcnt lgkmcnt(6)\n" \
    "v_mfma_f32_16x16x32_bf16 v[24:27], v[80:83], v[140:143], v[24:27]\n" \
    "v_mfma_f32_16x16x32_bf16 v[28:31], v[112:115], v[140:143], v[28:31]\n" \
    "ds_read_b128 v[140:143], v33 offset:3776\n" \
    "v_mfma_f32_16x16x32_bf16 v[24:27], v[84:87], v[136:139], v[24:27]\n" \
    "v_mfma_f32_16x16x32_bf16 v[28:31], v[116:119], v[136:139], v[28:31]\n" \
    "ds_read_b128 v[136:139], v33 offset:3712\n" \
    "v_mfma_f32_16x16x32_bf16 v[24:27], v[88:91], v[132:135], v[24:27]\n" \
    "v_mfma_f32_16x16x32_bf16 v[28:31], v[120:123], v[132:135], v[28:31]\n" \
    "ds_read_b128 v[132:135], v33 offset:3648\n" \
    "v_mfma_f32_16x16x32_bf16 v[24:27], v[92:95], v[128:131], v[24:27]\n" \
    "v_mfma_f32_16x16x32_bf16 v[28:31], v[124:127], v[128:131], v[28:31]\n" \
    "ds_read_b128 v[128:131], v33 offset:3584\n" \
    "s_waitcnt lgkmcnt(4)\n" \
    "v_mfma_f32_16x16x32_bf16 v[28:31], v[64:67], v[156:159], v[28:31]\n" \
    "v_mfma_f32_16x16x32_bf16 v[28:31], v[68:71], v[152:155], v[28:31]\n" \
    "v_mfma_f32_16x16x32_bf16 v[28:31], v[72:75], v[148:151], v[28:31]\n" \
    "v_mov_b32_e64 v61, 17\n" \
    "v_mfma_f32_16x16x32_bf16 v[28:31], v[76:79], v[144:147], v[28:31]\n" \
    "ds_write_b128 v37, v[24:27] offset:24960\n" \
    "ds_write_addtid_b32 v61 offset:54080\n" \
    "s_waitcnt lgkmcnt(2)\n" \
    "v_mfma_f32_16x16x32_bf16 v[28:31], v[80:83], v[140:143], v[28:31]\n" \
    "v_mfma_f32_16x16x32_bf16 v[28:31], v[84:87], v[136:139], v[28:31]\n" \
    "v_mfma_f32_16x16x32_bf16 v[28:31], v[88:91], v[132:135], v[28:31]\n" \
    "v_mfma_f32_16x16x32_bf16 v[28:31], v[92:95], v[128:131], v[28:31]\n" \
    "v_mov_b32_e64 v61, 18\n" \
    "s_nop 6\n" \
    "ds_write_b128 v37, v[28:31] offset:29120\n" \
    "ds_write_addtid_b32 v61 offset:54080\n" \
    "s_branch Lax_%=\n" \
    "Las3_%=:\n" \
    "s_mov_b32 s90, 0x100000\n" \
    "Lal3_%=:\n" \
    "ds_read_b32 v62, v34 offset:0\n" \
    "s_waitcnt lgkmcnt(0)\n" \
    "v_cmp_gt_u32_e32 vcc, 3, v62\n" \
    "s_cbranch_vccz Lae3_%=\n" \
    "s_sub_u32 s90, s90, 1\n" \
    "s_cbranch_scc0 Lal3_%=\n" \
    "s_branch Lae3_%=\n" \
    "Las4_%=:\n" \
    "s_mov_b32 s90, 0x100000\n" \
    "Lal4_%=:\n" \
    "ds_read_b32 v62, v34 offset:0\n" \
    "s_waitcnt lgkmcnt(0)\n" \
    "v_cmp_gt_u32_e32 vcc, 5, v62\n" \
    "s_cbranch_vccz Lae4_%=\n" \
    "s_sub_u32 s90, s90, 1\n" \
    "s_cbranch_scc0 Lal4_%=\n" \
    "s_branch Lae4_%=\n" \
    "Las5_%=:\n" \
    "s_mov_b32 s90, 0x100000\n" \
    "Lal5_%=:\n" \
    "ds_read_b32 v62, v34 offset:0\n" \
    "s_waitcnt lgkmcnt(0)\n" \
    "v_cmp_gt_u32_e32 vcc, 7, v62\n" \
    "s_cbranch_vccz Lae5_%=\n" \
    "s_sub_u32 s90, s90, 1\n" \
    "s_cbranch_scc0 Lal5_%=\n" \
    "s_branch Lae5_%=\n" \
    "Lax_%=:\n"
#define CONV_A_CLOB "v0", "v1", "v2", "v3", "v4", "v5", "v6", "v7", "v8", "v9", "v10", "v11", "v12", "v13", "v14", "v15", "v16", "v17", "v18", "v19", "v20", "v21", "v22", "v23", "v24", "v25", "v26", "v27", "v28", "v29", "v30", "v31", "v32", "v33", "v34", "v36", "v37", "v60", "v61", "v62", "v64", "v65", "v66", "v67", "v68", "v69", "v70", "v71", "v72", "v73", "v74", "v75", "v76", "v77", "v78", "v79", "v80", "v81", "v82", "v83", "v84", "v85", "v86", "v87", "v88", "v89", "v90", "v91", "v92", "v93", "v94", "v95", "v96", "v97", "v98", "v99", "v100", "v101", "v102", "v103", "v104", "v105", "v106", "v107", "v108", "v109", "v110", "v111", "v112", "v113", "v114", "v115", "v116", "v117", "v118", "v119", "v120", "v121", "v122", "v123", "v124", "v125", "v126", "v127", "v128", "v129", "v130", "v131", "v132", "v133", "v134", "v135", "v136", "v137", "v138", "v139", "v140", "v141", "v142", "v143", "v144", "v145", "v146", "v147", "v148", "v149", "v150", "v151", "v152", "v153", "v154", "v155", "v156", "v157", "v158", "v159"
#define CONV_B_ASM \
    "s_mov_b32 s92, 0x07060302\n" \
    "s_setprio 2\n" \
    "s_waitcnt vmcnt(4)\n" \
    "v_mul_f32_e32 v0, 0x3fb8aa3b, v70\n" \
    "v_exp_f32_e32 v0, v0\n" \
    "v_mul_f32_e32 v1, 0x3fb8aa3b, v64\n" \
    "v_cvt_pk_bf16_f32 v0, 0, v0\n" \
    "v_mul_f32_e32 v2, 0.15915494, v65\n" \
    "v_mul_f32_e32 v1, v0, v1\n" \
    "v_mul_f32_e32 v2, v0, v2\n" \
    "v_mul_f32_e32 v4, 0x41000000, v2\n" \
    "v_mul_f32_e32 v3, 0x41000000, v1\n" \
    "v_fract_f32_e32 v4, v4\n" \
    "v_exp_f32_e32 v3, v3\n" \
    "v_cos_f32_e32 v8, v4\n" \
    "v_sin_f32_e32 v9, v4\n" \
    "v_mul_f32_e32 v66, v0, v66\n" \
    "v_mul_f32_e64 v8, v3, v8\n" \
    "v_mul_f32_e64 v9, v3, v9\n" \
    "v_pk_mul_f32 v[18:19], v[8:9], v[8:9] op_sel_hi:[0,1]\n" \
    "v_cvt_pk_bf16_f32 v30, v8, v9\n" \
    "v_pk_fma_f32 v[14:15], v[8:9], v[8:9], v[18:19] op_sel:[1,1,0] op_sel_hi:[0,1,1] neg_lo:[0,1,0]\n" \
    "ds_write_addtid_b32 v30 offset:2080\n" \
    "v_pk_mul_f32 v[18:19], v[8:9], v[14:15] op_sel_hi:[0,1]\n" \
    "v_cvt_pk_bf16_f32 v31, v14, v15\n" \
    "v_pk_fma_f32 v[14:15], v[14:15], v[8:9], v[18:19] op_sel:[1,1,0] op_sel_hi:[0,1,1] neg_lo:[0,1,0]\n" \
    "ds_write_addtid_b32 v31 offset:4160\n" \
    "v_pk_mul_f32 v[18:19], v[8:9], v[14:15] op_sel_hi:[0,1]\n" \
    "v_cvt_pk_bf16_f32 v30, v14, v15\n" \
    "v_pk_fma_f32 v[14:15], v[14:15], v[8:9], v[18:19] op_sel:[1,1,0] op_sel_hi:[0,1,1] neg_lo:[0,1,0]\n" \
    "ds_write_addtid_b32 v30 offset:6240\n" \
    "v_pk_mul_f32 v[18:19], v[8:9], v[14:15] op_sel_hi:[0,1]\n" \
    "v_cvt_pk_bf16_f32 v31, v14, v15\n" \
    "v_pk_fma_f32 v[14:15], v[14:15], v[8:9], v[18:19] op_sel:[1,1,0] op_sel_hi:[0,1,1] neg_lo:[0,1,0]\n" \
    "ds_write_addtid_b32 v31 offset:8320\n" \
    "v_mul_f32_e32 v4, 0x43000000, v2\n" \
    "v_mul_f32_e32 v3, 0x43000000, v1\n" \
    "v_fract_f32_e32 v4, v4\n" \
    "v_exp_f32_e32 v3, v3\n" \
    "v_cos_f32_e32 v12, v4\n" \
    "v_sin_f32_e32 v13, v4\n" \
    "v_pk_mul_f32 v[18:19], v[8:9], v[14:15] op_sel_hi:[0,1]\n" \
    "v_cvt_pk_bf16_f32 v30, v14, v15\n" \
    "v_pk_fma_f32 v[14:15], v[14:15], v[8:9], v[18:19] op_sel:[1,1,0] op_sel_hi:[0,1,1] neg_lo:[0,1,0]\n" \
    "ds_write_addtid_b32 v30 offset:10400\n" \
    "v_pk_mul_f32 v[18:19], v[8:9], v[14:15] op_sel_hi:[0,1]\n" \
    "v_cvt_pk_bf16_f32 v31, v14, v15\n" \
    "v_pk_fma_f32 v[14:15], v[14:15], v[8:9], v[18:19] op_sel:[1,1,0] op_sel_hi:[0,1,1] neg_lo:[0,1,0]\n" \
    "ds_write_addtid_b32 v31 offset:12480\n" \
    "v_mul_f32_e64 v12, v3, v12\n" \
    "v_mul_f32_e64 v13, -v3, v13\n" \
    "v_pk_mul_f32 v[18:19], v[8:9], v[14:15] op_sel_hi:[0,1]\n" \
    "v_cvt_pk_bf16_f32 v30, v14, v15\n" \
    "v_pk_fma_f32 v[14:15], v[14:15], v[8:9], v[18:19] op_sel:[1,1,0] op_sel_hi:[0,1,1] neg_lo:[0,1,0]\n" \
    "ds_write_addtid_b32 v30 offset:14560\n" \
    "v_pk_mul_f32 v[18:19], v[8:9], v[14:15] op_sel_hi:[0,1]\n" \
    "v_cvt_pk_bf16_f32 v31, v14, v15\n" \
    "v_pk_fma_f32 v[14:15], v[14:15], v[8:9], v[18:19] op_sel:[1,1,0] op_sel_hi:[0,1,1] neg_lo:[0,1,0]\n" \
    "ds_write_addtid_b32 v31 offset:16640\n" \
    "v_fract_f32_e32 v6, v2\n" \
    "v_exp_f32_e32 v5, v1\n" \
    "v_cos_f32_e32 v10, v6\n" \
    "v_sin_f32_e32 v11, v6\n" \
    "v_pk_mul_f32 v[18:19], v[8:9], v[14:15] op_sel_hi:[0,1]\n" \
    "v_cvt_pk_bf16_f32 v30, v14, v15\n" \
    "v_pk_fma_f32 v[14:15], v[14:15], v[8:9], v[18:19] op_sel:[1,1,0] op_sel_hi:[0,1,1] neg_lo:[0,1,0]\n" \
    "ds_write_addtid_b32 v30 offset:18720\n" \
    "v_pk_mul_f32 v[18:19], v[8:9], v[14:15] op_sel_hi:[0,1]\n" \
    "v_cvt_pk_bf16_f32 v31, v14, v15\n" \
    "v_pk_fma_f32 v[14:15], v[14:15], v[8:9], v[18:19] op_sel:[1,1,0] op_sel_hi:[0,1,1] neg_lo:[0,1,0]\n" \
    "ds_write_addtid_b32 v31 offset:20800\n" \
    "v_mul_f32_e64 v10, v5, v10\n" \
    "v_mul_f32_e64 v11, -v5, v11\n" \
    "v_pk_mul_f32 v[18:19], v[8:9], v[14:15] op_sel_hi:[0,1]\n" \
    "v_cvt_pk_bf16_f32 v30, v14, v15\n" \
    "v_pk_fma_f32 v[14:15], v[14:15], v[8:9], v[18:19] op_sel:[1,1,0] op_sel_hi:[0,1,1] neg_lo:[0,1,0]\n" \
    "ds_write_addtid_b32 v30 offset:22880\n" \
    "v_mul_f32_e64 v67, v0, v67\n" \
    "v_mul_f32_e64 v16, v68, v66\n" \
    "v_mul_f32_e64 v17, -v68, v67\n" \
    "v_fma_f32 v16, -v69, v67, v16\n" \
    "v_fma_f32 v17, -v69, v66, v17\n" \
    "v_pk_mul_f32 v[18:19], v[8:9], v[14:15] op_sel_hi:[0,1]\n" \
    "v_cvt_pk_bf16_f32 v31, v14, v15\n" \
    "v_pk_fma_f32 v[14:15], v[14:15], v[8:9], v[18:19] op_sel:[1,1,0] op_sel_hi:[0,1,1] neg_lo:[0,1,0]\n" \
    "ds_write_addtid_b32 v31 offset:24960\n" \
    "v_pk_mul_f32 v[18:19], v[8:9], v[14:15] op_sel_hi:[0,1]\n" \
    "v_cvt_pk_bf16_f32 v30, v14, v15\n" \
    "v_pk_fma_f32 v[14:15], v[14:15], v[8:9], v[18:19] op_sel:[1,1,0] op_sel_hi:[0,1,1] neg_lo:[0,1,0]\n" \
    "ds_write_addtid_b32 v30 offset:27040\n" \
    "v_pk_mul_f32 v[18:19], v[8:9], v[14:15] op_sel_hi:[0,1]\n" \
    "v_cvt_pk_bf16_f32 v31, v14, v15\n" \
    "v_pk_fma_f32 v[14:15], v[14:15], v[8:9], v[18:19] op_sel:[1,1,0] op_sel_hi:[0,1,1] neg_lo:[0,1,0]\n" \
    "ds_write_addtid_b32 v31 offset:29120\n" \
    "v_pk_mul_f32 v[18:19], v[12:13], v[16:17] op_sel_hi:[0,1]\n" \
    "v_cvt_pk_bf16_f32 v30, v14, v15\n" \
    "v_pk_fma_f32 v[20:21], v[16:17], v[12:13], v[18:19] op_sel:[1,1,0] op_sel_hi:[0,1,1] neg_lo:[0,1,0]\n" \
    "ds_write_addtid_b32 v30 offset:31200\n" \
    "v_mov_b32_e32 v61, 1\n" \
    "ds_write_addtid_b32 v61 offset:54080\n" \
    "s_add_u32 m0, %[m0a], 54080\n" \
    "v_cvt_pk_bf16_f32 v31, v16, v17\n" \
    "ds_write_addtid_b32 v31 offset:4160\n" \
    "v_pk_mul_f32 v[18:19], v[10:11], v[16:17] op_sel_hi:[0,1]\n" \
    "v_cvt_pk_bf16_f32 v30, v20, v21\n" \
    "v_pk_fma_f32 v[16:17], v[16:17], v[10:11], v[18:19] op_sel:[1,1,0] op_sel_hi:[0,1,1] neg_lo:[0,1,0]\n" \
    "ds_write_addtid_b32 v30 offset:20800\n" \
    "v_pk_mul_f32 v[18:19], v[12:13], v[16:17] op_sel_hi:[0,1]\n" \
    "v_cvt_pk_bf16_f32 v31, v16, v17\n" \
    "v_pk_fma_f32 v[14:15], v[16:17], v[12:13], v[18:19] op_sel:[1,1,0] op_sel_hi:[0,1,1] neg_lo:[0,1,0]\n" \
    "ds_write_addtid_b32 v31 offset:6240\n" \
    "v_pk_mul_f32 v[18:19], v[10:11], v[16:17] op_sel_hi:[0,1]\n" \
    "v_cvt_pk_bf16_f32 v30, v14, v15\n" \
    "v_pk_fma_f32 v[16:17], v[16:17], v[10:11], v[18:19] op_sel:[1,1,0] op_sel_hi:[0,1,1] neg_lo:[0,1,0]\n" \
    "ds_write_addtid_b32 v30 offset:22880\n" \
    "v_pk_mul_f32 v[18:19], v[12:13], v[16:17] op_sel_hi:[0,1]\n" \
    "v_cvt_pk_bf16_f32 v31, v16, v17\n" \
    "v_pk_fma_f32 v[22:23], v[16:17], v[12:13], v[18:19] op_sel:[1,1,0] op_sel_hi:[0,1,1] neg_lo:[0,1,0]\n" \
    "ds_write_addtid_b32 v31 offset:8320\n" \
    "v_pk_mul_f32 v[18:19], v[10:11], v[16:17] op_sel_hi:[0,1]\n" \
    "v_cvt_pk_bf16_f32 v30, v22, v23\n" \
    "v_pk_fma_f32 v[16:17], v[16:17], v[10:11], v[18:19] op_sel:[1,1,0] op_sel_hi:[0,1,1] neg_lo:[0,1,0]\n" \
    "ds_write_addtid_b32 v30 offset:24960\n" \
    "v_pk_mul_f32 v[18:19], v[12:13], v[16:17] op_sel_hi:[0,1]\n" \
    "v_cvt_pk_bf16_f32 v31, v16, v17\n" \
    "v_pk_fma_f32 v[14:15], v[16:17], v[12:13], v[18:19] op_sel:[1,1,0] op_sel_hi:[0,1,1] neg_lo:[0,1,0]\n" \
    "ds_write_addtid_b32 v31 offset:10400\n" \
    "v_pk_mul_f32 v[18:19], v[10:11], v[16:17] op_sel_hi:[0,1]\n" \
    "v_cvt_pk_bf16_f32 v30, v14, v15\n" \
    "v_pk_fma_f32 v[16:17], v[16:17], v[10:11], v[18:19] op_sel:[1,1,0] op_sel_hi:[0,1,1] neg_lo:[0,1,0]\n" \
    "ds_write_addtid_b32 v30 offset:27040\n" \
    "v_pk_mul_f32 v[18:19], v[12:13], v[16:17] op_sel_hi:[0,1]\n" \
    "v_cvt_pk_bf16_f32 v31, v16, v17\n" \
    "v_pk_fma_f32 v[24:25], v[16:17], v[12:13], v[18:19] op_sel:[1,1,0] op_sel_hi:[0,1,1] neg_lo:[0,1,0]\n" \
    "ds_write_addtid_b32 v31 offset:12480\n" \
    "v_pk_mul_f32 v[18:19], v[10:11], v[16:17] op_sel_hi:[0,1]\n" \
    "v_cvt_pk_bf16_f32 v30, v24, v25\n" \
    "v_pk_fma_f32 v[16:17], v[16:17], v[10:11], v[18:19] op_sel:[1,1,0] op_sel_hi:[0,1,1] neg_lo:[0,1,0]\n" \
    "ds_write_addtid_b32 v30 offset:29120\n" \
    "v_pk_mul_f32 v[18:19], v[12:13], v[16:17] op_sel_hi:[0,1]\n" \
    "v_cvt_pk_bf16_f32 v31, v16, v17\n" \
    "v_pk_fma_f32 v[14:15], v[16:17], v[12:13], v[18:19] op_sel:[1,1,0] op_sel_hi:[0,1,1] neg_lo:[0,1,0]\n" \
    "ds_write_addtid_b32 v31 offset:14560\n" \
    "v_pk_mul_f32 v[18:19], v[10:11], v[16:17] op_sel_hi:[0,1]\n" \
    "v_cvt_pk_bf16_f32 v30, v14, v15\n" \
    "v_pk_fma_f32 v[16:17], v[16:17], v[10:11], v[18:19] op_sel:[1,1,0] op_sel_hi:[0,1,1] neg_lo:[0,1,0]\n" \
    "ds_write_addtid_b32 v30 offset:31200\n" \
    "v_pk_mul_f32 v[18:19], v[12:13], v[16:17] op_sel_hi:[0,1]\n" \
    "v_cvt_pk_bf16_f32 v31, v16, v17\n" \
    "v_pk_fma_f32 v[26:27], v[16:17], v[12:13], v[18:19] op_sel:[1,1,0] op_sel_hi:[0,1,1] neg_lo:[0,1,0]\n" \
    "ds_write_addtid_b32 v31 offset:16640\n" \
    "v_pk_mul_f32 v[18:19], v[10:11], v[16:17] op_sel_hi:[0,1]\n" \
    "v_cvt_pk_bf16_f32 v30, v26, v27\n" \
    "v_pk_fma_f32 v[16:17], v[16:17], v[10:11], v[18:19] op_sel:[1,1,0] op_sel_hi:[0,1,1] neg_lo:[0,1,0]\n" \
    "ds_write_addtid_b32 v30 offset:33280\n" \
    "v_pk_mul_f32 v[18:19], v[12:13], v[16:17] op_sel_hi:[0,1]\n" \
    "v_cvt_pk_bf16_f32 v31, v16, v17\n" \
    "v_pk_fma_f32 v[14:15], v[16:17], v[12:13], v[18:19] op_sel:[1,1,0] op_sel_hi:[0,1,1] neg_lo:[0,1,0]\n" \
    "ds_write_addtid_b32 v31 offset:18720\n" \
    "s_waitcnt vmcnt(0)\n" \
    "v_readfirstlane_b32 %[ep], v72\n" \
    "v_pk_mul_f32 v[18:19], v[12:13], v[20:21] op_sel_hi:[0,1]\n" \
    "v_cvt_pk_bf16_f32 v30, v14, v15\n" \
    "v_pk_fma_f32 v[20:21], v[20:21], v[12:13], v[18:19] op_sel:[1,1,0] op_sel_hi:[0,1,1] neg_lo:[0,1,0]\n" \
    "ds_write_addtid_b32 v30 offset:35360\n" \
    "v_mov_b32_e32 v61, 2\n" \
    "ds_write_addtid_b32 v61 offset:0\n" \
    "s_mov_b32 m0, %[m0a]\n" \
    "v_pk_mul_f32 v[18:19], v[12:13], v[22:23] op_sel_hi:[0,1]\n" \
    "v_cvt_pk_bf16_f32 v31, v20, v21\n" \
    "v_pk_fma_f32 v[22:23], v[22:23], v[12:13], v[18:19] op_sel:[1,1,0] op_sel_hi:[0,1,1] neg_lo:[0,1,0]\n" \
    "ds_write_addtid_b32 v31 offset:33280\n" \
    "v_pk_mul_f32 v[18:19], v[12:13], v[24:25] op_sel_hi:[0,1]\n" \
    "v_cvt_pk_bf16_f32 v30, v22, v23\n" \
    "v_pk_fma_f32 v[24:25], v[24:25], v[12:13], v[18:19] op_sel:[1,1,0] op_sel_hi:[0,1,1] neg_lo:[0,1,0]\n" \
    "ds_write_addtid_b32 v30 offset:35360\n" \
    "v_pk_mul_f32 v[18:19], v[12:13], v[26:27] op_sel_hi:[0,1]\n" \
    "v_cvt_pk_bf16_f32 v31, v24, v25\n" \
    "v_pk_fma_f32 v[26:27], v[26:27], v[12:13], v[18:19] op_sel:[1,1,0] op_sel_hi:[0,1,1] neg_lo:[0,1,0]\n" \
    "ds_write_addtid_b32 v31 offset:37440\n" \
    "v_pk_mul_f32 v[18:19], v[10:11], v[14:15] op_sel_hi:[0,1]\n" \
    "v_cvt_pk_bf16_f32 v30, v26, v27\n" \
    "v_pk_fma_f32 v[28:29], v[14:15], v[10:11], v[18:19] op_sel:[1,1,0] op_sel_hi:[0,1,1] neg_lo:[0,1,0]\n" \
    "ds_write_addtid_b32 v30 offset:39520\n" \
    "v_pk_mul_f32 v[18:19], v[12:13], v[28:29] op_sel_hi:[0,1]\n" \
    "v_pk_fma_f32 v[28:29], v[28:29], v[12:13], v[18:19] op_sel:[1,1,0] op_sel_hi:[0,1,1] neg_lo:[0,1,0]\n" \
    "v_pk_mul_f32 v[18:19], v[12:13], v[20:21] op_sel_hi:[0,1]\n" \
    "v_cvt_pk_bf16_f32 v31, v28, v29\n" \
    "v_pk_fma_f32 v[20:21], v[20:21], v[12:13], v[18:19] op_sel:[1,1,0] op_sel_hi:[0,1,1] neg_lo:[0,1,0]\n" \
    "ds_write_addtid_b32 v31 offset:41600\n" \
    "v_pk_mul_f32 v[18:19], v[12:13], v[22:23] op_sel_hi:[0,1]\n" \
    "v_cvt_pk_bf16_f32 v30, v20, v21\n" \
    "v_pk_fma_f32 v[22:23], v[22:23], v[12:13], v[18:19] op_sel:[1,1,0] op_sel_hi:[0,1,1] neg_lo:[0,1,0]\n" \
    "ds_write_addtid_b32 v30 offset:43680\n" \
    "v_pk_mul_f32 v[18:19], v[12:13], v[24:25] op_sel_hi:[0,1]\n" \
    "v_cvt_pk_bf16_f32 v31, v22, v23\n" \
    "v_pk_fma_f32 v[24:25], v[24:25], v[12:13], v[18:19] op_sel:[1,1,0] op_sel_hi:[0,1,1] neg_lo:[0,1,0]\n" \
    "ds_write_addtid_b32 v31 offset:45760\n" \
    "v_pk_mul_f32 v[18:19], v[12:13], v[26:27] op_sel_hi:[0,1]\n" \
    "v_cvt_pk_bf16_f32 v30, v24, v25\n" \
    "v_pk_fma_f32 v[26:27], v[26:27], v[12:13], v[18:19] op_sel:[1,1,0] op_sel_hi:[0,1,1] neg_lo:[0,1,0]\n" \
    "ds_write_addtid_b32 v30 offset:47840\n" \
    "v_pk_mul_f32 v[18:19], v[12:13], v[28:29] op_sel_hi:[0,1]\n" \
    "v_cvt_pk_bf16_f32 v31, v26, v27\n" \
    "v_pk_fma_f32 v[28:29], v[28:29], v[12:13], v[18:19] op_sel:[1,1,0] op_sel_hi:[0,1,1] neg_lo:[0,1,0]\n" \
    "ds_write_addtid_b32 v31 offset:49920\n" \
    "v_pk_mul_f32 v[18:19], v[12:13], v[24:25] op_sel_hi:[0,1]\n" \
    "v_cvt_pk_bf16_f32 v30, v28, v29\n" \
    "v_pk_fma_f32 v[24:25], v[24:25], v[12:13], v[18:19] op_sel:[1,1,0] op_sel_hi:[0,1,1] neg_lo:[0,1,0]\n" \
    "ds_write_addtid_b32 v30 offset:52000\n" \
    "v_mov_b32_e32 v61, 3\n" \
    "ds_write_addtid_b32 v61 offset:54080\n" \
    "s_add_u32 m0, %[m0a], 62400\n" \
    "v_pk_mul_f32 v[18:19], v[12:13], v[28:29] op_sel_hi:[0,1]\n" \
    "v_cvt_pk_bf16_f32 v31, v24, v25\n" \
    "v_pk_fma_f32 v[28:29], v[28:29], v[12:13], v[18:19] op_sel:[1,1,0] op_sel_hi:[0,1,1] neg_lo:[0,1,0]\n" \
    "ds_write_addtid_b32 v31 offset:29120\n" \
    "v_pk_mul_f32 v[18:19], v[12:13], v[24:25] op_sel_hi:[0,1]\n" \
    "v_cvt_pk_bf16_f32 v30, v28, v29\n" \
    "v_pk_fma_f32 v[24:25], v[24:25], v[12:13], v[18:19] op_sel:[1,1,0] op_sel_hi:[0,1,1] neg_lo:[0,1,0]\n" \
    "ds_write_addtid_b32 v30 offset:37440\n" \
    "v_pk_mul_f32 v[18:19], v[12:13], v[28:29] op_sel_hi:[0,1]\n" \
    "v_cvt_pk_bf16_f32 v31, v24, v25\n" \
    "v_pk_fma_f32 v[28:29], v[28:29], v[12:13], v[18:19] op_sel:[1,1,0] op_sel_hi:[0,1,1] neg_lo:[0,1,0]\n" \
    "ds_write_addtid_b32 v31 offset:31200\n" \
    "v_pk_mul_f32 v[18:19], v[12:13], v[24:25] op_sel_hi:[0,1]\n" \
    "v_cvt_pk_bf16_f32 v30, v28, v29\n" \
    "v_pk_fma_f32 v[24:25], v[24:25], v[12:13], v[18:19] op_sel:[1,1,0] op_sel_hi:[0,1,1] neg_lo:[0,1,0]\n" \
    "ds_write_addtid_b32 v30 offset:39520\n" \
    "v_pk_mul_f32 v[18:19], v[12:13], v[28:29] op_sel_hi:[0,1]\n" \
    "v_cvt_pk_bf16_f32 v31, v24, v25\n" \
    "v_pk_fma_f32 v[28:29], v[28:29], v[12:13], v[18:19] op_sel:[1,1,0] op_sel_hi:[0,1,1] neg_lo:[0,1,0]\n" \
    "ds_write_addtid_b32 v31 offset:33280\n" \
    "v_pk_mul_f32 v[18:19], v[12:13], v[24:25] op_sel_hi:[0,1]\n" \
    "v_cvt_pk_bf16_f32 v30, v28, v29\n" \
    "v_pk_fma_f32 v[24:25], v[24:25], v[12:13], v[18:19] op_sel:[1,1,0] op_sel_hi:[0,1,1] neg_lo:[0,1,0]\n" \
    "ds_write_addtid_b32 v30 offset:41600\n" \
    "v_pk_mul_f32 v[18:19], v[12:13], v[28:29] op_sel_hi:[0,1]\n" \
    "v_cvt_pk_bf16_f32 v31, v24, v25\n" \
    "v_pk_fma_f32 v[28:29], v[28:29], v[12:13], v[18:19] op_sel:[1,1,0] op_sel_hi:[0,1,1] neg_lo:[0,1,0]\n" \
    "ds_write_addtid_b32 v31 offset:35360\n" \
    "v_pk_mul_f32 v[18:19], v[12:13], v[28:29] op_sel_hi:[0,1]\n" \
    "v_cvt_pk_bf16_f32 v30, v28, v29\n" \
    "v_pk_fma_f32 v[28:29], v[28:29], v[12:13], v[18:19] op_sel:[1,1,0] op_sel_hi:[0,1,1] neg_lo:[0,1,0]\n" \
    "ds_write_addtid_b32 v30 offset:43680\n" \
    "v_pk_mul_f32 v[18:19], v[12:13], v[28:29] op_sel_hi:[0,1]\n" \
    "v_cvt_pk_bf16_f32 v31, v28, v29\n" \
    "v_pk_fma_f32 v[28:29], v[28:29], v[12:13], v[18:19] op_sel:[1,1,0] op_sel_hi:[0,1,1] neg_lo:[0,1,0]\n" \
    "ds_write_addtid_b32 v31 offset:45760\n" \
    "v_pk_mul_f32 v[18:19], v[12:13], v[28:29] op_sel_hi:[0,1]\n" \
    "v_cvt_pk_bf16_f32 v30, v28, v29\n" \
    "v_pk_fma_f32 v[28:29], v[28:29], v[12:13], v[18:19] op_sel:[1,1,0] op_sel_hi:[0,1,1] neg_lo:[0,1,0]\n" \
    "ds_write_addtid_b32 v30 offset:47840\n" \
    "v_pk_mul_f32 v[18:19], v[12:13], v[28:29] op_sel_hi:[0,1]\n" \
    "v_cvt_pk_bf16_f32 v31, v28, v29\n" \
    "v_pk_fma_f32 v[28:29], v[28:29], v[12:13], v[18:19] op_sel:[1,1,0] op_sel_hi:[0,1,1] neg_lo:[0,1,0]\n" \
    "ds_write_addtid_b32 v31 offset:49920\n" \
    "v_pk_mul_f32 v[18:19], v[12:13], v[28:29] op_sel_hi:[0,1]\n" \
    "v_cvt_pk_bf16_f32 v30, v28, v29\n" \
    "v_pk_fma_f32 v[28:29], v[28:29], v[12:13], v[18:19] op_sel:[1,1,0] op_sel_hi:[0,1,1] neg_lo:[0,1,0]\n" \
    "ds_write_addtid_b32 v30 offset:52000\n" \
    "v_pk_mul_f32 v[18:19], v[12:13], v[28:29] op_sel_hi:[0,1]\n" \
    "v_cvt_pk_bf16_f32 v31, v28, v29\n" \
    "v_pk_fma_f32 v[28:29], v[28:29], v[12:13], v[18:19] op_sel:[1,1,0] op_sel_hi:[0,1,1] neg_lo:[0,1,0]\n" \
    "ds_write_addtid_b32 v31 offset:54080\n" \
    "v_pk_mul_f32 v[18:19], v[12:13], v[28:29] op_sel_hi:[0,1]\n" \
    "v_cvt_pk_bf16_f32 v30, v28, v29\n" \
    "v_pk_fma_f32 v[28:29], v[28:29], v[12:13], v[18:19] op_sel:[1,1,0] op_sel_hi:[0,1,1] neg_lo:[0,1,0]\n" \
    "ds_write_addtid_b32 v30 offset:56160\n" \
    "v_pk_mul_f32 v[18:19], v[12:13], v[28:29] op_sel_hi:[0,1]\n" \
    "v_cvt_pk_bf16_f32 v31, v28, v29\n" \
    "v_pk_fma_f32 v[28:29], v[28:29], v[12:13], v[18:19] op_sel:[1,1,0] op_sel_hi:[0,1,1] neg_lo:[0,1,0]\n" \
    "ds_write_addtid_b32 v31 offset:58240\n" \
    "v_cvt_pk_bf16_f32 v30, v28, v29\n" \
    "ds_write_addtid_b32 v30 offset:60320\n" \
    "s_mov_b32 m0, %[m0a]\n" \
    "v_mov_b32_e32 v61, 4\n" \
    "ds_write_addtid_b32 v61 offset:54080\n" \
    "s_setprio 1\n" \
    "ds_read_b32 v63, v34\n" \
    "ds_read_b128 v[152:155], v38 offset:1024\n" \
    "ds_read_b128 v[160:163], v38 offset:3072\n" \
    "ds_read_b128 v[148:151], v38 offset:0\n" \
    "ds_read_b128 v[156:159], v38 offset:2048\n" \
    "ds_read_b128 v[164:167], v38 offset:4096\n" \
    "s_waitcnt lgkmcnt(5)\n" \
    "v_cmp_gt_u32_e32 vcc, 2, v63\n" \
    "s_cbranch_vccz Lbe0_%=\n" \
    "s_mov_b32 s90, 0x100000\n" \
    "Lbl0_%=:\n" \
    "s_sleep 1\n" \
    "ds_read_b32 v63, v34 offset:0\n" \
    "ds_read_b128 v[152:155], v38 offset:1024\n" \
    "ds_read_b128 v[160:163], v38 offset:3072\n" \
    "ds_read_b128 v[148:151], v38 offset:0\n" \
    "ds_read_b128 v[156:159], v38 offset:2048\n" \
    "ds_read_b128 v[164:167], v38 offset:4096\n" \
    "s_waitcnt lgkmcnt(0)\n" \
    "v_cmp_gt_u32_e32 vcc, 2, v63\n" \
    "s_cbranch_vccz Lbe0_%=\n" \
    "s_sub_u32 s90, s90, 1\n" \
    "s_cbranch_scc0 Lbl0_%=\n" \
    "Lbe0_%=:\n" \
    "ds_read_b128 v[108:111], v33 offset:448\n" \
    "ds_read_b128 v[112:115], v33 offset:432\n" \
    "ds_read_b128 v[116:119], v33 offset:960\n" \
    "ds_read_b128 v[120:123], v33 offset:944\n" \
    "s_waitcnt lgkmcnt(3)\n" \
    "v_mfma_f32_16x16x32_bf16 v[48:51], v[108:111], v[152:155], 0\n" \
    "ds_read_b32 v60, v32\n" \
    "s_waitcnt lgkmcnt(3)\n" \
    "v_mfma_f32_16x16x32_bf16 v[52:55], v[112:115], v[152:155], 0\n" \
    "v_mfma_f32_16x16x32_bf16 v[56:59], v[112:115], v[160:163], 0\n" \
    "v_mfma_f32_16x16x32_bf16 v[0:3], v[108:111], v[148:151], 0\n" \
    "v_mfma_f32_16x16x32_bf16 v[4:7], v[112:115], v[148:151], 0\n" \
    "v_mfma_f32_16x16x32_bf16 v[40:43], v[108:111], v[156:159], 0\n" \
    "s_waitcnt lgkmcnt(2)\n" \
    "v_mfma_f32_16x16x32_bf16 v[8:11], v[116:119], v[148:151], 0\n" \
    "s_nop 3\n" \
    "v_cvt_pk_bf16_f32 v0, v0, v48\n" \
    "v_cvt_pk_bf16_f32 v1, v1, v49\n" \
    "v_cvt_pk_bf16_f32 v2, v2, v50\n" \
    "v_cvt_pk_bf16_f32 v3, v3, v51\n" \
    "s_waitcnt lgkmcnt(1)\n" \
    "v_mfma_f32_16x16x32_bf16 v[12:15], v[120:123], v[148:151], 0\n" \
    "v_cvt_pk_bf16_f32 v4, v4, v52\n" \
    "v_cvt_pk_bf16_f32 v5, v5, v53\n" \
    "v_cvt_pk_bf16_f32 v6, v6, v54\n" \
    "v_cvt_pk_bf16_f32 v7, v7, v55\n" \
    "v_mfma_f32_16x16x32_bf16 v[44:47], v[116:119], v[156:159], 0\n" \
    "v_cvt_pk_bf16_f32 v40, v40, v56\n" \
    "v_cvt_pk_bf16_f32 v41, v41, v57\n" \
    "v_cvt_pk_bf16_f32 v42, v42, v58\n" \
    "v_cvt_pk_bf16_f32 v43, v43, v59\n" \
    "v_mfma_f32_16x16x32_bf16 v[48:51], v[116:119], v[152:155], 0\n" \
    "v_mfma_f32_16x16x32_bf16 v[52:55], v[120:123], v[152:155], 0\n" \
    "v_mfma_f32_16x16x32_bf16 v[56:59], v[120:123], v[160:163], 0\n" \
    "s_waitcnt lgkmcnt(0)\n" \
    "v_cmp_gt_u32_e32 vcc, 2, v60\n" \
    "s_cbranch_vccz Lbe1_%=\n" \
    "s_mov_b32 s90, 0x100000\n" \
    "Lbl1_%=:\n" \
    "s_sleep 1\n" \
    "ds_read_b32 v60, v32 offset:0\n" \
    "s_waitcnt lgkmcnt(0)\n" \
    "v_cmp_gt_u32_e32 vcc, 2, v60\n" \
    "s_cbranch_vccz Lbe1_%=\n" \
    "s_sub_u32 s90, s90, 1\n" \
    "s_cbranch_scc0 Lbl1_%=\n" \
    "Lbe1_%=:\n" \
    "ds_read2_b32 v[104:105], v37 offset0:0 offset1:1\n" \
    "ds_read2_b32 v[106:107], v37 offset0:2 offset1:3\n" \
    "s_waitcnt lgkmcnt(0)\n" \
    "v_mfma_f32_16x16x32_bf16 v[132:135], v[104:107], v[108:111], 0\n" \
    "v_mfma_f32_16x16x32_bf16 v[136:139], v[104:107], v[116:119], 0\n" \
    "ds_read_b32 v60, v32\n" \
    "s_waitcnt lgkmcnt(0)\n" \
    "v_cmp_gt_u32_e32 vcc, 3, v60\n" \
    "s_cbranch_vccz Lbe2_%=\n" \
    "s_mov_b32 s90, 0x100000\n" \
    "Lbl2_%=:\n" \
    "s_sleep 1\n" \
    "ds_read_b32 v60, v32 offset:0\n" \
    "s_waitcnt lgkmcnt(0)\n" \
    "v_cmp_gt_u32_e32 vcc, 3, v60\n" \
    "s_cbranch_vccz Lbe2_%=\n" \
    "s_sub_u32 s90, s90, 1\n" \
    "s_cbranch_scc0 Lbl2_%=\n" \
    "Lbe2_%=:\n" \
    "ds_read_b32 v62, v39 offset:320\n" \
    "ds_read_b32 v63, v34\n" \
    "ds_read2_b32 v[64:65], v36 offset0:32 offset1:33\n" \
    "ds_read2_b32 v[66:67], v36 offset0:34 offset1:35\n" \
    "ds_read2_b32 v[76:77], v36 offset0:96 offset1:97\n" \
    "ds_read2_b32 v[78:79], v36 offset0:98 offset1:99\n" \
    "s_waitcnt lgkmcnt(5)\n" \
    "v_perm_b32 v62, v62, v62, s92\n" \
    "v_and_b32_e64 v164, v62, v164\n" \
    "v_and_b32_e64 v165, v62, v165\n" \
    "v_and_b32_e64 v166, v62, v166\n" \
    "v_and_b32_e64 v167, v62, v167\n" \
    "s_nop 0\n" \
    "s_nop 0\n" \
    "v_mfma_f32_16x16x32_bf16 v[140:143], v[164:167], v[108:111], 0\n" \
    "v_mfma_f32_16x16x32_bf16 v[124:127], v[164:167], v[116:119], 0\n" \
    "s_waitcnt lgkmcnt(4)\n" \
    "v_cmp_gt_u32_e32 vcc, 3, v63\n" \
    "s_cbranch_vccnz Lbs3_%=\n" \
    "Lbe3_%=:\n" \
    "ds_read_b128 v[108:111], v33 offset:1472\n" \
    "ds_read_b128 v[112:115], v33 offset:1456\n" \
    "s_setprio 3\n" \
    "s_waitcnt lgkmcnt(4)\n" \
    "v_mfma_f32_16x16x32_bf16 v[132:135], v[0:3], v[64:67], v[132:135]\n" \
    "v_cvt_pk_bf16_f32 v8, v8, v48\n" \
    "v_cvt_pk_bf16_f32 v9, v9, v49\n" \
    "v_cvt_pk_bf16_f32 v10, v10, v50\n" \
    "v_cvt_pk_bf16_f32 v11, v11, v51\n" \
    "v_cvt_pk_bf16_f32 v12, v12, v52\n" \
    "v_cvt_pk_bf16_f32 v13, v13, v53\n" \
    "v_cvt_pk_bf16_f32 v14, v14, v54\n" \
    "v_cvt_pk_bf16_f32 v15, v15, v55\n" \
    "v_cvt_pk_bf16_f32 v44, v44, v56\n" \
    "v_cvt_pk_bf16_f32 v45, v45, v57\n" \
    "v_cvt_pk_bf16_f32 v46, v46, v58\n" \
    "v_cvt_pk_bf16_f32 v47, v47, v59\n" \
    "ds_read_b32 v63, v34\n" \
    "s_waitcnt lgkmcnt(3)\n" \
    "v_mfma_f32_16x16x32_bf16 v[132:135], v[4:7], v[76:79], v[132:135]\n" \
    "s_waitcnt lgkmcnt(2)\n" \
    "v_mfma_f32_16x16x32_bf16 v[128:131], v[164:167], v[108:111], 0\n" \
    "ds_read2_b32 v[68:69], v36 offset0:16 offset1:17\n" \
    "ds_read2_b32 v[70:71], v36 offset0:18 offset1:19\n" \
    "v_mfma_f32_16x16x32_bf16 v[140:143], v[104:107], v[108:111], v[140:143]\n" \
    "ds_read2_b32 v[80:81], v36 offset0:80 offset1:81\n" \
    "ds_read2_b32 v[82:83], v36 offset0:82 offset1:83\n" \
    "v_mfma_f32_16x16x32_bf16 v[16:19], v[108:111], v[148:151], 0\n" \
    "s_waitcnt lgkmcnt(5)\n" \
    "v_mfma_f32_16x16x32_bf16 v[20:23], v[112:115], v[148:151], 0\n" \
    "v_mfma_f32_16x16x32_bf16 v[144:147], v[108:111], v[156:159], 0\n" \
    "v_mfma_f32_16x16x32_bf16 v[48:51], v[108:111], v[152:155], 0\n" \
    "v_mfma_f32_16x16x32_bf16 v[52:55], v[112:115], v[152:155], 0\n" \
    "v_mfma_f32_16x16x32_bf16 v[56:59], v[112:115], v[160:163], 0\n" \
    "s_waitcnt lgkmcnt(4)\n" \
    "v_cmp_gt_u32_e32 vcc, 4, v63\n" \
    "s_cbranch_vccnz Lbs4_%=\n" \
    "Lbe4_%=:\n" \
    "ds_read_b128 v[116:119], v33 offset:1984\n" \
    "ds_read_b128 v[120:123], v33 offset:1968\n" \
    "v_mfma_f32_16x16x32_bf16 v[136:139], v[8:11], v[64:67], v[136:139]\n" \
    "v_mov_b32_e64 v61, 13\n" \
    "ds_write_b128 v35, v[132:135] offset:0\n" \
    "ds_write_addtid_b32 v61 offset:54080\n" \
    "v_cvt_pk_bf16_f32 v16, v16, v48\n" \
    "v_cvt_pk_bf16_f32 v17, v17, v49\n" \
    "v_cvt_pk_bf16_f32 v18, v18, v50\n" \
    "v_cvt_pk_bf16_f32 v19, v19, v51\n" \
    "v_cvt_pk_bf16_f32 v20, v20, v52\n" \
    "v_cvt_pk_bf16_f32 v21, v21, v53\n" \
    "v_cvt_pk_bf16_f32 v22, v22, v54\n" \
    "v_cvt_pk_bf16_f32 v23, v23, v55\n" \
    "v_cvt_pk_bf16_f32 v144, v144, v56\n" \
    "v_cvt_pk_bf16_f32 v145, v145, v57\n" \
    "v_cvt_pk_bf16_f32 v146, v146, v58\n" \
    "v_cvt_pk_bf16_f32 v147, v147, v59\n" \
    "ds_read_b32 v63, v34\n" \
    "v_mfma_f32_16x16x32_bf16 v[136:139], v[12:15], v[76:79], v[136:139]\n" \
    "s_waitcnt lgkmcnt(7)\n" \
    "v_mfma_f32_16x16x32_bf16 v[136:139], v[0:3], v[68:71], v[136:139]\n" \
    "s_waitcnt lgkmcnt(5)\n" \
    "v_mfma_f32_16x16x32_bf16 v[136:139], v[4:7], v[80:83], v[136:139]\n" \
    "s_waitcnt lgkmcnt(4)\n" \
    "v_mfma_f32_16x16x32_bf16 v[132:135], v[164:167], v[116:119], 0\n" \
    "ds_read2_b32 v[72:73], v36 offset0:0 offset1:1\n" \
    "ds_read2_b32 v[74:75], v36 offset0:2 offset1:3\n" \
    "v_mfma_f32_16x16x32_bf16 v[124:127], v[104:107], v[116:119], v[124:127]\n" \
    "ds_read2_b32 v[84:85], v36 offset0:64 offset1:65\n" \
    "ds_read2_b32 v[86:87], v36 offset0:66 offset1:67\n" \
    "v_mfma_f32_16x16x32_bf16 v[24:27], v[116:119], v[148:151], 0\n" \
    "ds_read2_b32 v[88:89], v36 offset0:176 offset1:177\n" \
    "ds_read2_b32 v[90:91], v36 offset0:178 offset1:179\n" \
    "s_waitcnt lgkmcnt(9)\n" \
    "v_mfma_f32_16x16x32_bf16 v[28:31], v[120:123], v[148:151], 0\n" \
    "v_mfma_f32_16x16x32_bf16 v[164:167], v[116:119], v[156:159], 0\n" \
    "v_mfma_f32_16x16x32_bf16 v[48:51], v[116:119], v[152:155], 0\n" \
    "v_mfma_f32_16x16x32_bf16 v[52:55], v[120:123], v[152:155], 0\n" \
    "v_mfma_f32_16x16x32_bf16 v[56:59], v[120:123], v[160:163], 0\n" \
    "s_waitcnt lgkmcnt(6)\n" \
    "v_cmp_gt_u32_e32 vcc, 5, v63\n" \
    "s_cbranch_vccnz Lbs5_%=\n" \
    "Lbe5_%=:\n" \
    "ds_read_b128 v[108:111], v33 offset:2496\n" \
    "ds_read_b128 v[112:115], v33 offset:2480\n" \
    "v_mfma_f32_16x16x32_bf16 v[140:143], v[16:19], v[64:67], v[140:143]\n" \
    "v_mov_b32_e64 v61, 14\n" \
    "ds_write_b128 v35, v[136:139] offset:4160\n" \
    "ds_write_addtid_b32 v61 offset:54080\n" \
    "v_mfma_f32_16x16x32_bf16 v[140:143], v[20:23], v[76:79], v[140:143]\n" \
    "v_cvt_pk_bf16_f32 v24, v24, v48\n" \
    "v_cvt_pk_bf16_f32 v25, v25, v49\n" \
    "v_cvt_pk_bf16_f32 v26, v26, v50\n" \
    "v_mfma_f32_16x16x32_bf16 v[140:143], v[8:11], v[68:71], v[140:143]\n" \
    "v_cvt_pk_bf16_f32 v27, v27, v51\n" \
    "v_cvt_pk_bf16_f32 v28, v28, v52\n" \
    "v_cvt_pk_bf16_f32 v29, v29, v53\n" \
    "v_mfma_f32_16x16x32_bf16 v[140:143], v[12:15], v[80:83], v[140:143]\n" \
    "v_cvt_pk_bf16_f32 v30, v30, v54\n" \
    "v_cvt_pk_bf16_f32 v31, v31, v55\n" \
    "v_cvt_pk_bf16_f32 v164, v164, v56\n" \
    "s_waitcnt lgkmcnt(8)\n" \
    "v_mfma_f32_16x16x32_bf16 v[140:143], v[0:3], v[72:75], v[140:143]\n" \
    "v_cvt_pk_bf16_f32 v165, v165, v57\n" \
    "v_cvt_pk_bf16_f32 v166, v166, v58\n" \
    "v_cvt_pk_bf16_f32 v167, v167, v59\n" \
    "s_waitcnt lgkmcnt(6)\n" \
    "v_mfma_f32_16x16x32_bf16 v[140:143], v[4:7], v[84:87], v[140:143]\n" \
    "ds_read_b32 v63, v34\n" \
    "s_waitcnt lgkmcnt(5)\n" \
    "v_mfma_f32_16x16x32_bf16 v[140:143], v[40:43], v[88:91], v[140:143]\n" \
    "s_waitcnt lgkmcnt(4)\n" \
    "v_mfma_f32_16x16x32_bf16 v[128:131], v[104:107], v[108:111], v[128:131]\n" \
    "ds_read2_b32 v[92:93], v36 offset0:160 offset1:161\n" \
    "ds_read2_b32 v[94:95], v36 offset0:162 offset1:163\n" \
    "v_mfma_f32_16x16x32_bf16 v[0:3], v[108:111], v[148:151], 0\n" \
    "s_waitcnt lgkmcnt(5)\n" \
    "v_mfma_f32_16x16x32_bf16 v[4:7], v[112:115], v[148:151], 0\n" \
    "v_mfma_f32_16x16x32_bf16 v[48:51], v[108:111], v[152:155], 0\n" \
    "v_mfma_f32_16x16x32_bf16 v[52:55], v[112:115], v[152:155], 0\n" \
    "s_waitcnt lgkmcnt(2)\n" \
    "v_cmp_gt_u32_e32 vcc, 6, v63\n" \
    "s_cbranch_vccnz Lbs6_%=\n" \
    "Lbe6_%=:\n" \
    "ds_read_b128 v[116:119], v33 offset:3008\n" \
    "ds_read_b128 v[120:123], v33 offset:2992\n" \
    "v_mfma_f32_16x16x32_bf16 v[124:127], v[24:27], v[64:67], v[124:127]\n" \
    "v_mov_b32_e64 v61, 15\n" \
    "ds_write_b128 v35, v[140:143] offset:8320\n" \
    "v_mfma_f32_16x16x32_bf16 v[124:127], v[28:31], v[76:79], v[124:127]\n" \
    "ds_write_addtid_b32 v61 offset:54080\n" \
    "v_cvt_pk_bf16_f32 v0, v0, v48\n" \
    "v_mfma_f32_16x16x32_bf16 v[124:127], v[16:19], v[68:71], v[124:127]\n" \
    "v_cvt_pk_bf16_f32 v1, v1, v49\n" \
    "v_cvt_pk_bf16_f32 v2, v2, v50\n" \
    "v_mfma_f32_16x16x32_bf16 v[124:127], v[20:23], v[80:83], v[124:127]\n" \
    "v_cvt_pk_bf16_f32 v3, v3, v51\n" \
    "v_cvt_pk_bf16_f32 v4, v4, v52\n" \
    "v_mfma_f32_16x16x32_bf16 v[124:127], v[8:11], v[72:75], v[124:127]\n" \
    "v_cvt_pk_bf16_f32 v5, v5, v53\n" \
    "v_cvt_pk_bf16_f32 v6, v6, v54\n" \
    "v_mfma_f32_16x16x32_bf16 v[124:127], v[12:15], v[84:87], v[124:127]\n" \
    "v_cvt_pk_bf16_f32 v7, v7, v55\n" \
    "v_mfma_f32_16x16x32_bf16 v[124:127], v[44:47], v[88:91], v[124:127]\n" \
    "s_waitcnt lgkmcnt(4)\n" \
    "v_mfma_f32_16x16x32_bf16 v[124:127], v[40:43], v[92:95], v[124:127]\n" \
    "s_waitcnt lgkmcnt(3)\n" \
    "v_mfma_f32_16x16x32_bf16 v[132:135], v[104:107], v[116:119], v[132:135]\n" \
    "ds_read2_b32 v[96:97], v36 offset0:144 offset1:145\n" \
    "ds_read2_b32 v[98:99], v36 offset0:146 offset1:147\n" \
    "v_mfma_f32_16x16x32_bf16 v[8:11], v[116:119], v[148:151], 0\n" \
    "ds_read2_b32 v[100:101], v36 offset0:128 offset1:129\n" \
    "ds_read2_b32 v[102:103], v36 offset0:130 offset1:131\n" \
    "s_waitcnt lgkmcnt(6)\n" \
    "v_mfma_f32_16x16x32_bf16 v[12:15], v[120:123], v[148:151], 0\n" \
    "v_mfma_f32_16x16x32_bf16 v[48:51], v[116:119], v[152:155], 0\n" \
    "v_mfma_f32_16x16x32_bf16 v[52:55], v[120:123], v[152:155], 0\n" \
    "v_mfma_f32_16x16x32_bf16 v[128:131], v[0:3], v[64:67], v[128:131]\n" \
    "v_mov_b32_e64 v61, 16\n" \
    "ds_write_b128 v35, v[124:127] offset:12480\n" \
    "v_mfma_f32_16x16x32_bf16 v[128:131], v[4:7], v[76:79], v[128:131]\n" \
    "ds_write_addtid_b32 v61 offset:54080\n" \
    "s_nop 1\n" \
    "v_cvt_pk_bf16_f32 v8, v8, v48\n" \
    "v_mfma_f32_16x16x32_bf16 v[128:131], v[24:27], v[68:71], v[128:131]\n" \
    "v_cvt_pk_bf16_f32 v9, v9, v49\n" \
    "v_cvt_pk_bf16_f32 v10, v10, v50\n" \
    "v_mfma_f32_16x16x32_bf16 v[128:131], v[28:31], v[80:83], v[128:131]\n" \
    "v_cvt_pk_bf16_f32 v11, v11, v51\n" \
    "v_cvt_pk_bf16_f32 v12, v12, v52\n" \
    "v_mfma_f32_16x16x32_bf16 v[128:131], v[16:19], v[72:75], v[128:131]\n" \
    "v_cvt_pk_bf16_f32 v13, v13, v53\n" \
    "v_cvt_pk_bf16_f32 v14, v14, v54\n" \
    "v_mfma_f32_16x16x32_bf16 v[128:131], v[20:23], v[84:87], v[128:131]\n" \
    "v_cvt_pk_bf16_f32 v15, v15, v55\n" \
    "v_mfma_f32_16x16x32_bf16 v[128:131], v[144:147], v[88:91], v[128:131]\n" \
    "v_mfma_f32_16x16x32_bf16 v[128:131], v[44:47], v[92:95], v[128:131]\n" \
    "s_waitcnt lgkmcnt(4)\n" \
    "v_mfma_f32_16x16x32_bf16 v[128:131], v[40:43], v[96:99], v[128:131]\n" \
    "v_mov_b32_e32 v108, %[ep]\n" \
    "v_mov_b32_e32 v112, 0\n" \
    "global_load_dwordx2 v[108:109], v112, %[tp] sc1\n" \
    "v_mfma_f32_16x16x32_bf16 v[132:135], v[8:11], v[64:67], v[132:135]\n" \
    "v_mov_b32_e64 v61, 17\n" \
    "s_nop 2\n" \
    "ds_write_b128 v35, v[128:131] offset:16640\n" \
    "v_mfma_f32_16x16x32_bf16 v[132:135], v[12:15], v[76:79], v[132:135]\n" \
    "ds_write_addtid_b32 v61 offset:54080\n" \
    "v_mfma_f32_16x16x32_bf16 v[132:135], v[0:3], v[68:71], v[132:135]\n" \
    "v_mfma_f32_16x16x32_bf16 v[132:135], v[4:7], v[80:83], v[132:135]\n" \
    "v_mfma_f32_16x16x32_bf16 v[132:135], v[24:27], v[72:75], v[132:135]\n" \
    "v_mfma_f32_16x16x32_bf16 v[132:135], v[28:31], v[84:87], v[132:135]\n" \
    "v_mfma_f32_16x16x32_bf16 v[132:135], v[164:167], v[88:91], v[132:135]\n" \
    "v_mfma_f32_16x16x32_bf16 v[132:135], v[144:147], v[92:95], v[132:135]\n" \
    "v_mfma_f32_16x16x32_bf16 v[132:135], v[44:47], v[96:99], v[132:135]\n" \
    "s_waitcnt lgkmcnt(4)\n" \
    "v_mfma_f32_16x16x32_bf16 v[132:135], v[40:43], v[100:103], v[132:135]\n" \
    "v_mov_b32_e64 v61, 18\n" \
    "s_nop 6\n" \
    "ds_write_b128 v35, v[132:135] offset:20800\n" \
    "ds_write_addtid_b32 v61 offset:54080\n" \
    "s_setprio 0\n" \
    "s_branch Lbx_%=\n" \
    "Lbs3_%=:\n" \
    "s_mov_b32 s90, 0x100000\n" \
    "Lbl3_%=:\n" \
    "ds_read_b32 v63, v34 offset:0\n" \
    "s_waitcnt lgkmcnt(0)\n" \
    "v_cmp_gt_u32_e32 vcc, 3, v63\n" \
    "s_cbranch_vccz Lbe3_%=\n" \
    "s_sub_u32 s90, s90, 1\n" \
    "s_cbranch_scc0 Lbl3_%=\n" \
    "s_branch Lbe3_%=\n" \
    "Lbs4_%=:\n" \
    "s_mov_b32 s90, 0x100000\n" \
    "Lbl4_%=:\n" \
    "ds_read_b32 v63, v34 offset:0\n" \
    "s_waitcnt lgkmcnt(0)\n" \
    "v_cmp_gt_u32_e32 vcc, 4, v63\n" \
    "s_cbranch_vccz Lbe4_%=\n" \
    "s_sub_u32 s90, s90, 1\n" \
    "s_cbranch_scc0 Lbl4_%=\n" \
    "s_branch Lbe4_%=\n" \
    "Lbs5_%=:\n" \
    "s_mov_b32 s90, 0x100000\n" \
    "Lbl5_%=:\n" \
    "ds_read_b32 v63, v34 offset:0\n" \
    "s_waitcnt lgkmcnt(0)\n" \
    "v_cmp_gt_u32_e32 vcc, 5, v63\n" \
    "s_cbranch_vccz Lbe5_%=\n" \
    "s_sub_u32 s90, s90, 1\n" \
    "s_cbranch_scc0 Lbl5_%=\n" \
    "s_branch Lbe5_%=\n" \
    "Lbs6_%=:\n" \
    "s_mov_b32 s90, 0x100000\n" \
    "Lbl6_%=:\n" \
    "ds_read_b32 v63, v34 offset:0\n" \
    "s_waitcnt lgkmcnt(0)\n" \
    "v_cmp_gt_u32_e32 vcc, 6, v63\n" \
    "s_cbranch_vccz Lbe6_%=\n" \
    "s_sub_u32 s90, s90, 1\n" \
    "s_cbranch_scc0 Lbl6_%=\n" \
    "s_branch Lbe6_%=\n" \
    "Lbx_%=:\n"
#define CONV_B_CLOB "v0", "v1", "v2", "v3", "v4", "v5", "v6", "v7", "v8", "v9", "v10", "v11", "v12", "v13", "v14", "v15", "v16", "v17", "v18", "v19", "v20", "v21", "v22", "v23", "v24", "v25", "v26", "v27", "v28", "v29", "v30", "v31", "v32", "v33", "v34", "v35", "v36", "v37", "v38", "v39", "v40", "v41", "v42", "v43", "v44", "v45", "v46", "v47", "v48", "v49", "v50", "v51", "v52", "v53", "v54", "v55", "v56", "v57", "v58", "v59", "v60", "v61", "v62", "v63", "v64", "v65", "v66", "v67", "v68", "v69", "v70", "v71", "v72", "v73", "v74", "v75", "v76", "v77", "v78", "v79", "v80", "v81", "v82", "v83", "v84", "v85", "v86", "v87", "v88", "v89", "v90", "v91", "v92", "v93", "v94", "v95", "v96", "v97", "v98", "v99", "v100", "v101", "v102", "v103", "v104", "v105", "v106", "v107", "v110", "v111", "v112", "v113", "v114", "v115", "v116", "v117", "v118", "v119", "v120", "v121", "v122", "v123", "v124", "v125", "v126", "v127", "v128", "v129", "v130", "v131", "v132", "v133", "v134", "v135", "v136", "v137", "v138", "v139", "v140", "v141", "v142", "v143", "v144", "v145", "v146", "v147", "v148", "v149", "v150", "v151", "v152", "v153", "v154", "v155", "v156", "v157", "v158", "v159", "v160", "v161", "v162", "v163", "v164", "v165", "v166", "v167"
#define CONV_C_ASM \
    "s_setprio 3\n" \
    "v_mbcnt_lo_u32_b32 v62, -1, 0\n" \
    "v_mbcnt_hi_u32_b32 v62, -1, v62\n" \
    "v_and_b32_e32 v57, 1, v62\n" \
    "v_min_u32_e32 v62, %[pl], v62\n" \
    "v_lshrrev_b32_e32 v62, %[ps], v62\n" \
    "v_mul_u32_u24_e32 v57, %[pd], v57\n" \
    "v_mul_u32_u24_e32 v62, 1152, v62\n" \
    "v_add_u32_e32 v62, v62, v57\n" \
    "global_load_dword v63, v62, %[xb]\n" \
    "s_mov_b32 s91, 0x05040100\n" \
    "s_mov_b32 s92, 0x07060302\n" \
    "s_mov_b64 s[96:97], %[o0]\n" \
    "s_mov_b64 s[94:95], %[x0]\n" \
    "v_mov_b64 v[160:161], 0\n" \
    "v_mov_b64 v[162:163], 0\n" \
    "s_waitcnt vmcnt(6)\n" \
    "s_mov_b32 s93, m0\n" \
    "v_lshrrev_b32_e32 v0, 3, v48\n" \
    "s_mov_b32 m0, %[bl]\n" \
    "s_sub_u32 s88, %[bl], s93\n" \
    "s_sub_u32 s88, s88, s93\n" \
    "global_load_lds_dwordx4 v0, %[bs]\n" \
    "s_add_u32 m0, s88, 4096\n" \
    "s_lshl_b32 s88, s93, 1\n" \
    "s_sub_u32 s88, 4096, s88\n" \
    "v_add_u32_e32 v0, s88, v0\n" \
    "global_load_lds_dwordx4 v0, %[bs]\n" \
    "s_mov_b32 m0, s93\n" \
    "ds_write_b128 v49, v[160:163]\n" \
    "ds_write_b128 v50, v[160:163]\n" \
    "s_waitcnt vmcnt(3)\n" \
    "v_add_u32_e32 v56, 91520, v33\n" \
    "s_mov_b64 exec, 1\n" \
    "v_mov_b32_e32 v160, v71\n" \
    "s_mov_b64 exec, -1\n" \
    "v_readfirstlane_b32 %[ep], v72\n" \
    "s_nop 2\n" \
    "s_add_u32 s98, %[ep], 1\n" \
    "s_max_u32 s98, s98, 1\n" \
    "s_mov_b32 s99, %[tg]\n" \
    "global_load_dwordx2 v[12:13], v41, s[94:95] offset:0\n" \
    "s_mov_b32 s90, 0x100000\n" \
    "Lcl0_%=:\n" \
    "ds_read_b32 v60, v32 offset:0\n" \
    "s_waitcnt lgkmcnt(0)\n" \
    "v_cmp_gt_u32_e32 vcc, 1, v60\n" \
    "s_cbranch_vccz Lce0_%=\n" \
    "s_sleep 2\n" \
    "s_sub_u32 s90, s90, 1\n" \
    "s_cbranch_scc0 Lcl0_%=\n" \
    "Lce0_%=:\n" \
    "ds_read_b128 v[96:99], v33 offset:0\n" \
    "ds_read_b128 v[100:103], v33 offset:64\n" \
    "ds_read_b128 v[104:107], v33 offset:128\n" \
    "ds_read_b128 v[108:111], v33 offset:192\n" \
    "ds_read_b128 v[112:115], v33 offset:256\n" \
    "ds_read_b128 v[116:119], v33 offset:320\n" \
    "ds_read_b128 v[120:123], v33 offset:384\n" \
    "ds_read_b128 v[124:127], v33 offset:448\n" \
    "global_load_dwordx2 v[14:15], v41, s[94:95] offset:1152\n" \
    "s_add_u32 s94, s94, 589824\n" \
    "s_addc_u32 s95, s95, 0\n" \
    "global_load_dwordx2 v[16:17], v41, s[94:95] offset:0\n" \
    "global_load_dwordx2 v[18:19], v41, s[94:95] offset:1152\n" \
    "s_add_u32 s94, s94, 589824\n" \
    "s_addc_u32 s95, s95, 0\n" \
    "s_mov_b32 s90, 0x100000\n" \
    "Lcl1_%=:\n" \
    "ds_read_b32 v60, v32 offset:0\n" \
    "s_waitcnt lgkmcnt(0)\n" \
    "v_cmp_gt_u32_e32 vcc, 2, v60\n" \
    "s_cbranch_vccz Lce1_%=\n" \
    "s_sleep 1\n" \
    "s_sub_u32 s90, s90, 1\n" \
    "s_cbranch_scc0 Lcl1_%=\n" \
    "Lce1_%=:\n" \
    "ds_read_b128 v[128:131], v33 offset:58240\n" \
    "ds_read_b128 v[132:135], v33 offset:58304\n" \
    "ds_read_b128 v[136:139], v33 offset:58368\n" \
    "ds_read_b128 v[140:143], v33 offset:58432\n" \
    "ds_read_b128 v[144:147], v33 offset:58496\n" \
    "ds_read_b128 v[148:151], v33 offset:58560\n" \
    "ds_read_b128 v[152:155], v33 offset:58624\n" \
    "ds_read_b128 v[156:159], v33 offset:58688\n" \
    "s_waitcnt lgkmcnt(6)\n" \
    "v_mfma_f32_16x16x32_bf16 v[160:163], v[128:131], v[96:99], v[160:163]\n" \
    "v_mfma_f32_16x16x32_bf16 v[160:163], v[132:135], v[100:103], v[160:163]\n" \
    "s_waitcnt lgkmcnt(4)\n" \
    "v_mfma_f32_16x16x32_bf16 v[160:163], v[136:139], v[104:107], v[160:163]\n" \
    "v_mfma_f32_16x16x32_bf16 v[160:163], v[140:143], v[108:111], v[160:163]\n" \
    "s_waitcnt lgkmcnt(2)\n" \
    "v_mfma_f32_16x16x32_bf16 v[160:163], v[144:147], v[112:115], v[160:163]\n" \
    "v_mfma_f32_16x16x32_bf16 v[160:163], v[148:151], v[116:119], v[160:163]\n" \
    "s_waitcnt lgkmcnt(0)\n" \
    "v_mfma_f32_16x16x32_bf16 v[160:163], v[152:155], v[120:123], v[160:163]\n" \
    "v_mfma_f32_16x16x32_bf16 v[160:163], v[156:159], v[124:127], v[160:163]\n" \
    "s_nop 7\n" \
    "v_cvt_pk_bf16_f32 v0, v163, v162\n" \
    "v_cvt_pk_bf16_f32 v1, v161, v160\n" \
    "v_cvt_pk_bf16_f32 v2, v162, v161\n" \
    "ds_write_b16 v35, v0\n" \
    "ds_write_b32 v35, v2 offset:2\n" \
    "ds_write_b16_d16_hi v35, v1 offset:6\n" \
    "ds_write_b32 v35, v0 offset:1206\n" \
    "ds_write_b32 v35, v1 offset:1210\n" \
    "v_readlane_b32 s93, v0, 63\n" \
    "v_mov_b32_e32 v61, 1\n" \
    "ds_write_addtid_b32 v61 offset:56160\n" \
    "global_load_dwordx2 v[20:21], v41, s[94:95] offset:0\n" \
    "global_load_dwordx2 v[22:23], v41, s[94:95] offset:1152\n" \
    "s_add_u32 s94, s94, 589824\n" \
    "s_addc_u32 s95, s95, 0\n" \
    "s_mov_b32 s90, 0x100000\n" \
    "Lcl2_%=:\n" \
    "ds_read_b32 v60, v32 offset:0\n" \
    "s_waitcnt lgkmcnt(0)\n" \
    "v_cmp_gt_u32_e32 vcc, 3, v60\n" \
    "s_cbranch_vccz Lce2_%=\n" \
    "s_sleep 1\n" \
    "s_sub_u32 s90, s90, 1\n" \
    "s_cbranch_scc0 Lcl2_%=\n" \
    "Lce2_%=:\n" \
    "ds_read_b128 v[128:131], v34 offset:0\n" \
    "ds_read_b128 v[132:135], v34 offset:64\n" \
    "ds_read_b128 v[136:139], v34 offset:128\n" \
    "ds_read_b128 v[140:143], v34 offset:192\n" \
    "ds_read_b128 v[144:147], v34 offset:256\n" \
    "ds_read_b128 v[148:151], v34 offset:320\n" \
    "ds_read_b128 v[152:155], v34 offset:384\n" \
    "ds_read_b128 v[156:159], v34 offset:448\n" \
    "s_waitcnt lgkmcnt(6)\n" \
    "v_mfma_f32_16x16x32_bf16 v[160:163], v[128:131], v[96:99], 0\n" \
    "v_mfma_f32_16x16x32_bf16 v[160:163], v[132:135], v[100:103], v[160:163]\n" \
    "s_waitcnt lgkmcnt(4)\n" \
    "v_mfma_f32_16x16x32_bf16 v[160:163], v[136:139], v[104:107], v[160:163]\n" \
    "v_mfma_f32_16x16x32_bf16 v[160:163], v[140:143], v[108:111], v[160:163]\n" \
    "s_waitcnt lgkmcnt(2)\n" \
    "v_mfma_f32_16x16x32_bf16 v[160:163], v[144:147], v[112:115], v[160:163]\n" \
    "v_mfma_f32_16x16x32_bf16 v[160:163], v[148:151], v[116:119], v[160:163]\n" \
    "s_waitcnt lgkmcnt(0)\n" \
    "v_mfma_f32_16x16x32_bf16 v[160:163], v[152:155], v[120:123], v[160:163]\n" \
    "v_mfma_f32_16x16x32_bf16 v[160:163], v[156:159], v[124:127], v[160:163]\n" \
    "ds_read_b32 v60, v32\n" \
    "s_nop 6\n" \
    "v_cvt_pk_bf16_f32 v1, v160, v161\n" \
    "v_cvt_pk_bf16_f32 v2, v162, v163\n" \
    "v_perm_b32 v6, v1, v1, v38\n" \
    "v_perm_b32 v7, s93, v1, v40\n" \
    "v_perm_b32 v4, v2, v2, v38\n" \
    "v_perm_b32 v5, v2, v2, v39\n" \
    "ds_write_b128 v36, v[4:7]\n" \
    "s_mov_b64 exec, 1\n" \
    "ds_write_b16 v51, v1 offset:1792\n" \
    "ds_write_b16 v51, v1 offset:1794\n" \
    "s_brev_b64 exec, 1\n" \
    "ds_write2_b32 v51, v4, v4 offset0:18 offset1:19\n" \
    "ds_write_b16 v51, v4 offset:1278\n" \
    "s_mov_b64 exec, -1\n" \
    "v_mov_b32_e32 v61, 2\n" \
    "ds_write_addtid_b32 v61 offset:56160\n" \
    "global_load_dwordx2 v[24:25], v41, s[94:95] offset:0\n" \
    "global_load_dwordx2 v[26:27], v41, s[94:95] offset:1152\n" \
    "v_mov_b32_e32 v61, 2\n" \
    "s_waitcnt vmcnt(6)\n" \
    "v_perm_b32 v8, v14, v12, s91\n" \
    "v_perm_b32 v9, v14, v12, s92\n" \
    "v_perm_b32 v10, v15, v13, s91\n" \
    "v_perm_b32 v11, v15, v13, s92\n" \
    "ds_write2st64_b32 v42, v8, v9 offset0:0 offset1:18\n" \
    "ds_write2st64_b32 v42, v10, v11 offset0:36 offset1:54\n" \
    "ds_write_addtid_b32 v61 offset:56416\n" \
    "v_mov_b32_e32 v61, 4\n" \
    "s_waitcnt vmcnt(4)\n" \
    "v_perm_b32 v8, v18, v16, s91\n" \
    "v_perm_b32 v9, v18, v16, s92\n" \
    "v_perm_b32 v10, v19, v17, s91\n" \
    "v_perm_b32 v11, v19, v17, s92\n" \
    "ds_write2st64_b32 v42, v8, v9 offset0:4 offset1:22\n" \
    "ds_write2st64_b32 v42, v10, v11 offset0:40 offset1:58\n" \
    "ds_write_addtid_b32 v61 offset:56416\n" \
    "s_waitcnt lgkmcnt(12)\n" \
    "v_cmp_gt_u32_e32 vcc, 4, v60\n" \
    "s_cbranch_vccz Lce3_%=\n" \
    "s_mov_b32 s90, 0x100000\n" \
    "Lcl3_%=:\n" \
    "s_sleep 1\n" \
    "ds_read_b32 v60, v32 offset:0\n" \
    "s_waitcnt lgkmcnt(0)\n" \
    "v_cmp_gt_u32_e32 vcc, 4, v60\n" \
    "s_cbranch_vccz Lce3_%=\n" \
    "s_sub_u32 s90, s90, 1\n" \
    "s_cbranch_scc0 Lcl3_%=\n" \
    "Lce3_%=:\n" \
    "ds_read_b128 v[128:131], v56 offset:0\n" \
    "ds_read_b128 v[132:135], v56 offset:64\n" \
    "ds_read_b128 v[136:139], v56 offset:128\n" \
    "ds_read_b128 v[140:143], v56 offset:192\n" \
    "ds_read_b128 v[144:147], v56 offset:256\n" \
    "ds_read_b128 v[148:151], v56 offset:320\n" \
    "ds_read_b128 v[152:155], v56 offset:384\n" \
    "ds_read_b128 v[156:159], v56 offset:448\n" \
    "s_waitcnt lgkmcnt(6)\n" \
    "v_mfma_f32_16x16x32_bf16 v[160:163], v[128:131], v[96:99], 0\n" \
    "v_mfma_f32_16x16x32_bf16 v[160:163], v[132:135], v[100:103], v[160:163]\n" \
    "s_waitcnt lgkmcnt(4)\n" \
    "v_mfma_f32_16x16x32_bf16 v[160:163], v[136:139], v[104:107], v[160:163]\n" \
    "v_mfma_f32_16x16x32_bf16 v[160:163], v[140:143], v[108:111], v[160:163]\n" \
    "s_waitcnt lgkmcnt(2)\n" \
    "v_mfma_f32_16x16x32_bf16 v[160:163], v[144:147], v[112:115], v[160:163]\n" \
    "v_mfma_f32_16x16x32_bf16 v[160:163], v[148:151], v[116:119], v[160:163]\n" \
    "s_waitcnt lgkmcnt(0)\n" \
    "v_mfma_f32_16x16x32_bf16 v[160:163], v[152:155], v[120:123], v[160:163]\n" \
    "v_mfma_f32_16x16x32_bf16 v[160:163], v[156:159], v[124:127], v[160:163]\n" \
    "s_nop 7\n" \
    "v_cvt_pk_bf16_f32 v0, v160, v161\n" \
    "v_cvt_pk_bf16_f32 v1, v162, v163\n" \
    "ds_write_b16 v37, v0 offset:96\n" \
    "ds_write_b16_d16_hi v37, v0 offset:64\n" \
    "ds_write_b16 v37, v1 offset:32\n" \
    "ds_write_b16_d16_hi v37, v1\n" \
    "v_mov_b32_e32 v61, 3\n" \
    "ds_write_addtid_b32 v61 offset:56160\n" \
    "global_load_dwordx4 v[80:83], v44, %[ws] sc1\n" \
    "v_add_u32_e32 v44, 0x1000, v44\n" \
    "global_load_dwordx4 v[84:87], v44, %[ws] sc1\n" \
    "v_add_u32_e32 v44, 0x1000, v44\n" \
    "global_load_dwordx4 v[88:91], v44, %[ws] sc1\n" \
    "v_add_u32_e32 v44, 0x1000, v44\n" \
    "global_load_dwordx4 v[92:95], v44, %[ws] sc1\n" \
    "v_add_u32_e32 v44, 0xffffd000, v44\n" \
    "v_mov_b32_e32 v54, %[tr]\n" \
    "global_load_dwordx2 v[54:55], v54, %[ws] sc1\n" \
    "v_mov_b32_e32 v61, 6\n" \
    "s_waitcnt vmcnt(7)\n" \
    "v_perm_b32 v8, v22, v20, s91\n" \
    "v_perm_b32 v9, v22, v20, s92\n" \
    "v_perm_b32 v10, v23, v21, s91\n" \
    "v_perm_b32 v11, v23, v21, s92\n" \
    "ds_write2st64_b32 v42, v8, v9 offset0:8 offset1:26\n" \
    "ds_write2st64_b32 v42, v10, v11 offset0:44 offset1:62\n" \
    "ds_write_addtid_b32 v61 offset:56416\n" \
    "v_mov_b32_e32 v61, 8\n" \
    "s_waitcnt vmcnt(5)\n" \
    "v_perm_b32 v8, v26, v24, s91\n" \
    "v_perm_b32 v9, v26, v24, s92\n" \
    "v_perm_b32 v10, v27, v25, s91\n" \
    "v_perm_b32 v11, v27, v25, s92\n" \
    "ds_write2st64_b32 v42, v8, v9 offset0:12 offset1:30\n" \
    "ds_write2st64_b32 v42, v10, v11 offset0:48 offset1:66\n" \
    "ds_write_addtid_b32 v61 offset:56416\n" \
    "s_mov_b32 s90, 0x100000\n" \
    "Lcl4_%=:\n" \
    "ds_read_b32 v60, v45 offset:2336\n" \
    "s_waitcnt lgkmcnt(0)\n" \
    "v_cmp_gt_u32_e32 vcc, 2, v60\n" \
    "s_cbranch_vccz Lce4_%=\n" \
    "s_sleep 1\n" \
    "s_sub_u32 s90, s90, 1\n" \
    "s_cbranch_scc0 Lcl4_%=\n" \
    "Lce4_%=:\n" \
    "v_mov_b32_e32 v0, %[ak]\n" \
    "v_mov_b32_e32 v1, s98\n" \
    "s_mov_b32 exec_lo, %[am]\n" \
    "s_mov_b32 exec_hi, 0\n" \
    "global_store_dword v0, v1, %[ws] sc1\n" \
    "s_mov_b64 exec, -1\n" \
    "v_min_u32_e32 v57, %[wl], v48\n" \
    "global_load_dword v63, v57, %[wd]\n" \
    "v_add_u32_e32 v57, 0x2000, v48\n" \
    "v_min_u32_e32 v57, %[wl], v57\n" \
    "global_load_dword v63, v57, %[wd]\n" \
    "s_mov_b32 s93, 0xffff0000\n" \
    "v_add_u32_e32 v58, 58240, v43\n" \
    "s_waitcnt vmcnt(7)\n" \
    "v_cmp_ne_u32_e32 vcc, s98, v81\n" \
    "v_cmp_ne_u32_e64 s[88:89], s98, v83\n" \
    "s_or_b64 vcc, vcc, s[88:89]\n" \
    "s_cbranch_scc1 Lcs5_%=\n" \
    "Lce5_%=:\n" \
    "v_cvt_f32_f16_e32 v104, v80\n" \
    "v_cvt_f32_f16_e32 v105, v82\n" \
    "v_lshlrev_b32_e32 v96, 16, v12\n" \
    "v_and_b32_e32 v97, s93, v12\n" \
    "v_lshlrev_b32_e32 v98, 16, v13\n" \
    "v_and_b32_e32 v99, s93, v13\n" \
    "v_lshlrev_b32_e32 v100, 16, v14\n" \
    "v_and_b32_e32 v101, s93, v14\n" \
    "v_lshlrev_b32_e32 v102, 16, v15\n" \
    "v_and_b32_e32 v103, s93, v15\n" \
    "v_sub_f32_e32 v106, 1.0, v104\n" \
    "v_sub_f32_e32 v107, 1.0, v105\n" \
    "v_mul_f32_e32 v96, v106, v96\n" \
    "v_mul_f32_e32 v97, v106, v97\n" \
    "v_mul_f32_e32 v98, v106, v98\n" \
    "v_mul_f32_e32 v99, v106, v99\n" \
    "v_mul_f32_e32 v100, v107, v100\n" \
    "v_mul_f32_e32 v101, v107, v101\n" \
    "v_mul_f32_e32 v102, v107, v102\n" \
    "v_mul_f32_e32 v103, v107, v103\n" \
    "s_mov_b32 s90, 0x100000\n" \
    "Lcl6_%=:\n" \
    "ds_read_b32 v60, v45 offset:0\n" \
    "ds_read_b64 v[64:65], v43 offset:0\n" \
    "ds_read_b64 v[66:67], v43 offset:512\n" \
    "ds_read_b64 v[68:69], v43 offset:1024\n" \
    "ds_read_b64 v[70:71], v43 offset:1536\n" \
    "s_waitcnt lgkmcnt(0)\n" \
    "v_cmp_gt_u32_e32 vcc, s99, v60\n" \
    "s_cbranch_vccz Lce6_%=\n" \
    "s_sleep 2\n" \
    "s_sub_u32 s90, s90, 1\n" \
    "s_cbranch_scc0 Lcl6_%=\n" \
    "Lce6_%=:\n" \
    "v_fmac_f32_e32 v96, v104, v64\n" \
    "v_fmac_f32_e32 v97, v104, v66\n" \
    "v_fmac_f32_e32 v98, v104, v68\n" \
    "v_fmac_f32_e32 v99, v104, v70\n" \
    "v_cvt_pk_bf16_f32 v108, v96, v97\n" \
    "v_cvt_pk_bf16_f32 v109, v98, v99\n" \
    "global_store_dwordx2 v41, v[108:109], s[96:97] offset:0\n" \
    "v_fmac_f32_e32 v100, v105, v65\n" \
    "v_fmac_f32_e32 v101, v105, v67\n" \
    "v_fmac_f32_e32 v102, v105, v69\n" \
    "v_fmac_f32_e32 v103, v105, v71\n" \
    "v_cvt_pk_bf16_f32 v110, v100, v101\n" \
    "v_cvt_pk_bf16_f32 v111, v102, v103\n" \
    "global_store_dwordx2 v41, v[110:111], s[96:97] offset:1152\n" \
    "s_add_u32 s96, s96, 589824\n" \
    "s_addc_u32 s97, s97, 0\n" \
    "s_add_u32 s99, s99, 2\n" \
    "v_lshrrev_b32_e32 v1, 1, v44\n" \
    "v_add_u32_e32 v1, %[gt], v1\n" \
    "ds_write2_b32 v1, v80, v82 offset0:0 offset1:1\n" \
    "s_waitcnt vmcnt(8)\n" \
    "v_cmp_ne_u32_e32 vcc, s98, v85\n" \
    "v_cmp_ne_u32_e64 s[88:89], s98, v87\n" \
    "s_or_b64 vcc, vcc, s[88:89]\n" \
    "s_cbranch_scc1 Lcs7_%=\n" \
    "Lce7_%=:\n" \
    "v_add_u32_e32 v1, 0x800, v1\n" \
    "ds_write2_b32 v1, v84, v86 offset0:0 offset1:1\n" \
    "s_waitcnt vmcnt(7)\n" \
    "v_cmp_ne_u32_e32 vcc, s98, v89\n" \
    "v_cmp_ne_u32_e64 s[88:89], s98, v91\n" \
    "s_or_b64 vcc, vcc, s[88:89]\n" \
    "s_cbranch_scc1 Lcs8_%=\n" \
    "Lce8_%=:\n" \
    "v_add_u32_e32 v1, 0x800, v1\n" \
    "ds_write2_b32 v1, v88, v90 offset0:0 offset1:1\n" \
    "s_waitcnt vmcnt(5)\n" \
    "v_cmp_ne_u32_e32 vcc, s98, v93\n" \
    "v_cmp_ne_u32_e64 s[88:89], s98, v95\n" \
    "s_or_b64 vcc, vcc, s[88:89]\n" \
    "s_cbranch_scc1 Lcs9_%=\n" \
    "Lce9_%=:\n" \
    "v_add_u32_e32 v1, 0x800, v1\n" \
    "ds_write2_b32 v1, v92, v94 offset0:0 offset1:1\n" \
    "s_memrealtime s[88:89]\n" \
    "v_cmp_eq_u32_e32 vcc, s98, v54\n" \
    "v_mov_b32_e32 v1, %[kt]\n" \
    "s_waitcnt lgkmcnt(0)\n" \
    "s_sub_u32 s88, s88, 390\n" \
    "v_mov_b32_e32 v2, s88\n" \
    "v_cndmask_b32_e32 v2, v2, v55, vcc\n" \
    "ds_write_b32 v1, v2\n" \
    "v_cvt_f32_f16_e32 v104, v84\n" \
    "v_cvt_f32_f16_e32 v105, v86\n" \
    "v_lshlrev_b32_e32 v96, 16, v16\n" \
    "v_and_b32_e32 v97, s93, v16\n" \
    "v_lshlrev_b32_e32 v98, 16, v17\n" \
    "v_and_b32_e32 v99, s93, v17\n" \
    "v_lshlrev_b32_e32 v100, 16, v18\n" \
    "v_and_b32_e32 v101, s93, v18\n" \
    "v_lshlrev_b32_e32 v102, 16, v19\n" \
    "v_and_b32_e32 v103, s93, v19\n" \
    "v_sub_f32_e32 v106, 1.0, v104\n" \
    "v_sub_f32_e32 v107, 1.0, v105\n" \
    "v_mul_f32_e32 v96, v106, v96\n" \
    "v_mul_f32_e32 v97, v106, v97\n" \
    "v_mul_f32_e32 v98, v106, v98\n" \
    "v_mul_f32_e32 v99, v106, v99\n" \
    "v_mul_f32_e32 v100, v107, v100\n" \
    "v_mul_f32_e32 v101, v107, v101\n" \
    "v_mul_f32_e32 v102, v107, v102\n" \
    "v_mul_f32_e32 v103, v107, v103\n" \
    "s_mov_b32 s90, 0x100000\n" \
    "Lcl10_%=:\n" \
    "ds_read_b32 v60, v47 offset:0\n" \
    "ds_read_b64 v[64:65], v43 offset:8320\n" \
    "ds_read_b64 v[66:67], v43 offset:8832\n" \
    "ds_read_b64 v[68:69], v43 offset:9344\n" \
    "ds_read_b64 v[70:71], v43 offset:9856\n" \
    "ds_read_b64 v[72:73], v58 offset:0\n" \
    "ds_read_b64 v[74:75], v58 offset:512\n" \
    "ds_read_b64 v[76:77], v58 offset:1024\n" \
    "ds_read_b64 v[78:79], v58 offset:1536\n" \
    "s_waitcnt lgkmcnt(0)\n" \
    "v_cmp_gt_u32_e32 vcc, s99, v60\n" \
    "s_cbranch_vccz Lce10_%=\n" \
    "s_sleep 2\n" \
    "s_sub_u32 s90, s90, 1\n" \
    "s_cbranch_scc0 Lcl10_%=\n" \
    "Lce10_%=:\n" \
    "v_fmac_f32_e32 v96, v104, v64\n" \
    "v_fmac_f32_e32 v97, v104, v66\n" \
    "v_fmac_f32_e32 v98, v104, v68\n" \
    "v_fmac_f32_e32 v99, v104, v70\n" \
    "v_fmac_f32_e32 v96, v104, v72\n" \
    "v_fmac_f32_e32 v97, v104, v74\n" \
    "v_fmac_f32_e32 v98, v104, v76\n" \
    "v_fmac_f32_e32 v99, v104, v78\n" \
    "v_cvt_pk_bf16_f32 v112, v96, v97\n" \
    "v_cvt_pk_bf16_f32 v113, v98, v99\n" \
    "global_store_dwordx2 v41, v[112:113], s[96:97] offset:0\n" \
    "v_fmac_f32_e32 v100, v105, v65\n" \
    "v_fmac_f32_e32 v101, v105, v67\n" \
    "v_fmac_f32_e32 v102, v105, v69\n" \
    "v_fmac_f32_e32 v103, v105, v71\n" \
    "v_fmac_f32_e32 v100, v105, v73\n" \
    "v_fmac_f32_e32 v101, v105, v75\n" \
    "v_fmac_f32_e32 v102, v105, v77\n" \
    "v_fmac_f32_e32 v103, v105, v79\n" \
    "v_cvt_pk_bf16_f32 v114, v100, v101\n" \
    "v_cvt_pk_bf16_f32 v115, v102, v103\n" \
    "global_store_dwordx2 v41, v[114:115], s[96:97] offset:1152\n" \
    "s_add_u32 s96, s96, 589824\n" \
    "s_addc_u32 s97, s97, 0\n" \
    "s_add_u32 s99, s99, 2\n" \
    "v_lshrrev_b32_e32 v120, 3, v44\n" \
    "v_sub_u32_e32 v120, %[ko], v120\n" \
    "v_and_or_b32 v124, v80, s93, v120\n" \
    "v_add_u32_e32 v120, -1, v120\n" \
    "v_and_or_b32 v121, v82, s93, v120\n" \
    "v_min_u32_e32 v125, v124, v121\n" \
    "v_max_u32_e32 v124, v124, v121\n" \
    "v_add_u32_e32 v120, 0xfffffe01, v120\n" \
    "v_and_or_b32 v121, v84, s93, v120\n" \
    "v_min3_u32 v126, v124, v125, v121\n" \
    "v_med3_u32 v125, v124, v125, v121\n" \
    "v_max_u32_e32 v124, v124, v121\n" \
    "v_add_u32_e32 v120, -1, v120\n" \
    "v_and_or_b32 v121, v86, s93, v120\n" \
    "v_med3_u32 v126, v125, v126, v121\n" \
    "v_med3_u32 v125, v124, v125, v121\n" \
    "v_max_u32_e32 v124, v124, v121\n" \
    "v_add_u32_e32 v120, 0xfffffe01, v120\n" \
    "v_and_or_b32 v121, v88, s93, v120\n" \
    "v_med3_u32 v126, v125, v126, v121\n" \
    "v_med3_u32 v125, v124, v125, v121\n" \
    "v_max_u32_e32 v124, v124, v121\n" \
    "v_add_u32_e32 v120, -1, v120\n" \
    "v_and_or_b32 v121, v90, s93, v120\n" \
    "v_med3_u32 v126, v125, v126, v121\n" \
    "v_med3_u32 v125, v124, v125, v121\n" \
    "v_max_u32_e32 v124, v124, v121\n" \
    "v_add_u32_e32 v120, 0xfffffe01, v120\n" \
    "v_and_or_b32 v121, v92, s93, v120\n" \
    "v_med3_u32 v126, v125, v126, v121\n" \
    "v_med3_u32 v125, v124, v125, v121\n" \
    "v_max_u32_e32 v124, v124, v121\n" \
    "v_add_u32_e32 v120, -1, v120\n" \
    "v_and_or_b32 v121, v94, s93, v120\n" \
    "v_med3_u32 v126, v125, v126, v121\n" \
    "v_med3_u32 v125, v124, v125, v121\n" \
    "v_max_u32_e32 v124, v124, v121\n" \
    "v_add_u32_e32 v120, %[kt2], v44\n" \
    "ds_write_b128 v120, v[124:127]\n" \
    "v_mov_b32_e32 v61, 10\n" \
    "ds_write_addtid_b32 v61 offset:56416\n" \
    "v_cvt_f32_f16_e32 v104, v88\n" \
    "v_cvt_f32_f16_e32 v105, v90\n" \
    "v_lshlrev_b32_e32 v96, 16, v20\n" \
    "v_and_b32_e32 v97, s93, v20\n" \
    "v_lshlrev_b32_e32 v98, 16, v21\n" \
    "v_and_b32_e32 v99, s93, v21\n" \
    "v_lshlrev_b32_e32 v100, 16, v22\n" \
    "v_and_b32_e32 v101, s93, v22\n" \
    "v_lshlrev_b32_e32 v102, 16, v23\n" \
    "v_and_b32_e32 v103, s93, v23\n" \
    "v_sub_f32_e32 v106, 1.0, v104\n" \
    "v_sub_f32_e32 v107, 1.0, v105\n" \
    "v_mul_f32_e32 v96, v106, v96\n" \
    "v_mul_f32_e32 v97, v106, v97\n" \
    "v_mul_f32_e32 v98, v106, v98\n" \
    "v_mul_f32_e32 v99, v106, v99\n" \
    "v_mul_f32_e32 v100, v107, v100\n" \
    "v_mul_f32_e32 v101, v107, v101\n" \
    "v_mul_f32_e32 v102, v107, v102\n" \
    "v_mul_f32_e32 v103, v107, v103\n" \
    "s_mov_b32 s90, 0x100000\n" \
    "Lcl11_%=:\n" \
    "ds_read_b32 v60, v47 offset:0\n" \
    "ds_read_b64 v[64:65], v43 offset:16640\n" \
    "ds_read_b64 v[66:67], v43 offset:17152\n" \
    "ds_read_b64 v[68:69], v43 offset:17664\n" \
    "ds_read_b64 v[70:71], v43 offset:18176\n" \
    "ds_read_b64 v[72:73], v58 offset:8320\n" \
    "ds_read_b64 v[74:75], v58 offset:8832\n" \
    "ds_read_b64 v[76:77], v58 offset:9344\n" \
    "ds_read_b64 v[78:79], v58 offset:9856\n" \
    "s_waitcnt lgkmcnt(0)\n" \
    "v_cmp_gt_u32_e32 vcc, s99, v60\n" \
    "s_cbranch_vccz Lce11_%=\n" \
    "s_sleep 2\n" \
    "s_sub_u32 s90, s90, 1\n" \
    "s_cbranch_scc0 Lcl11_%=\n" \
    "Lce11_%=:\n" \
    "v_fmac_f32_e32 v96, v104, v64\n" \
    "v_fmac_f32_e32 v97, v104, v66\n" \
    "v_fmac_f32_e32 v98, v104, v68\n" \
    "v_fmac_f32_e32 v99, v104, v70\n" \
    "v_fmac_f32_e32 v96, v104, v72\n" \
    "v_fmac_f32_e32 v97, v104, v74\n" \
    "v_fmac_f32_e32 v98, v104, v76\n" \
    "v_fmac_f32_e32 v99, v104, v78\n" \
    "v_cvt_pk_bf16_f32 v108, v96, v97\n" \
    "v_cvt_pk_bf16_f32 v109, v98, v99\n" \
    "global_store_dwordx2 v41, v[108:109], s[96:97] offset:0\n" \
    "v_fmac_f32_e32 v100, v105, v65\n" \
    "v_fmac_f32_e32 v101, v105, v67\n" \
    "v_fmac_f32_e32 v102, v105, v69\n" \
    "v_fmac_f32_e32 v103, v105, v71\n" \
    "v_fmac_f32_e32 v100, v105, v73\n" \
    "v_fmac_f32_e32 v101, v105, v75\n" \
    "v_fmac_f32_e32 v102, v105, v77\n" \
    "v_fmac_f32_e32 v103, v105, v79\n" \
    "v_cvt_pk_bf16_f32 v110, v100, v101\n" \
    "v_cvt_pk_bf16_f32 v111, v102, v103\n" \
    "global_store_dwordx2 v41, v[110:111], s[96:97] offset:1152\n" \
    "s_add_u32 s96, s96, 589824\n" \
    "s_addc_u32 s97, s97, 0\n" \
    "s_add_u32 s99, s99, 2\n" \
    "v_cvt_f32_f16_e32 v104, v92\n" \
    "v_cvt_f32_f16_e32 v105, v94\n" \
    "v_lshlrev_b32_e32 v96, 16, v24\n" \
    "v_and_b32_e32 v97, s93, v24\n" \
    "v_lshlrev_b32_e32 v98, 16, v25\n" \
    "v_and_b32_e32 v99, s93, v25\n" \
    "v_lshlrev_b32_e32 v100, 16, v26\n" \
    "v_and_b32_e32 v101, s93, v26\n" \
    "v_lshlrev_b32_e32 v102, 16, v27\n" \
    "v_and_b32_e32 v103, s93, v27\n" \
    "v_sub_f32_e32 v106, 1.0, v104\n" \
    "v_sub_f32_e32 v107, 1.0, v105\n" \
    "v_mul_f32_e32 v96, v106, v96\n" \
    "v_mul_f32_e32 v97, v106, v97\n" \
    "v_mul_f32_e32 v98, v106, v98\n" \
    "v_mul_f32_e32 v99, v106, v99\n" \
    "v_mul_f32_e32 v100, v107, v100\n" \
    "v_mul_f32_e32 v101, v107, v101\n" \
    "v_mul_f32_e32 v102, v107, v102\n" \
    "v_mul_f32_e32 v103, v107, v103\n" \
    "s_mov_b32 s90, 0x100000\n" \
    "Lcl12_%=:\n" \
    "ds_read_b32 v60, v47 offset:0\n" \
    "ds_read_b64 v[64:65], v43 offset:24960\n" \
    "ds_read_b64 v[66:67], v43 offset:25472\n" \
    "ds_read_b64 v[68:69], v43 offset:25984\n" \
    "ds_read_b64 v[70:71], v43 offset:26496\n" \
    "ds_read_b64 v[72:73], v58 offset:16640\n" \
    "ds_read_b64 v[74:75], v58 offset:17152\n" \
    "ds_read_b64 v[76:77], v58 offset:17664\n" \
    "ds_read_b64 v[78:79], v58 offset:18176\n" \
    "s_waitcnt lgkmcnt(0)\n" \
    "v_cmp_gt_u32_e32 vcc, s99, v60\n" \
    "s_cbranch_vccz Lce12_%=\n" \
    "s_sub_u32 s90, s90, 1\n" \
    "s_cbranch_scc0 Lcl12_%=\n" \
    "Lce12_%=:\n" \
    "v_fmac_f32_e32 v96, v104, v64\n" \
    "v_fmac_f32_e32 v97, v104, v66\n" \
    "v_fmac_f32_e32 v98, v104, v68\n" \
    "v_fmac_f32_e32 v99, v104, v70\n" \
    "v_fmac_f32_e32 v96, v104, v72\n" \
    "v_fmac_f32_e32 v97, v104, v74\n" \
    "v_fmac_f32_e32 v98, v104, v76\n" \
    "v_fmac_f32_e32 v99, v104, v78\n" \
    "v_cvt_pk_bf16_f32 v112, v96, v97\n" \
    "v_cvt_pk_bf16_f32 v113, v98, v99\n" \
    "global_store_dwordx2 v41, v[112:113], s[96:97] offset:0\n" \
    "v_fmac_f32_e32 v100, v105, v65\n" \
    "v_fmac_f32_e32 v101, v105, v67\n" \
    "v_fmac_f32_e32 v102, v105, v69\n" \
    "v_fmac_f32_e32 v103, v105, v71\n" \
    "v_fmac_f32_e32 v100, v105, v73\n" \
    "v_fmac_f32_e32 v101, v105, v75\n" \
    "v_fmac_f32_e32 v102, v105, v77\n" \
    "v_fmac_f32_e32 v103, v105, v79\n" \
    "v_cvt_pk_bf16_f32 v114, v100, v101\n" \
    "v_cvt_pk_bf16_f32 v115, v102, v103\n" \
    "global_store_dwordx2 v41, v[114:115], s[96:97] offset:1152\n" \
    "v_mov_b32_e32 v61, 20\n" \
    "s_waitcnt vmcnt(0)\n" \
    "ds_write_addtid_b32 v61 offset:56416\n" \
    "s_branch Lcx_%=\n" \
    "Lcs5_%=:\n" \
    "s_mov_b32 s90, 0x20000\n" \
    "v_add_u32_e32 v0, 0, v44\n" \
    "Lcl5_%=:\n" \
    "s_sleep 4\n" \
    "global_load_dwordx4 v[80:83], v0, %[ws] sc1\n" \
    "s_waitcnt vmcnt(0)\n" \
    "v_cmp_ne_u32_e32 vcc, s98, v81\n" \
    "v_cmp_ne_u32_e64 s[88:89], s98, v83\n" \
    "s_nop 2\n" \
    "s_or_b64 vcc, vcc, s[88:89]\n" \
    "s_cbranch_scc0 Lce5_%=\n" \
    "s_sub_u32 s90, s90, 1\n" \
    "s_cbranch_scc0 Lcl5_%=\n" \
    "s_branch Lce5_%=\n" \
    "Lcs7_%=:\n" \
    "s_mov_b32 s90, 0x20000\n" \
    "v_add_u32_e32 v0, 4096, v44\n" \
    "Lcl7_%=:\n" \
    "s_sleep 4\n" \
    "global_load_dwordx4 v[84:87], v0, %[ws] sc1\n" \
    "s_waitcnt vmcnt(0)\n" \
    "v_cmp_ne_u32_e32 vcc, s98, v85\n" \
    "v_cmp_ne_u32_e64 s[88:89], s98, v87\n" \
    "s_nop 2\n" \
    "s_or_b64 vcc, vcc, s[88:89]\n" \
    "s_cbranch_scc0 Lce7_%=\n" \
    "s_sub_u32 s90, s90, 1\n" \
    "s_cbranch_scc0 Lcl7_%=\n" \
    "s_branch Lce7_%=\n" \
    "Lcs8_%=:\n" \
    "s_mov_b32 s90, 0x20000\n" \
    "v_add_u32_e32 v0, 8192, v44\n" \
    "Lcl8_%=:\n" \
    "s_sleep 4\n" \
    "global_load_dwordx4 v[88:91], v0, %[ws] sc1\n" \
    "s_waitcnt vmcnt(0)\n" \
    "v_cmp_ne_u32_e32 vcc, s98, v89\n" \
    "v_cmp_ne_u32_e64 s[88:89], s98, v91\n" \
    "s_nop 2\n" \
    "s_or_b64 vcc, vcc, s[88:89]\n" \
    "s_cbranch_scc0 Lce8_%=\n" \
    "s_sub_u32 s90, s90, 1\n" \
    "s_cbranch_scc0 Lcl8_%=\n" \
    "s_branch Lce8_%=\n" \
    "Lcs9_%=:\n" \
    "s_mov_b32 s90, 0x20000\n" \
    "v_add_u32_e32 v0, 12288, v44\n" \
    "Lcl9_%=:\n" \
    "s_sleep 4\n" \
    "global_load_dwordx4 v[92:95], v0, %[ws] sc1\n" \
    "s_waitcnt vmcnt(0)\n" \
    "v_cmp_ne_u32_e32 vcc, s98, v93\n" \
    "v_cmp_ne_u32_e64 s[88:89], s98, v95\n" \
    "s_nop 2\n" \
    "s_or_b64 vcc, vcc, s[88:89]\n" \
    "s_cbranch_scc0 Lce9_%=\n" \
    "s_sub_u32 s90, s90, 1\n" \
    "s_cbranch_scc0 Lcl9_%=\n" \
    "s_branch Lce9_%=\n" \
    "Lcx_%=:\n"
#define CONV_C_CLOB "v0", "v1", "v2", "v4", "v5", "v6", "v7", "v8", "v9", "v10", "v11", "v12", "v13", "v14", "v15", "v16", "v17", "v18", "v19", "v20", "v21", "v22", "v23", "v24", "v25", "v26", "v27", "v32", "v33", "v34", "v35", "v36", "v37", "v38", "v39", "v40", "v41", "v42", "v43", "v44", "v45", "v47", "v48", "v49", "v50", "v51", "v54", "v55", "v56", "v57", "v58", "v60", "v61", "v62", "v63", "v64", "v65", "v66", "v67", "v68", "v69", "v70", "v71", "v72", "v73", "v74", "v75", "v76", "v77", "v78", "v79", "v80", "v81", "v82", "v83", "v84", "v85", "v86", "v87", "v88", "v89", "v90", "v91", "v92", "v93", "v94", "v95", "v96", "v97", "v98", "v99", "v100", "v101", "v102", "v103", "v104", "v105", "v106", "v107", "v108", "v109", "v110", "v111", "v112", "v113", "v114", "v115", "v116", "v117", "v118", "v119", "v120", "v121", "v122", "v123", "v124", "v125", "v126", "v127", "v128", "v129", "v130", "v131", "v132", "v133", "v134", "v135", "v136", "v137", "v138", "v139", "v140", "v141", "v142", "v143", "v144", "v145", "v146", "v147", "v148", "v149", "v150", "v151", "v152", "v153", "v154", "v155", "v156", "v157", "v158", "v159", "v160", "v161", "v162", "v163"
#define ITEM_ASM \
    "v_mbcnt_lo_u32_b32 v53, -1, 0\n" \
    "v_mbcnt_hi_u32_b32 v53, -1, v53\n" \
    "s_lshl_b32 s88, %[it], 7\n" \
    "s_add_u32 s89, s88, %[kb]\n" \
    "s_add_u32 s88, s88, %[cb]\n" \
    "v_and_b32_e32 v28, 3, v53\n" \
    "v_bfe_u32 v31, v53, 2, 2\n" \
    "v_lshrrev_b32_e32 v52, 4, v53\n" \
    "v_lshlrev_b32_e32 v30, 3, v31\n" \
    "v_lshl_or_b32 v30, v28, 5, v30\n" \
    "v_add_u32_e32 v30, s89, v30\n" \
    "v_lshlrev_b32_e32 v28, 4, v28\n" \
    "v_lshl_or_b32 v28, v52, 6, v28\n" \
    "v_and_b32_e32 v52, 1, v31\n" \
    "v_mul_u32_u24_e32 v52, 1152, v52\n" \
    "s_mov_b32 s89, %[wl]\n" \
    "v_add3_u32 v28, v28, v52, s89\n" \
    "v_lshlrev_b32_e32 v29, 14, v53\n" \
    "v_add_u32_e32 v29, s88, v29\n" \
    "v_sub_u32_e32 v31, 15, v31\n" \
    "v_mov_b64 v[16:17], 0\n" \
    "v_mov_b64 v[18:19], 0\n" \
    "v_mov_b64 v[20:21], 0\n" \
    "v_mov_b64 v[22:23], 0\n" \
    "s_mov_b32 vcc_lo, 0xaaaaaaaa\n" \
    "v_mov_b32_e32 v52, 0x07060302\n" \
    "s_mov_b32 s88, 0x05040100\n" \
    "v_mov_b32_e32 v53, s88\n" \
    "v_cndmask_b32_e32 v52, v53, v52, vcc\n" \
    "s_mov_b32 s88, 0x80008000\n" \
    "s_mov_b32 s89, 0xffff0000\n" \
    "s_mov_b32 s92, 0x07060304\n" \
    "s_mov_b32 s93, 0x07060204\n" \
    "s_mov_b32 vcc_lo, 0xcccccccc\n" \
    "v_mov_b32_e32 v91, %[fl]\n" \
    "v_mov_b32_e32 v92, 1\n" \
    "s_waitcnt vmcnt(18)\n" \
    "ds_read_b128 v[0:3], v28 offset:0\n" \
    "ds_read_b128 v[4:7], v28 offset:256\n" \
    "ds_read_b128 v[8:11], v28 offset:512\n" \
    "ds_read_b128 v[12:15], v28 offset:768\n" \
    "s_mov_b32 exec_hi, 0\n" \
    "ds_read_b128 v[16:19], v28 offset:1024\n" \
    "s_mov_b64 exec, -1\n" \
    "s_mov_b32 exec_lo, 0\n" \
    "ds_read_b128 v[20:23], v28 offset:896\n" \
    "s_mov_b32 exec_lo, -1\n" \
    "v_readfirstlane_b32 s98, v72\n" \
    "v_add_u32_e32 v54, -4, v31\n" \
    "v_add_u32_e32 v55, -8, v31\n" \
    "v_add_u32_e32 v90, -12, v31\n" \
    "s_add_u32 s98, s98, 1\n" \
    "s_max_u32 s98, s98, 1\n" \
    "v_mov_b32_e32 v25, s98\n" \
    "v_mov_b32_e32 v27, s98\n" \
    "s_waitcnt vmcnt(17)\n" \
    "s_waitcnt lgkmcnt(5)\n" \
    "v_mfma_f32_16x16x32_bf16 v[74:77], v[0:3], v[96:99], 0\n" \
    "s_waitcnt vmcnt(16)\n" \
    "v_mfma_f32_16x16x32_bf16 v[78:81], v[0:3], v[112:115], 0\n" \
    "s_waitcnt vmcnt(15)\n" \
    "v_mfma_f32_16x16x32_bf16 v[82:85], v[0:3], v[128:131], 0\n" \
    "s_waitcnt vmcnt(14)\n" \
    "v_mfma_f32_16x16x32_bf16 v[86:89], v[0:3], v[144:147], 0\n" \
    "s_waitcnt vmcnt(13)\n" \
    "s_waitcnt lgkmcnt(4)\n" \
    "v_mfma_f32_16x16x32_bf16 v[74:77], v[4:7], v[100:103], v[74:77]\n" \
    "s_waitcnt vmcnt(12)\n" \
    "v_mfma_f32_16x16x32_bf16 v[78:81], v[4:7], v[116:119], v[78:81]\n" \
    "s_waitcnt vmcnt(11)\n" \
    "v_mfma_f32_16x16x32_bf16 v[82:85], v[4:7], v[132:135], v[82:85]\n" \
    "s_waitcnt vmcnt(10)\n" \
    "v_mfma_f32_16x16x32_bf16 v[86:89], v[4:7], v[148:151], v[86:89]\n" \
    "s_waitcnt vmcnt(9)\n" \
    "s_waitcnt lgkmcnt(3)\n" \
    "v_mfma_f32_16x16x32_bf16 v[74:77], v[8:11], v[104:107], v[74:77]\n" \
    "s_waitcnt vmcnt(8)\n" \
    "v_mfma_f32_16x16x32_bf16 v[78:81], v[8:11], v[120:123], v[78:81]\n" \
    "s_waitcnt vmcnt(7)\n" \
    "v_mfma_f32_16x16x32_bf16 v[82:85], v[8:11], v[136:139], v[82:85]\n" \
    "s_waitcnt vmcnt(6)\n" \
    "v_mfma_f32_16x16x32_bf16 v[86:89], v[8:11], v[152:155], v[86:89]\n" \
    "s_waitcnt vmcnt(5)\n" \
    "s_waitcnt lgkmcnt(2)\n" \
    "v_mfma_f32_16x16x32_bf16 v[74:77], v[12:15], v[108:111], v[74:77]\n" \
    "s_waitcnt vmcnt(4)\n" \
    "v_mfma_f32_16x16x32_bf16 v[78:81], v[12:15], v[124:127], v[78:81]\n" \
    "s_waitcnt vmcnt(3)\n" \
    "v_mfma_f32_16x16x32_bf16 v[82:85], v[12:15], v[140:143], v[82:85]\n" \
    "s_waitcnt vmcnt(2)\n" \
    "v_mfma_f32_16x16x32_bf16 v[86:89], v[12:15], v[156:159], v[86:89]\n" \
    "s_waitcnt vmcnt(1)\n" \
    "s_waitcnt lgkmcnt(1)\n" \
    "v_mfma_f32_16x16x32_bf16 v[74:77], v[16:19], v[160:163], v[74:77]\n" \
    "s_waitcnt lgkmcnt(0)\n" \
    "v_mfma_f32_16x16x32_bf16 v[78:81], v[20:23], v[160:163], v[78:81]\n" \
    "s_waitcnt vmcnt(0)\n" \
    "v_mfma_f32_16x16x32_bf16 v[82:85], v[16:19], v[164:167], v[82:85]\n" \
    "v_mfma_f32_16x16x32_bf16 v[86:89], v[20:23], v[164:167], v[86:89]\n" \
    "s_nop 2\n" \
    "v_mov_b32_dpp v0, v74 quad_perm:[0,0,0,0] row_mask:0xf bank_mask:0xf\n" \
    "s_nop 0\n" \
    "v_mov_b32_dpp v1, v78 quad_perm:[0,0,0,0] row_mask:0xf bank_mask:0xf\n" \
    "s_nop 0\n" \
    "v_mov_b32_dpp v2, v82 quad_perm:[0,0,0,0] row_mask:0xf bank_mask:0xf\n" \
    "v_mov_b32_dpp v3, v86 quad_perm:[0,0,0,0] row_mask:0xf bank_mask:0xf\n" \
    "v_add_f32_dpp v0, v75, v0 quad_perm:[1,1,1,1] row_mask:0xf bank_mask:0xf\n" \
    "v_add_f32_dpp v1, v79, v1 quad_perm:[1,1,1,1] row_mask:0xf bank_mask:0xf\n" \
    "v_add_f32_dpp v2, v83, v2 quad_perm:[1,1,1,1] row_mask:0xf bank_mask:0xf\n" \
    "v_add_f32_dpp v3, v87, v3 quad_perm:[1,1,1,1] row_mask:0xf bank_mask:0xf\n" \
    "v_add_f32_dpp v0, v76, v0 quad_perm:[2,2,2,2] row_mask:0xf bank_mask:0xf\n" \
    "v_add_f32_dpp v1, v80, v1 quad_perm:[2,2,2,2] row_mask:0xf bank_mask:0xf\n" \
    "v_add_f32_dpp v2, v84, v2 quad_perm:[2,2,2,2] row_mask:0xf bank_mask:0xf\n" \
    "v_add_f32_dpp v3, v88, v3 quad_perm:[2,2,2,2] row_mask:0xf bank_mask:0xf\n" \
    "v_add_f32_dpp v0, v77, v0 quad_perm:[3,3,3,3] row_mask:0xf bank_mask:0xf\n" \
    "v_add_f32_dpp v1, v81, v1 quad_perm:[3,3,3,3] row_mask:0xf bank_mask:0xf\n" \
    "v_add_f32_dpp v2, v85, v2 quad_perm:[3,3,3,3] row_mask:0xf bank_mask:0xf\n" \
    "v_add_f32_dpp v3, v89, v3 quad_perm:[3,3,3,3] row_mask:0xf bank_mask:0xf\n" \
    "v_max_f32_e32 v4, 0xff800000, v0\n" \
    "v_max_f32_e32 v5, 0xff800000, v1\n" \
    "v_max_f32_e32 v6, 0xff800000, v2\n" \
    "v_max_f32_e32 v7, 0xff800000, v3\n" \
    "v_cvt_pk_bf16_f32 v8, v4, v5\n" \
    "v_cvt_pk_bf16_f32 v9, v6, v7\n" \
    "v_pk_ashrrev_i16 v4, 15, v8 op_sel_hi:[0,1]\n" \
    "v_pk_ashrrev_i16 v5, 15, v9 op_sel_hi:[0,1]\n" \
    "v_bitop3_b32 v8, v8, v4, s88 bitop3:0x1e\n" \
    "v_bitop3_b32 v9, v9, v5, s88 bitop3:0x1e\n" \
    "v_lshl_or_b32 v4, v8, 16, v31\n" \
    "v_and_or_b32 v5, v8, s89, v54\n" \
    "v_lshl_or_b32 v6, v9, 16, v55\n" \
    "v_and_or_b32 v7, v9, s89, v90\n" \
    "v_max_u32_e32 v10, v4, v5\n" \
    "v_max_u32_e32 v12, v6, v7\n" \
    "v_min_u32_e32 v11, v4, v5\n" \
    "v_min_u32_e32 v13, v6, v7\n" \
    "v_max_u32_e32 v14, v10, v12\n" \
    "v_min_u32_e32 v17, v10, v12\n" \
    "v_min_u32_e32 v18, v10, v13\n" \
    "v_max3_u32 v15, v17, v11, v13\n" \
    "v_min_u32_e32 v16, v11, v12\n" \
    "v_max_u32_e32 v16, v16, v18\n" \
    "v_min_u32_dpp v4, v14, v14 row_ror:4 row_mask:0xf bank_mask:0xf\n" \
    "v_mov_b32_dpp v5, v15 row_ror:4 row_mask:0xf bank_mask:0xf\n" \
    "v_max_u32_dpp v10, v14, v14 row_ror:4 row_mask:0xf bank_mask:0xf\n" \
    "v_min_u32_dpp v17, v14, v15 row_ror:4 row_mask:0xf bank_mask:0xf\n" \
    "v_max_u32_dpp v6, v16, v16 row_ror:4 row_mask:0xf bank_mask:0xf\n" \
    "v_max3_u32 v11, v4, v15, v5\n" \
    "v_min_u32_e32 v18, v14, v5\n" \
    "v_max3_u32 v12, v17, v18, v6\n" \
    "v_min_u32_dpp v4, v10, v10 row_ror:8 row_mask:0xf bank_mask:0xf\n" \
    "v_mov_b32_dpp v5, v11 row_ror:8 row_mask:0xf bank_mask:0xf\n" \
    "v_max_u32_dpp v14, v10, v10 row_ror:8 row_mask:0xf bank_mask:0xf\n" \
    "v_min_u32_dpp v17, v10, v11 row_ror:8 row_mask:0xf bank_mask:0xf\n" \
    "v_max_u32_dpp v6, v12, v12 row_ror:8 row_mask:0xf bank_mask:0xf\n" \
    "v_max3_u32 v15, v4, v11, v5\n" \
    "v_min_u32_e32 v18, v10, v5\n" \
    "v_max3_u32 v16, v17, v18, v6\n" \
    "s_mov_b64 exec, 0xff\n" \
    "v_perm_b32 v24, v14, v16, s92\n" \
    "v_perm_b32 v26, v15, v16, s93\n" \
    "global_store_dwordx4 v29, v[24:27], %[ws] sc1\n" \
    "ds_write_b32 v91, v92\n" \
    "s_mov_b64 exec, -1\n" \
    "v_mul_f32_e32 v0, 0xbfb8aa3b, v0\n" \
    "v_mul_f32_e32 v1, 0xbfb8aa3b, v1\n" \
    "v_mul_f32_e32 v2, 0xbfb8aa3b, v2\n" \
    "v_mul_f32_e32 v3, 0xbfb8aa3b, v3\n" \
    "v_exp_f32_e32 v0, v0\n" \
    "v_exp_f32_e32 v1, v1\n" \
    "v_exp_f32_e32 v2, v2\n" \
    "v_exp_f32_e32 v3, v3\n" \
    "v_add_f32_e32 v0, 1.0, v0\n" \
    "v_add_f32_e32 v1, 1.0, v1\n" \
    "v_add_f32_e32 v2, 1.0, v2\n" \
    "v_add_f32_e32 v3, 1.0, v3\n" \
    "v_rcp_f32_e32 v0, v0\n" \
    "v_rcp_f32_e32 v1, v1\n" \
    "v_rcp_f32_e32 v2, v2\n" \
    "v_rcp_f32_e32 v3, v3\n" \
    "v_cvt_f16_f32_e32 v0, v0\n" \
    "v_cvt_f16_f32_e32 v1, v1\n" \
    "v_cvt_f16_f32_e32 v2, v2\n" \
    "v_cvt_f16_f32_e32 v3, v3\n" \
    "v_pack_b32_f16 v0, v0, v1\n" \
    "v_pack_b32_f16 v2, v2, v3\n" \
    "v_cndmask_b32_e32 v8, v8, v9, vcc\n" \
    "v_cndmask_b32_e32 v0, v0, v2, vcc\n" \
    "v_mov_b32_e32 v11, s98\n" \
    "s_nop 0\n" \
    "v_permlane16_swap_b32 v0, v1\n" \
    "v_perm_b32 v10, v8, v1, v52\n" \
    "s_mov_b64 exec, 0xffff\n" \
    "global_store_dwordx2 v30, v[10:11], %[ws] sc1\n" \
    "s_mov_b64 exec, -1\n"
#define ITEM_CLOB "v0", "v1", "v2", "v3", "v4", "v5", "v6", "v7", "v8", "v9", "v10", "v11", "v12", "v13", "v14", "v15", "v16", "v17", "v18", "v19", "v20", "v21", "v22", "v23", "v24", "v25", "v26", "v27", "v28", "v29", "v30", "v31", "v52", "v53", "v54", "v55", "v72", "v74", "v75", "v76", "v77", "v78", "v79", "v80", "v81", "v82", "v83", "v84", "v85", "v86", "v87", "v88", "v89", "v90", "v91", "v92", "v96", "v97", "v98", "v99", "v100", "v101", "v102", "v103", "v104", "v105", "v106", "v107", "v108", "v109", "v110", "v111", "v112", "v113", "v114", "v115", "v116", "v117", "v118", "v119", "v120", "v121", "v122", "v123", "v124", "v125", "v126", "v127", "v128", "v129", "v130", "v131", "v132", "v133", "v134", "v135", "v136", "v137", "v138", "v139", "v140", "v141", "v142", "v143", "v144", "v145", "v146", "v147", "v148", "v149", "v150", "v151", "v152", "v153", "v154", "v155", "v156", "v157", "v158", "v159", "v160", "v161", "v162", "v163", "v164", "v165", "v166", "v167"
#define FFN_ASM \
    "v_mov_b64 v[96:97], 0\n" \
    "v_mov_b64 v[98:99], 0\n" \
    "v_mov_b64 v[100:101], 0\n" \
    "v_mov_b64 v[102:103], 0\n" \
    "v_mov_b64 v[104:105], 0\n" \
    "v_mov_b64 v[106:107], 0\n" \
    "v_mov_b64 v[108:109], 0\n" \
    "v_mov_b64 v[110:111], 0\n" \
    "v_mov_b64 v[112:113], 0\n" \
    "v_mov_b64 v[114:115], 0\n" \
    "v_mov_b64 v[116:117], 0\n" \
    "v_mov_b64 v[118:119], 0\n" \
    "v_mov_b64 v[120:121], 0\n" \
    "v_mov_b64 v[122:123], 0\n" \
    "s_waitcnt lgkmcnt(0)\n" \
    "s_barrier\n" \
    "ds_read_b128 v[124:127], %[xa] offset:0\n" \
    "ds_read_b128 v[128:131], %[xa] offset:3072\n" \
    "ds_read_b128 v[132:135], %[xa] offset:6144\n" \
    "ds_read_b32 v136, v112 offset:%[tg]\n" \
    "s_waitcnt lgkmcnt(3)\n" \
    "v_smfmac_f32_16x16x64_bf16 v[96:99], v[124:127], v[0:7], %[idx]\n" \
    "v_smfmac_f32_16x16x64_bf16 v[100:103], v[124:127], v[8:15], %[idx]\n" \
    "v_smfmac_f32_16x16x64_bf16 v[104:107], v[124:127], v[16:23], %[idx]\n" \
    "v_smfmac_f32_16x16x64_bf16 v[108:111], v[124:127], v[24:31], %[idx]\n" \
    "s_waitcnt lgkmcnt(2)\n" \
    "v_smfmac_f32_16x16x64_bf16 v[96:99], v[128:131], v[32:39], %[idx]\n" \
    "v_smfmac_f32_16x16x64_bf16 v[100:103], v[128:131], v[40:47], %[idx]\n" \
    "v_smfmac_f32_16x16x64_bf16 v[104:107], v[128:131], v[48:55], %[idx]\n" \
    "v_smfmac_f32_16x16x64_bf16 v[108:111], v[128:131], v[56:63], %[idx]\n" \
    "s_waitcnt lgkmcnt(1)\n" \
    "v_smfmac_f32_16x16x64_bf16 v[96:99], v[132:135], v[64:71], %[idx]\n" \
    "v_smfmac_f32_16x16x64_bf16 v[100:103], v[132:135], v[72:79], %[idx]\n" \
    "v_smfmac_f32_16x16x64_bf16 v[104:107], v[132:135], v[80:87], %[idx]\n" \
    "v_smfmac_f32_16x16x64_bf16 v[108:111], v[132:135], v[88:95], %[idx]\n" \
    "s_nop 4\n" \
    "ds_write_b128 %[rdw], v[96:99] offset:0\n" \
    "ds_write_b128 %[rdw], v[100:103] offset:1024\n" \
    "ds_write_b128 %[rdw], v[104:107] offset:2048\n" \
    "ds_write_b128 %[rdw], v[108:111] offset:3072\n" \
    "s_cmp_gt_u32 %[wv], 3\n" \
    "s_waitcnt lgkmcnt(4)\n" \
    "v_mov_b32_e32 v73, v136\n" \
    "v_mov_b32_e32 v75, v136\n" \
    "v_mov_b32_e32 v77, v136\n" \
    "v_mov_b32_e32 v79, v136\n" \
    "v_mov_b32_e32 v81, v136\n" \
    "v_mov_b32_e32 v83, v136\n" \
    "s_waitcnt vmcnt(0) lgkmcnt(0)\n" \
    "s_barrier\n" \
    "s_cbranch_scc1 Lfs_%=\n" \
    "ds_read_b64 v[48:49], %[rdr] offset:0\n" \
    "ds_read_b64 v[50:51], %[rdr] offset:4096\n" \
    "ds_read_b64 v[52:53], %[rdr] offset:8192\n" \
    "ds_read_b64 v[54:55], %[rdr] offset:12288\n" \
    "ds_read_b64 v[56:57], %[rdr] offset:16384\n" \
    "ds_read_b64 v[58:59], %[rdr] offset:20480\n" \
    "ds_read_b64 v[60:61], %[rdr] offset:128\n" \
    "ds_read_b64 v[62:63], %[rdr] offset:4224\n" \
    "ds_read_b64 v[64:65], %[rdr] offset:8320\n" \
    "ds_read_b64 v[66:67], %[rdr] offset:12416\n" \
    "ds_read_b64 v[68:69], %[rdr] offset:16512\n" \
    "ds_read_b64 v[70:71], %[rdr] offset:20608\n" \
    "ds_read_b64_tr_b16 v[0:1], %[d00] offset:0\n" \
    "ds_read_b64_tr_b16 v[2:3], %[d01] offset:256\n" \
    "ds_read_b64_tr_b16 v[4:5], %[d00] offset:2048\n" \
    "ds_read_b64_tr_b16 v[6:7], %[d01] offset:2304\n" \
    "ds_read_b64_tr_b16 v[8:9], %[d10] offset:0\n" \
    "ds_read_b64_tr_b16 v[10:11], %[d11] offset:256\n" \
    "ds_read_b64_tr_b16 v[12:13], %[d10] offset:2048\n" \
    "ds_read_b64_tr_b16 v[14:15], %[d11] offset:2304\n" \
    "ds_read_b64_tr_b16 v[16:17], %[d20] offset:0\n" \
    "ds_read_b64_tr_b16 v[18:19], %[d21] offset:256\n" \
    "ds_read_b64_tr_b16 v[20:21], %[d20] offset:2048\n" \
    "ds_read_b64_tr_b16 v[22:23], %[d21] offset:2304\n" \
    "s_waitcnt lgkmcnt(14)\n" \
    "v_pk_add_f32 v[48:49], v[48:49], v[50:51]\n" \
    "v_pk_add_f32 v[52:53], v[52:53], v[54:55]\n" \
    "v_pk_add_f32 v[56:57], v[56:57], v[58:59]\n" \
    "v_pk_add_f32 v[48:49], v[48:49], v[52:53]\n" \
    "v_pk_add_f32 v[60:61], v[60:61], v[62:63]\n" \
    "v_pk_add_f32 v[48:49], v[48:49], v[56:57]\n" \
    "v_pk_add_f32 v[64:65], v[64:65], v[66:67]\n" \
    "v_mul_f32_e32 v50, 0xbfb8aa3b, v48\n" \
    "v_mul_f32_e32 v51, 0xbfb8aa3b, v49\n" \
    "v_exp_f32_e32 v50, v50\n" \
    "v_exp_f32_e32 v51, v51\n" \
    "s_waitcnt lgkmcnt(12)\n" \
    "v_pk_add_f32 v[68:69], v[68:69], v[70:71]\n" \
    "v_add_f32_e32 v50, 1.0, v50\n" \
    "v_add_f32_e32 v51, 1.0, v51\n" \
    "v_rcp_f32_e32 v50, v50\n" \
    "v_rcp_f32_e32 v51, v51\n" \
    "v_pk_add_f32 v[60:61], v[60:61], v[64:65]\n" \
    "v_pk_add_f32 v[60:61], v[60:61], v[68:69]\n" \
    "v_pk_mul_f32 v[48:49], v[48:49], v[50:51]\n" \
    "v_pk_mul_f32 v[48:49], v[48:49], v[60:61]\n" \
    "v_cvt_pk_bf16_f32 v48, v48, v49\n" \
    "ds_write_b16 %[hbw], v48\n" \
    "ds_write_b16_d16_hi %[hbw], v48 offset:144\n" \
    "s_branch Lfj_%=\n" \
    "Lfs_%=:\n" \
    "ds_read_b64_tr_b16 v[0:1], %[d00] offset:0\n" \
    "ds_read_b64_tr_b16 v[2:3], %[d01] offset:256\n" \
    "ds_read_b64_tr_b16 v[4:5], %[d00] offset:2048\n" \
    "ds_read_b64_tr_b16 v[6:7], %[d01] offset:2304\n" \
    "ds_read_b64_tr_b16 v[8:9], %[d10] offset:0\n" \
    "ds_read_b64_tr_b16 v[10:11], %[d11] offset:256\n" \
    "ds_read_b64_tr_b16 v[12:13], %[d10] offset:2048\n" \
    "ds_read_b64_tr_b16 v[14:15], %[d11] offset:2304\n" \
    "ds_read_b64_tr_b16 v[16:17], %[d20] offset:0\n" \
    "ds_read_b64_tr_b16 v[18:19], %[d21] offset:256\n" \
    "ds_read_b64_tr_b16 v[20:21], %[d20] offset:2048\n" \
    "ds_read_b64_tr_b16 v[22:23], %[d21] offset:2304\n" \
    "Lfj_%=:\n" \
    "s_waitcnt vmcnt(0) lgkmcnt(0)\n" \
    "s_barrier\n" \
    "ds_read_b128 v[84:87], %[hs]\n" \
    "ds_read_b128 v[88:91], %[hs] offset:64\n" \
    "ds_read_b64_tr_b16 v[24:25], %[d00] offset:4096\n" \
    "ds_read_b64_tr_b16 v[26:27], %[d01] offset:4352\n" \
    "ds_read_b64_tr_b16 v[28:29], %[d00] offset:6144\n" \
    "ds_read_b64_tr_b16 v[30:31], %[d01] offset:6400\n" \
    "ds_read_b64_tr_b16 v[32:33], %[d10] offset:4096\n" \
    "ds_read_b64_tr_b16 v[34:35], %[d11] offset:4352\n" \
    "ds_read_b64_tr_b16 v[36:37], %[d10] offset:6144\n" \
    "ds_read_b64_tr_b16 v[38:39], %[d11] offset:6400\n" \
    "ds_read_b64_tr_b16 v[40:41], %[d20] offset:4096\n" \
    "ds_read_b64_tr_b16 v[42:43], %[d21] offset:4352\n" \
    "ds_read_b64_tr_b16 v[44:45], %[d20] offset:6144\n" \
    "ds_read_b64_tr_b16 v[46:47], %[d21] offset:6400\n" \
    "s_waitcnt lgkmcnt(13)\n" \
    "v_smfmac_f32_16x16x64_bf16 v[112:115], v[84:87], v[0:7], %[idx]\n" \
    "v_smfmac_f32_16x16x64_bf16 v[116:119], v[84:87], v[8:15], %[idx]\n" \
    "v_smfmac_f32_16x16x64_bf16 v[120:123], v[84:87], v[16:23], %[idx]\n" \
    "s_waitcnt lgkmcnt(8)\n" \
    "v_smfmac_f32_16x16x64_bf16 v[112:115], v[88:91], v[24:31], %[idx]\n" \
    "s_waitcnt lgkmcnt(4)\n" \
    "v_smfmac_f32_16x16x64_bf16 v[116:119], v[88:91], v[32:39], %[idx]\n" \
    "s_waitcnt lgkmcnt(0)\n" \
    "v_smfmac_f32_16x16x64_bf16 v[120:123], v[88:91], v[40:47], %[idx]\n" \
    "s_nop 3\n" \
    "v_cvt_pk_bf16_f32 v72, v112, v113\n" \
    "v_cvt_pk_bf16_f32 v74, v114, v115\n" \
    "global_store_dwordx4 %[po0], v[72:75], %[ws] sc1\n" \
    "v_cvt_pk_bf16_f32 v76, v116, v117\n" \
    "v_cvt_pk_bf16_f32 v78, v118, v119\n" \
    "global_store_dwordx4 %[po1], v[76:79], %[ws] sc1\n" \
    "v_cvt_pk_bf16_f32 v80, v120, v121\n" \
    "v_cvt_pk_bf16_f32 v82, v122, v123\n" \
    "global_store_dwordx4 %[po2], v[80:83], %[ws] sc1\n"
#define FFN_CLOB "v96", "v97", "v98", "v99", "v100", "v101", "v102", "v103", "v104", "v105", "v106", "v107", "v108", "v109", "v110", "v111", "v112", "v113", "v114", "v115", "v116", "v117", "v118", "v119", "v120", "v121", "v122", "v123", "v124", "v125", "v126", "v127", "v128", "v129", "v130", "v131", "v132", "v133", "v134", "v135", "v136"
#define SEL_ASM \
    "s_mov_b32 %[ps], 0\n" \
    "s_branch Lsn_%=\n" \
    "Lsg_%=:\n" \
    "s_mov_b32 %[ps], 4096\n" \
    "Lsw_%=:\n" \
    "s_memrealtime s[92:93]\n" \
    "s_waitcnt lgkmcnt(0)\n" \
    "s_sub_u32 s92, s92, %[at]\n" \
    "s_cmp_lt_i32 s92, 0\n" \
    "s_cbranch_scc0 Lsr_%=\n" \
    "s_sleep 1\n" \
    "s_sub_u32 %[ps], %[ps], 1\n" \
    "s_cbranch_scc0 Lsw_%=\n" \
    "Lsr_%=:\n" \
    "s_mov_b32 %[ps], 0x20000\n" \
    "Lsp_%=:\n" \
    "global_load_dwordx4 v[0:3], %[co], %[ws] sc1\n" \
    "global_load_dwordx4 v[4:7], %[co2], %[ws] sc1\n" \
    "s_mov_b64 exec, -1\n" \
    "s_waitcnt vmcnt(1)\n" \
    "v_bitop3_b32 v10, v1, v3, %[tag] bitop3:0x7e\n" \
    "s_waitcnt vmcnt(0)\n" \
    "v_bitop3_b32 v11, v5, v7, %[tag] bitop3:0x7e\n" \
    "v_or_b32_e32 v10, v10, v11\n" \
    "v_cmp_ne_u32_e32 vcc, 0, v10\n" \
    "s_cbranch_vccz Lsn_%=\n" \
    "s_sleep %[gap]\n" \
    "s_mov_b64 exec, vcc\n" \
    "s_sub_u32 %[ps], %[ps], 1\n" \
    "s_cbranch_scc0 Lsp_%=\n" \
    "s_mov_b64 exec, -1\n" \
    "s_mov_b32 %[ps], 1\n" \
    "Lsn_%=:\n" \
    "v_and_or_b32 v15, v0, %[mk], %[f0]\n" \
    "v_and_or_b32 v17, v4, %[mk], %[fi]\n" \
    "v_and_or_b32 v16, v2, %[mk], %[f0]\n" \
    "v_and_or_b32 v18, v6, %[mk], %[fi]\n" \
    "v_max_u32_e32 v8, v15, v17\n" \
    "v_min_u32_e32 v9, v15, v17\n" \
    "v_max3_u32 v9, v9, v16, v18\n" \
    "v_mov_b32_dpp v10, v8 quad_perm:[1,0,3,2] row_mask:0xf bank_mask:0xf\n" \
    "v_med3_u32 v8, v8, v10, %[c1]\n" \
    "v_mov_b32_dpp v11, v9 quad_perm:[1,0,3,2] row_mask:0xf bank_mask:0xf\n" \
    "v_med3_u32 v9, v9, v11, %[c1]\n" \
    "v_mov_b32_dpp v10, v8 quad_perm:[3,2,1,0] row_mask:0xf bank_mask:0xf\n" \
    "v_med3_u32 v8, v8, v10, %[c2]\n" \
    "v_mov_b32_dpp v11, v9 quad_perm:[3,2,1,0] row_mask:0xf bank_mask:0xf\n" \
    "v_med3_u32 v9, v9, v11, %[c2]\n" \
    "v_mov_b32_dpp v10, v8 quad_perm:[1,0,3,2] row_mask:0xf bank_mask:0xf\n" \
    "v_med3_u32 v8, v8, v10, %[c1]\n" \
    "v_mov_b32_dpp v11, v9 quad_perm:[1,0,3,2] row_mask:0xf bank_mask:0xf\n" \
    "v_med3_u32 v9, v9, v11, %[c1]\n" \
    "v_mov_b32_dpp v10, v8 row_half_mirror row_mask:0xf bank_mask:0xf\n" \
    "v_med3_u32 v8, v8, v10, %[c4]\n" \
    "v_max_u32_dpp v9, v9, v9 row_half_mirror row_mask:0xf bank_mask:0xf\n" \
    "v_perm_b32 v19, v0, v2, %[pm]\n" \
    "v_mov_b32_dpp v10, v8 quad_perm:[2,3,0,1] row_mask:0xf bank_mask:0xf\n" \
    "v_med3_u32 v8, v8, v10, %[c2]\n" \
    "v_mov_b32_dpp v11, v9 quad_perm:[2,3,0,1] row_mask:0xf bank_mask:0xf\n" \
    "v_med3_u32 v9, v9, v11, %[c2]\n" \
    "v_mov_b32_dpp v10, v8 quad_perm:[1,0,3,2] row_mask:0xf bank_mask:0xf\n" \
    "v_med3_u32 v8, v8, v10, %[c1]\n" \
    "v_mov_b32_dpp v11, v9 quad_perm:[1,0,3,2] row_mask:0xf bank_mask:0xf\n" \
    "v_med3_u32 v9, v9, v11, %[c1]\n" \
    "v_max_u32_dpp v8, v8, v8 row_mirror row_mask:0xf bank_mask:0xf\n" \
    "s_nop 0\n" \
    "v_max_u32_dpp v9, v9, v9 row_mirror row_mask:0xf bank_mask:0xf\n" \
    "v_max_u32_dpp v10, v8, v8 row_shl:4 row_mask:0xf bank_mask:0x5\n" \
    "v_min_u32_dpp v10, v8, v8 row_shr:4 row_mask:0xf bank_mask:0xa\n" \
    "v_mov_b32_dpp v11, v9 quad_perm:[2,3,0,1] row_mask:0xf bank_mask:0xf\n" \
    "v_med3_u32 v9, v9, v11, %[c2]\n" \
    "v_mov_b32_dpp v8, v10 quad_perm:[2,3,0,1] row_mask:0xf bank_mask:0xf\n" \
    "v_med3_u32 v10, v10, v8, %[c2]\n" \
    "v_mov_b32_dpp v11, v9 quad_perm:[1,0,3,2] row_mask:0xf bank_mask:0xf\n" \
    "v_med3_u32 v9, v9, v11, %[c1]\n" \
    "v_mov_b32_dpp v8, v10 quad_perm:[1,0,3,2] row_mask:0xf bank_mask:0xf\n" \
    "v_med3_u32 v10, v10, v8, %[c1]\n" \
    "v_mov_b32_e32 v13, v9\n" \
    "v_perm_b32 v20, v4, v6, %[pm]\n" \
    "v_mov_b32_e32 v12, v10\n" \
    "v_permlane16_swap_b32 v9, v13\n" \
    "v_min_u32_e32 v15, v15, v18\n" \
    "v_permlane16_swap_b32 v10, v12\n" \
    "v_max_u32_dpp v9, v13, v9 quad_perm:[3,2,1,0] row_mask:0xf bank_mask:0xf\n" \
    "v_min_u32_e32 v16, v16, v17\n" \
    "v_max_u32_dpp v10, v12, v10 row_half_mirror row_mask:0xf bank_mask:0xf\n" \
    "v_mov_b32_dpp v11, v9 quad_perm:[2,3,0,1] row_mask:0xf bank_mask:0xf\n" \
    "v_med3_u32 v9, v9, v11, %[c2]\n" \
    "v_mov_b32_dpp v8, v10 row_ror:4 row_mask:0xf bank_mask:0xf\n" \
    "v_med3_u32 v10, v10, v8, %[c4]\n" \
    "v_mov_b32_dpp v11, v9 quad_perm:[1,0,3,2] row_mask:0xf bank_mask:0xf\n" \
    "v_med3_u32 v9, v9, v11, %[c1]\n" \
    "v_mov_b32_dpp v8, v10 quad_perm:[2,3,0,1] row_mask:0xf bank_mask:0xf\n" \
    "v_med3_u32 v10, v10, v8, %[c2]\n" \
    "v_mov_b32_e32 v13, v9\n" \
    "v_max3_u32 v19, v19, v20, v15\n" \
    "v_mov_b32_dpp v8, v10 quad_perm:[1,0,3,2] row_mask:0xf bank_mask:0xf\n" \
    "v_med3_u32 v10, v10, v8, %[c1]\n" \
    "v_permlane32_swap_b32 v9, v13\n" \
    "v_max_u32_e32 %[tc], v19, v16\n" \
    "v_mov_b32_e32 v12, v10\n" \
    "v_max_u32_dpp v9, v13, v9 quad_perm:[3,2,1,0] row_mask:0xf bank_mask:0xf\n" \
    "s_nop 0\n" \
    "v_permlane32_swap_b32 v10, v12\n" \
    "v_mov_b32_dpp v11, v9 quad_perm:[2,3,0,1] row_mask:0xf bank_mask:0xf\n" \
    "v_med3_u32 v9, v9, v11, %[c2]\n" \
    "v_max_u32_dpp v10, v12, v10 row_half_mirror row_mask:0xf bank_mask:0xf\n" \
    "s_nop 0\n" \
    "v_mov_b32_dpp v11, v9 quad_perm:[1,0,3,2] row_mask:0xf bank_mask:0xf\n" \
    "v_med3_u32 v9, v9, v11, %[c1]\n" \
    "v_mov_b32_dpp v8, v10 row_ror:4 row_mask:0xf bank_mask:0xf\n" \
    "v_med3_u32 v10, v10, v8, %[c4]\n" \
    "v_and_b32_e32 v9, v9, %[c4]\n" \
    "s_nop 0\n" \
    "v_mov_b32_dpp v8, v10 quad_perm:[2,3,0,1] row_mask:0xf bank_mask:0xf\n" \
    "v_med3_u32 v10, v10, v8, %[c2]\n" \
    "s_nop 1\n" \
    "v_mov_b32_dpp v8, v10 quad_perm:[1,0,3,2] row_mask:0xf bank_mask:0xf\n" \
    "v_med3_u32 v10, v10, v8, %[c1]\n" \
    "v_max_u32_dpp v10, v9, v10 row_half_mirror row_mask:0xf bank_mask:0xf\n" \
    "s_nop 1\n" \
    "v_mov_b32_dpp v8, v10 row_ror:4 row_mask:0xf bank_mask:0xf\n" \
    "v_med3_u32 v10, v10, v8, %[c4]\n" \
    "s_nop 1\n" \
    "v_mov_b32_dpp v8, v10 quad_perm:[2,3,0,1] row_mask:0xf bank_mask:0xf\n" \
    "v_med3_u32 v10, v10, v8, %[c2]\n" \
    "s_nop 1\n" \
    "v_mov_b32_dpp v8, v10 quad_perm:[1,0,3,2] row_mask:0xf bank_mask:0xf\n" \
    "v_med3_u32 v10, v10, v8, %[c1]\n" \
    "s_cmp_eq_u32 %[ps], 0\n" \
    "s_cbranch_scc1 Lsg_%=\n" \
    "v_mov_b32_dpp v14, v10 row_newbcast:0 row_mask:0x1 bank_mask:0x3\n" \
    "v_mov_b32_dpp v14, v10 row_newbcast:1 row_mask:0x1 bank_mask:0xc\n" \
    "v_mov_b32_dpp v14, v10 row_newbcast:2 row_mask:0x2 bank_mask:0x3\n" \
    "v_mov_b32_dpp v14, v10 row_newbcast:3 row_mask:0x2 bank_mask:0xc\n" \
    "v_mov_b32_dpp v14, v10 row_newbcast:4 row_mask:0x4 bank_mask:0x3\n" \
    "v_mov_b32_dpp v14, v10 row_newbcast:5 row_mask:0x4 bank_mask:0xc\n" \
    "v_mov_b32_dpp v14, v10 row_newbcast:6 row_mask:0x8 bank_mask:0x3\n" \
    "v_mov_b32_dpp v14, v10 row_newbcast:7 row_mask:0x8 bank_mask:0xc\n" \
    "s_mov_b32 m0, %[xs]\n" \
    "v_and_b32_e32 v14, 0x7ff, v14\n" \
    "v_mul_u32_u24_e32 v14, 1152, v14\n" \
    "v_sub_u32_e32 v14, %[xk], v14\n" \
    "global_load_lds_dwordx4 v14, %[x] offset:0\n" \
    "s_addk_i32 m0, 896\n" \
    "s_nop 0\n" \
    "global_load_lds_dwordx4 v14, %[x] offset:128\n" \
    "s_addk_i32 m0, 896\n" \
    "s_nop 0\n" \
    "global_load_lds_dwordx4 v14, %[x] offset:256\n" \
    "s_addk_i32 m0, 896\n" \
    "s_nop 0\n" \
    "global_load_lds_dwordx4 v14, %[x] offset:384\n" \
    "s_addk_i32 m0, 896\n" \
    "s_nop 0\n" \
    "global_load_lds_dwordx4 v14, %[x] offset:512\n" \
    "s_addk_i32 m0, 896\n" \
    "s_nop 0\n" \
    "global_load_lds_dwordx4 v14, %[x] offset:640\n" \
    "s_addk_i32 m0, 896\n" \
    "s_nop 0\n" \
    "global_load_lds_dwordx4 v14, %[x] offset:768\n" \
    "s_addk_i32 m0, 896\n" \
    "s_nop 0\n" \
    "global_load_lds_dwordx4 v14, %[x] offset:896\n" \
    "s_addk_i32 m0, 896\n" \
    "s_nop 0\n" \
    "global_load_lds_dwordx4 v14, %[x] offset:1024\n" \
    "v_mov_b32_e32 %[g8], v10\n"
#define SEL_CLOB "v0", "v1", "v2", "v3", "v4", "v5", "v6", "v7", "v8", "v9", "v10", "v11", "v12", "v13", "v14", "v15", "v16", "v17", "v18", "v19", "v20"
#define NET_ASM \
    "s_nop 1\n" \
    "v_min_u32_e32 v15, v0, v4\n" \
    "v_min_u32_e32 v16, v0, v5\n" \
    "v_min_u32_e32 v17, v1, v4\n" \
    "v_max_u32_e32 v18, v2, v6\n" \
    "v_max_u32_e32 v28, v0, v4\n" \
    "v_max3_u32 v29, v15, v1, v5\n" \
    "v_max3_u32 v30, v16, v17, v18\n" \
    "v_min_u32_e32 v15, v20, v24\n" \
    "v_min_u32_e32 v16, v20, v25\n" \
    "v_min_u32_e32 v17, v21, v24\n" \
    "v_max_u32_e32 v18, v22, v26\n" \
    "v_max_u32_e32 v0, v20, v24\n" \
    "v_max3_u32 v1, v15, v21, v25\n" \
    "v_max3_u32 v2, v16, v17, v18\n" \
    "v_min_u32_e32 v15, v28, v0\n" \
    "v_min_u32_e32 v16, v28, v1\n" \
    "v_min_u32_e32 v17, v29, v0\n" \
    "v_max_u32_e32 v18, v30, v2\n" \
    "v_max_u32_e32 v8, v28, v0\n" \
    "v_max3_u32 v9, v15, v29, v1\n" \
    "v_max3_u32 %[tc], v16, v17, v18\n" \
    "v_mov_b32_dpp v10, v8 quad_perm:[1,0,3,2] row_mask:0xf bank_mask:0xf\n" \
    "v_med3_u32 v8, v8, v10, %[c1]\n" \
    "v_mov_b32_dpp v11, v9 quad_perm:[1,0,3,2] row_mask:0xf bank_mask:0xf\n" \
    "v_med3_u32 v9, v9, v11, %[c1]\n" \
    "v_mov_b32_dpp v10, v8 quad_perm:[3,2,1,0] row_mask:0xf bank_mask:0xf\n" \
    "v_med3_u32 v8, v8, v10, %[c2]\n" \
    "v_mov_b32_dpp v11, v9 quad_perm:[3,2,1,0] row_mask:0xf bank_mask:0xf\n" \
    "v_med3_u32 v9, v9, v11, %[c2]\n" \
    "v_mov_b32_dpp v10, v8 quad_perm:[1,0,3,2] row_mask:0xf bank_mask:0xf\n" \
    "v_med3_u32 v8, v8, v10, %[c1]\n" \
    "v_mov_b32_dpp v11, v9 quad_perm:[1,0,3,2] row_mask:0xf bank_mask:0xf\n" \
    "v_med3_u32 v9, v9, v11, %[c1]\n" \
    "v_mov_b32_dpp v10, v8 row_half_mirror row_mask:0xf bank_mask:0xf\n" \
    "v_med3_u32 v8, v8, v10, %[c4]\n" \
    "v_max_u32_dpp v9, v9, v9 row_half_mirror row_mask:0xf bank_mask:0xf\n" \
    "s_nop 0\n" \
    "v_mov_b32_dpp v10, v8 quad_perm:[2,3,0,1] row_mask:0xf bank_mask:0xf\n" \
    "v_med3_u32 v8, v8, v10, %[c2]\n" \
    "v_mov_b32_dpp v11, v9 quad_perm:[2,3,0,1] row_mask:0xf bank_mask:0xf\n" \
    "v_med3_u32 v9, v9, v11, %[c2]\n" \
    "v_mov_b32_dpp v10, v8 quad_perm:[1,0,3,2] row_mask:0xf bank_mask:0xf\n" \
    "v_med3_u32 v8, v8, v10, %[c1]\n" \
    "v_mov_b32_dpp v11, v9 quad_perm:[1,0,3,2] row_mask:0xf bank_mask:0xf\n" \
    "v_med3_u32 v9, v9, v11, %[c1]\n" \
    "v_max_u32_dpp v8, v8, v8 row_mirror row_mask:0xf bank_mask:0xf\n" \
    "s_nop 0\n" \
    "v_max_u32_dpp v9, v9, v9 row_mirror row_mask:0xf bank_mask:0xf\n" \
    "v_max_u32_dpp v10, v8, v8 row_shl:4 row_mask:0xf bank_mask:0x5\n" \
    "v_min_u32_dpp v10, v8, v8 row_shr:4 row_mask:0xf bank_mask:0xa\n" \
    "v_mov_b32_dpp v11, v9 quad_perm:[2,3,0,1] row_mask:0xf bank_mask:0xf\n" \
    "v_med3_u32 v9, v9, v11, %[c2]\n" \
    "v_mov_b32_dpp v8, v10 quad_perm:[2,3,0,1] row_mask:0xf bank_mask:0xf\n" \
    "v_med3_u32 v10, v10, v8, %[c2]\n" \
    "v_mov_b32_dpp v11, v9 quad_perm:[1,0,3,2] row_mask:0xf bank_mask:0xf\n" \
    "v_med3_u32 v9, v9, v11, %[c1]\n" \
    "v_mov_b32_dpp v8, v10 quad_perm:[1,0,3,2] row_mask:0xf bank_mask:0xf\n" \
    "v_med3_u32 v10, v10, v8, %[c1]\n" \
    "v_mov_b32_e32 v13, v9\n" \
    "v_mov_b32_e32 v12, v10\n" \
    "s_nop 0\n" \
    "v_permlane16_swap_b32 v9, v13\n" \
    "v_permlane16_swap_b32 v10, v12\n" \
    "s_nop 0\n" \
    "v_max_u32_dpp v9, v13, v9 quad_perm:[3,2,1,0] row_mask:0xf bank_mask:0xf\n" \
    "v_max_u32_dpp v10, v12, v10 row_half_mirror row_mask:0xf bank_mask:0xf\n" \
    "s_nop 0\n" \
    "v_mov_b32_dpp v11, v9 quad_perm:[2,3,0,1] row_mask:0xf bank_mask:0xf\n" \
    "v_med3_u32 v9, v9, v11, %[c2]\n" \
    "v_mov_b32_dpp v8, v10 row_ror:4 row_mask:0xf bank_mask:0xf\n" \
    "v_med3_u32 v10, v10, v8, %[c4]\n" \
    "v_mov_b32_dpp v11, v9 quad_perm:[1,0,3,2] row_mask:0xf bank_mask:0xf\n" \
    "v_med3_u32 v9, v9, v11, %[c1]\n" \
    "v_mov_b32_dpp v8, v10 quad_perm:[2,3,0,1] row_mask:0xf bank_mask:0xf\n" \
    "v_med3_u32 v10, v10, v8, %[c2]\n" \
    "v_mov_b32_e32 v13, v9\n" \
    "s_nop 0\n" \
    "v_mov_b32_dpp v8, v10 quad_perm:[1,0,3,2] row_mask:0xf bank_mask:0xf\n" \
    "v_med3_u32 v10, v10, v8, %[c1]\n" \
    "v_permlane32_swap_b32 v9, v13\n" \
    "v_mov_b32_e32 v12, v10\n" \
    "s_nop 0\n" \
    "v_max_u32_dpp v9, v13, v9 quad_perm:[3,2,1,0] row_mask:0xf bank_mask:0xf\n" \
    "v_permlane32_swap_b32 v10, v12\n" \
    "s_nop 0\n" \
    "v_mov_b32_dpp v11, v9 quad_perm:[2,3,0,1] row_mask:0xf bank_mask:0xf\n" \
    "v_med3_u32 v9, v9, v11, %[c2]\n" \
    "v_max_u32_dpp v10, v12, v10 row_half_mirror row_mask:0xf bank_mask:0xf\n" \
    "s_nop 0\n" \
    "v_mov_b32_dpp v11, v9 quad_perm:[1,0,3,2] row_mask:0xf bank_mask:0xf\n" \
    "v_med3_u32 v9, v9, v11, %[c1]\n" \
    "v_mov_b32_dpp v8, v10 row_ror:4 row_mask:0xf bank_mask:0xf\n" \
    "v_med3_u32 v10, v10, v8, %[c4]\n" \
    "v_and_b32_e32 v9, v9, %[c4]\n" \
    "s_nop 0\n" \
    "v_mov_b32_dpp v8, v10 quad_perm:[2,3,0,1] row_mask:0xf bank_mask:0xf\n" \
    "v_med3_u32 v10, v10, v8, %[c2]\n" \
    "s_nop 1\n" \
    "v_mov_b32_dpp v8, v10 quad_perm:[1,0,3,2] row_mask:0xf bank_mask:0xf\n" \
    "v_med3_u32 v10, v10, v8, %[c1]\n" \
    "v_max_u32_dpp v10, v9, v10 row_half_mirror row_mask:0xf bank_mask:0xf\n" \
    "s_nop 1\n" \
    "v_mov_b32_dpp v8, v10 row_ror:4 row_mask:0xf bank_mask:0xf\n" \
    "v_med3_u32 v10, v10, v8, %[c4]\n" \
    "s_nop 1\n" \
    "v_mov_b32_dpp v8, v10 quad_perm:[2,3,0,1] row_mask:0xf bank_mask:0xf\n" \
    "v_med3_u32 v10, v10, v8, %[c2]\n" \
    "s_nop 1\n" \
    "v_mov_b32_dpp v8, v10 quad_perm:[1,0,3,2] row_mask:0xf bank_mask:0xf\n" \
    "v_med3_u32 v10, v10, v8, %[c1]\n" \
    "v_mov_b32_e32 %[g8], v10\n" \
    "s_nop 0\n"
#define NET_CLOB "v8", "v9", "v10", "v11", "v12", "v13", "v15", "v16", "v17", "v18", "v28", "v29", "v30"

DI void conv_final(const Params& P, lptr sm, int cg, int xcd, u32 tag, v2u t1) {
    const int lane = fresh_lane();
    const __amdgpu_buffer_rsrc_t rws = ws_rsrc(P), ro = mk_rsrc(P.out, L_ * D_ * 2);
    const int j = (lane & 4) + (lane >> 4), cc = lane & 3;
    u32 po = WS_PART + (u32)cg * (72u * 128u) + (u32)lane * 16u;
    PIN(po);
    clptr fw = sm + LD_TB + ROW_FU * TR + (lane & 3) * 516 + 256;
    __builtin_amdgcn_s_setprio(FPRIO);
    wait_ge<1>(fw, 10);
    u32 sel;
    {
        v4u e0 = W128(sm + LD_KT + 8448 + lane * 16), e1 = W128(sm + LD_KT + 9472 + lane * 16), e2 = W128(sm + LD_KT + 10496 + lane * 16), e3 = W128(sm + LD_KT + 11520 + lane * 16);
        u32 g8, tc;
        asm volatile(NET_ASM : [g8] "=&v"(g8), [tc] "=&v"(tc), "+{v[0:3]}"(e0), "+{v[4:7]}"(e1), "+{v[20:23]}"(e2), "+{v[24:27]}"(e3)
                     : [c4] "v"((lane & 4) ? 0u : ~0u), [c2] "v"((lane & 2) ? 0u : ~0u), [c1] "v"((lane & 1) ? 0u : ~0u) : NET_CLOB);
        if (__builtin_expect(__any(tc > rdl(g8, 7)), 0)) {
            u32 all[32];
#pragma unroll
            for (int q = 0; q < 32; ++q) all[q] = (W32(sm + LD_KT + lane * 4 + q * 256) & 0xffff0000u) | (u32)(2047 - 64 * q - lane);
            g8 = top8_slow(all, lane);
        }
        sel = from_lane(g8, j);
    }
    const u32 tok = 2047u - (sel & 0x7ffu);
    wait_ge<1>(sm + LD_TB + ROW_FL * TR + (lane & 3) * 516 + (lane & 4) * 64, 18);
    const float rs = sigmoidf_(bf_hi(sel ^ ((~(u32)((int)sel >> 31)) | 0x80000000u)));
    const float x = bf_lo((u32)W16(sm + LD_U + cc * U_CH + 2 * U_PAD + tok * 2u));
    const u32 c = (tok >> 4) & 15u;
    clptr ap = sm + LD_TB + (tok >> 7) * TR + cc * 512 + (c & 7u) * 64u + (((tok >> 2) ^ (c >> 1)) & 3u) * 16u + (tok & 3u) * 4u;
    const float a2 = *(const volatile LAS float*)(ap + (ROW_T0 - 4) * TR);
    const float a = *(const volatile LAS float*)ap + (tok >= 512u ? a2 : 0.f);
    const float g = g_of(W32(sm + LD_KT + tok * 4u));
    float gs = (1.f - g) * rs, base = x + g * (a - x);
    u32 oo = (lane & 9) ? OOB : (tok * D_ + (u32)(4 * cg + cc)) * 2u;
    PIN(base); PIN(gs); PIN(oo); PIN(fw);
    u32 po2 = po + 5120u;
    const u32 at0 = rfl(W32(sm + LD_KT + 8192)) + PART_AT;
    PIN(po2);
#pragma unroll 1
    for (int w = 0; w < 4096; ++w) {
        u32 tg1, at1;
        asm volatile("v_readfirstlane_b32 %0, v108\n\tv_readfirstlane_b32 %1, v109" : "=s"(tg1), "=s"(at1), "+{v[108:109]}"(t1));
        if ((int)((u32)__builtin_amdgcn_s_memrealtime() - (tg1 == tag ? at1 : at0)) >= 0) break;
        __builtin_amdgcn_s_sleep(1);
    }
    float s0, s1, s2, s3;
    u32 n;
#define PLD(k, a, o) "global_load_dwordx4 v[64+4*" #k ":67+4*" #k "], " a ", %[ws] offset:" #o " sc1\n\t"
#define PAD(k, w) "s_waitcnt vmcnt(" #w ")\n\tv_dot2c_f32_bf16 %[s0], %[lo], v[64+4*" #k "]\n\tv_dot2c_f32_bf16 %[s1], %[hi], v[64+4*" #k "]\n\t" \
                  "v_dot2c_f32_bf16 %[s2], %[lo], v[66+4*" #k "]\n\tv_dot2c_f32_bf16 %[s3], %[hi], v[66+4*" #k "]\n\t" \
                  "v_bitop3_b32 v[65+4*" #k "], v[65+4*" #k "], v[67+4*" #k "], %[tag] bitop3:0x7e\n\t"
    asm volatile("s_mov_b32 %[n], 0x20000\n\tv_mov_b32 v100, 1\n"
                 "Lr%=:\n\tv_cmp_ne_u32 vcc, 0, v100\n\tv_mov_b32 %[s0], 0\n\tv_mov_b32 %[s1], 0\n\tv_mov_b32 %[s2], 0\n\tv_mov_b32 %[s3], 0\n\ts_mov_b64 exec, vcc\n\t"
                 PLD(0, "%[p0]", 0) PLD(1, "%[p0]", 1024) PLD(2, "%[p0]", 2048) PLD(3, "%[p0]", 3072) PLD(4, "%[p1]", -1024)
                 PLD(5, "%[p1]", 0) PLD(6, "%[p1]", 1024) PLD(7, "%[p1]", 2048) PLD(8, "%[p1]", 3072) "s_mov_b64 exec, -1\n\t"
                 PAD(0, 8) PAD(1, 7) PAD(2, 6) "v_or3_b32 v100, v65, v69, v73\n\t" PAD(3, 5) PAD(4, 4) "v_or3_b32 v100, v100, v77, v81\n\t"
                 PAD(5, 3) PAD(6, 2) "v_or3_b32 v100, v100, v85, v89\n\t" PAD(7, 1) PAD(8, 0) "v_or3_b32 v100, v100, v93, v97\n\t"
                 "v_cmp_ne_u32 vcc, 0, v100\n\ts_cbranch_vccz Le%=\n\t"
                 "v_mov_b32 v65, %[tag]\n\tv_mov_b32 v69, %[tag]\n\tv_mov_b32 v73, %[tag]\n\tv_mov_b32 v77, %[tag]\n\tv_mov_b32 v81, %[tag]\n\t"
                 "v_mov_b32 v85, %[tag]\n\tv_mov_b32 v89, %[tag]\n\tv_mov_b32 v93, %[tag]\n\tv_mov_b32 v97, %[tag]\n\t"
                 "s_sleep %[gap]\n\ts_sub_u32 %[n], %[n], 1\n\ts_cbranch_scc0 Lr%=\n"
                 "Le%=:\n\ts_nop 1"
                 : [s0] "=&v"(s0), [s1] "=&v"(s1), [s2] "=&v"(s2), [s3] "=&v"(s3), [n] "=&s"(n), "+{v[108:109]}"(t1)
                 : [p0] "v"(po), [p1] "v"(po2), [ws] "s"(P.ws), [tag] "s"(tag), [lo] "s"(0x00003f80u), [hi] "s"(0x3f800000u), [gap] "n"(PART_GAP)
                 : "memory", "vcc", "scc", "v64", "v65", "v66", "v67", "v68", "v69", "v70", "v71", "v72", "v73", "v74", "v75", "v76", "v77", "v78", "v79", "v80", "v81", "v82", "v83",
                   "v84", "v85", "v86", "v87", "v88", "v89", "v90", "v91", "v92", "v93", "v94", "v95", "v96", "v97", "v98", "v99", "v100");
    wait_ge<0>(fw, 20);
    const auto a3 = __builtin_amdgcn_permlane32_swap(__float_as_uint(s0), __float_as_uint(s2), false, false);
    const auto b2 = __builtin_amdgcn_permlane32_swap(__float_as_uint(s1), __float_as_uint(s3), false, false);
    const auto e = __builtin_amdgcn_permlane16_swap(__float_as_uint(__uint_as_float(a3[0]) + __uint_as_float(a3[1])), __float_as_uint(__uint_as_float(b2[0]) + __uint_as_float(b2[1])), false, false);
    float y = __uint_as_float(e[0]) + __uint_as_float(e[1]);
    y += dppf<0x128, 0xf>(y);
    const float o = base + gs * y;
    __builtin_amdgcn_raw_buffer_store_b32(pk_bf16(o, dppf<0xB1, 0xf>(o)), ro, oo, 0, 0);
}

template <int kind> DI void conv_wave(const Params& P, lptr sm, int ch, int xcd, int ii, u32 labw) {
    const int cg = cg_of(xcd, ii);
    const u32 m0a = (u32)(LD_TB + ch * 512 + (kind & 1) * 256);
    u32 epoch;
    if (kind == 0) {
        asm volatile("; claim\n" CONV_A_ASM : : [m0a] "s"(m0a) : "memory", "vcc", "scc", "s90", CONV_A_CLOB);
    } else if (kind == 1) {
        v2u t1;
        asm volatile("; claim\n" CONV_B_ASM : [ep] "=&s"(epoch), "=&{v[108:109]}"(t1) : [m0a] "s"(m0a), [tp] "s"(P.ws + WS_T1 + xcd * 128) : "memory", "vcc", "scc", "s90", "s92", CONV_B_CLOB);
        if (ch == 0) conv_final(P, sm, cg, xcd, tag_of(epoch), t1);
    } else {
        const Lab lb = lab_of(labw);
        const u32 nl = xcd < 2 ? 6u * lb.n : 9u * lb.n, u0 = (u32)(4 * ii + ch);
        const char* pw = (const char*)P.wd + (lb.m0 * 576u + u0 * nl) * 128u;
        asm volatile("; claim\n" CONV_C_ASM : [ep] "=&s"(epoch)
                     : [xb] "s"((const char*)P.x + (umin((xcd < 2 ? 22u : 32u) * u0, xcd < 2 ? 2026u : 2016u) * (D_ * 2) + 128 * xcd)), [pl] "s"(xcd < 2 ? 43 : 31), [ps] "s"(1u >> (xcd >> 1)), [pd] "s"(xcd < 2 ? 1024 - 128 * xcd : 0), [x0] "s"((const char*)P.x + cg * 8), [wd] "s"(pw), [wl] "s"((nl - 1u) * 128u), [o0] "s"((char*)P.out + cg * 8), [ws] "s"(P.ws), [ak] "s"(WS_ACK + (u32)(8 * ii + xcd) * 128u),
                       [bs] "s"((const char*)&conv_tab + 61440 + ch * 1024), [bl] "s"(LD_BD + ch * 1024), [tg] "s"(11 + (ch >> 1)), [am] "s"(1u >> ch), [gt] "n"(LD_KT - (int)WS_KEYS / 2), [kt] "n"(LD_KT + 8192), [kt2] "n"(LD_KT + 8448 - (int)WS_KEYS), [tr] "s"(WS_T0 + (u32)xcd * 128u), [ko] "n"(2047 + (int)WS_KEYS / 8), [pp] "s"(P.ws + cg * (72 * 128))
                     : "memory", "vcc", "scc", "s88", "s89", "s90", "s91", "s92", "s93", "s94", "s95", "s96", "s97", "s98", "s99", CONV_C_CLOB);
    }
}

DI void select8(const Params& P, int lane, int xcd, u32 tag, u32 t_in) {
    const __amdgpu_buffer_rsrc_t rws = ws_rsrc(P);
    const v2u t0 = ld2_sc1(rws, TREF && xcd >= 4 ? WS_T0 + (u32)xcd * 128u : OOB);
    const u32 co = WS_CAND + (u32)xcd * 16384u + (u32)lane * 128u, fi = (u32)(63 - lane) << 4;
    const u32 xk = 2047u * (D_ * 2) + (u32)(((lane & 7) ^ ((lane >> 3) & 6)) * 16);
    const u32 d = rfl(t0[0]) == tag ? t_in - rfl(t0[1]) : 0u, at = t_in + SEL_AT - (d < (u32)SEL_AT ? d : 0u);
    u32 g8, tc, ps;
    asm volatile(SEL_ASM
                 : [g8] "=&v"(g8), [tc] "=&v"(tc), [ps] "=&s"(ps)
                 : [co] "v"(co), [co2] "v"(co + 8192u), [fi] "v"(fi), [f0] "v"(fi | 1024u), [c4] "v"((lane & 4) ? 0u : ~0u), [c2] "v"((lane & 2) ? 0u : ~0u), [c1] "v"((lane & 1) ? 0u : ~0u),
                   [mk] "v"(0xffff000fu), [xk] "v"(xk), [pm] "s"(0x05010d0du), [ws] "s"(P.ws), [x] "s"(P.x), [tag] "s"(tag), [at] "s"(at), [xs] "n"(LD_XS), [gap] "n"(SEL_GAP)
                 : "memory", "vcc", "scc", "s92", "s93", SEL_CLOB);
    if (__builtin_expect(__any(tc > rdl(g8, 7)), 0)) {
        u32 all[32];
#pragma unroll 1
        for (int spin = 0; spin < SPIN_CAP; ++spin) {
            MEMFENCE();
            bool ok = true;
#pragma unroll
            for (int q = 0; q < 16; ++q) {
                const v4u e = ld_sc1(rws, WS_KEYS + (u32)lane * 16u + q * 1024);
                ok &= tag_ok(e, tag);
                all[2 * q] = (e[0] & 0xffff0000u) | (u32)(2047 - 128 * q - 2 * lane); all[2 * q + 1] = (e[2] & 0xffff0000u) | (u32)(2046 - 128 * q - 2 * lane);
            }
            if (__all(ok)) break;
            __builtin_amdgcn_s_sleep(8);
        }
        g8 = top8_slow(all, lane);
        VMWAIT(0);
        dma_rows<1024>(P.x, xk - (from_lane(g8, lane >> 3) & 0x7ffu) * (D_ * 2), LD_XS);
    }
}

DI void moe_weights(const Params& P, lptr sm, int wave, int midx, u32 nit) {
    v4u wb[24];
    u64 base;
    asm volatile("; claim4" : "={v[96:99]}"(wb[0]), "={v[100:103]}"(wb[1]), "={v[104:107]}"(wb[2]), "={v[108:111]}"(wb[3]), "={v[12:13]}"(base));
    const int lane = fresh_lane();
    const u32 wo = (u32)(((lane & 7) ^ ((lane >> 3) & 6)) * 16);
    const int c = lane & 15, kg = lane >> 4;
    auto ldw = [&](int k) {
        asm volatile("global_load_dwordx4 %0, %1, off" : "=v"(wb[k]) : "v"(base) : "memory");
        base += (u64)(((k & 7) == 7 ? 173 : ((k & 7) == 3 ? 13 : 1)) * (F_ * 2));
        SB();
    };
    base += (u64)(12 * (F_ * 2));
#pragma unroll
    for (int k = 4; k < WIN0; ++k) ldw(k);
    const int w3 = wave >= 3 ? wave - 3 : wave, r8 = wave >= 3;
    u32 src = (u32)((midx * 64 + 8 * r8 + (lane >> 3)) * (D_ * 2) + w3 * 384) + wo, dst = LD_WD + w3 * 24576 + r8 * 1024;
    lptr rdw = sm + LD_RED + wave * 4096 + lane * 16;
    clptr rdr = sm + LD_RED + (wave & 3) * 1024 + (lane & 55) * 16 + (c >> 3) * 8;
    lptr hbw = sm + LD_HB + (4 * (kg & 1) + 2 * (c >> 3)) * HB_ROW + 2 * (8 * (c & 7) + (wave & 3) + 4 * (kg >> 1));
    clptr hs = sm + LD_HB + (c & 7) * HB_ROW + kg * 16;
    u32 po[3];
#pragma unroll
    for (int t = 0; t < 3; ++t) { po[t] = WS_PART + (u32)(((4 * (6 * wave + t + 3 * (kg >> 1)) + (c >> 2)) * 72 + midx) * 8 + 4 * (kg & 1) + (c & 3)) * 16u; PIN(po[t]); }
    const int kq = 4 * wave + kg;
    clptr xa = sm + LD_XS + (kq >> 3) * 1024 + (c & 7) * 128 + (((kq & 7) ^ (c & 6)) * 16);
    u32 idx = c < 8 ? 0x4444u : 0xeeeeu;
    clptr db[3][2];
#pragma unroll
    for (int t = 0; t < 3; ++t) {
        const int r = (lane >> 2) & 3, p = lane & 3, tile = 6 * wave + t + 3 * (r >> 1);
        db[t][0] = sm + LD_WD + (tile >> 2) * 8192 + (4 * kg + (r & 1)) * 128 + (((2 * (tile & 3) + (p >> 1)) ^ (4 * (kg & 1))) * 16) + 8 * (p & 1);
        db[t][1] = (clptr)((u32)(uintptr_t)db[t][0] ^ 32u);
        PIN(db[t][0]); PIN(db[t][1]);
    }
    PIN(rdw); PIN(rdr); PIN(hbw); PIN(hs); PIN(xa); PIN(idx); PIN(src);
    SB();
    v8u gf[3][4];
    {
        if (WIN0 < WIN && nit) wait_ge<1>(sm + FL_IT + 4 * umin(lane, nit - 1), 1);
#pragma unroll
        for (int k = WIN0; k < WIN; ++k) ldw(k);
        auto done = [&](int d) {
            if (d & 1) {
                asm volatile("" : "+v"(wb[d - 1]), "+v"(wb[d]));
                const v4u lo = wb[d - 1], hi = wb[d];
#pragma unroll
                for (int m = 0; m < 4; ++m) {
                    const int e = (d & 6) + (m >> 1);
                    gf[d >> 3][2 * (m & 1)][e] = perm_lo(hi[m], lo[m]); gf[d >> 3][2 * (m & 1) + 1][e] = perm_hi(hi[m], lo[m]);
                    PIN(gf[d >> 3][2 * (m & 1)][e]); PIN(gf[d >> 3][2 * (m & 1) + 1][e]);
                }
                SB();
            }
        };
#pragma unroll
        for (int k = WIN; k < 24; ++k) {
            VMWAIT(WIN - 1);
            ldw(k);
            done(k - WIN);
        }
        int nw = 0;
#pragma unroll
        for (int k = 24 - WIN; k < 24; ++k) {
            VMWAIT(23 - k + nw);
#pragma unroll
            for (; nw < (k - 23 + WIN) * NWE / WIN; ++nw) {
                asm volatile("s_mov_b32 m0, %2\n\ts_nop 0\n\tglobal_load_lds_dwordx4 %0, %1 offset:%3" ::"v"(src), "s"(P.wd), "s"(dst + (u32)((nw % 3) * 8064 + nw / 3 * 2048)), "n"((nw % 3) * 128) : "memory");
                if (nw % 3 == 2) src += 16 * D_ * 2;
            }
            done(k);
        }
    }

    asm volatile(FFN_ASM
                 : "+{v[0:7]}"(gf[0][0]), "+{v[8:15]}"(gf[0][1]), "+{v[16:23]}"(gf[0][2]), "+{v[24:31]}"(gf[0][3]), "+{v[32:39]}"(gf[1][0]), "+{v[40:47]}"(gf[1][1]),
                   "+{v[48:55]}"(gf[1][2]), "+{v[56:63]}"(gf[1][3]), "+{v[64:71]}"(gf[2][0]), "+{v[72:79]}"(gf[2][1]), "+{v[80:87]}"(gf[2][2]), "+{v[88:95]}"(gf[2][3])
                 : [xa] "v"(xa), [rdw] "v"(rdw), [rdr] "v"(rdr), [hbw] "v"(hbw), [hs] "v"(hs), [idx] "v"(idx), [d00] "v"(db[0][0]), [d01] "v"(db[0][1]), [d10] "v"(db[1][0]), [d11] "v"(db[1][1]),
                   [d20] "v"(db[2][0]), [d21] "v"(db[2][1]), [po0] "v"(po[0]), [po1] "v"(po[1]), [po2] "v"(po[2]), [ws] "s"(P.ws), [wv] "s"(wave), [tg] "n"(LD_TAG)
                 : "memory", "scc", FFN_CLOB);
}

DI void helper_prefetch(const Params& P, int lane, u32 idx, u32 cnt, Lab lb) {
    const u32 c3 = (3u - (u32)((u64)P.wg >> 7) - lb.m0) & 7u, c3u = (3u - (u32)((u64)P.wu >> 7) - lb.m0) & 7u;
    const u32 n3 = (c3 < lb.n ? 1u : 0u) + (c3 + 8u < lb.n ? 1u : 0u), nl = lb.n - n3;
    const u32 mg2 = (u32)(65536.f * __builtin_amdgcn_rcpf((float)nl)) + 1u;
    const u32 rpu = (64u * mg2) >> 16, r = umin(((u32)lane * mg2) >> 16, rpu - 1u);
    u32 l = umin((u32)lane - nl * r, nl - 1u);
    l += l >= c3 ? 1u : 0u; l += l >= c3 + 8u ? 1u : 0u;
    const u32 go = r * (F_ * 2) + (lb.m0 + l) * 128u;
    constexpr u32 NSG = (D_ - SLOW0) / 64;
    u32 u = idx;
#pragma unroll 1
    for (; u < 2u * NSG * n3; u += cnt) {
        VMWAIT(HWIN - 1);
        const u32 h = n3 == 2u ? u >> 2 : u >> 1;
        touch((u & 1) ? P.wu : P.wg, (SLOW0 + h * 64u + (u32)lane) * (F_ * 2) + umin(lb.m0 + (n3 == 2u && (u & 2u) ? 8u : 0u) + ((u & 1) ? c3u : c3), 71u) * 128u);
    }
#pragma unroll 1
    for (u -= 2u * NSG * n3; HR0 + (u >> 1) * rpu < D_; u += cnt) {
        VMWAIT(HWIN - 1);
        touch((u & 1) ? P.wu : P.wg, go + umin(HR0 + (u >> 1) * rpu, D_ - rpu) * (F_ * 2));
    }
}

#define TAB_CLOB "v148", "v149", "v150", "v151", "v152", "v153", "v154", "v155", "v156", "v157", "v158", "v159", "v160", "v161", "v162", "v163", "v164", "v165", "v166", "v167", "v32", "v33", "v34", "v35", "v36", "v37", "v38", "v39", "v40", "v41", "v42", "v43", "v44", "v45", "v46", "v47", "v48", "v49", "v50", "v51"
#define ENTRY_CLOB TAB_CLOB, "v137", "v138", "v139", "v140", "v141", "v142", "v143", "v144", "v145", "v146", "v147", "v64", "v65", "v66", "v67", "v68", "v69", "v70", "v71", "v72", "v73", "v74", "v75", "v76", "v77", "v78", "v79", "v80", "v81", "v82", "v83", "v84", "v85", "v86", "v87", "v88", "v89", "v90", "v91", "v92", "v93", "v94", "v95", "v96", "v97", "v98", "v99", "v100", "v101", "v102", "v103", "v104", "v105", "v106", "v107", "v108", "v109", "v110", "v111", "v112", "v113", "v114", "v115", "v116", "v117", "v118", "v119", "v120", "v121", "v122", "v123", "v124", "v125", "v126", "v127", "v128", "v129", "v130", "v131", "v132", "v133", "v134", "v135", "v136"
__global__ void __launch_bounds__(NTHR, 3) fused_kernel(Args) {
    __shared__ __attribute__((aligned(16))) unsigned char smem[LDS_BYTES];
    lptr sm = (lptr)smem;
    const int b = blockIdx.x;
    v16u ka0, ka1;
    int wave, item;
    u32 role, labw, tid = threadIdx.x;
    u64 t64;
#define LX(q, a, o) "global_load_dwordx4 v[96+4*(" #q "):99+4*(" #q ")], " a ", s[54:55] offset:" #o "\n\t"
#define LXS(s) LX(s, "v7", -2304+256*s) LX(4+s, "v7", 2304+256*s) LX(8+s, "v9", -2304+256*s) LX(12+s, "v9", 2304+256*s)
#define LG(q) "global_load_dwordx4 v[96+4*" #q ":99+4*" #q "], v[12:13], off\n\tv_lshl_add_u64 v[12:13], v[12:13], 0, s[54:55]\n\t"
#define NPRE 0
#define EARLY_LOADS "s_barrier\n\t" LG(0) LG(1) LG(2) LG(3)
#define LWS(p) "global_load_lds_dwordx4 v2, " p "\n\ts_mov_b64 exec, 0xff\n\tglobal_load_lds_dwordx4 v2, " p " offset:1024\n\ts_mov_b64 exec, -1\n\t"
    asm volatile("s_load_dwordx16 s[36:51], %[kap], 0x0\n\ts_load_dwordx16 s[16:31], %[kap], 0x40\n\t"
                 "v_readfirstlane_b32 %[w], %[tid]\n\ts_lshr_b32 s53, %[b], 3\n\tv_and_b32 v1, 63, %[tid]\n\ts_sub_u32 s56, s53, 24\n\t"
                 "s_cmpk_eq_u32 %[w], 640\n\ts_cselect_b32 s56, s56, -1\n\ts_cmp_lt_u32 s56, 8\n\ts_cbranch_scc0 Lb%=\n\t"
                 "s_getpc_b64 s[54:55]\n\ts_lshl_b32 s56, s56, 12\n\tv_min_u32 v3, 31, v1\n\tv_lshl_add_u32 v3, v3, 7, s56\n\tv_min_u32 v3, %[ce], v3\n\t"
                 "global_load_dword v3, v3, s[54:55]\n\ts_barrier\n\ts_waitcnt vmcnt(0)\n"
                 "Lx%=:\n\ts_waitcnt lgkmcnt(0)\n\ts_endpgm\n"
                 "Lb%=:\n\ts_and_b32 s52, %[b], 7\n\ts_lshl_b32 s57, s52, 7\n\ts_lshl2_add_u32 s55, s53, s57\n\t"
                 "s_load_dword %[rl], %[kap], s55 offset:0x80\n\ts_load_dword %[lb], %[kap], s57 offset:0x80\n\t"
                 "v_mov_b32 v0, 0\n\ts_cmp_lt_u32 s52, 2\n\ts_cselect_b32 s54, 24, 16\n\t"
                 "s_lshr_b32 %[w], %[w], 6\n\tv_lshlrev_b32 v2, 4, v1\n\ts_cmp_ge_u32 s53, s54\n\ts_cbranch_scc1 Ln%=\n\t"
                 "s_mul_i32 s54, %[w], 5120\n\tv_add_u32 v3, s54, v2\n\ts_and_b32 s55, %[w], 3\n\t"
                 "s_cmp_lt_u32 %[w], 8\n\ts_cbranch_scc1 Lt%=\n\ts_setprio 3\n\tv_add_u32 v4, 0x1000, v3\n\t"
                 "global_load_dwordx4 v[44:47], v3, %[tab] offset:3072\n\tglobal_load_dwordx4 v[48:51], v4, %[tab]\n\tglobal_load_dwordx4 v[40:43], v3, %[tab] offset:2048\n"
                 "Lt%=:\n\ts_lshl4_add_u32 s54, s52, s53\n\ts_lshl3_add_u32 s56, s52, s53\n\ts_addk_i32 s56, 112\n\ts_cmp_lt_u32 s53, 16\n\ts_cselect_b32 s54, s54, s56\n\t"
                 "s_lshl2_add_u32 s54, s54, s55\n\t"
                 "s_bfe_u32 s56, %[w], 0x10002\n\ts_lshl1_add_u32 s55, s55, s56\n\ts_lshl_b32 m0, s55, 8\n\t"
                 "s_lshl1_add_u32 s56, s54, s56\n\ts_lshl_b32 s56, s56, 7\n\ts_lshl_b32 s54, s54, 1\n\t"
                 "v_lshl_add_u32 v4, v1, 1, s56\n\tv_mov_b32 v5, s54\n\tv_mov_b32 v6, 0x3f80\n\t"
                 "ds_write_addtid_b32 v0 offset:%[fl]\n\tds_write_addtid_b32 v0 offset:%[fu]\n\tds_write_addtid_b32 v6\n\t"
                 "v_mov_b64 v[64:65], 0\n\tv_mov_b64 v[66:67], 0\n\tv_mov_b64 v[68:69], 0\n\tv_mov_b64 v[70:71], 0\n\ts_waitcnt lgkmcnt(0)\n\t"
                 "s_cmp_ge_u32 %[w], 8\n\ts_cbranch_scc1 Lc%=\n\t"
                 "global_load_short_d16_hi v64, v4, s[38:39]\n\tglobal_load_short_d16_hi v65, v4, s[40:41]\n\tglobal_load_short_d16_hi v70, v5, s[50:51]\n\t"
                 "global_load_short_d16_hi v66, v4, s[42:43]\n\tglobal_load_short_d16_hi v67, v4, s[44:45]\n\tglobal_load_short_d16_hi v68, v4, s[46:47]\n\t"
                 "global_load_short_d16_hi v69, v4, s[48:49]\n"
                 "Lc%=:\n\ts_barrier\n\tglobal_load_short_d16_hi v71, v5, s[16:17]\n\tglobal_load_dword v72, v0, s[30:31] sc1\n\t"
                 "global_load_dwordx4 v[32:35], v3, %[tab]\n\tglobal_load_dwordx4 v[36:39], v3, %[tab] offset:1024\n\ts_branch Lw%=\n"
                 "Ln%=:\n\ts_mov_b32 m0, 0\n\ts_bitcmp1_b32 0x302, %[w]\n\tds_write_addtid_b32 v0\n\ts_cbranch_scc0 Lo%=\n\t"
                 "v_and_b32 v9, 0x330, v2\n\tv_bfe_u32 v7, v1, 2, 2\n\tv_lshrrev_b32 v8, 2, v9\n\tv_and_b32 v9, 48, v9\n\tv_and_or_b32 v9, v8, %[c0c], v9\n\t"
                 "v_mad_u32_u24 v7, v7, %[rb], v9\n\tv_lshrrev_b32 v8, 5, v1\n\tv_add_u32 v7, 2304, v7\n\ts_sub_i32 s56, %[w], 7\n\ts_max_i32 s56, s56, 0\n\t"
                 "v_mul_u32_u24 v8, 4480, v8\n\tv_add_u32 v9, 9216, v7\n\tv_add_u32 v8, v8, v7\n\tv_add_u32 v11, 9216, v8\n\ts_mov_b32 m0, %[wl]\n\t"
                 "s_waitcnt lgkmcnt(0)\n\ts_lshr_b32 s54, %[rl], 16\n\ts_cmp_ge_u32 s56, s54\n\ts_cbranch_scc1 Lx%=\n\t"
                 "s_bfe_u32 s54, %[rl], 0x80008\n\ts_add_u32 %[it], s54, s56\n\ts_mul_i32 s54, %[it], 18432\n\ts_add_u32 s54, s54, s36\n\ts_addc_u32 s55, s37, 0\n\t"
                 "global_load_dword v72, v0, s[30:31] sc1\n\t"
                 LWS("s[18:19]") "s_addk_i32 m0, 1152\n\ts_nop 0\n\t" LWS("s[26:27]")
                 LXS(0) LXS(1) LXS(2) LXS(3) LX(16, "v8", -1280) LX(17, "v11", -1280) "s_barrier\n\ts_branch Lw%=\n"
                 "Lo%=:\n\ts_cmp_lt_u32 %[w], 8\n\ts_cbranch_scc0 Lx%=\n\t"
                 "v_lshrrev_b32 v7, 4, v1\n\ts_lshl_b32 s56, %[w], 3\n\ts_sub_u32 s56, s56, 16\n\tv_add_u32 v7, s56, v7\n\tv_and_b32 v8, 0x70, v2\n\tv_mul_u32_u24 v7, 36864, v7\n\t"
                 "v_add_u32 v7, v7, v8\n\ts_mov_b32 s58, 0xff00ff00\n\ts_mov_b32 s59, s58\n\ts_mov_b64 s[54:55], 9216\n\ts_cmp_lg_u32 %[w], 0\n\t"
                 "s_waitcnt lgkmcnt(0)\n\ts_cbranch_scc1 Lp%=\n\ts_memrealtime %[ti]\n\tglobal_load_dword v136, v0, s[30:31] sc1\n\ts_barrier\n\ts_branch Lw%=\n"
                 "Lp%=:\n\ts_lshl_b32 s56, %[rl], 7\n\ts_and_b32 s56, s56, 0x7f80\n\ts_cbranch_scc0 Lv%=\n\ts_sub_u32 s56, s56, 128\n\t"
                 "v_add_u32 v8, s56, v7\n\tv_mov_b32 v9, 0\n\tv_lshl_add_u64 v[12:13], v[8:9], 0, s[20:21]\n\ts_mov_b64 exec, s[58:59]\n\tv_lshl_add_u64 v[12:13], v[8:9], 0, s[22:23]\n\ts_mov_b64 exec, -1\n\t"
                 EARLY_LOADS "s_branch Lw%=\n"
                 "Lv%=:\n\ts_barrier\n"
                 "Lw%=:\n\ts_waitcnt lgkmcnt(0)"
                 : "=&{s[36:51]}"(ka0), "=&{s[16:31]}"(ka1), [w] "=&s"(wave), [it] "=&s"(item), [rl] "=&s"(role), [lb] "=&s"(labw), [ti] "=&s"(t64), [tid] "+{v0}"(tid)
                 : [b] "s"(b), [kap] "s"(__builtin_amdgcn_kernarg_segment_ptr()), [tab] "s"(&conv_tab), [rb] "s"(D_ * 2), [c0c] "s"(0xc0),
                   [fl] "n"(ROW_FL * TR), [fu] "n"(ROW_FU * TR), [ce] "n"(CODE_KB * 1024 - 128), [wl] "n"(LD_KA)
                 : "memory", "scc", "s52", "s53", "s54", "s55", "s56", "s57", "s58", "s59", "v1", "v2", "v3", "v4", "v5", "v6", "v7", "v8", "v9", "v10", "v11", "v12", "v13", ENTRY_CLOB);
    Params P;
#define KARG(T, v, i) (T)(((u64)v[2 * (i) + 1] << 32) | v[2 * (i)])
    P.x = KARG(const u16*, ka0, 0); P.wg = KARG(const u16*, ka1, 2); P.wu = KARG(const u16*, ka1, 3); P.wd = KARG(const u16*, ka1, 4);
    P.out = KARG(u16*, ka1, 6); P.ws = KARG(unsigned char*, ka1, 7);
    const int xcd = b & 7, ii = b >> 3, nconv = xcd < 2 ? 24 : 16;
    if (ii < nconv) {
        if (wave >= 8) { conv_wave<2>(P, sm, wave - 8, xcd, ii, labw); EXIT(); }
        if (wave >= 4) { conv_wave<1>(P, sm, wave - 4, xcd, ii, labw); EXIT(); }
        conv_wave<0>(P, sm, wave, xcd, ii, labw);
        EXIT();
    }
    if (wave == 1 || wave >= 8) {
        asm volatile("; claim\n\ts_setprio 3\n" ITEM_ASM : : [ws] "s"(P.ws), [it] "s"(item), [fl] "s"(FL_IT + 4 * (wave == 1 ? 0 : wave - 7)), [kb] "n"(WS_KEYS), [cb] "n"(WS_CAND), [wl] "n"(LD_KA)
                     : "memory", "vcc", "scc", "s88", "s89", "s92", "s93", "s98", ITEM_CLOB);
        EXIT();
    }
    const bool moe = (role & 255u) != 0u;
    if (moe && wave >= 2) { moe_weights(P, sm, wave - 2, (int)(role & 255u) - 1, role >> 16); EXIT(); }
    u32 tag0 = 0;
    if (!moe && wave >= 8 - HWAVES) {
        const int lane = fresh_lane();
        const Lab lb = lab_of(labw);
        if (b == RB) EXIT();
        const u32 nh = (u32)(32 - nconv) - lb.n - (xcd == (RB & 7) ? 1u : 0u), nit = role >> 16;
        if (nit && 7 - wave >= HT0) wait_ge<1>(sm + FL_IT + 4 * umin(lane, nit - 1), 1);
        helper_prefetch(P, lane, (u32)(7 - wave) * nh + (u32)(ii - nconv) - lb.n, HWAVES * nh, lb);
        EXIT();
    }
    if (wave) EXIT();
    const u32 t_in = (u32)t64;
    {
        u32 epoch;
        asm volatile("s_waitcnt vmcnt(0) ; epoch" : "={v136}"(epoch)::"memory");
        tag0 = tag_of(rfl(epoch));
    }
    const int lane = fresh_lane();
    st2_sc1(ws_rsrc(P), lane == 0 ? WS_ACK + (u32)b * 128u : (b == TB && lane >= 8 && lane < 16 ? WS_T0 + (u32)(lane - 8) * 128u : OOB), (v2u){tag0, t_in});
    if (!moe && b != RB) EXIT();
    if (moe) {
        flag1(sm + LD_TAG, tag0);
        select8(P, lane, xcd, tag0, t_in);
        u32 src[4];
#pragma unroll
        for (int j = 0; j < 4; ++j) { src[j] = (((role & 255u) - 1u) * 64u + (u32)(32 + 8 * j + (lane >> 3))) * (D_ * 2) + (u32)(((lane & 7) ^ ((lane >> 3) & 6)) * 16); PIN(src[j]); }
        VMWAIT(0);
        wg_barrier();
        if (W2S) __builtin_amdgcn_s_sleep(W2S);
#pragma unroll
        for (int j = 0; j < W2A; ++j) dma_rows<8192>(P.wd, src[j], LD_WD + 4096 + j * 1024);
        wg_barrier();
#pragma unroll
        for (int j = W2A; j < 4; ++j) dma_rows<8192>(P.wd, src[j], LD_WD + 4096 + j * 1024);
        VMWAIT(0);
        wg_barrier();
        EXIT();
    }
    {
        const __amdgpu_buffer_rsrc_t rws = ws_rsrc(P);
#pragma unroll 1
        for (int w = 0; w < 4096 && (int)((u32)__builtin_amdgcn_s_memrealtime() - t_in - SEL_AT) < 0; ++w) __builtin_amdgcn_s_sleep(1);
        const v2u a0 = ld2_sc1(rws, (lane & 6) ? WS_ACK + (u32)(128 + lane) * 128u : OOB), a1 = ld2_sc1(rws, WS_ACK + (u32)(192 + lane) * 128u);
#pragma unroll 1
        for (int spin = 0; spin < SPIN_CAP; ++spin) {
            MEMFENCE();
            if (__all((ld2_sc1(rws, WS_CAND + (u32)(xcd * 16384 + lane * 128))[1] == tag0) & (ld2_sc1(rws, WS_CAND + 8192u + (u32)(xcd * 16384 + lane * 128))[1] == tag0))) break;
            __builtin_amdgcn_s_sleep(SEL_GAP);
        }
        const u32 t_sel = (u32)__builtin_amdgcn_s_memrealtime() - t_in;
        const u32 late = wave_max(umax(a0[0] == tag0 ? umin(a0[1] - t_in + 64u, 255u) : 0u, a1[0] == tag0 ? umin(a1[1] - t_in + 64u, 255u) : 0u));
        st2_sc1(rws, lane < 8 ? WS_T1 + (u32)lane * 128u : OOB, (v2u){tag0, t_in + umax(PART_AT1 + (late * PART_K >> 3), t_sel + PART_SEL)});
        __builtin_amdgcn_s_sleep(BUMP_DELAY);
#pragma unroll 1
        for (int spin = 0; spin < SPIN_CAP; ++spin) {
            MEMFENCE();
            u32 bad = 0;
#pragma unroll
            for (int k = 0; k < 4; ++k) bad |= ld2_sc1(rws, WS_ACK + (u32)(k * 64 + lane) * 128u)[0] ^ tag0;
            if (__all(bad == 0u)) break;
            __builtin_amdgcn_s_sleep(10);
        }
        if (lane == 0) __hip_atomic_store((GAS u32*)(P.ws + WS_EPOCH), tag0, RLX_AGENT);
    }
    VMWAIT(0);
}

extern "C" void kernel_launch(void* const* d_in, const int* in_sizes, int n_in, void* d_out, int out_size,
                              void* d_ws, size_t ws_size, hipStream_t stream) {
    Args A = mk_roles();
    Params& P = A.p;
    P.x = (const u16*)d_in[0];
    P.Are = (const u16*)d_in[1]; P.Aim = (const u16*)d_in[2];
    P.Bre = (const u16*)d_in[3]; P.Bim = (const u16*)d_in[4];
    P.Cre = (const u16*)d_in[5]; P.Cim = (const u16*)d_in[6];
    P.logdt = (const u16*)d_in[7]; P.dskip = (const u16*)d_in[8];
    P.wr = (const u16*)d_in[10]; P.wg = (const u16*)d_in[11]; P.wu = (const u16*)d_in[12]; P.wd = (const u16*)d_in[13];
    P.gw = (const u16*)d_in[14];
    P.out = (u16*)d_out;
    P.ws = (unsigned char*)d_ws;
    fused_kernel<<<256, NTHR, 0, stream>>>(A);
}
```

```cpp
#include <hip/hip_runtime.h>
#include <stdint.h>

typedef unsigned int u32;
typedef unsigned long long u64;
typedef unsigned short u16;
typedef u32 v4u __attribute__((ext_vector_type(4)));
typedef u32 v2u __attribute__((ext_vector_type(2)));
typedef u32 v8u __attribute__((ext_vector_type(8)));
typedef u32 v16u __attribute__((ext_vector_type(16)));
typedef float v4f __attribute__((ext_vector_type(4)));
typedef float v2f __attribute__((ext_vector_type(2)));
typedef short v8s __attribute__((ext_vector_type(8)));
typedef __bf16 v2bf __attribute__((ext_vector_type(2)));
#define GAS __attribute__((address_space(1)))
#define LAS __attribute__((address_space(3)))
#define DI __device__ __forceinline__
#define RLX_AGENT __ATOMIC_RELAXED, __HIP_MEMORY_SCOPE_AGENT
#define SB() __builtin_amdgcn_sched_barrier(0)
#define MEMFENCE() asm volatile("" ::: "memory")
#define PIN(x) asm volatile("" : "+v"(x))
#define VMWAIT(n) asm volatile("s_waitcnt vmcnt(%0)" ::"n"(n) : "memory")

#define CODE_KB 25
#define WIN 5
#define WIN0 5
#define PART_AT 592
#define FPRIO 2
#define PART_AT1 545
#define PART_K 4
#define PART_SEL 249
#define GFL 23
#define WIN2 WIN
#define WSW 16
#define HWAVES 5
#define HT0 1
#define HWIN 2
#define HR0 320
#define PART_GAP 2
#define SLOW0 128
#define SEL_AT 240
#define TREF 1
#define SEL_GAP 3
#define BUMP_DELAY 36
#define NM_LIST 6, 6, 10, 10, 10, 10, 10, 10
#define NH_LIST 3, 3, 2, 2, 1, 0, 2, 2
#define NI_LIST 12, 12, 22, 22, 16, 0, 22, 22
constexpr int TB = 248;
constexpr int RB = 250;

constexpr int L_ = 2048, D_ = 576, N_ = 128, F_ = 4608;
constexpr int NTHR = 768;
constexpr int SPIN_CAP = 1 << 17;
constexpr u32 OOB = 0x7ffffff0u;
__device__ __forceinline__ unsigned tag_of(unsigned epoch) { return epoch == 0xffffffffu ? 1u : epoch + 1u; }

constexpr u32 WS_EPOCH = 0;
constexpr u32 WS_T1 = 1024;
constexpr u32 WS_T0 = 2048;
constexpr u32 WS_KEYS = 4096;
constexpr u32 WS_CAND = WS_KEYS + 16384;
constexpr u32 WS_ACK = WS_CAND + 8 * 128 * 128;
constexpr u32 WS_PART = WS_ACK + 256 * 128;
constexpr u32 WS_BYTES = WS_PART + 144 * 72 * 128;

constexpr int TR = 2080;
constexpr int LD_TB = 0;
constexpr int ROW_T1 = 16, ROW_FL = 26, ROW_FU = 27, ROW_T0 = 28, ROW_T2 = 44;
constexpr int LD_KT = LD_TB + ROW_T1 * TR;
constexpr int U_PAD = 256, U_CH = 2 * (U_PAD + 2048);
constexpr int LD_U = LD_TB + 60 * TR;
constexpr int PA = 550, PB = 545, KA_CP = 1216, KA_CH = 2 * KA_CP;
constexpr int LD_KA = LD_U + 4 * U_CH;
constexpr int G_CH = 896;
constexpr int LD_G = LD_KA + 4 * KA_CH;
constexpr int LD_BD = LD_G + 4 * G_CH;
constexpr int LD_CONV_END = LD_BD + 5120;
constexpr int FL_G = 16;
constexpr int FL_IT = 64;
constexpr int LD_TAG = 48;
constexpr int LD_SCR = 512;
constexpr int LD_XS = 1024 + 2 * 9216;
constexpr int LD_RED = LD_XS + 9216;
constexpr int HB_ROW = 144;
constexpr int LD_HB = LD_RED + 6 * 4096;
constexpr int LD_MOE_END = LD_HB + 8 * HB_ROW;
constexpr int LDS_BYTES = LD_CONV_END > LD_MOE_END ? LD_CONV_END : LD_MOE_END;
static_assert(LDS_BYTES > 81920 && LDS_BYTES <= 163840, "lds: one workgroup per CU");
static_assert(LD_BD % 128 == 0 && LD_MOE_END <= LD_KA && LD_U % 16 == 0 && LD_KA % 16 == 0 && LD_G % 16 == 0, "align");

struct Params {
    const u16 *x, *Are, *Aim, *Bre, *Bim, *Cre, *Cim, *logdt, *dskip, *wr, *wg, *wu, *wd, *gw;
    u16* out;
    unsigned char* ws;
};
struct Args { Params p; unsigned role[256]; };
constexpr int nconv_of(int label) { return label < 2 ? 24 : 16; }
constexpr Args mk_roles() {
    Args A{};
    const int nm[8] = {NM_LIST}, nh[8] = {NH_LIST}, ni[8] = {NI_LIST};
    int m0 = 0, i0 = 0;
    bool ok = true;
    for (int x = 0; x < 8; ++x) {
        const int nc = nconv_of(x), first = x * 32, w = 32 - nc;
        for (int ii = 0; ii < nc; ++ii) A.role[first + ii] = (unsigned)(m0 | nm[x] << 8 | (65536 / nm[x] + 1) << 16);
        const int rest = ni[x] - (w - nm[x]) * nh[x];
        ok = ok && rest >= 0;
        for (int ii = 31; ii >= nc; --ii) {
            const int cnt = ii - nc >= nm[x] ? nh[x] : rest / nm[x] + (ii - nc < rest % nm[x] ? 1 : 0);
            A.role[first + ii] = (unsigned)((ii - nc < nm[x] ? m0 + ii - nc + 1 : 0) | (cnt ? i0 : 0) << 8 | cnt << 16);
            i0 += cnt; ok = ok && cnt <= 3;
        }
        m0 += nm[x];
    }
    A.role[255] |= (m0 != 72 || i0 != 128 || !ok) ? 0x80000000u : 0u;
    return A;
}
static_assert(!(mk_roles().role[255] >> 31) && (mk_roles().role[(RB & 7) * 32 + (RB >> 3)] & 255) == 0, "roles");

#define EXIT() do { VMWAIT(0); __builtin_amdgcn_endpgm(); } while (0)

typedef LAS unsigned char* lptr;
typedef const LAS unsigned char* clptr;
DI float bf_lo(u32 v) { return __uint_as_float(v << 16); }
DI float bf_hi(u32 v) { return __uint_as_float(v & 0xffff0000u); }
DI u32 pk_bf16(float a, float b) { v2f f = {a, b}; return __builtin_bit_cast(u32, __builtin_convertvector(f, v2bf)); }
DI float dot2(u32 a, u32 b, float c) { return __builtin_amdgcn_fdot2_f32_bf16(__builtin_bit_cast(v2bf, a), __builtin_bit_cast(v2bf, b), c, false); }
DI float sigmoidf_(float s) { return __builtin_amdgcn_rcpf(1.f + __builtin_amdgcn_exp2f(-1.44269504f * s)); }
template <int CTRL, int RM> DI u32 dpp0(u32 v) { return (u32)__builtin_amdgcn_update_dpp(0, (int)v, CTRL, RM, 0xf, true); }
template <int CTRL, int RM> DI float dppf(float v) { return __uint_as_float(dpp0<CTRL, RM>(__float_as_uint(v))); }
DI u32 umax(u32 a, u32 b) { return a > b ? a : b; }
DI u32 umin(u32 a, u32 b) { return a < b ? a : b; }
DI int fresh_lane() { int l; asm volatile("v_mbcnt_lo_u32_b32 %0, -1, 0\n\tv_mbcnt_hi_u32_b32 %0, -1, %0" : "=v"(l)); return l; }
DI u32 rfl(u32 v) { return (u32)__builtin_amdgcn_readfirstlane((int)v); }
DI u32 rdl(u32 v, int l) { return (u32)__builtin_amdgcn_readlane((int)v, l); }
DI u32 wave_max(u32 v) {
    v = umax(v, dpp0<0x111, 0xf>(v)); v = umax(v, dpp0<0x112, 0xf>(v)); v = umax(v, dpp0<0x114, 0xf>(v)); v = umax(v, dpp0<0x118, 0xf>(v));
    v = umax(v, dpp0<0x142, 0xa>(v)); v = umax(v, dpp0<0x143, 0xc>(v));
    return rdl(v, 63);
}
DI u32 perm_lo(u32 hi, u32 lo) { return __builtin_amdgcn_perm(hi, lo, 0x05040100u); }
DI u32 perm_hi(u32 hi, u32 lo) { return __builtin_amdgcn_perm(hi, lo, 0x07060302u); }
typedef __bf16 v8bf __attribute__((ext_vector_type(8)));
typedef __bf16 v16bf __attribute__((ext_vector_type(16)));
DI v4f smfmac(v4u a, v8u b, v4f c, u32 idx) { return __builtin_amdgcn_smfmac_f32_16x16x64_bf16(__builtin_bit_cast(v8bf, a), __builtin_bit_cast(v16bf, b), c, (int)idx, 0, 0); }
DI __amdgpu_buffer_rsrc_t mk_rsrc(const void* p, u32 n) { return __builtin_amdgcn_make_buffer_rsrc((void*)p, 0, n, 0x00020000); }
DI __amdgpu_buffer_rsrc_t ws_rsrc(const Params& P) { return mk_rsrc(P.ws, WS_BYTES); }
DI v4u ld_sc1(__amdgpu_buffer_rsrc_t r, u32 off) { return __builtin_amdgcn_raw_buffer_load_b128(r, off, 0, 16); }
DI v2u ld2_sc1(__amdgpu_buffer_rsrc_t r, u32 off) { return __builtin_amdgcn_raw_buffer_load_b64(r, off, 0, 16); }
DI void st_sc1(__amdgpu_buffer_rsrc_t r, u32 off, v4u v) { __builtin_amdgcn_raw_buffer_store_b128(v, r, off, 0, 16); }
DI void st2_sc1(__amdgpu_buffer_rsrc_t r, u32 off, v2u v) { __builtin_amdgcn_raw_buffer_store_b64(v, r, off, 0, 16); }
DI float g_of(u32 p) { return (float)__builtin_bit_cast(_Float16, (u16)p); }
DI bool tag_ok(v4u v, u32 tag) { return (v[1] == tag) & (v[3] == tag); }
DI v2u tr_read(clptr p) {
    typedef short v4s __attribute__((ext_vector_type(4)));
    return __builtin_bit_cast(v2u, __builtin_amdgcn_ds_read_tr16_b64_v4i16((LAS v4s*)p));
}
DI void fmac_(float& acc, float a, float b) { asm("v_fmac_f32 %0, %1, %2" : "+v"(acc) : "v"(a), "v"(b)); }
DI float mul_(float a, float b) { float r; asm("v_mul_f32 %0, %1, %2" : "=v"(r) : "v"(a), "v"(b)); return r; }
#define W16(p) (*(volatile LAS u16*)(p))
#define W32(p) (*(volatile LAS u32*)(p))
#define W64(p) (*(volatile LAS v2u*)(p))
#define W128(p) (*(volatile LAS v4u*)(p))

template <int SLEEP> DI void wait_ge(clptr p, u32 target) {
    u32 v, n;
    asm volatile("s_mov_b32 %1, 0x100000\n"
                 "Lw%=:\n\tds_read_b32 %0, %2\n\ts_waitcnt lgkmcnt(0)\n\tv_cmp_gt_u32 vcc, %3, %0\n\ts_cbranch_vccz Ld%=\n\t"
                 "s_sleep %4\n\ts_sub_u32 %1, %1, 1\n\ts_cbranch_scc0 Lw%=\n"
                 "Ld%=:"
                 : "=&v"(v), "=&s"(n) : "v"(p), "s"(target), "n"(SLEEP) : "memory", "vcc", "scc");
}
DI void flag1(lptr p, u32 v) {
    asm volatile("s_mov_b64 exec, 1\n\tds_write_b32 %0, %1\n\ts_mov_b64 exec, -1" ::"v"(p), "v"(v) : "memory");
}
template <int OFF> DI void put_row(u32 v) { asm volatile("ds_write_addtid_b32 %0 offset:%1" ::"v"(v), "n"(OFF) : "memory"); }
DI void wg_barrier() { asm volatile("s_waitcnt lgkmcnt(0)\n\ts_barrier" ::: "memory"); }

DI void touch(const void* base, u32 off) {
    asm volatile("s_movk_i32 m0, %2\n\ts_nop 0\n\tglobal_load_lds_dword %0, %1" ::"v"(off), "s"(base), "n"(LD_SCR) : "memory");
}
template <int STEP> DI void dma_rows(const void* base, u32 off, u32 dst) {
#define DR(q) "global_load_lds_dwordx4 %0, %1 offset:128*" #q "\n\ts_addk_i32 m0, %3\n\ts_nop 0\n\t"
    asm volatile("s_mov_b32 m0, %2\n\ts_nop 0\n\t" DR(0) DR(1) DR(2) DR(3) DR(4) DR(5) DR(6) DR(7) "global_load_lds_dwordx4 %0, %1 offset:1024" ::"v"(off), "s"(base), "s"(dst), "n"(STEP - 128) : "memory", "scc");
}
DI int cg_of(int xcd, int ii) { return ii < 16 ? 16 * xcd + ii : 112 + 8 * xcd + ii; }
struct Lab { u32 m0, n, mg; };
DI Lab lab_of(u32 w) { return {w & 255u, (w >> 8) & 255u, w >> 16}; }

DI u32 top8_slow(const u32 (&all)[32], int lane) {
    u32 g8 = 0, bound = 0xffffffffu;
#pragma unroll 1
    for (int rd = 0; rd < 8; ++rd) {
        u32 m = 0;
#pragma unroll
        for (int q = 0; q < 32; ++q) { m = umax(m, all[q] <= bound ? all[q] : 0u); PIN(m); }
        const u32 M = wave_max(m);
        if ((lane & 7) == rd) g8 = M;
        bound = M - 1u;
    }
    return g8;
}
DI u32 from_lane(u32 v, int l) { return (u32)__builtin_amdgcn_ds_bpermute(4 * l, (int)v); }

struct ConvTab { u32 v[(12 * 5 + 5) * 256]; };
constexpr ConvTab mk_tab() {
    ConvTab t{};
    for (int kind = 0; kind < 3; ++kind) for (int ch = 0; ch < 4; ++ch) for (int lane = 0; lane < 64; ++lane) {
        const int c = lane & 15, kg = lane >> 4;
        const int ka = LD_KA + ch * KA_CH, gq = LD_G + ch * G_CH;
        const int kb = ka + ((c & 1) ? KA_CP + 2 * PB : 2 * PA) - 2 * (c + 16 - 8 * kg) - 64 * 15;
        u32 ad[20] = {};
        if (kind < 2) {
            ad[0] = LD_TB + ROW_FU * TR + ch * 512;
            ad[1] = LD_U + ch * U_CH + 2 * (U_PAD - 16 + 16 * c + 8 * kg) - 7 * 64;
            ad[2] = LD_TB + ROW_FU * TR + (lane & 3) * 516 + 256;
            ad[3] = LD_TB + (ROW_T0 + (c >> 3)) * TR + ch * 512 + 64 * (c & 7) + 16 * (kg ^ ((c >> 1) & 3));
            ad[4] = kind ? gq + 4 * (15 - c + 4 * kg) : kb;
            ad[5] = kind ? kb - 64 : LD_TB + (c >> 3) * TR + ch * 512 + 64 * (c & 7) + 16 * (kg ^ ((c >> 1) & 3));
            ad[6] = kind ? LD_BD + lane * 16 : LD_TB + ROW_FL * TR + ch * 512 + 256;
            ad[7] = gq;
        } else {
            ad[0] = LD_TB + ROW_FL * TR + ch * 512 + (lane & 1) * 260;
            ad[1] = LD_TB + c * TR + ch * 512 + kg * 16;
            ad[2] = LD_TB + (ROW_T1 + (c & 3) + (c >> 3) + 5 * ((c >> 2) & 1)) * TR + ch * 512 + kg * 16;
            ad[3] = ka + 2 * (PA - 3 - (4 * (kg & 1) + 128 * (kg >> 1) + 8 * c));
            ad[4] = ka + (kg < 2 ? 2 * (PA - 6) : KA_CP + 2 * (PB - 9)) - 2 * (256 + 128 * (kg & 1) + 8 * c);
            ad[5] = gq + (kg < 2 ? (c & 1) * 256 + 4 * (47 - 24) + 2 * kg : 512 + 4 * (63 - 24) + 2 * (c & 1) - 128 * (kg - 2)) - 4 * (c >> 1);
            ad[6] = kg < 2 ? 0x01000302u : 0x03020302u;
            ad[7] = kg < 2 ? 0x01000100u : 0x01000302u;
            ad[8] = lane == 0 ? 0x05040100u : ad[7];
            ad[9] = (128 * ch + 2 * lane) * (D_ * 2);
            ad[10] = LD_U + 2 * (U_PAD + 128 * ch + 2 * lane);
            ad[11] = LD_TB + ch * TR + 64 * (lane >> 3) + 16 * (((lane >> 1) ^ (lane >> 4)) & 3) + 8 * (lane & 1);
            ad[12] = WS_KEYS + (64 * ch + lane) * 16;
            ad[13] = LD_TB + ROW_FL * TR + (lane & 3) * 516;
            ad[14] = WS_PART + (18 * ch + (lane < 17 ? lane : 17)) * 128;
            ad[15] = LD_TB + ROW_FL * TR + (lane & 3) * 516 + (lane & 4) * 64;
            ad[16] = lane * 128;
            ad[17] = lane < 32 ? ka + (lane >> 4) * KA_CP + (c < 6 ? c * 16 : 1088 + (c & 3) * 16) : LD_U + ch * U_CH + (lane - 32) * 16;
            ad[18] = gq + (lane < 55 ? lane : 55) * 16;
            ad[19] = ka;
        }
        for (int k = 0; k < 20; ++k) t.v[((kind * 4 + ch) * 5 + (k >> 2)) * 256 + lane * 4 + (k & 3)] = ad[k];
    }
    const int lo[5] = {3, 6, 5, 4, 3}, hi[5] = {5, 10, 11, 12, 4};
    for (int k = 0; k < 5; ++k) for (int lane = 0; lane < 64; ++lane) for (int e = 0; e < 8; ++e) {
        const int l = (lane & 15) + 16 - 8 * (lane >> 4) - e;
        if (l >= lo[k] && l <= hi[k]) t.v[(60 + k) * 256 + lane * 4 + (e >> 1)] |= (k < 4 ? 0x3f80u : 0xffffu) << (16 * (e & 1));
    }
    return t;
}
static __device__ const ConvTab conv_tab = mk_tab();
#define CONV_A_ASM \
    "s_setprio 1\n" \
    "s_waitcnt vmcnt(4)\n" \
    "v_mul_f32_e32 v0, 0x3fb8aa3b, v70\n" \
    "v_exp_f32_e32 v0, v0\n" \
    "v_mul_f32_e32 v1, 0x3fb8aa3b, v64\n" \
    "v_cvt_pk_bf16_f32 v0, 0, v0\n" \
    "v_mul_f32_e32 v2, 0.15915494, v65\n" \
    "v_mul_f32_e32 v1, v0, v1\n" \
    "v_mul_f32_e32 v2, v0, v2\n" \
    "v_mul_f32_e32 v4, 0x41000000, v2\n" \
    "v_mul_f32_e32 v3, 0x41000000, v1\n" \
    "v_fract_f32_e32 v4, v4\n" \
    "v_exp_f32_e32 v3, v3\n" \
    "v_cos_f32_e32 v8, v4\n" \
    "v_sin_f32_e32 v9, v4\n" \
    "v_mul_f32_e32 v66, v0, v66\n" \
    "v_mul_f32_e64 v8, v3, v8\n" \
    "v_mul_f32_e64 v9, v3, v9\n" \
    "v_pk_mul_f32 v[18:19], v[8:9], v[8:9] op_sel_hi:[0,1]\n" \
    "v_cvt_pk_bf16_f32 v30, v8, v9\n" \
    "v_pk_fma_f32 v[14:15], v[8:9], v[8:9], v[18:19] op_sel:[1,1,0] op_sel_hi:[0,1,1] neg_lo:[0,1,0]\n" \
    "ds_write_addtid_b32 v30 offset:2080\n" \
    "v_pk_mul_f32 v[18:19], v[8:9], v[14:15] op_sel_hi:[0,1]\n" \
    "v_cvt_pk_bf16_f32 v31, v14, v15\n" \
    "v_pk_fma_f32 v[14:15], v[14:15], v[8:9], v[18:19] op_sel:[1,1,0] op_sel_hi:[0,1,1] neg_lo:[0,1,0]\n" \
    "ds_write_addtid_b32 v31 offset:4160\n" \
    "v_pk_mul_f32 v[18:19], v[8:9], v[14:15] op_sel_hi:[0,1]\n" \
    "v_cvt_pk_bf16_f32 v30, v14, v15\n" \
    "v_pk_fma_f32 v[14:15], v[14:15], v[8:9], v[18:19] op_sel:[1,1,0] op_sel_hi:[0,1,1] neg_lo:[0,1,0]\n" \
    "ds_write_addtid_b32 v30 offset:6240\n" \
    "v_pk_mul_f32 v[18:19], v[8:9], v[14:15] op_sel_hi:[0,1]\n" \
    "v_cvt_pk_bf16_f32 v31, v14, v15\n" \
    "v_pk_fma_f32 v[14:15], v[14:15], v[8:9], v[18:19] op_sel:[1,1,0] op_sel_hi:[0,1,1] neg_lo:[0,1,0]\n" \
    "ds_write_addtid_b32 v31 offset:8320\n" \
    "v_mul_f32_e32 v4, 0x43000000, v2\n" \
    "v_mul_f32_e32 v3, 0x43000000, v1\n" \
    "v_fract_f32_e32 v4, v4\n" \
    "v_exp_f32_e32 v3, v3\n" \
    "v_cos_f32_e32 v12, v4\n" \
    "v_sin_f32_e32 v13, v4\n" \
    "v_pk_mul_f32 v[18:19], v[8:9], v[14:15] op_sel_hi:[0,1]\n" \
    "v_cvt_pk_bf16_f32 v30, v14, v15\n" \
    "v_pk_fma_f32 v[14:15], v[14:15], v[8:9], v[18:19] op_sel:[1,1,0] op_sel_hi:[0,1,1] neg_lo:[0,1,0]\n" \
    "ds_write_addtid_b32 v30 offset:10400\n" \
    "v_pk_mul_f32 v[18:19], v[8:9], v[14:15] op_sel_hi:[0,1]\n" \
    "v_cvt_pk_bf16_f32 v31, v14, v15\n" \
    "v_pk_fma_f32 v[14:15], v[14:15], v[8:9], v[18:19] op_sel:[1,1,0] op_sel_hi:[0,1,1] neg_lo:[0,1,0]\n" \
    "ds_write_addtid_b32 v31 offset:12480\n" \
    "v_mul_f32_e64 v12, v3, v12\n" \
    "v_mul_f32_e64 v13, -v3, v13\n" \
    "v_pk_mul_f32 v[18:19], v[8:9], v[14:15] op_sel_hi:[0,1]\n" \
    "v_cvt_pk_bf16_f32 v30, v14, v15\n" \
    "v_pk_fma_f32 v[14:15], v[14:15], v[8:9], v[18:19] op_sel:[1,1,0] op_sel_hi:[0,1,1] neg_lo:[0,1,0]\n" \
    "ds_write_addtid_b32 v30 offset:14560\n" \
    "v_pk_mul_f32 v[18:19], v[8:9], v[14:15] op_sel_hi:[0,1]\n" \
    "v_cvt_pk_bf16_f32 v31, v14, v15\n" \
    "v_pk_fma_f32 v[14:15], v[14:15], v[8:9], v[18:19] op_sel:[1,1,0] op_sel_hi:[0,1,1] neg_lo:[0,1,0]\n" \
    "ds_write_addtid_b32 v31 offset:16640\n" \
    "v_fract_f32_e32 v6, v2\n" \
    "v_exp_f32_e32 v5, v1\n" \
    "v_cos_f32_e32 v10, v6\n" \
    "v_sin_f32_e32 v11, v6\n" \
    "v_pk_mul_f32 v[18:19], v[8:9], v[14:15] op_sel_hi:[0,1]\n" \
    "v_cvt_pk_bf16_f32 v30, v14, v15\n" \
    "v_pk_fma_f32 v[14:15], v[14:15], v[8:9], v[18:19] op_sel:[1,1,0] op_sel_hi:[0,1,1] neg_lo:[0,1,0]\n" \
    "ds_write_addtid_b32 v30 offset:18720\n" \
    "v_pk_mul_f32 v[18:19], v[8:9], v[14:15] op_sel_hi:[0,1]\n" \
    "v_cvt_pk_bf16_f32 v31, v14, v15\n" \
    "v_pk_fma_f32 v[14:15], v[14:15], v[8:9], v[18:19] op_sel:[1,1,0] op_sel_hi:[0,1,1] neg_lo:[0,1,0]\n" \
    "ds_write_addtid_b32 v31 offset:20800\n" \
    "v_mul_f32_e64 v10, v5, v10\n" \
    "v_mul_f32_e64 v11, -v5, v11\n" \
    "v_pk_mul_f32 v[18:19], v[8:9], v[14:15] op_sel_hi:[0,1]\n" \
    "v_cvt_pk_bf16_f32 v30, v14, v15\n" \
    "v_pk_fma_f32 v[14:15], v[14:15], v[8:9], v[18:19] op_sel:[1,1,0] op_sel_hi:[0,1,1] neg_lo:[0,1,0]\n" \
    "ds_write_addtid_b32 v30 offset:22880\n" \
    "v_mul_f32_e64 v67, v0, v67\n" \
    "v_mul_f32_e64 v16, v68, v66\n" \
    "v_mul_f32_e64 v17, -v68, v67\n" \
    "v_fma_f32 v16, -v69, v67, v16\n" \
    "v_fma_f32 v17, -v69, v66, v17\n" \
    "v_pk_mul_f32 v[18:19], v[8:9], v[14:15] op_sel_hi:[0,1]\n" \
    "v_cvt_pk_bf16_f32 v31, v14, v15\n" \
    "v_pk_fma_f32 v[14:15], v[14:15], v[8:9], v[18:19] op_sel:[1,1,0] op_sel_hi:[0,1,1] neg_lo:[0,1,0]\n" \
    "ds_write_addtid_b32 v31 offset:24960\n" \
    "v_pk_mul_f32 v[18:19], v[8:9], v[14:15] op_sel_hi:[0,1]\n" \
    "v_cvt_pk_bf16_f32 v30, v14, v15\n" \
    "v_pk_fma_f32 v[14:15], v[14:15], v[8:9], v[18:19] op_sel:[1,1,0] op_sel_hi:[0,1,1] neg_lo:[0,1,0]\n" \
    "ds_write_addtid_b32 v30 offset:27040\n" \
    "v_pk_mul_f32 v[18:19], v[8:9], v[14:15] op_sel_hi:[0,1]\n" \
    "v_cvt_pk_bf16_f32 v31, v14, v15\n" \
    "v_pk_fma_f32 v[14:15], v[14:15], v[8:9], v[18:19] op_sel:[1,1,0] op_sel_hi:[0,1,1] neg_lo:[0,1,0]\n" \
    "ds_write_addtid_b32 v31 offset:29120\n" \
    "v_pk_mul_f32 v[18:19], v[12:13], v[16:17] op_sel_hi:[0,1]\n" \
    "v_cvt_pk_bf16_f32 v30, v14, v15\n" \
    "v_pk_fma_f32 v[20:21], v[16:17], v[12:13], v[18:19] op_sel:[1,1,0] op_sel_hi:[0,1,1] neg_lo:[0,1,0]\n" \
    "ds_write_addtid_b32 v30 offset:31200\n" \
    "v_mov_b32_e32 v61, 1\n" \
    "ds_write_addtid_b32 v61 offset:54080\n" \
    "s_add_u32 m0, %[m0a], 54080\n" \
    "v_cvt_pk_bf16_f32 v31, v16, v17\n" \
    "ds_write_addtid_b32 v31 offset:4160\n" \
    "v_pk_mul_f32 v[18:19], v[10:11], v[16:17] op_sel_hi:[0,1]\n" \
    "v_cvt_pk_bf16_f32 v30, v20, v21\n" \
    "v_pk_fma_f32 v[16:17], v[16:17], v[10:11], v[18:19] op_sel:[1,1,0] op_sel_hi:[0,1,1] neg_lo:[0,1,0]\n" \
    "ds_write_addtid_b32 v30 offset:20800\n" \
    "v_pk_mul_f32 v[18:19], v[12:13], v[16:17] op_sel_hi:[0,1]\n" \
    "v_cvt_pk_bf16_f32 v31, v16, v17\n" \
    "v_pk_fma_f32 v[14:15], v[16:17], v[12:13], v[18:19] op_sel:[1,1,0] op_sel_hi:[0,1,1] neg_lo:[0,1,0]\n" \
    "ds_write_addtid_b32 v31 offset:6240\n" \
    "v_pk_mul_f32 v[18:19], v[10:11], v[16:17] op_sel_hi:[0,1]\n" \
    "v_cvt_pk_bf16_f32 v30, v14, v15\n" \
    "v_pk_fma_f32 v[16:17], v[16:17], v[10:11], v[18:19] op_sel:[1,1,0] op_sel_hi:[0,1,1] neg_lo:[0,1,0]\n" \
    "ds_write_addtid_b32 v30 offset:22880\n" \
    "v_pk_mul_f32 v[18:19], v[12:13], v[16:17] op_sel_hi:[0,1]\n" \
    "v_cvt_pk_bf16_f32 v31, v16, v17\n" \
    "v_pk_fma_f32 v[22:23], v[16:17], v[12:13], v[18:19] op_sel:[1,1,0] op_sel_hi:[0,1,1] neg_lo:[0,1,0]\n" \
    "ds_write_addtid_b32 v31 offset:8320\n" \
    "v_pk_mul_f32 v[18:19], v[10:11], v[16:17] op_sel_hi:[0,1]\n" \
    "v_cvt_pk_bf16_f32 v30, v22, v23\n" \
    "v_pk_fma_f32 v[16:17], v[16:17], v[10:11], v[18:19] op_sel:[1,1,0] op_sel_hi:[0,1,1] neg_lo:[0,1,0]\n" \
    "ds_write_addtid_b32 v30 offset:24960\n" \
    "v_pk_mul_f32 v[18:19], v[12:13], v[16:17] op_sel_hi:[0,1]\n" \
    "v_cvt_pk_bf16_f32 v31, v16, v17\n" \
    "v_pk_fma_f32 v[14:15], v[16:17], v[12:13], v[18:19] op_sel:[1,1,0] op_sel_hi:[0,1,1] neg_lo:[0,1,0]\n" \
    "ds_write_addtid_b32 v31 offset:10400\n" \
    "v_pk_mul_f32 v[18:19], v[10:11], v[16:17] op_sel_hi:[0,1]\n" \
    "v_cvt_pk_bf16_f32 v30, v14, v15\n" \
    "v_pk_fma_f32 v[16:17], v[16:17], v[10:11], v[18:19] op_sel:[1,1,0] op_sel_hi:[0,1,1] neg_lo:[0,1,0]\n" \
    "ds_write_addtid_b32 v30 offset:27040\n" \
    "v_pk_mul_f32 v[18:19], v[12:13], v[16:17] op_sel_hi:[0,1]\n" \
    "v_cvt_pk_bf16_f32 v31, v16, v17\n" \
    "v_pk_fma_f32 v[24:25], v[16:17], v[12:13], v[18:19] op_sel:[1,1,0] op_sel_hi:[0,1,1] neg_lo:[0,1,0]\n" \
    "ds_write_addtid_b32 v31 offset:12480\n" \
    "v_pk_mul_f32 v[18:19], v[10:11], v[16:17] op_sel_hi:[0,1]\n" \
    "v_cvt_pk_bf16_f32 v30, v24, v25\n" \
    "v_pk_fma_f32 v[16:17], v[16:17], v[10:11], v[18:19] op_sel:[1,1,0] op_sel_hi:[0,1,1] neg_lo:[0,1,0]\n" \
    "ds_write_addtid_b32 v30 offset:29120\n" \
    "v_pk_mul_f32 v[18:19], v[12:13], v[16:17] op_sel_hi:[0,1]\n" \
    "v_cvt_pk_bf16_f32 v31, v16, v17\n" \
    "v_pk_fma_f32 v[14:15], v[16:17], v[12:13], v[18:19] op_sel:[1,1,0] op_sel_hi:[0,1,1] neg_lo:[0,1,0]\n" \
    "ds_write_addtid_b32 v31 offset:14560\n" \
    "v_pk_mul_f32 v[18:19], v[10:11], v[16:17] op_sel_hi:[0,1]\n" \
    "v_cvt_pk_bf16_f32 v30, v14, v15\n" \
    "v_pk_fma_f32 v[16:17], v[16:17], v[10:11], v[18:19] op_sel:[1,1,0] op_sel_hi:[0,1,1] neg_lo:[0,1,0]\n" \
    "ds_write_addtid_b32 v30 offset:31200\n" \
    "v_pk_mul_f32 v[18:19], v[12:13], v[16:17] op_sel_hi:[0,1]\n" \
    "v_cvt_pk_bf16_f32 v31, v16, v17\n" \
    "v_pk_fma_f32 v[26:27], v[16:17], v[12:13], v[18:19] op_sel:[1,1,0] op_sel_hi:[0,1,1] neg_lo:[0,1,0]\n" \
    "ds_write_addtid_b32 v31 offset:16640\n" \
    "v_pk_mul_f32 v[18:19], v[10:11], v[16:17] op_sel_hi:[0,1]\n" \
    "v_cvt_pk_bf16_f32 v30, v26, v27\n" \
    "v_pk_fma_f32 v[16:17], v[16:17], v[10:11], v[18:19] op_sel:[1,1,0] op_sel_hi:[0,1,1] neg_lo:[0,1,0]\n" \
    "ds_write_addtid_b32 v30 offset:33280\n" \
    "v_pk_mul_f32 v[18:19], v[12:13], v[16:17] op_sel_hi:[0,1]\n" \
    "v_cvt_pk_bf16_f32 v31, v16, v17\n" \
    "v_pk_fma_f32 v[14:15], v[16:17], v[12:13], v[18:19] op_sel:[1,1,0] op_sel_hi:[0,1,1] neg_lo:[0,1,0]\n" \
    "ds_write_addtid_b32 v31 offset:18720\n" \
    "s_waitcnt vmcnt(0)\n" \
    "v_pk_mul_f32 v[18:19], v[12:13], v[20:21] op_sel_hi:[0,1]\n" \
    "v_cvt_pk_bf16_f32 v30, v14, v15\n" \
    "v_pk_fma_f32 v[20:21], v[20:21], v[12:13], v[18:19] op_sel:[1,1,0] op_sel_hi:[0,1,1] neg_lo:[0,1,0]\n" \
    "ds_write_addtid_b32 v30 offset:35360\n" \
    "v_mov_b32_e32 v61, 2\n" \
    "ds_write_addtid_b32 v61 offset:0\n" \
    "s_mov_b32 m0, %[m0a]\n" \
    "v_pk_mul_f32 v[18:19], v[12:13], v[22:23] op_sel_hi:[0,1]\n" \
    "v_cvt_pk_bf16_f32 v31, v20, v21\n" \
    "v_pk_fma_f32 v[22:23], v[22:23], v[12:13], v[18:19] op_sel:[1,1,0] op_sel_hi:[0,1,1] neg_lo:[0,1,0]\n" \
    "ds_write_addtid_b32 v31 offset:33280\n" \
    "v_pk_mul_f32 v[18:19], v[12:13], v[24:25] op_sel_hi:[0,1]\n" \
    "v_cvt_pk_bf16_f32 v30, v22, v23\n" \
    "v_pk_fma_f32 v[24:25], v[24:25], v[12:13], v[18:19] op_sel:[1,1,0] op_sel_hi:[0,1,1] neg_lo:[0,1,0]\n" \
    "ds_write_addtid_b32 v30 offset:35360\n" \
    "v_pk_mul_f32 v[18:19], v[12:13], v[26:27] op_sel_hi:[0,1]\n" \
    "v_cvt_pk_bf16_f32 v31, v24, v25\n" \
    "v_pk_fma_f32 v[26:27], v[26:27], v[12:13], v[18:19] op_sel:[1,1,0] op_sel_hi:[0,1,1] neg_lo:[0,1,0]\n" \
    "ds_write_addtid_b32 v31 offset:37440\n" \
    "v_pk_mul_f32 v[18:19], v[10:11], v[14:15] op_sel_hi:[0,1]\n" \
    "v_cvt_pk_bf16_f32 v30, v26, v27\n" \
    "v_pk_fma_f32 v[28:29], v[14:15], v[10:11], v[18:19] op_sel:[1,1,0] op_sel_hi:[0,1,1] neg_lo:[0,1,0]\n" \
    "ds_write_addtid_b32 v30 offset:39520\n" \
    "v_pk_mul_f32 v[18:19], v[12:13], v[28:29] op_sel_hi:[0,1]\n" \
    "v_pk_fma_f32 v[28:29], v[28:29], v[12:13], v[18:19] op_sel:[1,1,0] op_sel_hi:[0,1,1] neg_lo:[0,1,0]\n" \
    "v_pk_mul_f32 v[18:19], v[12:13], v[20:21] op_sel_hi:[0,1]\n" \
    "v_cvt_pk_bf16_f32 v31, v28, v29\n" \
    "v_pk_fma_f32 v[20:21], v[20:21], v[12:13], v[18:19] op_sel:[1,1,0] op_sel_hi:[0,1,1] neg_lo:[0,1,0]\n" \
    "ds_write_addtid_b32 v31 offset:41600\n" \
    "v_pk_mul_f32 v[18:19], v[12:13], v[22:23] op_sel_hi:[0,1]\n" \
    "v_cvt_pk_bf16_f32 v30, v20, v21\n" \
    "v_pk_fma_f32 v[22:23], v[22:23], v[12:13], v[18:19] op_sel:[1,1,0] op_sel_hi:[0,1,1] neg_lo:[0,1,0]\n" \
    "ds_write_addtid_b32 v30 offset:43680\n" \
    "v_pk_mul_f32 v[18:19], v[12:13], v[24:25] op_sel_hi:[0,1]\n" \
    "v_cvt_pk_bf16_f32 v31, v22, v23\n" \
    "v_pk_fma_f32 v[24:25], v[24:25], v[12:13], v[18:19] op_sel:[1,1,0] op_sel_hi:[0,1,1] neg_lo:[0,1,0]\n" \
    "ds_write_addtid_b32 v31 offset:45760\n" \
    "v_pk_mul_f32 v[18:19], v[12:13], v[26:27] op_sel_hi:[0,1]\n" \
    "v_cvt_pk_bf16_f32 v30, v24, v25\n" \
    "v_pk_fma_f32 v[26:27], v[26:27], v[12:13], v[18:19] op_sel:[1,1,0] op_sel_hi:[0,1,1] neg_lo:[0,1,0]\n" \
    "ds_write_addtid_b32 v30 offset:47840\n" \
    "v_pk_mul_f32 v[18:19], v[12:13], v[28:29] op_sel_hi:[0,1]\n" \
    "v_cvt_pk_bf16_f32 v31, v26, v27\n" \
    "v_pk_fma_f32 v[28:29], v[28:29], v[12:13], v[18:19] op_sel:[1,1,0] op_sel_hi:[0,1,1] neg_lo:[0,1,0]\n" \
    "ds_write_addtid_b32 v31 offset:49920\n" \
    "v_pk_mul_f32 v[18:19], v[12:13], v[24:25] op_sel_hi:[0,1]\n" \
    "v_cvt_pk_bf16_f32 v30, v28, v29\n" \
    "v_pk_fma_f32 v[24:25], v[24:25], v[12:13], v[18:19] op_sel:[1,1,0] op_sel_hi:[0,1,1] neg_lo:[0,1,0]\n" \
    "ds_write_addtid_b32 v30 offset:52000\n" \
    "v_mov_b32_e32 v61, 3\n" \
    "ds_write_addtid_b32 v61 offset:54080\n" \
    "s_add_u32 m0, %[m0a], 62400\n" \
    "v_pk_mul_f32 v[18:19], v[12:13], v[28:29] op_sel_hi:[0,1]\n" \
    "v_cvt_pk_bf16_f32 v31, v24, v25\n" \
    "v_pk_fma_f32 v[28:29], v[28:29], v[12:13], v[18:19] op_sel:[1,1,0] op_sel_hi:[0,1,1] neg_lo:[0,1,0]\n" \
    "ds_write_addtid_b32 v31 offset:29120\n" \
    "v_pk_mul_f32 v[18:19], v[12:13], v[24:25] op_sel_hi:[0,1]\n" \
    "v_cvt_pk_bf16_f32 v30, v28, v29\n" \
    "v_pk_fma_f32 v[24:25], v[24:25], v[12:13], v[18:19] op_sel:[1,1,0] op_sel_hi:[0,1,1] neg_lo:[0,1,0]\n" \
    "ds_write_addtid_b32 v30 offset:37440\n" \
    "v_pk_mul_f32 v[18:19], v[12:13], v[28:29] op_sel_hi:[0,1]\n" \
    "v_cvt_pk_bf16_f32 v31, v24, v25\n" \
    "v_pk_fma_f32 v[28:29], v[28:29], v[12:13], v[18:19] op_sel:[1,1,0] op_sel_hi:[0,1,1] neg_lo:[0,1,0]\n" \
    "ds_write_addtid_b32 v31 offset:31200\n" \
    "v_pk_mul_f32 v[18:19], v[12:13], v[24:25] op_sel_hi:[0,1]\n" \
    "v_cvt_pk_bf16_f32 v30, v28, v29\n" \
    "v_pk_fma_f32 v[24:25], v[24:25], v[12:13], v[18:19] op_sel:[1,1,0] op_sel_hi:[0,1,1] neg_lo:[0,1,0]\n" \
    "ds_write_addtid_b32 v30 offset:39520\n" \
    "v_pk_mul_f32 v[18:19], v[12:13], v[28:29] op_sel_hi:[0,1]\n" \
    "v_cvt_pk_bf16_f32 v31, v24, v25\n" \
    "v_pk_fma_f32 v[28:29], v[28:29], v[12:13], v[18:19] op_sel:[1,1,0] op_sel_hi:[0,1,1] neg_lo:[0,1,0]\n" \
    "ds_write_addtid_b32 v31 offset:33280\n" \
    "v_pk_mul_f32 v[18:19], v[12:13], v[24:25] op_sel_hi:[0,1]\n" \
    "v_cvt_pk_bf16_f32 v30, v28, v29\n" \
    "v_pk_fma_f32 v[24:25], v[24:25], v[12:13], v[18:19] op_sel:[1,1,0] op_sel_hi:[0,1,1] neg_lo:[0,1,0]\n" \
    "ds_write_addtid_b32 v30 offset:41600\n" \
    "v_pk_mul_f32 v[18:19], v[12:13], v[28:29] op_sel_hi:[0,1]\n" \
    "v_cvt_pk_bf16_f32 v31, v24, v25\n" \
    "v_pk_fma_f32 v[28:29], v[28:29], v[12:13], v[18:19] op_sel:[1,1,0] op_sel_hi:[0,1,1] neg_lo:[0,1,0]\n" \
    "ds_write_addtid_b32 v31 offset:35360\n" \
    "v_pk_mul_f32 v[18:19], v[12:13], v[28:29] op_sel_hi:[0,1]\n" \
    "v_cvt_pk_bf16_f32 v30, v28, v29\n" \
    "v_pk_fma_f32 v[28:29], v[28:29], v[12:13], v[18:19] op_sel:[1,1,0] op_sel_hi:[0,1,1] neg_lo:[0,1,0]\n" \
    "ds_write_addtid_b32 v30 offset:43680\n" \
    "v_pk_mul_f32 v[18:19], v[12:13], v[28:29] op_sel_hi:[0,1]\n" \
    "v_cvt_pk_bf16_f32 v31, v28, v29\n" \
    "v_pk_fma_f32 v[28:29], v[28:29], v[12:13], v[18:19] op_sel:[1,1,0] op_sel_hi:[0,1,1] neg_lo:[0,1,0]\n" \
    "ds_write_addtid_b32 v31 offset:45760\n" \
    "v_pk_mul_f32 v[18:19], v[12:13], v[28:29] op_sel_hi:[0,1]\n" \
    "v_cvt_pk_bf16_f32 v30, v28, v29\n" \
    "v_pk_fma_f32 v[28:29], v[28:29], v[12:13], v[18:19] op_sel:[1,1,0] op_sel_hi:[0,1,1] neg_lo:[0,1,0]\n" \
    "ds_write_addtid_b32 v30 offset:47840\n" \
    "ds_read_b32 v60, v32\n" \
    "v_pk_mul_f32 v[18:19], v[12:13], v[28:29] op_sel_hi:[0,1]\n" \
    "v_cvt_pk_bf16_f32 v31, v28, v29\n" \
    "v_pk_fma_f32 v[28:29], v[28:29], v[12:13], v[18:19] op_sel:[1,1,0] op_sel_hi:[0,1,1] neg_lo:[0,1,0]\n" \
    "ds_write_addtid_b32 v31 offset:49920\n" \
    "v_pk_mul_f32 v[18:19], v[12:13], v[28:29] op_sel_hi:[0,1]\n" \
    "v_cvt_pk_bf16_f32 v30, v28, v29\n" \
    "v_pk_fma_f32 v[28:29], v[28:29], v[12:13], v[18:19] op_sel:[1,1,0] op_sel_hi:[0,1,1] neg_lo:[0,1,0]\n" \
    "ds_write_addtid_b32 v30 offset:52000\n" \
    "v_pk_mul_f32 v[18:19], v[12:13], v[28:29] op_sel_hi:[0,1]\n" \
    "v_cvt_pk_bf16_f32 v31, v28, v29\n" \
    "v_pk_fma_f32 v[28:29], v[28:29], v[12:13], v[18:19] op_sel:[1,1,0] op_sel_hi:[0,1,1] neg_lo:[0,1,0]\n" \
    "ds_write_addtid_b32 v31 offset:54080\n" \
    "s_waitcnt lgkmcnt(3)\n" \
    "v_cmp_gt_u32_e32 vcc, 1, v60\n" \
    "s_cbranch_vccz Lae0_%=\n" \
    "s_mov_b32 s90, 0x100000\n" \
    "Lal0_%=:\n" \
    "s_sleep 1\n" \
    "ds_read_b32 v60, v32 offset:0\n" \
    "s_waitcnt lgkmcnt(0)\n" \
    "v_cmp_gt_u32_e32 vcc, 1, v60\n" \
    "s_cbranch_vccz Lae0_%=\n" \
    "s_sub_u32 s90, s90, 1\n" \
    "s_cbranch_scc0 Lal0_%=\n" \
    "Lae0_%=:\n" \
    "ds_read2_b32 v[64:65], v36 offset0:240 offset1:241\n" \
    "ds_read2_b32 v[66:67], v36 offset0:242 offset1:243\n" \
    "ds_read2_b32 v[68:69], v36 offset0:224 offset1:225\n" \
    "ds_read2_b32 v[70:71], v36 offset0:226 offset1:227\n" \
    "v_pk_mul_f32 v[18:19], v[12:13], v[28:29] op_sel_hi:[0,1]\n" \
    "v_cvt_pk_bf16_f32 v30, v28, v29\n" \
    "v_pk_fma_f32 v[28:29], v[28:29], v[12:13], v[18:19] op_sel:[1,1,0] op_sel_hi:[0,1,1] neg_lo:[0,1,0]\n" \
    "ds_write_addtid_b32 v30 offset:56160\n" \
    "ds_read2_b32 v[72:73], v36 offset0:208 offset1:209\n" \
    "ds_read2_b32 v[74:75], v36 offset0:210 offset1:211\n" \
    "ds_read2_b32 v[76:77], v36 offset0:192 offset1:193\n" \
    "ds_read2_b32 v[78:79], v36 offset0:194 offset1:195\n" \
    "v_pk_mul_f32 v[18:19], v[12:13], v[28:29] op_sel_hi:[0,1]\n" \
    "v_cvt_pk_bf16_f32 v31, v28, v29\n" \
    "v_pk_fma_f32 v[28:29], v[28:29], v[12:13], v[18:19] op_sel:[1,1,0] op_sel_hi:[0,1,1] neg_lo:[0,1,0]\n" \
    "ds_write_addtid_b32 v31 offset:58240\n" \
    "ds_read2_b32 v[80:81], v36 offset0:176 offset1:177\n" \
    "ds_read2_b32 v[82:83], v36 offset0:178 offset1:179\n" \
    "ds_read2_b32 v[84:85], v36 offset0:160 offset1:161\n" \
    "ds_read2_b32 v[86:87], v36 offset0:162 offset1:163\n" \
    "v_cvt_pk_bf16_f32 v30, v28, v29\n" \
    "ds_write_addtid_b32 v30 offset:60320\n" \
    "s_mov_b32 m0, %[m0a]\n" \
    "v_mov_b32_e32 v61, 4\n" \
    "ds_write_addtid_b32 v61 offset:54080\n" \
    "ds_read2_b32 v[88:89], v36 offset0:144 offset1:145\n" \
    "ds_read2_b32 v[90:91], v36 offset0:146 offset1:147\n" \
    "ds_read2_b32 v[92:93], v36 offset0:128 offset1:129\n" \
    "ds_read2_b32 v[94:95], v36 offset0:130 offset1:131\n" \
    "s_setprio 2\n" \
    "s_mov_b32 s90, 0x100000\n" \
    "Lal1_%=:\n" \
    "ds_read_b32 v62, v34 offset:0\n" \
    "s_waitcnt lgkmcnt(0)\n" \
    "v_cmp_gt_u32_e32 vcc, 1, v62\n" \
    "s_cbranch_vccz Lae1_%=\n" \
    "s_sleep 1\n" \
    "s_sub_u32 s90, s90, 1\n" \
    "s_cbranch_scc0 Lal1_%=\n" \
    "Lae1_%=:\n" \
    "ds_read_b128 v[156:159], v33 offset:448\n" \
    "ds_read_b128 v[152:155], v33 offset:384\n" \
    "ds_read_b128 v[148:151], v33 offset:320\n" \
    "ds_read_b128 v[144:147], v33 offset:256\n" \
    "ds_read_b128 v[140:143], v33 offset:192\n" \
    "ds_read_b128 v[136:139], v33 offset:128\n" \
    "ds_read_b128 v[132:135], v33 offset:64\n" \
    "ds_read_b128 v[128:131], v33 offset:0\n" \
    "ds_read_b32 v60, v32\n" \
    "s_waitcnt lgkmcnt(8)\n" \
    "v_mfma_f32_16x16x32_bf16 v[0:3], v[64:67], v[156:159], 0\n" \
    "s_waitcnt lgkmcnt(7)\n" \
    "v_mfma_f32_16x16x32_bf16 v[0:3], v[68:71], v[152:155], v[0:3]\n" \
    "s_waitcnt lgkmcnt(6)\n" \
    "v_mfma_f32_16x16x32_bf16 v[0:3], v[72:75], v[148:151], v[0:3]\n" \
    "s_waitcnt lgkmcnt(0)\n" \
    "v_cmp_gt_u32_e32 vcc, 2, v60\n" \
    "s_cbranch_vccz Lae2_%=\n" \
    "s_mov_b32 s90, 0x100000\n" \
    "Lal2_%=:\n" \
    "s_sleep 1\n" \
    "ds_read_b32 v60, v32 offset:0\n" \
    "s_waitcnt lgkmcnt(0)\n" \
    "v_cmp_gt_u32_e32 vcc, 2, v60\n" \
    "s_cbranch_vccz Lae2_%=\n" \
    "s_sub_u32 s90, s90, 1\n" \
    "s_cbranch_scc0 Lal2_%=\n" \
    "Lae2_%=:\n" \
    "v_mfma_f32_16x16x32_bf16 v[0:3], v[76:79], v[144:147], v[0:3]\n" \
    "ds_read2_b32 v[96:97], v36 offset0:112 offset1:113\n" \
    "ds_read2_b32 v[98:99], v36 offset0:114 offset1:115\n" \
    "ds_read2_b32 v[100:101], v36 offset0:96 offset1:97\n" \
    "ds_read2_b32 v[102:103], v36 offset0:98 offset1:99\n" \
    "v_mfma_f32_16x16x32_bf16 v[0:3], v[80:83], v[140:143], v[0:3]\n" \
    "ds_read2_b32 v[104:105], v36 offset0:80 offset1:81\n" \
    "ds_read2_b32 v[106:107], v36 offset0:82 offset1:83\n" \
    "ds_read2_b32 v[108:109], v36 offset0:64 offset1:65\n" \
    "ds_read2_b32 v[110:111], v36 offset0:66 offset1:67\n" \
    "v_mfma_f32_16x16x32_bf16 v[0:3], v[84:87], v[136:139], v[0:3]\n" \
    "ds_read2_b32 v[112:113], v36 offset0:48 offset1:49\n" \
    "ds_read2_b32 v[114:115], v36 offset0:50 offset1:51\n" \
    "ds_read2_b32 v[116:117], v36 offset0:32 offset1:33\n" \
    "ds_read2_b32 v[118:119], v36 offset0:34 offset1:35\n" \
    "v_mfma_f32_16x16x32_bf16 v[0:3], v[88:91], v[132:135], v[0:3]\n" \
    "ds_read2_b32 v[120:121], v36 offset0:16 offset1:17\n" \
    "ds_read2_b32 v[122:123], v36 offset0:18 offset1:19\n" \
    "ds_read2_b32 v[124:125], v36 offset0:0 offset1:1\n" \
    "ds_read2_b32 v[126:127], v36 offset0:2 offset1:3\n" \
    "v_mfma_f32_16x16x32_bf16 v[0:3], v[92:95], v[128:131], v[0:3]\n" \
    "s_waitcnt lgkmcnt(14)\n" \
    "v_mfma_f32_16x16x32_bf16 v[4:7], v[96:99], v[156:159], 0\n" \
    "ds_read_b128 v[156:159], v33 offset:960\n" \
    "s_waitcnt lgkmcnt(13)\n" \
    "v_mfma_f32_16x16x32_bf16 v[4:7], v[100:103], v[152:155], v[4:7]\n" \
    "ds_read_b128 v[152:155], v33 offset:896\n" \
    "ds_read_b32 v62, v34\n" \
    "s_waitcnt lgkmcnt(13)\n" \
    "v_mfma_f32_16x16x32_bf16 v[4:7], v[104:107], v[148:151], v[4:7]\n" \
    "ds_read_b128 v[148:151], v33 offset:832\n" \
    "s_waitcnt lgkmcnt(12)\n" \
    "v_mfma_f32_16x16x32_bf16 v[4:7], v[108:111], v[144:147], v[4:7]\n" \
    "ds_read_b128 v[144:147], v33 offset:768\n" \
    "s_waitcnt lgkmcnt(11)\n" \
    "v_mfma_f32_16x16x32_bf16 v[4:7], v[112:115], v[140:143], v[4:7]\n" \
    "ds_read_b128 v[140:143], v33 offset:704\n" \
    "s_waitcnt lgkmcnt(10)\n" \
    "v_mfma_f32_16x16x32_bf16 v[4:7], v[116:119], v[136:139], v[4:7]\n" \
    "ds_read_b128 v[136:139], v33 offset:640\n" \
    "s_waitcnt lgkmcnt(9)\n" \
    "v_mfma_f32_16x16x32_bf16 v[4:7], v[120:123], v[132:135], v[4:7]\n" \
    "ds_read_b128 v[132:135], v33 offset:576\n" \
    "s_waitcnt lgkmcnt(8)\n" \
    "v_mfma_f32_16x16x32_bf16 v[4:7], v[124:127], v[128:131], v[4:7]\n" \
    "ds_read_b128 v[128:131], v33 offset:512\n" \
    "s_waitcnt lgkmcnt(6)\n" \
    "v_cmp_gt_u32_e32 vcc, 3, v62\n" \
    "s_cbranch_vccnz Las3_%=\n" \
    "Lae3_%=:\n" \
    "s_waitcnt lgkmcnt(4)\n" \
    "v_mfma_f32_16x16x32_bf16 v[4:7], v[64:67], v[156:159], v[4:7]\n" \
    "v_mfma_f32_16x16x32_bf16 v[8:11], v[96:99], v[156:159], 0\n" \
    "ds_read_b128 v[156:159], v33 offset:1472\n" \
    "v_mfma_f32_16x16x32_bf16 v[4:7], v[68:71], v[152:155], v[4:7]\n" \
    "v_mfma_f32_16x16x32_bf16 v[8:11], v[100:103], v[152:155], v[8:11]\n" \
    "ds_read_b128 v[152:155], v33 offset:1408\n" \
    "v_mov_b32_e64 v61, 11\n" \
    "v_mfma_f32_16x16x32_bf16 v[4:7], v[72:75], v[148:151], v[4:7]\n" \
    "ds_write_b128 v37, v[0:3] offset:0\n" \
    "ds_write_addtid_b32 v61 offset:54080\n" \
    "v_mfma_f32_16x16x32_bf16 v[8:11], v[104:107], v[148:151], v[8:11]\n" \
    "ds_read_b128 v[148:151], v33 offset:1344\n" \
    "v_mfma_f32_16x16x32_bf16 v[4:7], v[76:79], v[144:147], v[4:7]\n" \
    "v_mfma_f32_16x16x32_bf16 v[8:11], v[108:111], v[144:147], v[8:11]\n" \
    "ds_read_b128 v[144:147], v33 offset:1280\n" \
    "s_waitcnt lgkmcnt(6)\n" \
    "v_mfma_f32_16x16x32_bf16 v[4:7], v[80:83], v[140:143], v[4:7]\n" \
    "v_mfma_f32_16x16x32_bf16 v[8:11], v[112:115], v[140:143], v[8:11]\n" \
    "ds_read_b128 v[140:143], v33 offset:1216\n" \
    "v_mfma_f32_16x16x32_bf16 v[4:7], v[84:87], v[136:139], v[4:7]\n" \
    "v_mfma_f32_16x16x32_bf16 v[8:11], v[116:119], v[136:139], v[8:11]\n" \
    "ds_read_b128 v[136:139], v33 offset:1152\n" \
    "v_mfma_f32_16x16x32_bf16 v[4:7], v[88:91], v[132:135], v[4:7]\n" \
    "v_mfma_f32_16x16x32_bf16 v[8:11], v[120:123], v[132:135], v[8:11]\n" \
    "ds_read_b128 v[132:135], v33 offset:1088\n" \
    "v_mfma_f32_16x16x32_bf16 v[4:7], v[92:95], v[128:131], v[4:7]\n" \
    "v_mfma_f32_16x16x32_bf16 v[8:11], v[124:127], v[128:131], v[8:11]\n" \
    "ds_read_b128 v[128:131], v33 offset:1024\n" \
    "s_waitcnt lgkmcnt(4)\n" \
    "v_mfma_f32_16x16x32_bf16 v[8:11], v[64:67], v[156:159], v[8:11]\n" \
    "v_mfma_f32_16x16x32_bf16 v[12:15], v[96:99], v[156:159], 0\n" \
    "ds_read_b128 v[156:159], v33 offset:1984\n" \
    "v_mfma_f32_16x16x32_bf16 v[8:11], v[68:71], v[152:155], v[8:11]\n" \
    "v_mfma_f32_16x16x32_bf16 v[12:15], v[100:103], v[152:155], v[12:15]\n" \
    "ds_read_b128 v[152:155], v33 offset:1920\n" \
    "v_mov_b32_e64 v61, 12\n" \
    "v_mfma_f32_16x16x32_bf16 v[8:11], v[72:75], v[148:151], v[8:11]\n" \
    "ds_write_b128 v37, v[4:7] offset:4160\n" \
    "ds_write_addtid_b32 v61 offset:54080\n" \
    "v_mfma_f32_16x16x32_bf16 v[12:15], v[104:107], v[148:151], v[12:15]\n" \
    "ds_read_b128 v[148:151], v33 offset:1856\n" \
    "v_mfma_f32_16x16x32_bf16 v[8:11], v[76:79], v[144:147], v[8:11]\n" \
    "v_mfma_f32_16x16x32_bf16 v[12:15], v[108:111], v[144:147], v[12:15]\n" \
    "ds_read_b128 v[144:147], v33 offset:1792\n" \
    "s_waitcnt lgkmcnt(6)\n" \
    "v_mfma_f32_16x16x32_bf16 v[8:11], v[80:83], v[140:143], v[8:11]\n" \
    "v_mfma_f32_16x16x32_bf16 v[12:15], v[112:115], v[140:143], v[12:15]\n" \
    "ds_read_b128 v[140:143], v33 offset:1728\n" \
    "v_mfma_f32_16x16x32_bf16 v[8:11], v[84:87], v[136:139], v[8:11]\n" \
    "v_mfma_f32_16x16x32_bf16 v[12:15], v[116:119], v[136:139], v[12:15]\n" \
    "ds_read_b128 v[136:139], v33 offset:1664\n" \
    "v_mfma_f32_16x16x32_bf16 v[8:11], v[88:91], v[132:135], v[8:11]\n" \
    "ds_read_b32 v62, v34\n" \
    "v_mfma_f32_16x16x32_bf16 v[12:15], v[120:123], v[132:135], v[12:15]\n" \
    "ds_read_b128 v[132:135], v33 offset:1600\n" \
    "v_mfma_f32_16x16x32_bf16 v[8:11], v[92:95], v[128:131], v[8:11]\n" \
    "v_mfma_f32_16x16x32_bf16 v[12:15], v[124:127], v[128:131], v[12:15]\n" \
    "ds_read_b128 v[128:131], v33 offset:1536\n" \
    "s_waitcnt lgkmcnt(2)\n" \
    "v_cmp_gt_u32_e32 vcc, 5, v62\n" \
    "s_cbranch_vccnz Las4_%=\n" \
    "Lae4_%=:\n" \
    "v_mfma_f32_16x16x32_bf16 v[12:15], v[64:67], v[156:159], v[12:15]\n" \
    "v_mfma_f32_16x16x32_bf16 v[16:19], v[96:99], v[156:159], 0\n" \
    "ds_read_b128 v[156:159], v33 offset:2496\n" \
    "v_mfma_f32_16x16x32_bf16 v[12:15], v[68:71], v[152:155], v[12:15]\n" \
    "v_mfma_f32_16x16x32_bf16 v[16:19], v[100:103], v[152:155], v[16:19]\n" \
    "ds_read_b128 v[152:155], v33 offset:2432\n" \
    "v_mov_b32_e64 v61, 13\n" \
    "v_mfma_f32_16x16x32_bf16 v[12:15], v[72:75], v[148:151], v[12:15]\n" \
    "ds_write_b128 v37, v[8:11] offset:8320\n" \
    "ds_write_addtid_b32 v61 offset:54080\n" \
    "v_mfma_f32_16x16x32_bf16 v[16:19], v[104:107], v[148:151], v[16:19]\n" \
    "ds_read_b128 v[148:151], v33 offset:2368\n" \
    "v_mfma_f32_16x16x32_bf16 v[12:15], v[76:79], v[144:147], v[12:15]\n" \
    "v_mfma_f32_16x16x32_bf16 v[16:19], v[108:111], v[144:147], v[16:19]\n" \
    "ds_read_b128 v[144:147], v33 offset:2304\n" \
    "s_waitcnt lgkmcnt(6)\n" \
    "v_mfma_f32_16x16x32_bf16 v[12:15], v[80:83], v[140:143], v[12:15]\n" \
    "v_mfma_f32_16x16x32_bf16 v[16:19], v[112:115], v[140:143], v[16:19]\n" \
    "ds_read_b128 v[140:143], v33 offset:2240\n" \
    "v_mfma_f32_16x16x32_bf16 v[12:15], v[84:87], v[136:139], v[12:15]\n" \
    "v_mfma_f32_16x16x32_bf16 v[16:19], v[116:119], v[136:139], v[16:19]\n" \
    "ds_read_b128 v[136:139], v33 offset:2176\n" \
    "v_mfma_f32_16x16x32_bf16 v[12:15], v[88:91], v[132:135], v[12:15]\n" \
    "v_mfma_f32_16x16x32_bf16 v[16:19], v[120:123], v[132:135], v[16:19]\n" \
    "ds_read_b128 v[132:135], v33 offset:2112\n" \
    "v_mfma_f32_16x16x32_bf16 v[12:15], v[92:95], v[128:131], v[12:15]\n" \
    "v_mfma_f32_16x16x32_bf16 v[16:19], v[124:127], v[128:131], v[16:19]\n" \
    "ds_read_b128 v[128:131], v33 offset:2048\n" \
    "s_waitcnt lgkmcnt(4)\n" \
    "v_mfma_f32_16x16x32_bf16 v[16:19], v[64:67], v[156:159], v[16:19]\n" \
    "v_mfma_f32_16x16x32_bf16 v[20:23], v[96:99], v[156:159], 0\n" \
    "ds_read_b128 v[156:159], v33 offset:3008\n" \
    "v_mfma_f32_16x16x32_bf16 v[16:19], v[68:71], v[152:155], v[16:19]\n" \
    "v_mfma_f32_16x16x32_bf16 v[20:23], v[100:103], v[152:155], v[20:23]\n" \
    "ds_read_b128 v[152:155], v33 offset:2944\n" \
    "v_mov_b32_e64 v61, 14\n" \
    "v_mfma_f32_16x16x32_bf16 v[16:19], v[72:75], v[148:151], v[16:19]\n" \
    "ds_write_b128 v37, v[12:15] offset:12480\n" \
    "ds_write_addtid_b32 v61 offset:54080\n" \
    "v_mfma_f32_16x16x32_bf16 v[20:23], v[104:107], v[148:151], v[20:23]\n" \
    "ds_read_b128 v[148:151], v33 offset:2880\n" \
    "v_mfma_f32_16x16x32_bf16 v[16:19], v[76:79], v[144:147], v[16:19]\n" \
    "v_mfma_f32_16x16x32_bf16 v[20:23], v[108:111], v[144:147], v[20:23]\n" \
    "ds_read_b128 v[144:147], v33 offset:2816\n" \
    "s_waitcnt lgkmcnt(6)\n" \
    "v_mfma_f32_16x16x32_bf16 v[16:19], v[80:83], v[140:143], v[16:19]\n" \
    "v_mfma_f32_16x16x32_bf16 v[20:23], v[112:115], v[140:143], v[20:23]\n" \
    "ds_read_b128 v[140:143], v33 offset:2752\n" \
    "v_mfma_f32_16x16x32_bf16 v[16:19], v[84:87], v[136:139], v[16:19]\n" \
    "v_mfma_f32_16x16x32_bf16 v[20:23], v[116:119], v[136:139], v[20:23]\n" \
    "ds_read_b128 v[136:139], v33 offset:2688\n" \
    "v_mfma_f32_16x16x32_bf16 v[16:19], v[88:91], v[132:135], v[16:19]\n" \
    "ds_read_b32 v62, v34\n" \
    "v_mfma_f32_16x16x32_bf16 v[20:23], v[120:123], v[132:135], v[20:23]\n" \
    "ds_read_b128 v[132:135], v33 offset:2624\n" \
    "v_mfma_f32_16x16x32_bf16 v[16:19], v[92:95], v[128:131], v[16:19]\n" \
    "v_mfma_f32_16x16x32_bf16 v[20:23], v[124:127], v[128:131], v[20:23]\n" \
    "ds_read_b128 v[128:131], v33 offset:2560\n" \
    "s_waitcnt lgkmcnt(2)\n" \
    "v_cmp_gt_u32_e32 vcc, 7, v62\n" \
    "s_cbranch_vccnz Las5_%=\n" \
    "Lae5_%=:\n" \
    "v_mfma_f32_16x16x32_bf16 v[20:23], v[64:67], v[156:159], v[20:23]\n" \
    "v_mfma_f32_16x16x32_bf16 v[24:27], v[96:99], v[156:159], 0\n" \
    "ds_read_b128 v[156:159], v33 offset:3520\n" \
    "v_mfma_f32_16x16x32_bf16 v[20:23], v[68:71], v[152:155], v[20:23]\n" \
    "v_mfma_f32_16x16x32_bf16 v[24:27], v[100:103], v[152:155], v[24:27]\n" \
    "ds_read_b128 v[152:155], v33 offset:3456\n" \
    "v_mov_b32_e64 v61, 15\n" \
    "v_mfma_f32_16x16x32_bf16 v[20:23], v[72:75], v[148:151], v[20:23]\n" \
    "ds_write_b128 v37, v[16:19] offset:16640\n" \
    "ds_write_addtid_b32 v61 offset:54080\n" \
    "v_mfma_f32_16x16x32_bf16 v[24:27], v[104:107], v[148:151], v[24:27]\n" \
    "ds_read_b128 v[148:151], v33 offset:3392\n" \
    "v_mfma_f32_16x16x32_bf16 v[20:23], v[76:79], v[144:147], v[20:23]\n" \
    "v_mfma_f32_16x16x32_bf16 v[24:27], v[108:111], v[144:147], v[24:27]\n" \
    "ds_read_b128 v[144:147], v33 offset:3328\n" \
    "s_waitcnt lgkmcnt(6)\n" \
    "v_mfma_f32_16x16x32_bf16 v[20:23], v[80:83], v[140:143], v[20:23]\n" \
    "v_mfma_f32_16x16x32_bf16 v[24:27], v[112:115], v[140:143], v[24:27]\n" \
    "ds_read_b128 v[140:143], v33 offset:3264\n" \
    "v_mfma_f32_16x16x32_bf16 v[20:23], v[84:87], v[136:139], v[20:23]\n" \
    "v_mfma_f32_16x16x32_bf16 v[24:27], v[116:119], v[136:139], v[24:27]\n" \
    "ds_read_b128 v[136:139], v33 offset:3200\n" \
    "v_mfma_f32_16x16x32_bf16 v[20:23], v[88:91], v[132:135], v[20:23]\n" \
    "v_mfma_f32_16x16x32_bf16 v[24:27], v[120:123], v[132:135], v[24:27]\n" \
    "ds_read_b128 v[132:135], v33 offset:3136\n" \
    "v_mfma_f32_16x16x32_bf16 v[20:23], v[92:95], v[128:131], v[20:23]\n" \
    "v_mfma_f32_16x16x32_bf16 v[24:27], v[124:127], v[128:131], v[24:27]\n" \
    "ds_read_b128 v[128:131], v33 offset:3072\n" \
    "s_waitcnt lgkmcnt(4)\n" \
    "v_mfma_f32_16x16x32_bf16 v[24:27], v[64:67], v[156:159], v[24:27]\n" \
    "v_mfma_f32_16x16x32_bf16 v[28:31], v[96:99], v[156:159], 0\n" \
    "ds_read_b128 v[156:159], v33 offset:4032\n" \
    "v_mfma_f32_16x16x32_bf16 v[24:27], v[68:71], v[152:155], v[24:27]\n" \
    "v_mfma_f32_16x16x32_bf16 v[28:31], v[100:103], v[152:155], v[28:31]\n" \
    "ds_read_b128 v[152:155], v33 offset:3968\n" \
    "v_mov_b32_e64 v61, 16\n" \
    "v_mfma_f32_16x16x32_bf16 v[24:27], v[72:75], v[148:151], v[24:27]\n" \
    "ds_write_b128 v37, v[20:23] offset:20800\n" \
    "ds_write_addtid_b32 v61 offset:54080\n" \
    "v_mfma_f32_16x16x32_bf16 v[28:31], v[104:107], v[148:151], v[28:31]\n" \
    "ds_read_b128 v[148:151], v33 offset:3904\n" \
    "v_mfma_f32_16x16x32_bf16 v[24:27], v[76:79], v[144:147], v[24:27]\n" \
    "v_mfma_f32_16x16x32_bf16 v[28:31], v[108:111], v[144:147], v[28:31]\n" \
    "ds_read_b128 v[144:147], v33 offset:3840\n" \
    "s_waitcnt lgkmcnt(6)\n" \
    "v_mfma_f32_16x16x32_bf16 v[24:27], v[80:83], v[140:143], v[24:27]\n" \
    "v_mfma_f32_16x16x32_bf16 v[28:31], v[112:115], v[140:143], v[28:31]\n" \
    "ds_read_b128 v[140:143], v33 offset:3776\n" \
    "v_mfma_f32_16x16x32_bf16 v[24:27], v[84:87], v[136:139], v[24:27]\n" \
    "v_mfma_f32_16x16x32_bf16 v[28:31], v[116:119], v[136:139], v[28:31]\n" \
    "ds_read_b128 v[136:139], v33 offset:3712\n" \
    "v_mfma_f32_16x16x32_bf16 v[24:27], v[88:91], v[132:135], v[24:27]\n" \
    "v_mfma_f32_16x16x32_bf16 v[28:31], v[120:123], v[132:135], v[28:31]\n" \
    "ds_read_b128 v[132:135], v33 offset:3648\n" \
    "v_mfma_f32_16x16x32_bf16 v[24:27], v[92:95], v[128:131], v[24:27]\n" \
    "v_mfma_f32_16x16x32_bf16 v[28:31], v[124:127], v[128:131], v[28:31]\n" \
    "ds_read_b128 v[128:131], v33 offset:3584\n" \
    "s_waitcnt lgkmcnt(4)\n" \
    "v_mfma_f32_16x16x32_bf16 v[28:31], v[64:67], v[156:159], v[28:31]\n" \
    "v_mfma_f32_16x16x32_bf16 v[28:31], v[68:71], v[152:155], v[28:31]\n" \
    "v_mfma_f32_16x16x32_bf16 v[28:31], v[72:75], v[148:151], v[28:31]\n" \
    "v_mov_b32_e64 v61, 17\n" \
    "v_mfma_f32_16x16x32_bf16 v[28:31], v[76:79], v[144:147], v[28:31]\n" \
    "ds_write_b128 v37, v[24:27] offset:24960\n" \
    "ds_write_addtid_b32 v61 offset:54080\n" \
    "s_waitcnt lgkmcnt(2)\n" \
    "v_mfma_f32_16x16x32_bf16 v[28:31], v[80:83], v[140:143], v[28:31]\n" \
    "v_mfma_f32_16x16x32_bf16 v[28:31], v[84:87], v[136:139], v[28:31]\n" \
    "v_mfma_f32_16x16x32_bf16 v[28:31], v[88:91], v[132:135], v[28:31]\n" \
    "v_mfma_f32_16x16x32_bf16 v[28:31], v[92:95], v[128:131], v[28:31]\n" \
    "v_mov_b32_e64 v61, 18\n" \
    "s_nop 6\n" \
    "ds_write_b128 v37, v[28:31] offset:29120\n" \
    "ds_write_addtid_b32 v61 offset:54080\n" \
    "s_branch Lax_%=\n" \
    "Las3_%=:\n" \
    "s_mov_b32 s90, 0x100000\n" \
    "Lal3_%=:\n" \
    "ds_read_b32 v62, v34 offset:0\n" \
    "s_waitcnt lgkmcnt(0)\n" \
    "v_cmp_gt_u32_e32 vcc, 3, v62\n" \
    "s_cbranch_vccz Lae3_%=\n" \
    "s_sub_u32 s90, s90, 1\n" \
    "s_cbranch_scc0 Lal3_%=\n" \
    "s_branch Lae3_%=\n" \
    "Las4_%=:\n" \
    "s_mov_b32 s90, 0x100000\n" \
    "Lal4_%=:\n" \
    "ds_read_b32 v62, v34 offset:0\n" \
    "s_waitcnt lgkmcnt(0)\n" \
    "v_cmp_gt_u32_e32 vcc, 5, v62\n" \
    "s_cbranch_vccz Lae4_%=\n" \
    "s_sub_u32 s90, s90, 1\n" \
    "s_cbranch_scc0 Lal4_%=\n" \
    "s_branch Lae4_%=\n" \
    "Las5_%=:\n" \
    "s_mov_b32 s90, 0x100000\n" \
    "Lal5_%=:\n" \
    "ds_read_b32 v62, v34 offset:0\n" \
    "s_waitcnt lgkmcnt(0)\n" \
    "v_cmp_gt_u32_e32 vcc, 7, v62\n" \
    "s_cbranch_vccz Lae5_%=\n" \
    "s_sub_u32 s90, s90, 1\n" \
    "s_cbranch_scc0 Lal5_%=\n" \
    "s_branch Lae5_%=\n" \
    "Lax_%=:\n"
#define CONV_A_CLOB "v0", "v1", "v2", "v3", "v4", "v5", "v6", "v7", "v8", "v9", "v10", "v11", "v12", "v13", "v14", "v15", "v16", "v17", "v18", "v19", "v20", "v21", "v22", "v23", "v24", "v25", "v26", "v27", "v28", "v29", "v30", "v31", "v32", "v33", "v34", "v36", "v37", "v60", "v61", "v62", "v64", "v65", "v66", "v67", "v68", "v69", "v70", "v71", "v72", "v73", "v74", "v75", "v76", "v77", "v78", "v79", "v80", "v81", "v82", "v83", "v84", "v85", "v86", "v87", "v88", "v89", "v90", "v91", "v92", "v93", "v94", "v95", "v96", "v97", "v98", "v99", "v100", "v101", "v102", "v103", "v104", "v105", "v106", "v107", "v108", "v109", "v110", "v111", "v112", "v113", "v114", "v115", "v116", "v117", "v118", "v119", "v120", "v121", "v122", "v123", "v124", "v125", "v126", "v127", "v128", "v129", "v130", "v131", "v132", "v133", "v134", "v135", "v136", "v137", "v138", "v139", "v140", "v141", "v142", "v143", "v144", "v145", "v146", "v147", "v148", "v149", "v150", "v151", "v152", "v153", "v154", "v155", "v156", "v157", "v158", "v159"
#define CONV_B_ASM \
    "s_mov_b32 s92, 0x07060302\n" \
    "s_setprio 2\n" \
    "s_waitcnt vmcnt(4)\n" \
    "v_mul_f32_e32 v0, 0x3fb8aa3b, v70\n" \
    "v_exp_f32_e32 v0, v0\n" \
    "v_mul_f32_e32 v1, 0x3fb8aa3b, v64\n" \
    "v_cvt_pk_bf16_f32 v0, 0, v0\n" \
    "v_mul_f32_e32 v2, 0.15915494, v65\n" \
    "v_mul_f32_e32 v1, v0, v1\n" \
    "v_mul_f32_e32 v2, v0, v2\n" \
    "v_mul_f32_e32 v4, 0x41000000, v2\n" \
    "v_mul_f32_e32 v3, 0x41000000, v1\n" \
    "v_fract_f32_e32 v4, v4\n" \
    "v_exp_f32_e32 v3, v3\n" \
    "v_cos_f32_e32 v8, v4\n" \
    "v_sin_f32_e32 v9, v4\n" \
    "v_mul_f32_e32 v66, v0, v66\n" \
    "v_mul_f32_e64 v8, v3, v8\n" \
    "v_mul_f32_e64 v9, v3, v9\n" \
    "v_pk_mul_f32 v[18:19], v[8:9], v[8:9] op_sel_hi:[0,1]\n" \
    "v_cvt_pk_bf16_f32 v30, v8, v9\n" \
    "v_pk_fma_f32 v[14:15], v[8:9], v[8:9], v[18:19] op_sel:[1,1,0] op_sel_hi:[0,1,1] neg_lo:[0,1,0]\n" \
    "ds_write_addtid_b32 v30 offset:2080\n" \
    "v_pk_mul_f32 v[18:19], v[8:9], v[14:15] op_sel_hi:[0,1]\n" \
    "v_cvt_pk_bf16_f32 v31, v14, v15\n" \
    "v_pk_fma_f32 v[14:15], v[14:15], v[8:9], v[18:19] op_sel:[1,1,0] op_sel_hi:[0,1,1] neg_lo:[0,1,0]\n" \
    "ds_write_addtid_b32 v31 offset:4160\n" \
    "v_pk_mul_f32 v[18:19], v[8:9], v[14:15] op_sel_hi:[0,1]\n" \
    "v_cvt_pk_bf16_f32 v30, v14, v15\n" \
    "v_pk_fma_f32 v[14:15], v[14:15], v[8:9], v[18:19] op_sel:[1,1,0] op_sel_hi:[0,1,1] neg_lo:[0,1,0]\n" \
    "ds_write_addtid_b32 v30 offset:6240\n" \
    "v_pk_mul_f32 v[18:19], v[8:9], v[14:15] op_sel_hi:[0,1]\n" \
    "v_cvt_pk_bf16_f32 v31, v14, v15\n" \
    "v_pk_fma_f32 v[14:15], v[14:15], v[8:9], v[18:19] op_sel:[1,1,0] op_sel_hi:[0,1,1] neg_lo:[0,1,0]\n" \
    "ds_write_addtid_b32 v31 offset:8320\n" \
    "v_mul_f32_e32 v4, 0x43000000, v2\n" \
    "v_mul_f32_e32 v3, 0x43000000, v1\n" \
    "v_fract_f32_e32 v4, v4\n" \
    "v_exp_f32_e32 v3, v3\n" \
    "v_cos_f32_e32 v12, v4\n" \
    "v_sin_f32_e32 v13, v4\n" \
    "v_pk_mul_f32 v[18:19], v[8:9], v[14:15] op_sel_hi:[0,1]\n" \
    "v_cvt_pk_bf16_f32 v30, v14, v15\n" \
    "v_pk_fma_f32 v[14:15], v[14:15], v[8:9], v[18:19] op_sel:[1,1,0] op_sel_hi:[0,1,1] neg_lo:[0,1,0]\n" \
    "ds_write_addtid_b32 v30 offset:10400\n" \
    "v_pk_mul_f32 v[18:19], v[8:9], v[14:15] op_sel_hi:[0,1]\n" \
    "v_cvt_pk_bf16_f32 v31, v14, v15\n" \
    "v_pk_fma_f32 v[14:15], v[14:15], v[8:9], v[18:19] op_sel:[1,1,0] op_sel_hi:[0,1,1] neg_lo:[0,1,0]\n" \
    "ds_write_addtid_b32 v31 offset:12480\n" \
    "v_mul_f32_e64 v12, v3, v12\n" \
    "v_mul_f32_e64 v13, -v3, v13\n" \
    "v_pk_mul_f32 v[18:19], v[8:9], v[14:15] op_sel_hi:[0,1]\n" \
    "v_cvt_pk_bf16_f32 v30, v14, v15\n" \
    "v_pk_fma_f32 v[14:15], v[14:15], v[8:9], v[18:19] op_sel:[1,1,0] op_sel_hi:[0,1,1] neg_lo:[0,1,0]\n" \
    "ds_write_addtid_b32 v30 offset:14560\n" \
    "v_pk_mul_f32 v[18:19], v[8:9], v[14:15] op_sel_hi:[0,1]\n" \
    "v_cvt_pk_bf16_f32 v31, v14, v15\n" \
    "v_pk_fma_f32 v[14:15], v[14:15], v[8:9], v[18:19] op_sel:[1,1,0] op_sel_hi:[0,1,1] neg_lo:[0,1,0]\n" \
    "ds_write_addtid_b32 v31 offset:16640\n" \
    "v_fract_f32_e32 v6, v2\n" \
    "v_exp_f32_e32 v5, v1\n" \
    "v_cos_f32_e32 v10, v6\n" \
    "v_sin_f32_e32 v11, v6\n" \
    "v_pk_mul_f32 v[18:19], v[8:9], v[14:15] op_sel_hi:[0,1]\n" \
    "v_cvt_pk_bf16_f32 v30, v14, v15\n" \
    "v_pk_fma_f32 v[14:15], v[14:15], v[8:9], v[18:19] op_sel:[1,1,0] op_sel_hi:[0,1,1] neg_lo:[0,1,0]\n" \
    "ds_write_addtid_b32 v30 offset:18720\n" \
    "v_pk_mul_f32 v[18:19], v[8:9], v[14:15] op_sel_hi:[0,1]\n" \
    "v_cvt_pk_bf16_f32 v31, v14, v15\n" \
    "v_pk_fma_f32 v[14:15], v[14:15], v[8:9], v[18:19] op_sel:[1,1,0] op_sel_hi:[0,1,1] neg_lo:[0,1,0]\n" \
    "ds_write_addtid_b32 v31 offset:20800\n" \
    "v_mul_f32_e64 v10, v5, v10\n" \
    "v_mul_f32_e64 v11, -v5, v11\n" \
    "v_pk_mul_f32 v[18:19], v[8:9], v[14:15] op_sel_hi:[0,1]\n" \
    "v_cvt_pk_bf16_f32 v30, v14, v15\n" \
    "v_pk_fma_f32 v[14:15], v[14:15], v[8:9], v[18:19] op_sel:[1,1,0] op_sel_hi:[0,1,1] neg_lo:[0,1,0]\n" \
    "ds_write_addtid_b32 v30 offset:22880\n" \
    "v_mul_f32_e64 v67, v0, v67\n" \
    "v_mul_f32_e64 v16, v68, v66\n" \
    "v_mul_f32_e64 v17, -v68, v67\n" \
    "v_fma_f32 v16, -v69, v67, v16\n" \
    "v_fma_f32 v17, -v69, v66, v17\n" \
    "v_pk_mul_f32 v[18:19], v[8:9], v[14:15] op_sel_hi:[0,1]\n" \
    "v_cvt_pk_bf16_f32 v31, v14, v15\n" \
    "v_pk_fma_f32 v[14:15], v[14:15], v[8:9], v[18:19] op_sel:[1,1,0] op_sel_hi:[0,1,1] neg_lo:[0,1,0]\n" \
    "ds_write_addtid_b32 v31 offset:24960\n" \
    "v_pk_mul_f32 v[18:19], v[8:9], v[14:15] op_sel_hi:[0,1]\n" \
    "v_cvt_pk_bf16_f32 v30, v14, v15\n" \
    "v_pk_fma_f32 v[14:15], v[14:15], v[8:9], v[18:19] op_sel:[1,1,0] op_sel_hi:[0,1,1] neg_lo:[0,1,0]\n" \
    "ds_write_addtid_b32 v30 offset:27040\n" \
    "v_pk_mul_f32 v[18:19], v[8:9], v[14:15] op_sel_hi:[0,1]\n" \
    "v_cvt_pk_bf16_f32 v31, v14, v15\n" \
    "v_pk_fma_f32 v[14:15], v[14:15], v[8:9], v[18:19] op_sel:[1,1,0] op_sel_hi:[0,1,1] neg_lo:[0,1,0]\n" \
    "ds_write_addtid_b32 v31 offset:29120\n" \
    "v_pk_mul_f32 v[18:19], v[12:13], v[16:17] op_sel_hi:[0,1]\n" \
    "v_cvt_pk_bf16_f32 v30, v14, v15\n" \
    "v_pk_fma_f32 v[20:21], v[16:17], v[12:13], v[18:19] op_sel:[1,1,0] op_sel_hi:[0,1,1] neg_lo:[0,1,0]\n" \
    "ds_write_addtid_b32 v30 offset:31200\n" \
    "v_mov_b32_e32 v61, 1\n" \
    "ds_write_addtid_b32 v61 offset:54080\n" \
    "s_add_u32 m0, %[m0a], 54080\n" \
    "v_cvt_pk_bf16_f32 v31, v16, v17\n" \
    "ds_write_addtid_b32 v31 offset:4160\n" \
    "v_pk_mul_f32 v[18:19], v[10:11], v[16:17] op_sel_hi:[0,1]\n" \
    "v_cvt_pk_bf16_f32 v30, v20, v21\n" \
    "v_pk_fma_f32 v[16:17], v[16:17], v[10:11], v[18:19] op_sel:[1,1,0] op_sel_hi:[0,1,1] neg_lo:[0,1,0]\n" \
    "ds_write_addtid_b32 v30 offset:20800\n" \
    "v_pk_mul_f32 v[18:19], v[12:13], v[16:17] op_sel_hi:[0,1]\n" \
    "v_cvt_pk_bf16_f32 v31, v16, v17\n" \
    "v_pk_fma_f32 v[14:15], v[16:17], v[12:13], v[18:19] op_sel:[1,1,0] op_sel_hi:[0,1,1] neg_lo:[0,1,0]\n" \
    "ds_write_addtid_b32 v31 offset:6240\n" \
    "v_pk_mul_f32 v[18:19], v[10:11], v[16:17] op_sel_hi:[0,1]\n" \
    "v_cvt_pk_bf16_f32 v30, v14, v15\n" \
    "v_pk_fma_f32 v[16:17], v[16:17], v[10:11], v[18:19] op_sel:[1,1,0] op_sel_hi:[0,1,1] neg_lo:[0,1,0]\n" \
    "ds_write_addtid_b32 v30 offset:22880\n" \
    "v_pk_mul_f32 v[18:19], v[12:13], v[16:17] op_sel_hi:[0,1]\n" \
    "v_cvt_pk_bf16_f32 v31, v16, v17\n" \
    "v_pk_fma_f32 v[22:23], v[16:17], v[12:13], v[18:19] op_sel:[1,1,0] op_sel_hi:[0,1,1] neg_lo:[0,1,0]\n" \
    "ds_write_addtid_b32 v31 offset:8320\n" \
    "v_pk_mul_f32 v[18:19], v[10:11], v[16:17] op_sel_hi:[0,1]\n" \
    "v_cvt_pk_bf16_f32 v30, v22, v23\n" \
    "v_pk_fma_f32 v[16:17], v[16:17], v[10:11], v[18:19] op_sel:[1,1,0] op_sel_hi:[0,1,1] neg_lo:[0,1,0]\n" \
    "ds_write_addtid_b32 v30 offset:24960\n" \
    "v_pk_mul_f32 v[18:19], v[12:13], v[16:17] op_sel_hi:[0,1]\n" \
    "v_cvt_pk_bf16_f32 v31, v16, v17\n" \
    "v_pk_fma_f32 v[14:15], v[16:17], v[12:13], v[18:19] op_sel:[1,1,0] op_sel_hi:[0,1,1] neg_lo:[0,1,0]\n" \
    "ds_write_addtid_b32 v31 offset:10400\n" \
    "v_pk_mul_f32 v[18:19], v[10:11], v[16:17] op_sel_hi:[0,1]\n" \
    "v_cvt_pk_bf16_f32 v30, v14, v15\n" \
    "v_pk_fma_f32 v[16:17], v[16:17], v[10:11], v[18:19] op_sel:[1,1,0] op_sel_hi:[0,1,1] neg_lo:[0,1,0]\n" \
    "ds_write_addtid_b32 v30 offset:27040\n" \
    "v_pk_mul_f32 v[18:19], v[12:13], v[16:17] op_sel_hi:[0,1]\n" \
    "v_cvt_pk_bf16_f32 v31, v16, v17\n" \
    "v_pk_fma_f32 v[24:25], v[16:17], v[12:13], v[18:19] op_sel:[1,1,0] op_sel_hi:[0,1,1] neg_lo:[0,1,0]\n" \
    "ds_write_addtid_b32 v31 offset:12480\n" \
    "v_pk_mul_f32 v[18:19], v[10:11], v[16:17] op_sel_hi:[0,1]\n" \
    "v_cvt_pk_bf16_f32 v30, v24, v25\n" \
    "v_pk_fma_f32 v[16:17], v[16:17], v[10:11], v[18:19] op_sel:[1,1,0] op_sel_hi:[0,1,1] neg_lo:[0,1,0]\n" \
    "ds_write_addtid_b32 v30 offset:29120\n" \
    "v_pk_mul_f32 v[18:19], v[12:13], v[16:17] op_sel_hi:[0,1]\n" \
    "v_cvt_pk_bf16_f32 v31, v16, v17\n" \
    "v_pk_fma_f32 v[14:15], v[16:17], v[12:13], v[18:19] op_sel:[1,1,0] op_sel_hi:[0,1,1] neg_lo:[0,1,0]\n" \
    "ds_write_addtid_b32 v31 offset:14560\n" \
    "v_pk_mul_f32 v[18:19], v[10:11], v[16:17] op_sel_hi:[0,1]\n" \
    "v_cvt_pk_bf16_f32 v30, v14, v15\n" \
    "v_pk_fma_f32 v[16:17], v[16:17], v[10:11], v[18:19] op_sel:[1,1,0] op_sel_hi:[0,1,1] neg_lo:[0,1,0]\n" \
    "ds_write_addtid_b32 v30 offset:31200\n" \
    "v_pk_mul_f32 v[18:19], v[12:13], v[16:17] op_sel_hi:[0,1]\n" \
    "v_cvt_pk_bf16_f32 v31, v16, v17\n" \
    "v_pk_fma_f32 v[26:27], v[16:17], v[12:13], v[18:19] op_sel:[1,1,0] op_sel_hi:[0,1,1] neg_lo:[0,1,0]\n" \
    "ds_write_addtid_b32 v31 offset:16640\n" \
    "v_pk_mul_f32 v[18:19], v[10:11], v[16:17] op_sel_hi:[0,1]\n" \
    "v_cvt_pk_bf16_f32 v30, v26, v27\n" \
    "v_pk_fma_f32 v[16:17], v[16:17], v[10:11], v[18:19] op_sel:[1,1,0] op_sel_hi:[0,1,1] neg_lo:[0,1,0]\n" \
    "ds_write_addtid_b32 v30 offset:33280\n" \
    "v_pk_mul_f32 v[18:19], v[12:13], v[16:17] op_sel_hi:[0,1]\n" \
    "v_cvt_pk_bf16_f32 v31, v16, v17\n" \
    "v_pk_fma_f32 v[14:15], v[16:17], v[12:13], v[18:19] op_sel:[1,1,0] op_sel_hi:[0,1,1] neg_lo:[0,1,0]\n" \
    "ds_write_addtid_b32 v31 offset:18720\n" \
    "s_waitcnt vmcnt(0)\n" \
    "v_readfirstlane_b32 %[ep], v72\n" \
    "v_pk_mul_f32 v[18:19], v[12:13], v[20:21] op_sel_hi:[0,1]\n" \
    "v_cvt_pk_bf16_f32 v30, v14, v15\n" \
    "v_pk_fma_f32 v[20:21], v[20:21], v[12:13], v[18:19] op_sel:[1,1,0] op_sel_hi:[0,1,1] neg_lo:[0,1,0]\n" \
    "ds_write_addtid_b32 v30 offset:35360\n" \
    "v_mov_b32_e32 v61, 2\n" \
    "ds_write_addtid_b32 v61 offset:0\n" \
    "s_mov_b32 m0, %[m0a]\n" \
    "v_pk_mul_f32 v[18:19], v[12:13], v[22:23] op_sel_hi:[0,1]\n" \
    "v_cvt_pk_bf16_f32 v31, v20, v21\n" \
    "v_pk_fma_f32 v[22:23], v[22:23], v[12:13], v[18:19] op_sel:[1,1,0] op_sel_hi:[0,1,1] neg_lo:[0,1,0]\n" \
    "ds_write_addtid_b32 v31 offset:33280\n" \
    "v_pk_mul_f32 v[18:19], v[12:13], v[24:25] op_sel_hi:[0,1]\n" \
    "v_cvt_pk_bf16_f32 v30, v22, v23\n" \
    "v_pk_fma_f32 v[24:25], v[24:25], v[12:13], v[18:19] op_sel:[1,1,0] op_sel_hi:[0,1,1] neg_lo:[0,1,0]\n" \
    "ds_write_addtid_b32 v30 offset:35360\n" \
    "v_pk_mul_f32 v[18:19], v[12:13], v[26:27] op_sel_hi:[0,1]\n" \
    "v_cvt_pk_bf16_f32 v31, v24, v25\n" \
    "v_pk_fma_f32 v[26:27], v[26:27], v[12:13], v[18:19] op_sel:[1,1,0] op_sel_hi:[0,1,1] neg_lo:[0,1,0]\n" \
    "ds_write_addtid_b32 v31 offset:37440\n" \
    "v_pk_mul_f32 v[18:19], v[10:11], v[14:15] op_sel_hi:[0,1]\n" \
    "v_cvt_pk_bf16_f32 v30, v26, v27\n" \
    "v_pk_fma_f32 v[28:29], v[14:15], v[10:11], v[18:19] op_sel:[1,1,0] op_sel_hi:[0,1,1] neg_lo:[0,1,0]\n" \
    "ds_write_addtid_b32 v30 offset:39520\n" \
    "v_pk_mul_f32 v[18:19], v[12:13], v[28:29] op_sel_hi:[0,1]\n" \
    "v_pk_fma_f32 v[28:29], v[28:29], v[12:13], v[18:19] op_sel:[1,1,0] op_sel_hi:[0,1,1] neg_lo:[0,1,0]\n" \
    "v_pk_mul_f32 v[18:19], v[12:13], v[20:21] op_sel_hi:[0,1]\n" \
    "v_cvt_pk_bf16_f32 v31, v28, v29\n" \
    "v_pk_fma_f32 v[20:21], v[20:21], v[12:13], v[18:19] op_sel:[1,1,0] op_sel_hi:[0,1,1] neg_lo:[0,1,0]\n" \
    "ds_write_addtid_b32 v31 offset:41600\n" \
    "v_pk_mul_f32 v[18:19], v[12:13], v[22:23] op_sel_hi:[0,1]\n" \
    "v_cvt_pk_bf16_f32 v30, v20, v21\n" \
    "v_pk_fma_f32 v[22:23], v[22:23], v[12:13], v[18:19] op_sel:[1,1,0] op_sel_hi:[0,1,1] neg_lo:[0,1,0]\n" \
    "ds_write_addtid_b32 v30 offset:43680\n" \
    "v_pk_mul_f32 v[18:19], v[12:13], v[24:25] op_sel_hi:[0,1]\n" \
    "v_cvt_pk_bf16_f32 v31, v22, v23\n" \
    "v_pk_fma_f32 v[24:25], v[24:25], v[12:13], v[18:19] op_sel:[1,1,0] op_sel_hi:[0,1,1] neg_lo:[0,1,0]\n" \
    "ds_write_addtid_b32 v31 offset:45760\n" \
    "v_pk_mul_f32 v[18:19], v[12:13], v[26:27] op_sel_hi:[0,1]\n" \
    "v_cvt_pk_bf16_f32 v30, v24, v25\n" \
    "v_pk_fma_f32 v[26:27], v[26:27], v[12:13], v[18:19] op_sel:[1,1,0] op_sel_hi:[0,1,1] neg_lo:[0,1,0]\n" \
    "ds_write_addtid_b32 v30 offset:47840\n" \
    "v_pk_mul_f32 v[18:19], v[12:13], v[28:29] op_sel_hi:[0,1]\n" \
    "v_cvt_pk_bf16_f32 v31, v26, v27\n" \
    "v_pk_fma_f32 v[28:29], v[28:29], v[12:13], v[18:19] op_sel:[1,1,0] op_sel_hi:[0,1,1] neg_lo:[0,1,0]\n" \
    "ds_write_addtid_b32 v31 offset:49920\n" \
    "v_pk_mul_f32 v[18:19], v[12:13], v[24:25] op_sel_hi:[0,1]\n" \
    "v_cvt_pk_bf16_f32 v30, v28, v29\n" \
    "v_pk_fma_f32 v[24:25], v[24:25], v[12:13], v[18:19] op_sel:[1,1,0] op_sel_hi:[0,1,1] neg_lo:[0,1,0]\n" \
    "ds_write_addtid_b32 v30 offset:52000\n" \
    "v_mov_b32_e32 v61, 3\n" \
    "ds_write_addtid_b32 v61 offset:54080\n" \
    "s_add_u32 m0, %[m0a], 62400\n" \
    "v_pk_mul_f32 v[18:19], v[12:13], v[28:29] op_sel_hi:[0,1]\n" \
    "v_cvt_pk_bf16_f32 v31, v24, v25\n" \
    "v_pk_fma_f32 v[28:29], v[28:29], v[12:13], v[18:19] op_sel:[1,1,0] op_sel_hi:[0,1,1] neg_lo:[0,1,0]\n" \
    "ds_write_addtid_b32 v31 offset:29120\n" \
    "v_pk_mul_f32 v[18:19], v[12:13], v[24:25] op_sel_hi:[0,1]\n" \
    "v_cvt_pk_bf16_f32 v30, v28, v29\n" \
    "v_pk_fma_f32 v[24:25], v[24:25], v[12:13], v[18:19] op_sel:[1,1,0] op_sel_hi:[0,1,1] neg_lo:[0,1,0]\n" \
    "ds_write_addtid_b32 v30 offset:37440\n" \
    "v_pk_mul_f32 v[18:19], v[12:13], v[28:29] op_sel_hi:[0,1]\n" \
    "v_cvt_pk_bf16_f32 v31, v24, v25\n" \
    "v_pk_fma_f32 v[28:29], v[28:29], v[12:13], v[18:19] op_sel:[1,1,0] op_sel_hi:[0,1,1] neg_lo:[0,1,0]\n" \
    "ds_write_addtid_b32 v31 offset:31200\n" \
    "v_pk_mul_f32 v[18:19], v[12:13], v[24:25] op_sel_hi:[0,1]\n" \
    "v_cvt_pk_bf16_f32 v30, v28, v29\n" \
    "v_pk_fma_f32 v[24:25], v[24:25], v[12:13], v[18:19] op_sel:[1,1,0] op_sel_hi:[0,1,1] neg_lo:[0,1,0]\n" \
    "ds_write_addtid_b32 v30 offset:39520\n" \
    "v_pk_mul_f32 v[18:19], v[12:13], v[28:29] op_sel_hi:[0,1]\n" \
    "v_cvt_pk_bf16_f32 v31, v24, v25\n" \
    "v_pk_fma_f32 v[28:29], v[28:29], v[12:13], v[18:19] op_sel:[1,1,0] op_sel_hi:[0,1,1] neg_lo:[0,1,0]\n" \
    "ds_write_addtid_b32 v31 offset:33280\n" \
    "v_pk_mul_f32 v[18:19], v[12:13], v[24:25] op_sel_hi:[0,1]\n" \
    "v_cvt_pk_bf16_f32 v30, v28, v29\n" \
    "v_pk_fma_f32 v[24:25], v[24:25], v[12:13], v[18:19] op_sel:[1,1,0] op_sel_hi:[0,1,1] neg_lo:[0,1,0]\n" \
    "ds_write_addtid_b32 v30 offset:41600\n" \
    "v_pk_mul_f32 v[18:19], v[12:13], v[28:29] op_sel_hi:[0,1]\n" \
    "v_cvt_pk_bf16_f32 v31, v24, v25\n" \
    "v_pk_fma_f32 v[28:29], v[28:29], v[12:13], v[18:19] op_sel:[1,1,0] op_sel_hi:[0,1,1] neg_lo:[0,1,0]\n" \
    "ds_write_addtid_b32 v31 offset:35360\n" \
    "v_pk_mul_f32 v[18:19], v[12:13], v[28:29] op_sel_hi:[0,1]\n" \
    "v_cvt_pk_bf16_f32 v30, v28, v29\n" \
    "v_pk_fma_f32 v[28:29], v[28:29], v[12:13], v[18:19] op_sel:[1,1,0] op_sel_hi:[0,1,1] neg_lo:[0,1,0]\n" \
    "ds_write_addtid_b32 v30 offset:43680\n" \
    "v_pk_mul_f32 v[18:19], v[12:13], v[28:29] op_sel_hi:[0,1]\n" \
    "v_cvt_pk_bf16_f32 v31, v28, v29\n" \
    "v_pk_fma_f32 v[28:29], v[28:29], v[12:13], v[18:19] op_sel:[1,1,0] op_sel_hi:[0,1,1] neg_lo:[0,1,0]\n" \
    "ds_write_addtid_b32 v31 offset:45760\n" \
    "v_pk_mul_f32 v[18:19], v[12:13], v[28:29] op_sel_hi:[0,1]\n" \
    "v_cvt_pk_bf16_f32 v30, v28, v29\n" \
    "v_pk_fma_f32 v[28:29], v[28:29], v[12:13], v[18:19] op_sel:[1,1,0] op_sel_hi:[0,1,1] neg_lo:[0,1,0]\n" \
    "ds_write_addtid_b32 v30 offset:47840\n" \
    "v_pk_mul_f32 v[18:19], v[12:13], v[28:29] op_sel_hi:[0,1]\n" \
    "v_cvt_pk_bf16_f32 v31, v28, v29\n" \
    "v_pk_fma_f32 v[28:29], v[28:29], v[12:13], v[18:19] op_sel:[1,1,0] op_sel_hi:[0,1,1] neg_lo:[0,1,0]\n" \
    "ds_write_addtid_b32 v31 offset:49920\n" \
    "v_pk_mul_f32 v[18:19], v[12:13], v[28:29] op_sel_hi:[0,1]\n" \
    "v_cvt_pk_bf16_f32 v30, v28, v29\n" \
    "v_pk_fma_f32 v[28:29], v[28:29], v[12:13], v[18:19] op_sel:[1,1,0] op_sel_hi:[0,1,1] neg_lo:[0,1,0]\n" \
    "ds_write_addtid_b32 v30 offset:52000\n" \
    "v_pk_mul_f32 v[18:19], v[12:13], v[28:29] op_sel_hi:[0,1]\n" \
    "v_cvt_pk_bf16_f32 v31, v28, v29\n" \
    "v_pk_fma_f32 v[28:29], v[28:29], v[12:13], v[18:19] op_sel:[1,1,0] op_sel_hi:[0,1,1] neg_lo:[0,1,0]\n" \
    "ds_write_addtid_b32 v31 offset:54080\n" \
    "v_pk_mul_f32 v[18:19], v[12:13], v[28:29] op_sel_hi:[0,1]\n" \
    "v_cvt_pk_bf16_f32 v30, v28, v29\n" \
    "v_pk_fma_f32 v[28:29], v[28:29], v[12:13], v[18:19] op_sel:[1,1,0] op_sel_hi:[0,1,1] neg_lo:[0,1,0]\n" \
    "ds_write_addtid_b32 v30 offset:56160\n" \
    "v_pk_mul_f32 v[18:19], v[12:13], v[28:29] op_sel_hi:[0,1]\n" \
    "v_cvt_pk_bf16_f32 v31, v28, v29\n" \
    "v_pk_fma_f32 v[28:29], v[28:29], v[12:13], v[18:19] op_sel:[1,1,0] op_sel_hi:[0,1,1] neg_lo:[0,1,0]\n" \
    "ds_write_addtid_b32 v31 offset:58240\n" \
    "v_cvt_pk_bf16_f32 v30, v28, v29\n" \
    "ds_write_addtid_b32 v30 offset:60320\n" \
    "s_mov_b32 m0, %[m0a]\n" \
    "v_mov_b32_e32 v61, 4\n" \
    "ds_write_addtid_b32 v61 offset:54080\n" \
    "s_setprio 1\n" \
    "ds_read_b32 v63, v34\n" \
    "ds_read_b128 v[152:155], v38 offset:1024\n" \
    "ds_read_b128 v[160:163], v38 offset:3072\n" \
    "ds_read_b128 v[148:151], v38 offset:0\n" \
    "ds_read_b128 v[156:159], v38 offset:2048\n" \
    "ds_read_b128 v[164:167], v38 offset:4096\n" \
    "s_waitcnt lgkmcnt(5)\n" \
    "v_cmp_gt_u32_e32 vcc, 2, v63\n" \
    "s_cbranch_vccz Lbe0_%=\n" \
    "s_mov_b32 s90, 0x100000\n" \
    "Lbl0_%=:\n" \
    "s_sleep 1\n" \
    "ds_read_b32 v63, v34 offset:0\n" \
    "ds_read_b128 v[152:155], v38 offset:1024\n" \
    "ds_read_b128 v[160:163], v38 offset:3072\n" \
    "ds_read_b128 v[148:151], v38 offset:0\n" \
    "ds_read_b128 v[156:159], v38 offset:2048\n" \
    "ds_read_b128 v[164:167], v38 offset:4096\n" \
    "s_waitcnt lgkmcnt(0)\n" \
    "v_cmp_gt_u32_e32 vcc, 2, v63\n" \
    "s_cbranch_vccz Lbe0_%=\n" \
    "s_sub_u32 s90, s90, 1\n" \
    "s_cbranch_scc0 Lbl0_%=\n" \
    "Lbe0_%=:\n" \
    "ds_read_b128 v[108:111], v33 offset:448\n" \
    "ds_read_b128 v[112:115], v33 offset:432\n" \
    "ds_read_b128 v[116:119], v33 offset:960\n" \
    "ds_read_b128 v[120:123], v33 offset:944\n" \
    "s_waitcnt lgkmcnt(3)\n" \
    "v_mfma_f32_16x16x32_bf16 v[48:51], v[108:111], v[152:155], 0\n" \
    "ds_read_b32 v60, v32\n" \
    "s_waitcnt lgkmcnt(3)\n" \
    "v_mfma_f32_16x16x32_bf16 v[52:55], v[112:115], v[152:155], 0\n" \
    "v_mfma_f32_16x16x32_bf16 v[56:59], v[112:115], v[160:163], 0\n" \
    "v_mfma_f32_16x16x32_bf16 v[0:3], v[108:111], v[148:151], 0\n" \
    "v_mfma_f32_16x16x32_bf16 v[4:7], v[112:115], v[148:151], 0\n" \
    "v_mfma_f32_16x16x32_bf16 v[40:43], v[108:111], v[156:159], 0\n" \
    "s_waitcnt lgkmcnt(2)\n" \
    "v_mfma_f32_16x16x32_bf16 v[8:11], v[116:119], v[148:151], 0\n" \
    "s_nop 3\n" \
    "v_cvt_pk_bf16_f32 v0, v0, v48\n" \
    "v_cvt_pk_bf16_f32 v1, v1, v49\n" \
    "v_cvt_pk_bf16_f32 v2, v2, v50\n" \
    "v_cvt_pk_bf16_f32 v3, v3, v51\n" \
    "s_waitcnt lgkmcnt(1)\n" \
    "v_mfma_f32_16x16x32_bf16 v[12:15], v[120:123], v[148:151], 0\n" \
    "v_cvt_pk_bf16_f32 v4, v4, v52\n" \
    "v_cvt_pk_bf16_f32 v5, v5, v53\n" \
    "v_cvt_pk_bf16_f32 v6, v6, v54\n" \
    "v_cvt_pk_bf16_f32 v7, v7, v55\n" \
    "v_mfma_f32_16x16x32_bf16 v[44:47], v[116:119], v[156:159], 0\n" \
    "v_cvt_pk_bf16_f32 v40, v40, v56\n" \
    "v_cvt_pk_bf16_f32 v41, v41, v57\n" \
    "v_cvt_pk_bf16_f32 v42, v42, v58\n" \
    "v_cvt_pk_bf16_f32 v43, v43, v59\n" \
    "v_mfma_f32_16x16x32_bf16 v[48:51], v[116:119], v[152:155], 0\n" \
    "v_mfma_f32_16x16x32_bf16 v[52:55], v[120:123], v[152:155], 0\n" \
    "v_mfma_f32_16x16x32_bf16 v[56:59], v[120:123], v[160:163], 0\n" \
    "s_waitcnt lgkmcnt(0)\n" \
    "v_cmp_gt_u32_e32 vcc, 2, v60\n" \
    "s_cbranch_vccz Lbe1_%=\n" \
    "s_mov_b32 s90, 0x100000\n" \
    "Lbl1_%=:\n" \
    "s_sleep 1\n" \
    "ds_read_b32 v60, v32 offset:0\n" \
    "s_waitcnt lgkmcnt(0)\n" \
    "v_cmp_gt_u32_e32 vcc, 2, v60\n" \
    "s_cbranch_vccz Lbe1_%=\n" \
    "s_sub_u32 s90, s90, 1\n" \
    "s_cbranch_scc0 Lbl1_%=\n" \
    "Lbe1_%=:\n" \
    "ds_read2_b32 v[104:105], v37 offset0:0 offset1:1\n" \
    "ds_read2_b32 v[106:107], v37 offset0:2 offset1:3\n" \
    "s_waitcnt lgkmcnt(0)\n" \
    "v_mfma_f32_16x16x32_bf16 v[132:135], v[104:107], v[108:111], 0\n" \
    "v_mfma_f32_16x16x32_bf16 v[136:139], v[104:107], v[116:119], 0\n" \
    "ds_read_b32 v60, v32\n" \
    "s_waitcnt lgkmcnt(0)\n" \
    "v_cmp_gt_u32_e32 vcc, 3, v60\n" \
    "s_cbranch_vccz Lbe2_%=\n" \
    "s_mov_b32 s90, 0x100000\n" \
    "Lbl2_%=:\n" \
    "s_sleep 1\n" \
    "ds_read_b32 v60, v32 offset:0\n" \
    "s_waitcnt lgkmcnt(0)\n" \
    "v_cmp_gt_u32_e32 vcc, 3, v60\n" \
    "s_cbranch_vccz Lbe2_%=\n" \
    "s_sub_u32 s90, s90, 1\n" \
    "s_cbranch_scc0 Lbl2_%=\n" \
    "Lbe2_%=:\n" \
    "ds_read_b32 v62, v39 offset:320\n" \
    "ds_read_b32 v63, v34\n" \
    "ds_read2_b32 v[64:65], v36 offset0:32 offset1:33\n" \
    "ds_read2_b32 v[66:67], v36 offset0:34 offset1:35\n" \
    "ds_read2_b32 v[76:77], v36 offset0:96 offset1:97\n" \
    "ds_read2_b32 v[78:79], v36 offset0:98 offset1:99\n" \
    "s_waitcnt lgkmcnt(5)\n" \
    "v_perm_b32 v62, v62, v62, s92\n" \
    "v_and_b32_e64 v164, v62, v164\n" \
    "v_and_b32_e64 v165, v62, v165\n" \
    "v_and_b32_e64 v166, v62, v166\n" \
    "v_and_b32_e64 v167, v62, v167\n" \
    "s_nop 0\n" \
    "s_nop 0\n" \
    "v_mfma_f32_16x16x32_bf16 v[140:143], v[164:167], v[108:111], 0\n" \
    "v_mfma_f32_16x16x32_bf16 v[124:127], v[164:167], v[116:119], 0\n" \
    "s_waitcnt lgkmcnt(4)\n" \
    "v_cmp_gt_u32_e32 vcc, 3, v63\n" \
    "s_cbranch_vccnz Lbs3_%=\n" \
    "Lbe3_%=:\n" \
    "ds_read_b128 v[108:111], v33 offset:1472\n" \
    "ds_read_b128 v[112:115], v33 offset:1456\n" \
    "s_setprio 3\n" \
    "s_waitcnt lgkmcnt(4)\n" \
    "v_mfma_f32_16x16x32_bf16 v[132:135], v[0:3], v[64:67], v[132:135]\n" \
    "v_cvt_pk_bf16_f32 v8, v8, v48\n" \
    "v_cvt_pk_bf16_f32 v9, v9, v49\n" \
    "v_cvt_pk_bf16_f32 v10, v10, v50\n" \
    "v_cvt_pk_bf16_f32 v11, v11, v51\n" \
    "v_cvt_pk_bf16_f32 v12, v12, v52\n" \
    "v_cvt_pk_bf16_f32 v13, v13, v53\n" \
    "v_cvt_pk_bf16_f32 v14, v14, v54\n" \
    "v_cvt_pk_bf16_f32 v15, v15, v55\n" \
    "v_cvt_pk_bf16_f32 v44, v44, v56\n" \
    "v_cvt_pk_bf16_f32 v45, v45, v57\n" \
    "v_cvt_pk_bf16_f32 v46, v46, v58\n" \
    "v_cvt_pk_bf16_f32 v47, v47, v59\n" \
    "ds_read_b32 v63, v34\n" \
    "s_waitcnt lgkmcnt(3)\n" \
    "v_mfma_f32_16x16x32_bf16 v[132:135], v[4:7], v[76:79], v[132:135]\n" \
    "s_waitcnt lgkmcnt(2)\n" \
    "v_mfma_f32_16x16x32_bf16 v[128:131], v[164:167], v[108:111], 0\n" \
    "ds_read2_b32 v[68:69], v36 offset0:16 offset1:17\n" \
    "ds_read2_b32 v[70:71], v36 offset0:18 offset1:19\n" \
    "v_mfma_f32_16x16x32_bf16 v[140:143], v[104:107], v[108:111], v[140:143]\n" \
    "ds_read2_b32 v[80:81], v36 offset0:80 offset1:81\n" \
    "ds_read2_b32 v[82:83], v36 offset0:82 offset1:83\n" \
    "v_mfma_f32_16x16x32_bf16 v[16:19], v[108:111], v[148:151], 0\n" \
    "s_waitcnt lgkmcnt(5)\n" \
    "v_mfma_f32_16x16x32_bf16 v[20:23], v[112:115], v[148:151], 0\n" \
    "v_mfma_f32_16x16x32_bf16 v[144:147], v[108:111], v[156:159], 0\n" \
    "v_mfma_f32_16x16x32_bf16 v[48:51], v[108:111], v[152:155], 0\n" \
    "v_mfma_f32_16x16x32_bf16 v[52:55], v[112:115], v[152:155], 0\n" \
    "v_mfma_f32_16x16x32_bf16 v[56:59], v[112:115], v[160:163], 0\n" \
    "s_waitcnt lgkmcnt(4)\n" \
    "v_cmp_gt_u32_e32 vcc, 4, v63\n" \
    "s_cbranch_vccnz Lbs4_%=\n" \
    "Lbe4_%=:\n" \
    "ds_read_b128 v[116:119], v33 offset:1984\n" \
    "ds_read_b128 v[120:123], v33 offset:1968\n" \
    "v_mfma_f32_16x16x32_bf16 v[136:139], v[8:11], v[64:67], v[136:139]\n" \
    "v_mov_b32_e64 v61, 13\n" \
    "ds_write_b128 v35, v[132:135] offset:0\n" \
    "ds_write_addtid_b32 v61 offset:54080\n" \
    "v_cvt_pk_bf16_f32 v16, v16, v48\n" \
    "v_cvt_pk_bf16_f32 v17, v17, v49\n" \
    "v_cvt_pk_bf16_f32 v18, v18, v50\n" \
    "v_cvt_pk_bf16_f32 v19, v19, v51\n" \
    "v_cvt_pk_bf16_f32 v20, v20, v52\n" \
    "v_cvt_pk_bf16_f32 v21, v21, v53\n" \
    "v_cvt_pk_bf16_f32 v22, v22, v54\n" \
    "v_cvt_pk_bf16_f32 v23, v23, v55\n" \
    "v_cvt_pk_bf16_f32 v144, v144, v56\n" \
    "v_cvt_pk_bf16_f32 v145, v145, v57\n" \
    "v_cvt_pk_bf16_f32 v146, v146, v58\n" \
    "v_cvt_pk_bf16_f32 v147, v147, v59\n" \
    "ds_read_b32 v63, v34\n" \
    "v_mfma_f32_16x16x32_bf16 v[136:139], v[12:15], v[76:79], v[136:139]\n" \
    "s_waitcnt lgkmcnt(7)\n" \
    "v_mfma_f32_16x16x32_bf16 v[136:139], v[0:3], v[68:71], v[136:139]\n" \
    "s_waitcnt lgkmcnt(5)\n" \
    "v_mfma_f32_16x16x32_bf16 v[136:139], v[4:7], v[80:83], v[136:139]\n" \
    "s_waitcnt lgkmcnt(4)\n" \
    "v_mfma_f32_16x16x32_bf16 v[132:135], v[164:167], v[116:119], 0\n" \
    "ds_read2_b32 v[72:73], v36 offset0:0 offset1:1\n" \
    "ds_read2_b32 v[74:75], v36 offset0:2 offset1:3\n" \
    "v_mfma_f32_16x16x32_bf16 v[124:127], v[104:107], v[116:119], v[124:127]\n" \
    "ds_read2_b32 v[84:85], v36 offset0:64 offset1:65\n" \
    "ds_read2_b32 v[86:87], v36 offset0:66 offset1:67\n" \
    "v_mfma_f32_16x16x32_bf16 v[24:27], v[116:119], v[148:151], 0\n" \
    "ds_read2_b32 v[88:89], v36 offset0:176 offset1:177\n" \
    "ds_read2_b32 v[90:91], v36 offset0:178 offset1:179\n" \
    "s_waitcnt lgkmcnt(9)\n" \
    "v_mfma_f32_16x16x32_bf16 v[28:31], v[120:123], v[148:151], 0\n" \
    "v_mfma_f32_16x16x32_bf16 v[164:167], v[116:119], v[156:159], 0\n" \
    "v_mfma_f32_16x16x32_bf16 v[48:51], v[116:119], v[152:155], 0\n" \
    "v_mfma_f32_16x16x32_bf16 v[52:55], v[120:123], v[152:155], 0\n" \
    "v_mfma_f32_16x16x32_bf16 v[56:59], v[120:123], v[160:163], 0\n" \
    "s_waitcnt lgkmcnt(6)\n" \
    "v_cmp_gt_u32_e32 vcc, 5, v63\n" \
    "s_cbranch_vccnz Lbs5_%=\n" \
    "Lbe5_%=:\n" \
    "ds_read_b128 v[108:111], v33 offset:2496\n" \
    "ds_read_b128 v[112:115], v33 offset:2480\n" \
    "v_mfma_f32_16x16x32_bf16 v[140:143], v[16:19], v[64:67], v[140:143]\n" \
    "v_mov_b32_e64 v61, 14\n" \
    "ds_write_b128 v35, v[136:139] offset:4160\n" \
    "ds_write_addtid_b32 v61 offset:54080\n" \
    "v_mfma_f32_16x16x32_bf16 v[140:143], v[20:23], v[76:79], v[140:143]\n" \
    "v_cvt_pk_bf16_f32 v24, v24, v48\n" \
    "v_cvt_pk_bf16_f32 v25, v25, v49\n" \
    "v_cvt_pk_bf16_f32 v26, v26, v50\n" \
    "v_mfma_f32_16x16x32_bf16 v[140:143], v[8:11], v[68:71], v[140:143]\n" \
    "v_cvt_pk_bf16_f32 v27, v27, v51\n" \
    "v_cvt_pk_bf16_f32 v28, v28, v52\n" \
    "v_cvt_pk_bf16_f32 v29, v29, v53\n" \
    "v_mfma_f32_16x16x32_bf16 v[140:143], v[12:15], v[80:83], v[140:143]\n" \
    "v_cvt_pk_bf16_f32 v30, v30, v54\n" \
    "v_cvt_pk_bf16_f32 v31, v31, v55\n" \
    "v_cvt_pk_bf16_f32 v164, v164, v56\n" \
    "s_waitcnt lgkmcnt(8)\n" \
    "v_mfma_f32_16x16x32_bf16 v[140:143], v[0:3], v[72:75], v[140:143]\n" \
    "v_cvt_pk_bf16_f32 v165, v165, v57\n" \
    "v_cvt_pk_bf16_f32 v166, v166, v58\n" \
    "v_cvt_pk_bf16_f32 v167, v167, v59\n" \
    "s_waitcnt lgkmcnt(6)\n" \
    "v_mfma_f32_16x16x32_bf16 v[140:143], v[4:7], v[84:87], v[140:143]\n" \
    "ds_read_b32 v63, v34\n" \
    "s_waitcnt lgkmcnt(5)\n" \
    "v_mfma_f32_16x16x32_bf16 v[140:143], v[40:43], v[88:91], v[140:143]\n" \
    "s_waitcnt lgkmcnt(4)\n" \
    "v_mfma_f32_16x16x32_bf16 v[128:131], v[104:107], v[108:111], v[128:131]\n" \
    "ds_read2_b32 v[92:93], v36 offset0:160 offset1:161\n" \
    "ds_read2_b32 v[94:95], v36 offset0:162 offset1:163\n" \
    "v_mfma_f32_16x16x32_bf16 v[0:3], v[108:111], v[148:151], 0\n" \
    "s_waitcnt lgkmcnt(5)\n" \
    "v_mfma_f32_16x16x32_bf16 v[4:7], v[112:115], v[148:151], 0\n" \
    "v_mfma_f32_16x16x32_bf16 v[48:51], v[108:111], v[152:155], 0\n" \
    "v_mfma_f32_16x16x32_bf16 v[52:55], v[112:115], v[152:155], 0\n" \
    "s_waitcnt lgkmcnt(2)\n" \
    "v_cmp_gt_u32_e32 vcc, 6, v63\n" \
    "s_cbranch_vccnz Lbs6_%=\n" \
    "Lbe6_%=:\n" \
    "ds_read_b128 v[116:119], v33 offset:3008\n" \
    "ds_read_b128 v[120:123], v33 offset:2992\n" \
    "v_mfma_f32_16x16x32_bf16 v[124:127], v[24:27], v[64:67], v[124:127]\n" \
    "v_mov_b32_e64 v61, 15\n" \
    "ds_write_b128 v35, v[140:143] offset:8320\n" \
    "v_mfma_f32_16x16x32_bf16 v[124:127], v[28:31], v[76:79], v[124:127]\n" \
    "ds_write_addtid_b32 v61 offset:54080\n" \
    "v_cvt_pk_bf16_f32 v0, v0, v48\n" \
    "v_mfma_f32_16x16x32_bf16 v[124:127], v[16:19], v[68:71], v[124:127]\n" \
    "v_cvt_pk_bf16_f32 v1, v1, v49\n" \
    "v_cvt_pk_bf16_f32 v2, v2, v50\n" \
    "v_mfma_f32_16x16x32_bf16 v[124:127], v[20:23], v[80:83], v[124:127]\n" \
    "v_cvt_pk_bf16_f32 v3, v3, v51\n" \
    "v_cvt_pk_bf16_f32 v4, v4, v52\n" \
    "v_mfma_f32_16x16x32_bf16 v[124:127], v[8:11], v[72:75], v[124:127]\n" \
    "v_cvt_pk_bf16_f32 v5, v5, v53\n" \
    "v_cvt_pk_bf16_f32 v6, v6, v54\n" \
    "v_mfma_f32_16x16x32_bf16 v[124:127], v[12:15], v[84:87], v[124:127]\n" \
    "v_cvt_pk_bf16_f32 v7, v7, v55\n" \
    "v_mfma_f32_16x16x32_bf16 v[124:127], v[44:47], v[88:91], v[124:127]\n" \
    "s_waitcnt lgkmcnt(4)\n" \
    "v_mfma_f32_16x16x32_bf16 v[124:127], v[40:43], v[92:95], v[124:127]\n" \
    "s_waitcnt lgkmcnt(3)\n" \
    "v_mfma_f32_16x16x32_bf16 v[132:135], v[104:107], v[116:119], v[132:135]\n" \
    "ds_read2_b32 v[96:97], v36 offset0:144 offset1:145\n" \
    "ds_read2_b32 v[98:99], v36 offset0:146 offset1:147\n" \
    "v_mfma_f32_16x16x32_bf16 v[8:11], v[116:119], v[148:151], 0\n" \
    "ds_read2_b32 v[100:101], v36 offset0:128 offset1:129\n" \
    "ds_read2_b32 v[102:103], v36 offset0:130 offset1:131\n" \
    "s_waitcnt lgkmcnt(6)\n" \
    "v_mfma_f32_16x16x32_bf16 v[12:15], v[120:123], v[148:151], 0\n" \
    "v_mfma_f32_16x16x32_bf16 v[48:51], v[116:119], v[152:155], 0\n" \
    "v_mfma_f32_16x16x32_bf16 v[52:55], v[120:123], v[152:155], 0\n" \
    "v_mfma_f32_16x16x32_bf16 v[128:131], v[0:3], v[64:67], v[128:131]\n" \
    "v_mov_b32_e64 v61, 16\n" \
    "ds_write_b128 v35, v[124:127] offset:12480\n" \
    "v_mfma_f32_16x16x32_bf16 v[128:131], v[4:7], v[76:79], v[128:131]\n" \
    "ds_write_addtid_b32 v61 offset:54080\n" \
    "s_nop 1\n" \
    "v_cvt_pk_bf16_f32 v8, v8, v48\n" \
    "v_mfma_f32_16x16x32_bf16 v[128:131], v[24:27], v[68:71], v[128:131]\n" \
    "v_cvt_pk_bf16_f32 v9, v9, v49\n" \
    "v_cvt_pk_bf16_f32 v10, v10, v50\n" \
    "v_mfma_f32_16x16x32_bf16 v[128:131], v[28:31], v[80:83], v[128:131]\n" \
    "v_cvt_pk_bf16_f32 v11, v11, v51\n" \
    "v_cvt_pk_bf16_f32 v12, v12, v52\n" \
    "v_mfma_f32_16x16x32_bf16 v[128:131], v[16:19], v[72:75], v[128:131]\n" \
    "v_cvt_pk_bf16_f32 v13, v13, v53\n" \
    "v_cvt_pk_bf16_f32 v14, v14, v54\n" \
    "v_mfma_f32_16x16x32_bf16 v[128:131], v[20:23], v[84:87], v[128:131]\n" \
    "v_cvt_pk_bf16_f32 v15, v15, v55\n" \
    "v_mfma_f32_16x16x32_bf16 v[128:131], v[144:147], v[88:91], v[128:131]\n" \
    "v_mfma_f32_16x16x32_bf16 v[128:131], v[44:47], v[92:95], v[128:131]\n" \
    "s_waitcnt lgkmcnt(4)\n" \
    "v_mfma_f32_16x16x32_bf16 v[128:131], v[40:43], v[96:99], v[128:131]\n" \
    "v_mov_b32_e32 v108, %[ep]\n" \
    "v_mov_b32_e32 v112, 0\n" \
    "global_load_dwordx2 v[108:109], v112, %[tp] sc1\n" \
    "v_mfma_f32_16x16x32_bf16 v[132:135], v[8:11], v[64:67], v[132:135]\n" \
    "v_mov_b32_e64 v61, 17\n" \
    "s_nop 2\n" \
    "ds_write_b128 v35, v[128:131] offset:16640\n" \
    "v_mfma_f32_16x16x32_bf16 v[132:135], v[12:15], v[76:79], v[132:135]\n" \
    "ds_write_addtid_b32 v61 offset:54080\n" \
    "v_mfma_f32_16x16x32_bf16 v[132:135], v[0:3], v[68:71], v[132:135]\n" \
    "v_mfma_f32_16x16x32_bf16 v[132:135], v[4:7], v[80:83], v[132:135]\n" \
    "v_mfma_f32_16x16x32_bf16 v[132:135], v[24:27], v[72:75], v[132:135]\n" \
    "v_mfma_f32_16x16x32_bf16 v[132:135], v[28:31], v[84:87], v[132:135]\n" \
    "v_mfma_f32_16x16x32_bf16 v[132:135], v[164:167], v[88:91], v[132:135]\n" \
    "v_mfma_f32_16x16x32_bf16 v[132:135], v[144:147], v[92:95], v[132:135]\n" \
    "v_mfma_f32_16x16x32_bf16 v[132:135], v[44:47], v[96:99], v[132:135]\n" \
    "s_waitcnt lgkmcnt(4)\n" \
    "v_mfma_f32_16x16x32_bf16 v[132:135], v[40:43], v[100:103], v[132:135]\n" \
    "v_mov_b32_e64 v61, 18\n" \
    "s_nop 6\n" \
    "ds_write_b128 v35, v[132:135] offset:20800\n" \
    "ds_write_addtid_b32 v61 offset:54080\n" \
    "s_setprio 0\n" \
    "s_branch Lbx_%=\n" \
    "Lbs3_%=:\n" \
    "s_mov_b32 s90, 0x100000\n" \
    "Lbl3_%=:\n" \
    "ds_read_b32 v63, v34 offset:0\n" \
    "s_waitcnt lgkmcnt(0)\n" \
    "v_cmp_gt_u32_e32 vcc, 3, v63\n" \
    "s_cbranch_vccz Lbe3_%=\n" \
    "s_sub_u32 s90, s90, 1\n" \
    "s_cbranch_scc0 Lbl3_%=\n" \
    "s_branch Lbe3_%=\n" \
    "Lbs4_%=:\n" \
    "s_mov_b32 s90, 0x100000\n" \
    "Lbl4_%=:\n" \
    "ds_read_b32 v63, v34 offset:0\n" \
    "s_waitcnt lgkmcnt(0)\n" \
    "v_cmp_gt_u32_e32 vcc, 4, v63\n" \
    "s_cbranch_vccz Lbe4_%=\n" \
    "s_sub_u32 s90, s90, 1\n" \
    "s_cbranch_scc0 Lbl4_%=\n" \
    "s_branch Lbe4_%=\n" \
    "Lbs5_%=:\n" \
    "s_mov_b32 s90, 0x100000\n" \
    "Lbl5_%=:\n" \
    "ds_read_b32 v63, v34 offset:0\n" \
    "s_waitcnt lgkmcnt(0)\n" \
    "v_cmp_gt_u32_e32 vcc, 5, v63\n" \
    "s_cbranch_vccz Lbe5_%=\n" \
    "s_sub_u32 s90, s90, 1\n" \
    "s_cbranch_scc0 Lbl5_%=\n" \
    "s_branch Lbe5_%=\n" \
    "Lbs6_%=:\n" \
    "s_mov_b32 s90, 0x100000\n" \
    "Lbl6_%=:\n" \
    "ds_read_b32 v63, v34 offset:0\n" \
    "s_waitcnt lgkmcnt(0)\n" \
    "v_cmp_gt_u32_e32 vcc, 6, v63\n" \
    "s_cbranch_vccz Lbe6_%=\n" \
    "s_sub_u32 s90, s90, 1\n" \
    "s_cbranch_scc0 Lbl6_%=\n" \
    "s_branch Lbe6_%=\n" \
    "Lbx_%=:\n"
#define CONV_B_CLOB "v0", "v1", "v2", "v3", "v4", "v5", "v6", "v7", "v8", "v9", "v10", "v11", "v12", "v13", "v14", "v15", "v16", "v17", "v18", "v19", "v20", "v21", "v22", "v23", "v24", "v25", "v26", "v27", "v28", "v29", "v30", "v31", "v32", "v33", "v34", "v35", "v36", "v37", "v38", "v39", "v40", "v41", "v42", "v43", "v44", "v45", "v46", "v47", "v48", "v49", "v50", "v51", "v52", "v53", "v54", "v55", "v56", "v57", "v58", "v59", "v60", "v61", "v62", "v63", "v64", "v65", "v66", "v67", "v68", "v69", "v70", "v71", "v72", "v73", "v74", "v75", "v76", "v77", "v78", "v79", "v80", "v81", "v82", "v83", "v84", "v85", "v86", "v87", "v88", "v89", "v90", "v91", "v92", "v93", "v94", "v95", "v96", "v97", "v98", "v99", "v100", "v101", "v102", "v103", "v104", "v105", "v106", "v107", "v110", "v111", "v112", "v113", "v114", "v115", "v116", "v117", "v118", "v119", "v120", "v121", "v122", "v123", "v124", "v125", "v126", "v127", "v128", "v129", "v130", "v131", "v132", "v133", "v134", "v135", "v136", "v137", "v138", "v139", "v140", "v141", "v142", "v143", "v144", "v145", "v146", "v147", "v148", "v149", "v150", "v151", "v152", "v153", "v154", "v155", "v156", "v157", "v158", "v159", "v160", "v161", "v162", "v163", "v164", "v165", "v166", "v167"
#define CONV_C_ASM \
    "s_setprio 3\n" \
    "v_mbcnt_lo_u32_b32 v62, -1, 0\n" \
    "v_mbcnt_hi_u32_b32 v62, -1, v62\n" \
    "v_and_b32_e32 v57, 1, v62\n" \
    "v_min_u32_e32 v62, %[pl], v62\n" \
    "v_lshrrev_b32_e32 v62, %[ps], v62\n" \
    "v_mul_u32_u24_e32 v57, %[pd], v57\n" \
    "v_mul_u32_u24_e32 v62, 1152, v62\n" \
    "v_add_u32_e32 v62, v62, v57\n" \
    "global_load_dword v63, v62, %[xb]\n" \
    "s_mov_b32 s91, 0x05040100\n" \
    "s_mov_b32 s92, 0x07060302\n" \
    "s_mov_b64 s[96:97], %[o0]\n" \
    "s_mov_b64 s[94:95], %[x0]\n" \
    "v_mov_b64 v[160:161], 0\n" \
    "v_mov_b64 v[162:163], 0\n" \
    "s_waitcnt vmcnt(6)\n" \
    "s_mov_b32 s93, m0\n" \
    "v_lshrrev_b32_e32 v0, 3, v48\n" \
    "s_mov_b32 m0, %[bl]\n" \
    "s_sub_u32 s88, %[bl], s93\n" \
    "s_sub_u32 s88, s88, s93\n" \
    "global_load_lds_dwordx4 v0, %[bs]\n" \
    "s_add_u32 m0, s88, 4096\n" \
    "s_lshl_b32 s88, s93, 1\n" \
    "s_sub_u32 s88, 4096, s88\n" \
    "v_add_u32_e32 v0, s88, v0\n" \
    "global_load_lds_dwordx4 v0, %[bs]\n" \
    "s_mov_b32 m0, s93\n" \
    "ds_write_b128 v49, v[160:163]\n" \
    "ds_write_b128 v50, v[160:163]\n" \
    "s_waitcnt vmcnt(3)\n" \
    "v_add_u32_e32 v56, 91520, v33\n" \
    "s_mov_b64 exec, 1\n" \
    "v_mov_b32_e32 v160, v71\n" \
    "s_mov_b64 exec, -1\n" \
    "v_readfirstlane_b32 %[ep], v72\n" \
    "s_nop 2\n" \
    "s_add_u32 s98, %[ep], 1\n" \
    "s_max_u32 s98, s98, 1\n" \
    "s_mov_b32 s99, %[tg]\n" \
    "global_load_dwordx2 v[12:13], v41, s[94:95] offset:0\n" \
    "s_mov_b32 s90, 0x100000\n" \
    "Lcl0_%=:\n" \
    "ds_read_b32 v60, v32 offset:0\n" \
    "s_waitcnt lgkmcnt(0)\n" \
    "v_cmp_gt_u32_e32 vcc, 1, v60\n" \
    "s_cbranch_vccz Lce0_%=\n" \
    "s_sleep 2\n" \
    "s_sub_u32 s90, s90, 1\n" \
    "s_cbranch_scc0 Lcl0_%=\n" \
    "Lce0_%=:\n" \
    "ds_read_b128 v[96:99], v33 offset:0\n" \
    "ds_read_b128 v[100:103], v33 offset:64\n" \
    "ds_read_b128 v[104:107], v33 offset:128\n" \
    "ds_read_b128 v[108:111], v33 offset:192\n" \
    "ds_read_b128 v[112:115], v33 offset:256\n" \
    "ds_read_b128 v[116:119], v33 offset:320\n" \
    "ds_read_b128 v[120:123], v33 offset:384\n" \
    "ds_read_b128 v[124:127], v33 offset:448\n" \
    "global_load_dwordx2 v[14:15], v41, s[94:95] offset:1152\n" \
    "s_add_u32 s94, s94, 589824\n" \
    "s_addc_u32 s95, s95, 0\n" \
    "global_load_dwordx2 v[16:17], v41, s[94:95] offset:0\n" \
    "global_load_dwordx2 v[18:19], v41, s[94:95] offset:1152\n" \
    "s_add_u32 s94, s94, 589824\n" \
    "s_addc_u32 s95, s95, 0\n" \
    "s_mov_b32 s90, 0x100000\n" \
    "Lcl1_%=:\n" \
    "ds_read_b32 v60, v32 offset:0\n" \
    "s_waitcnt lgkmcnt(0)\n" \
    "v_cmp_gt_u32_e32 vcc, 2, v60\n" \
    "s_cbranch_vccz Lce1_%=\n" \
    "s_sleep 1\n" \
    "s_sub_u32 s90, s90, 1\n" \
    "s_cbranch_scc0 Lcl1_%=\n" \
    "Lce1_%=:\n" \
    "ds_read_b128 v[128:131], v33 offset:58240\n" \
    "ds_read_b128 v[132:135], v33 offset:58304\n" \
    "ds_read_b128 v[136:139], v33 offset:58368\n" \
    "ds_read_b128 v[140:143], v33 offset:58432\n" \
    "ds_read_b128 v[144:147], v33 offset:58496\n" \
    "ds_read_b128 v[148:151], v33 offset:58560\n" \
    "ds_read_b128 v[152:155], v33 offset:58624\n" \
    "ds_read_b128 v[156:159], v33 offset:58688\n" \
    "s_waitcnt lgkmcnt(6)\n" \
    "v_mfma_f32_16x16x32_bf16 v[160:163], v[128:131], v[96:99], v[160:163]\n" \
    "v_mfma_f32_16x16x32_bf16 v[160:163], v[132:135], v[100:103], v[160:163]\n" \
    "s_waitcnt lgkmcnt(4)\n" \
    "v_mfma_f32_16x16x32_bf16 v[160:163], v[136:139], v[104:107], v[160:163]\n" \
    "v_mfma_f32_16x16x32_bf16 v[160:163], v[140:143], v[108:111], v[160:163]\n" \
    "s_waitcnt lgkmcnt(2)\n" \
    "v_mfma_f32_16x16x32_bf16 v[160:163], v[144:147], v[112:115], v[160:163]\n" \
    "v_mfma_f32_16x16x32_bf16 v[160:163], v[148:151], v[116:119], v[160:163]\n" \
    "s_waitcnt lgkmcnt(0)\n" \
    "v_mfma_f32_16x16x32_bf16 v[160:163], v[152:155], v[120:123], v[160:163]\n" \
    "v_mfma_f32_16x16x32_bf16 v[160:163], v[156:159], v[124:127], v[160:163]\n" \
    "s_nop 7\n" \
    "v_cvt_pk_bf16_f32 v0, v163, v162\n" \
    "v_cvt_pk_bf16_f32 v1, v161, v160\n" \
    "v_cvt_pk_bf16_f32 v2, v162, v161\n" \
    "ds_write_b16 v35, v0\n" \
    "ds_write_b32 v35, v2 offset:2\n" \
    "ds_write_b16_d16_hi v35, v1 offset:6\n" \
    "ds_write_b32 v35, v0 offset:1206\n" \
    "ds_write_b32 v35, v1 offset:1210\n" \
    "v_readlane_b32 s93, v0, 63\n" \
    "v_mov_b32_e32 v61, 1\n" \
    "ds_write_addtid_b32 v61 offset:56160\n" \
    "global_load_dwordx2 v[20:21], v41, s[94:95] offset:0\n" \
    "global_load_dwordx2 v[22:23], v41, s[94:95] offset:1152\n" \
    "s_add_u32 s94, s94, 589824\n" \
    "s_addc_u32 s95, s95, 0\n" \
    "s_mov_b32 s90, 0x100000\n" \
    "Lcl2_%=:\n" \
    "ds_read_b32 v60, v32 offset:0\n" \
    "s_waitcnt lgkmcnt(0)\n" \
    "v_cmp_gt_u32_e32 vcc, 3, v60\n" \
    "s_cbranch_vccz Lce2_%=\n" \
    "s_sleep 1\n" \
    "s_sub_u32 s90, s90, 1\n" \
    "s_cbranch_scc0 Lcl2_%=\n" \
    "Lce2_%=:\n" \
    "ds_read_b128 v[128:131], v34 offset:0\n" \
    "ds_read_b128 v[132:135], v34 offset:64\n" \
    "ds_read_b128 v[136:139], v34 offset:128\n" \
    "ds_read_b128 v[140:143], v34 offset:192\n" \
    "ds_read_b128 v[144:147], v34 offset:256\n" \
    "ds_read_b128 v[148:151], v34 offset:320\n" \
    "ds_read_b128 v[152:155], v34 offset:384\n" \
    "ds_read_b128 v[156:159], v34 offset:448\n" \
    "s_waitcnt lgkmcnt(6)\n" \
    "v_mfma_f32_16x16x32_bf16 v[160:163], v[128:131], v[96:99], 0\n" \
    "v_mfma_f32_16x16x32_bf16 v[160:163], v[132:135], v[100:103], v[160:163]\n" \
    "s_waitcnt lgkmcnt(4)\n" \
    "v_mfma_f32_16x16x32_bf16 v[160:163], v[136:139], v[104:107], v[160:163]\n" \
    "v_mfma_f32_16x16x32_bf16 v[160:163], v[140:143], v[108:111], v[160:163]\n" \
    "s_waitcnt lgkmcnt(2)\n" \
    "v_mfma_f32_16x16x32_bf16 v[160:163], v[144:147], v[112:115], v[160:163]\n" \
    "v_mfma_f32_16x16x32_bf16 v[160:163], v[148:151], v[116:119], v[160:163]\n" \
    "s_waitcnt lgkmcnt(0)\n" \
    "v_mfma_f32_16x16x32_bf16 v[160:163], v[152:155], v[120:123], v[160:163]\n" \
    "v_mfma_f32_16x16x32_bf16 v[160:163], v[156:159], v[124:127], v[160:163]\n" \
    "ds_read_b32 v60, v32\n" \
    "s_nop 6\n" \
    "v_cvt_pk_bf16_f32 v1, v160, v161\n" \
    "v_cvt_pk_bf16_f32 v2, v162, v163\n" \
    "v_perm_b32 v6, v1, v1, v38\n" \
    "v_perm_b32 v7, s93, v1, v40\n" \
    "v_perm_b32 v4, v2, v2, v38\n" \
    "v_perm_b32 v5, v2, v2, v39\n" \
    "ds_write_b128 v36, v[4:7]\n" \
    "s_mov_b64 exec, 1\n" \
    "ds_write_b16 v51, v1 offset:1792\n" \
    "ds_write_b16 v51, v1 offset:1794\n" \
    "s_brev_b64 exec, 1\n" \
    "ds_write2_b32 v51, v4, v4 offset0:18 offset1:19\n" \
    "ds_write_b16 v51, v4 offset:1278\n" \
    "s_mov_b64 exec, -1\n" \
    "v_mov_b32_e32 v61, 2\n" \
    "ds_write_addtid_b32 v61 offset:56160\n" \
    "global_load_dwordx2 v[24:25], v41, s[94:95] offset:0\n" \
    "global_load_dwordx2 v[26:27], v41, s[94:95] offset:1152\n" \
    "v_mov_b32_e32 v61, 2\n" \
    "s_waitcnt vmcnt(6)\n" \
    "v_perm_b32 v8, v14, v12, s91\n" \
    "v_perm_b32 v9, v14, v12, s92\n" \
    "v_perm_b32 v10, v15, v13, s91\n" \
    "v_perm_b32 v11, v15, v13, s92\n" \
    "ds_write2st64_b32 v42, v8, v9 offset0:0 offset1:18\n" \
    "ds_write2st64_b32 v42, v10, v11 offset0:36 offset1:54\n" \
    "ds_write_addtid_b32 v61 offset:56416\n" \
    "v_mov_b32_e32 v61, 4\n" \
    "s_waitcnt vmcnt(4)\n" \
    "v_perm_b32 v8, v18, v16, s91\n" \
    "v_perm_b32 v9, v18, v16, s92\n" \
    "v_perm_b32 v10, v19, v17, s91\n" \
    "v_perm_b32 v11, v19, v17, s92\n" \
    "ds_write2st64_b32 v42, v8, v9 offset0:4 offset1:22\n" \
    "ds_write2st64_b32 v42, v10, v11 offset0:40 offset1:58\n" \
    "ds_write_addtid_b32 v61 offset:56416\n" \
    "s_waitcnt lgkmcnt(12)\n" \
    "v_cmp_gt_u32_e32 vcc, 4, v60\n" \
    "s_cbranch_vccz Lce3_%=\n" \
    "s_mov_b32 s90, 0x100000\n" \
    "Lcl3_%=:\n" \
    "s_sleep 1\n" \
    "ds_read_b32 v60, v32 offset:0\n" \
    "s_waitcnt lgkmcnt(0)\n" \
    "v_cmp_gt_u32_e32 vcc, 4, v60\n" \
    "s_cbranch_vccz Lce3_%=\n" \
    "s_sub_u32 s90, s90, 1\n" \
    "s_cbranch_scc0 Lcl3_%=\n" \
    "Lce3_%=:\n" \
    "ds_read_b128 v[128:131], v56 offset:0\n" \
    "ds_read_b128 v[132:135], v56 offset:64\n" \
    "ds_read_b128 v[136:139], v56 offset:128\n" \
    "ds_read_b128 v[140:143], v56 offset:192\n" \
    "ds_read_b128 v[144:147], v56 offset:256\n" \
    "ds_read_b128 v[148:151], v56 offset:320\n" \
    "ds_read_b128 v[152:155], v56 offset:384\n" \
    "ds_read_b128 v[156:159], v56 offset:448\n" \
    "s_waitcnt lgkmcnt(6)\n" \
    "v_mfma_f32_16x16x32_bf16 v[160:163], v[128:131], v[96:99], 0\n" \
    "v_mfma_f32_16x16x32_bf16 v[160:163], v[132:135], v[100:103], v[160:163]\n" \
    "s_waitcnt lgkmcnt(4)\n" \
    "v_mfma_f32_16x16x32_bf16 v[160:163], v[136:139], v[104:107], v[160:163]\n" \
    "v_mfma_f32_16x16x32_bf16 v[160:163], v[140:143], v[108:111], v[160:163]\n" \
    "s_waitcnt lgkmcnt(2)\n" \
    "v_mfma_f32_16x16x32_bf16 v[160:163], v[144:147], v[112:115], v[160:163]\n" \
    "v_mfma_f32_16x16x32_bf16 v[160:163], v[148:151], v[116:119], v[160:163]\n" \
    "s_waitcnt lgkmcnt(0)\n" \
    "v_mfma_f32_16x16x32_bf16 v[160:163], v[152:155], v[120:123], v[160:163]\n" \
    "v_mfma_f32_16x16x32_bf16 v[160:163], v[156:159], v[124:127], v[160:163]\n" \
    "s_nop 7\n" \
    "v_cvt_pk_bf16_f32 v0, v160, v161\n" \
    "v_cvt_pk_bf16_f32 v1, v162, v163\n" \
    "ds_write_b16 v37, v0 offset:96\n" \
    "ds_write_b16_d16_hi v37, v0 offset:64\n" \
    "ds_write_b16 v37, v1 offset:32\n" \
    "ds_write_b16_d16_hi v37, v1\n" \
    "v_mov_b32_e32 v61, 3\n" \
    "ds_write_addtid_b32 v61 offset:56160\n" \
    "global_load_dwordx4 v[80:83], v44, %[ws] sc1\n" \
    "v_add_u32_e32 v44, 0x1000, v44\n" \
    "global_load_dwordx4 v[84:87], v44, %[ws] sc1\n" \
    "v_add_u32_e32 v44, 0x1000, v44\n" \
    "global_load_dwordx4 v[88:91], v44, %[ws] sc1\n" \
    "v_add_u32_e32 v44, 0x1000, v44\n" \
    "global_load_dwordx4 v[92:95], v44, %[ws] sc1\n" \
    "v_add_u32_e32 v44, 0xffffd000, v44\n" \
    "v_mov_b32_e32 v54, %[tr]\n" \
    "global_load_dwordx2 v[54:55], v54, %[ws] sc1\n" \
    "v_mov_b32_e32 v61, 6\n" \
    "s_waitcnt vmcnt(7)\n" \
    "v_perm_b32 v8, v22, v20, s91\n" \
    "v_perm_b32 v9, v22, v20, s92\n" \
    "v_perm_b32 v10, v23, v21, s91\n" \
    "v_perm_b32 v11, v23, v21, s92\n" \
    "ds_write2st64_b32 v42, v8, v9 offset0:8 offset1:26\n" \
    "ds_write2st64_b32 v42, v10, v11 offset0:44 offset1:62\n" \
    "ds_write_addtid_b32 v61 offset:56416\n" \
    "v_mov_b32_e32 v61, 8\n" \
    "s_waitcnt vmcnt(5)\n" \
    "v_perm_b32 v8, v26, v24, s91\n" \
    "v_perm_b32 v9, v26, v24, s92\n" \
    "v_perm_b32 v10, v27, v25, s91\n" \
    "v_perm_b32 v11, v27, v25, s92\n" \
    "ds_write2st64_b32 v42, v8, v9 offset0:12 offset1:30\n" \
    "ds_write2st64_b32 v42, v10, v11 offset0:48 offset1:66\n" \
    "ds_write_addtid_b32 v61 offset:56416\n" \
    "s_mov_b32 s90, 0x100000\n" \
    "Lcl4_%=:\n" \
    "ds_read_b32 v60, v45 offset:2336\n" \
    "s_waitcnt lgkmcnt(0)\n" \
    "v_cmp_gt_u32_e32 vcc, 2, v60\n" \
    "s_cbranch_vccz Lce4_%=\n" \
    "s_sleep 1\n" \
    "s_sub_u32 s90, s90, 1\n" \
    "s_cbranch_scc0 Lcl4_%=\n" \
    "Lce4_%=:\n" \
    "v_mov_b32_e32 v0, %[ak]\n" \
    "v_mov_b32_e32 v1, s98\n" \
    "s_mov_b32 exec_lo, %[am]\n" \
    "s_mov_b32 exec_hi, 0\n" \
    "global_store_dword v0, v1, %[ws] sc1\n" \
    "s_mov_b64 exec, -1\n" \
    "v_min_u32_e32 v57, %[wl], v48\n" \
    "global_load_dword v63, v57, %[wd]\n" \
    "v_add_u32_e32 v57, 0x2000, v48\n" \
    "v_min_u32_e32 v57, %[wl], v57\n" \
    "global_load_dword v63, v57, %[wd]\n" \
    "s_mov_b32 s93, 0xffff0000\n" \
    "v_add_u32_e32 v58, 58240, v43\n" \
    "s_waitcnt vmcnt(7)\n" \
    "v_cmp_ne_u32_e32 vcc, s98, v81\n" \
    "v_cmp_ne_u32_e64 s[88:89], s98, v83\n" \
    "s_or_b64 vcc, vcc, s[88:89]\n" \
    "s_cbranch_scc1 Lcs5_%=\n" \
    "Lce5_%=:\n" \
    "v_cvt_f32_f16_e32 v104, v80\n" \
    "v_cvt_f32_f16_e32 v105, v82\n" \
    "v_lshlrev_b32_e32 v96, 16, v12\n" \
    "v_and_b32_e32 v97, s93, v12\n" \
    "v_lshlrev_b32_e32 v98, 16, v13\n" \
    "v_and_b32_e32 v99, s93, v13\n" \
    "v_lshlrev_b32_e32 v100, 16, v14\n" \
    "v_and_b32_e32 v101, s93, v14\n" \
    "v_lshlrev_b32_e32 v102, 16, v15\n" \
    "v_and_b32_e32 v103, s93, v15\n" \
    "v_sub_f32_e32 v106, 1.0, v104\n" \
    "v_sub_f32_e32 v107, 1.0, v105\n" \
    "v_mul_f32_e32 v96, v106, v96\n" \
    "v_mul_f32_e32 v97, v106, v97\n" \
    "v_mul_f32_e32 v98, v106, v98\n" \
    "v_mul_f32_e32 v99, v106, v99\n" \
    "v_mul_f32_e32 v100, v107, v100\n" \
    "v_mul_f32_e32 v101, v107, v101\n" \
    "v_mul_f32_e32 v102, v107, v102\n" \
    "v_mul_f32_e32 v103, v107, v103\n" \
    "s_mov_b32 s90, 0x100000\n" \
    "Lcl6_%=:\n" \
    "ds_read_b32 v60, v45 offset:0\n" \
    "ds_read_b64 v[64:65], v43 offset:0\n" \
    "ds_read_b64 v[66:67], v43 offset:512\n" \
    "ds_read_b64 v[68:69], v43 offset:1024\n" \
    "ds_read_b64 v[70:71], v43 offset:1536\n" \
    "s_waitcnt lgkmcnt(0)\n" \
    "v_cmp_gt_u32_e32 vcc, s99, v60\n" \
    "s_cbranch_vccz Lce6_%=\n" \
    "s_sleep 2\n" \
    "s_sub_u32 s90, s90, 1\n" \
    "s_cbranch_scc0 Lcl6_%=\n" \
    "Lce6_%=:\n" \
    "v_fmac_f32_e32 v96, v104, v64\n" \
    "v_fmac_f32_e32 v97, v104, v66\n" \
    "v_fmac_f32_e32 v98, v104, v68\n" \
    "v_fmac_f32_e32 v99, v104, v70\n" \
    "v_cvt_pk_bf16_f32 v108, v96, v97\n" \
    "v_cvt_pk_bf16_f32 v109, v98, v99\n" \
    "global_store_dwordx2 v41, v[108:109], s[96:97] offset:0\n" \
    "v_fmac_f32_e32 v100, v105, v65\n" \
    "v_fmac_f32_e32 v101, v105, v67\n" \
    "v_fmac_f32_e32 v102, v105, v69\n" \
    "v_fmac_f32_e32 v103, v105, v71\n" \
    "v_cvt_pk_bf16_f32 v110, v100, v101\n" \
    "v_cvt_pk_bf16_f32 v111, v102, v103\n" \
    "global_store_dwordx2 v41, v[110:111], s[96:97] offset:1152\n" \
    "s_add_u32 s96, s96, 589824\n" \
    "s_addc_u32 s97, s97, 0\n" \
    "s_add_u32 s99, s99, 2\n" \
    "v_lshrrev_b32_e32 v1, 1, v44\n" \
    "v_add_u32_e32 v1, %[gt], v1\n" \
    "ds_write2_b32 v1, v80, v82 offset0:0 offset1:1\n" \
    "s_waitcnt vmcnt(8)\n" \
    "v_cmp_ne_u32_e32 vcc, s98, v85\n" \
    "v_cmp_ne_u32_e64 s[88:89], s98, v87\n" \
    "s_or_b64 vcc, vcc, s[88:89]\n" \
    "s_cbranch_scc1 Lcs7_%=\n" \
    "Lce7_%=:\n" \
    "v_add_u32_e32 v1, 0x800, v1\n" \
    "ds_write2_b32 v1, v84, v86 offset0:0 offset1:1\n" \
    "s_waitcnt vmcnt(7)\n" \
    "v_cmp_ne_u32_e32 vcc, s98, v89\n" \
    "v_cmp_ne_u32_e64 s[88:89], s98, v91\n" \
    "s_or_b64 vcc, vcc, s[88:89]\n" \
    "s_cbranch_scc1 Lcs8_%=\n" \
    "Lce8_%=:\n" \
    "v_add_u32_e32 v1, 0x800, v1\n" \
    "ds_write2_b32 v1, v88, v90 offset0:0 offset1:1\n" \
    "s_waitcnt vmcnt(5)\n" \
    "v_cmp_ne_u32_e32 vcc, s98, v93\n" \
    "v_cmp_ne_u32_e64 s[88:89], s98, v95\n" \
    "s_or_b64 vcc, vcc, s[88:89]\n" \
    "s_cbranch_scc1 Lcs9_%=\n" \
    "Lce9_%=:\n" \
    "v_add_u32_e32 v1, 0x800, v1\n" \
    "ds_write2_b32 v1, v92, v94 offset0:0 offset1:1\n" \
    "s_memrealtime s[88:89]\n" \
    "v_cmp_eq_u32_e32 vcc, s98, v54\n" \
    "v_mov_b32_e32 v1, %[kt]\n" \
    "s_waitcnt lgkmcnt(0)\n" \
    "s_sub_u32 s88, s88, 390\n" \
    "v_mov_b32_e32 v2, s88\n" \
    "v_cndmask_b32_e32 v2, v2, v55, vcc\n" \
    "ds_write_b32 v1, v2\n" \
    "v_cvt_f32_f16_e32 v104, v84\n" \
    "v_cvt_f32_f16_e32 v105, v86\n" \
    "v_lshlrev_b32_e32 v96, 16, v16\n" \
    "v_and_b32_e32 v97, s93, v16\n" \
    "v_lshlrev_b32_e32 v98, 16, v17\n" \
    "v_and_b32_e32 v99, s93, v17\n" \
    "v_lshlrev_b32_e32 v100, 16, v18\n" \
    "v_and_b32_e32 v101, s93, v18\n" \
    "v_lshlrev_b32_e32 v102, 16, v19\n" \
    "v_and_b32_e32 v103, s93, v19\n" \
    "v_sub_f32_e32 v106, 1.0, v104\n" \
    "v_sub_f32_e32 v107, 1.0, v105\n" \
    "v_mul_f32_e32 v96, v106, v96\n" \
    "v_mul_f32_e32 v97, v106, v97\n" \
    "v_mul_f32_e32 v98, v106, v98\n" \
    "v_mul_f32_e32 v99, v106, v99\n" \
    "v_mul_f32_e32 v100, v107, v100\n" \
    "v_mul_f32_e32 v101, v107, v101\n" \
    "v_mul_f32_e32 v102, v107, v102\n" \
    "v_mul_f32_e32 v103, v107, v103\n" \
    "s_mov_b32 s90, 0x100000\n" \
    "Lcl10_%=:\n" \
    "ds_read_b32 v60, v47 offset:0\n" \
    "ds_read_b64 v[64:65], v43 offset:8320\n" \
    "ds_read_b64 v[66:67], v43 offset:8832\n" \
    "ds_read_b64 v[68:69], v43 offset:9344\n" \
    "ds_read_b64 v[70:71], v43 offset:9856\n" \
    "ds_read_b64 v[72:73], v58 offset:0\n" \
    "ds_read_b64 v[74:75], v58 offset:512\n" \
    "ds_read_b64 v[76:77], v58 offset:1024\n" \
    "ds_read_b64 v[78:79], v58 offset:1536\n" \
    "s_waitcnt lgkmcnt(0)\n" \
    "v_cmp_gt_u32_e32 vcc, s99, v60\n" \
    "s_cbranch_vccz Lce10_%=\n" \
    "s_sleep 2\n" \
    "s_sub_u32 s90, s90, 1\n" \
    "s_cbranch_scc0 Lcl10_%=\n" \
    "Lce10_%=:\n" \
    "v_fmac_f32_e32 v96, v104, v64\n" \
    "v_fmac_f32_e32 v97, v104, v66\n" \
    "v_fmac_f32_e32 v98, v104, v68\n" \
    "v_fmac_f32_e32 v99, v104, v70\n" \
    "v_fmac_f32_e32 v96, v104, v72\n" \
    "v_fmac_f32_e32 v97, v104, v74\n" \
    "v_fmac_f32_e32 v98, v104, v76\n" \
    "v_fmac_f32_e32 v99, v104, v78\n" \
    "v_cvt_pk_bf16_f32 v112, v96, v97\n" \
    "v_cvt_pk_bf16_f32 v113, v98, v99\n" \
    "global_store_dwordx2 v41, v[112:113], s[96:97] offset:0\n" \
    "v_fmac_f32_e32 v100, v105, v65\n" \
    "v_fmac_f32_e32 v101, v105, v67\n" \
    "v_fmac_f32_e32 v102, v105, v69\n" \
    "v_fmac_f32_e32 v103, v105, v71\n" \
    "v_fmac_f32_e32 v100, v105, v73\n" \
    "v_fmac_f32_e32 v101, v105, v75\n" \
    "v_fmac_f32_e32 v102, v105, v77\n" \
    "v_fmac_f32_e32 v103, v105, v79\n" \
    "v_cvt_pk_bf16_f32 v114, v100, v101\n" \
    "v_cvt_pk_bf16_f32 v115, v102, v103\n" \
    "global_store_dwordx2 v41, v[114:115], s[96:97] offset:1152\n" \
    "s_add_u32 s96, s96, 589824\n" \
    "s_addc_u32 s97, s97, 0\n" \
    "s_add_u32 s99, s99, 2\n" \
    "v_lshrrev_b32_e32 v120, 3, v44\n" \
    "v_sub_u32_e32 v120, %[ko], v120\n" \
    "v_and_or_b32 v124, v80, s93, v120\n" \
    "v_add_u32_e32 v120, -1, v120\n" \
    "v_and_or_b32 v121, v82, s93, v120\n" \
    "v_min_u32_e32 v125, v124, v121\n" \
    "v_max_u32_e32 v124, v124, v121\n" \
    "v_add_u32_e32 v120, 0xfffffe01, v120\n" \
    "v_and_or_b32 v121, v84, s93, v120\n" \
    "v_min3_u32 v126, v124, v125, v121\n" \
    "v_med3_u32 v125, v124, v125, v121\n" \
    "v_max_u32_e32 v124, v124, v121\n" \
    "v_add_u32_e32 v120, -1, v120\n" \
    "v_and_or_b32 v121, v86, s93, v120\n" \
    "v_med3_u32 v126, v125, v126, v121\n" \
    "v_med3_u32 v125, v124, v125, v121\n" \
    "v_max_u32_e32 v124, v124, v121\n" \
    "v_add_u32_e32 v120, 0xfffffe01, v120\n" \
    "v_and_or_b32 v121, v88, s93, v120\n" \
    "v_med3_u32 v126, v125, v126, v121\n" \
    "v_med3_u32 v125, v124, v125, v121\n" \
    "v_max_u32_e32 v124, v124, v121\n" \
    "v_add_u32_e32 v120, -1, v120\n" \
    "v_and_or_b32 v121, v90, s93, v120\n" \
    "v_med3_u32 v126, v125, v126, v121\n" \
    "v_med3_u32 v125, v124, v125, v121\n" \
    "v_max_u32_e32 v124, v124, v121\n" \
    "v_add_u32_e32 v120, 0xfffffe01, v120\n" \
    "v_and_or_b32 v121, v92, s93, v120\n" \
    "v_med3_u32 v126, v125, v126, v121\n" \
    "v_med3_u32 v125, v124, v125, v121\n" \
    "v_max_u32_e32 v124, v124, v121\n" \
    "v_add_u32_e32 v120, -1, v120\n" \
    "v_and_or_b32 v121, v94, s93, v120\n" \
    "v_med3_u32 v126, v125, v126, v121\n" \
    "v_med3_u32 v125, v124, v125, v121\n" \
    "v_max_u32_e32 v124, v124, v121\n" \
    "v_add_u32_e32 v120, %[kt2], v44\n" \
    "ds_write_b128 v120, v[124:127]\n" \
    "v_mov_b32_e32 v61, 10\n" \
    "ds_write_addtid_b32 v61 offset:56416\n" \
    "v_cvt_f32_f16_e32 v104, v88\n" \
    "v_cvt_f32_f16_e32 v105, v90\n" \
    "v_lshlrev_b32_e32 v96, 16, v20\n" \
    "v_and_b32_e32 v97, s93, v20\n" \
    "v_lshlrev_b32_e32 v98, 16, v21\n" \
    "v_and_b32_e32 v99, s93, v21\n" \
    "v_lshlrev_b32_e32 v100, 16, v22\n" \
    "v_and_b32_e32 v101, s93, v22\n" \
    "v_lshlrev_b32_e32 v102, 16, v23\n" \
    "v_and_b32_e32 v103, s93, v23\n" \
    "v_sub_f32_e32 v106, 1.0, v104\n" \
    "v_sub_f32_e32 v107, 1.0, v105\n" \
    "v_mul_f32_e32 v96, v106, v96\n" \
    "v_mul_f32_e32 v97, v106, v97\n" \
    "v_mul_f32_e32 v98, v106, v98\n" \
    "v_mul_f32_e32 v99, v106, v99\n" \
    "v_mul_f32_e32 v100, v107, v100\n" \
    "v_mul_f32_e32 v101, v107, v101\n" \
    "v_mul_f32_e32 v102, v107, v102\n" \
    "v_mul_f32_e32 v103, v107, v103\n" \
    "s_mov_b32 s90, 0x100000\n" \
    "Lcl11_%=:\n" \
    "ds_read_b32 v60, v47 offset:0\n" \
    "ds_read_b64 v[64:65], v43 offset:16640\n" \
    "ds_read_b64 v[66:67], v43 offset:17152\n" \
    "ds_read_b64 v[68:69], v43 offset:17664\n" \
    "ds_read_b64 v[70:71], v43 offset:18176\n" \
    "ds_read_b64 v[72:73], v58 offset:8320\n" \
    "ds_read_b64 v[74:75], v58 offset:8832\n" \
    "ds_read_b64 v[76:77], v58 offset:9344\n" \
    "ds_read_b64 v[78:79], v58 offset:9856\n" \
    "s_waitcnt lgkmcnt(0)\n" \
    "v_cmp_gt_u32_e32 vcc, s99, v60\n" \
    "s_cbranch_vccz Lce11_%=\n" \
    "s_sleep 2\n" \
    "s_sub_u32 s90, s90, 1\n" \
    "s_cbranch_scc0 Lcl11_%=\n" \
    "Lce11_%=:\n" \
    "v_fmac_f32_e32 v96, v104, v64\n" \
    "v_fmac_f32_e32 v97, v104, v66\n" \
    "v_fmac_f32_e32 v98, v104, v68\n" \
    "v_fmac_f32_e32 v99, v104, v70\n" \
    "v_fmac_f32_e32 v96, v104, v72\n" \
    "v_fmac_f32_e32 v97, v104, v74\n" \
    "v_fmac_f32_e32 v98, v104, v76\n" \
    "v_fmac_f32_e32 v99, v104, v78\n" \
    "v_cvt_pk_bf16_f32 v108, v96, v97\n" \
    "v_cvt_pk_bf16_f32 v109, v98, v99\n" \
    "global_store_dwordx2 v41, v[108:109], s[96:97] offset:0\n" \
    "v_fmac_f32_e32 v100, v105, v65\n" \
    "v_fmac_f32_e32 v101, v105, v67\n" \
    "v_fmac_f32_e32 v102, v105, v69\n" \
    "v_fmac_f32_e32 v103, v105, v71\n" \
    "v_fmac_f32_e32 v100, v105, v73\n" \
    "v_fmac_f32_e32 v101, v105, v75\n" \
    "v_fmac_f32_e32 v102, v105, v77\n" \
    "v_fmac_f32_e32 v103, v105, v79\n" \
    "v_cvt_pk_bf16_f32 v110, v100, v101\n" \
    "v_cvt_pk_bf16_f32 v111, v102, v103\n" \
    "global_store_dwordx2 v41, v[110:111], s[96:97] offset:1152\n" \
    "s_add_u32 s96, s96, 589824\n" \
    "s_addc_u32 s97, s97, 0\n" \
    "s_add_u32 s99, s99, 2\n" \
    "v_cvt_f32_f16_e32 v104, v92\n" \
    "v_cvt_f32_f16_e32 v105, v94\n" \
    "v_lshlrev_b32_e32 v96, 16, v24\n" \
    "v_and_b32_e32 v97, s93, v24\n" \
    "v_lshlrev_b32_e32 v98, 16, v25\n" \
    "v_and_b32_e32 v99, s93, v25\n" \
    "v_lshlrev_b32_e32 v100, 16, v26\n" \
    "v_and_b32_e32 v101, s93, v26\n" \
    "v_lshlrev_b32_e32 v102, 16, v27\n" \
    "v_and_b32_e32 v103, s93, v27\n" \
    "v_sub_f32_e32 v106, 1.0, v104\n" \
    "v_sub_f32_e32 v107, 1.0, v105\n" \
    "v_mul_f32_e32 v96, v106, v96\n" \
    "v_mul_f32_e32 v97, v106, v97\n" \
    "v_mul_f32_e32 v98, v106, v98\n" \
    "v_mul_f32_e32 v99, v106, v99\n" \
    "v_mul_f32_e32 v100, v107, v100\n" \
    "v_mul_f32_e32 v101, v107, v101\n" \
    "v_mul_f32_e32 v102, v107, v102\n" \
    "v_mul_f32_e32 v103, v107, v103\n" \
    "s_mov_b32 s90, 0x100000\n" \
    "Lcl12_%=:\n" \
    "ds_read_b32 v60, v47 offset:0\n" \
    "ds_read_b64 v[64:65], v43 offset:24960\n" \
    "ds_read_b64 v[66:67], v43 offset:25472\n" \
    "ds_read_b64 v[68:69], v43 offset:25984\n" \
    "ds_read_b64 v[70:71], v43 offset:26496\n" \
    "ds_read_b64 v[72:73], v58 offset:16640\n" \
    "ds_read_b64 v[74:75], v58 offset:17152\n" \
    "ds_read_b64 v[76:77], v58 offset:17664\n" \
    "ds_read_b64 v[78:79], v58 offset:18176\n" \
    "s_waitcnt lgkmcnt(0)\n" \
    "v_cmp_gt_u32_e32 vcc, s99, v60\n" \
    "s_cbranch_vccz Lce12_%=\n" \
    "s_sub_u32 s90, s90, 1\n" \
    "s_cbranch_scc0 Lcl12_%=\n" \
    "Lce12_%=:\n" \
    "v_fmac_f32_e32 v96, v104, v64\n" \
    "v_fmac_f32_e32 v97, v104, v66\n" \
    "v_fmac_f32_e32 v98, v104, v68\n" \
    "v_fmac_f32_e32 v99, v104, v70\n" \
    "v_fmac_f32_e32 v96, v104, v72\n" \
    "v_fmac_f32_e32 v97, v104, v74\n" \
    "v_fmac_f32_e32 v98, v104, v76\n" \
    "v_fmac_f32_e32 v99, v104, v78\n" \
    "v_cvt_pk_bf16_f32 v112, v96, v97\n" \
    "v_cvt_pk_bf16_f32 v113, v98, v99\n" \
    "global_store_dwordx2 v41, v[112:113], s[96:97] offset:0\n" \
    "v_fmac_f32_e32 v100, v105, v65\n" \
    "v_fmac_f32_e32 v101, v105, v67\n" \
    "v_fmac_f32_e32 v102, v105, v69\n" \
    "v_fmac_f32_e32 v103, v105, v71\n" \
    "v_fmac_f32_e32 v100, v105, v73\n" \
    "v_fmac_f32_e32 v101, v105, v75\n" \
    "v_fmac_f32_e32 v102, v105, v77\n" \
    "v_fmac_f32_e32 v103, v105, v79\n" \
    "v_cvt_pk_bf16_f32 v114, v100, v101\n" \
    "v_cvt_pk_bf16_f32 v115, v102, v103\n" \
    "global_store_dwordx2 v41, v[114:115], s[96:97] offset:1152\n" \
    "v_mov_b32_e32 v61, 20\n" \
    "s_waitcnt vmcnt(0)\n" \
    "ds_write_addtid_b32 v61 offset:56416\n" \
    "s_branch Lcx_%=\n" \
    "Lcs5_%=:\n" \
    "s_mov_b32 s90, 0x20000\n" \
    "v_add_u32_e32 v0, 0, v44\n" \
    "Lcl5_%=:\n" \
    "s_sleep 4\n" \
    "global_load_dwordx4 v[80:83], v0, %[ws] sc1\n" \
    "s_waitcnt vmcnt(0)\n" \
    "v_cmp_ne_u32_e32 vcc, s98, v81\n" \
    "v_cmp_ne_u32_e64 s[88:89], s98, v83\n" \
    "s_nop 2\n" \
    "s_or_b64 vcc, vcc, s[88:89]\n" \
    "s_cbranch_scc0 Lce5_%=\n" \
    "s_sub_u32 s90, s90, 1\n" \
    "s_cbranch_scc0 Lcl5_%=\n" \
    "s_branch Lce5_%=\n" \
    "Lcs7_%=:\n" \
    "s_mov_b32 s90, 0x20000\n" \
    "v_add_u32_e32 v0, 4096, v44\n" \
    "Lcl7_%=:\n" \
    "s_sleep 4\n" \
    "global_load_dwordx4 v[84:87], v0, %[ws] sc1\n" \
    "s_waitcnt vmcnt(0)\n" \
    "v_cmp_ne_u32_e32 vcc, s98, v85\n" \
    "v_cmp_ne_u32_e64 s[88:89], s98, v87\n" \
    "s_nop 2\n" \
    "s_or_b64 vcc, vcc, s[88:89]\n" \
    "s_cbranch_scc0 Lce7_%=\n" \
    "s_sub_u32 s90, s90, 1\n" \
    "s_cbranch_scc0 Lcl7_%=\n" \
    "s_branch Lce7_%=\n" \
    "Lcs8_%=:\n" \
    "s_mov_b32 s90, 0x20000\n" \
    "v_add_u32_e32 v0, 8192, v44\n" \
    "Lcl8_%=:\n" \
    "s_sleep 4\n" \
    "global_load_dwordx4 v[88:91], v0, %[ws] sc1\n" \
    "s_waitcnt vmcnt(0)\n" \
    "v_cmp_ne_u32_e32 vcc, s98, v89\n" \
    "v_cmp_ne_u32_e64 s[88:89], s98, v91\n" \
    "s_nop 2\n" \
    "s_or_b64 vcc, vcc, s[88:89]\n" \
    "s_cbranch_scc0 Lce8_%=\n" \
    "s_sub_u32 s90, s90, 1\n" \
    "s_cbranch_scc0 Lcl8_%=\n" \
    "s_branch Lce8_%=\n" \
    "Lcs9_%=:\n" \
    "s_mov_b32 s90, 0x20000\n" \
    "v_add_u32_e32 v0, 12288, v44\n" \
    "Lcl9_%=:\n" \
    "s_sleep 4\n" \
    "global_load_dwordx4 v[92:95], v0, %[ws] sc1\n" \
    "s_waitcnt vmcnt(0)\n" \
    "v_cmp_ne_u32_e32 vcc, s98, v93\n" \
    "v_cmp_ne_u32_e64 s[88:89], s98, v95\n" \
    "s_nop 2\n" \
    "s_or_b64 vcc, vcc, s[88:89]\n" \
    "s_cbranch_scc0 Lce9_%=\n" \
    "s_sub_u32 s90, s90, 1\n" \
    "s_cbranch_scc0 Lcl9_%=\n" \
    "s_branch Lce9_%=\n" \
    "Lcx_%=:\n"
#define CONV_C_CLOB "v0", "v1", "v2", "v4", "v5", "v6", "v7", "v8", "v9", "v10", "v11", "v12", "v13", "v14", "v15", "v16", "v17", "v18", "v19", "v20", "v21", "v22", "v23", "v24", "v25", "v26", "v27", "v32", "v33", "v34", "v35", "v36", "v37", "v38", "v39", "v40", "v41", "v42", "v43", "v44", "v45", "v47", "v48", "v49", "v50", "v51", "v54", "v55", "v56", "v57", "v58", "v60", "v61", "v62", "v63", "v64", "v65", "v66", "v67", "v68", "v69", "v70", "v71", "v72", "v73", "v74", "v75", "v76", "v77", "v78", "v79", "v80", "v81", "v82", "v83", "v84", "v85", "v86", "v87", "v88", "v89", "v90", "v91", "v92", "v93", "v94", "v95", "v96", "v97", "v98", "v99", "v100", "v101", "v102", "v103", "v104", "v105", "v106", "v107", "v108", "v109", "v110", "v111", "v112", "v113", "v114", "v115", "v116", "v117", "v118", "v119", "v120", "v121", "v122", "v123", "v124", "v125", "v126", "v127", "v128", "v129", "v130", "v131", "v132", "v133", "v134", "v135", "v136", "v137", "v138", "v139", "v140", "v141", "v142", "v143", "v144", "v145", "v146", "v147", "v148", "v149", "v150", "v151", "v152", "v153", "v154", "v155", "v156", "v157", "v158", "v159", "v160", "v161", "v162", "v163"
#define ITEM_ASM \
    "v_mbcnt_lo_u32_b32 v53, -1, 0\n" \
    "v_mbcnt_hi_u32_b32 v53, -1, v53\n" \
    "s_lshl_b32 s88, %[it], 7\n" \
    "s_add_u32 s89, s88, %[kb]\n" \
    "s_add_u32 s88, s88, %[cb]\n" \
    "v_and_b32_e32 v28, 3, v53\n" \
    "v_bfe_u32 v31, v53, 2, 2\n" \
    "v_lshrrev_b32_e32 v52, 4, v53\n" \
    "v_lshlrev_b32_e32 v30, 3, v31\n" \
    "v_lshl_or_b32 v30, v28, 5, v30\n" \
    "v_add_u32_e32 v30, s89, v30\n" \
    "v_lshlrev_b32_e32 v28, 4, v28\n" \
    "v_lshl_or_b32 v28, v52, 6, v28\n" \
    "v_and_b32_e32 v52, 1, v31\n" \
    "v_mul_u32_u24_e32 v52, 1152, v52\n" \
    "s_mov_b32 s89, %[wl]\n" \
    "v_add3_u32 v28, v28, v52, s89\n" \
    "v_lshlrev_b32_e32 v29, 14, v53\n" \
    "v_add_u32_e32 v29, s88, v29\n" \
    "v_sub_u32_e32 v31, 15, v31\n" \
    "v_mov_b64 v[16:17], 0\n" \
    "v_mov_b64 v[18:19], 0\n" \
    "v_mov_b64 v[20:21], 0\n" \
    "v_mov_b64 v[22:23], 0\n" \
    "s_mov_b32 vcc_lo, 0xaaaaaaaa\n" \
    "v_mov_b32_e32 v52, 0x07060302\n" \
    "s_mov_b32 s88, 0x05040100\n" \
    "v_mov_b32_e32 v53, s88\n" \
    "v_cndmask_b32_e32 v52, v53, v52, vcc\n" \
    "s_mov_b32 s88, 0x80008000\n" \
    "s_mov_b32 s89, 0xffff0000\n" \
    "s_mov_b32 s92, 0x07060304\n" \
    "s_mov_b32 s93, 0x07060204\n" \
    "s_mov_b32 vcc_lo, 0xcccccccc\n" \
    "v_mov_b32_e32 v91, %[fl]\n" \
    "v_mov_b32_e32 v92, 1\n" \
    "s_waitcnt vmcnt(18)\n" \
    "ds_read_b128 v[0:3], v28 offset:0\n" \
    "ds_read_b128 v[4:7], v28 offset:256\n" \
    "ds_read_b128 v[8:11], v28 offset:512\n" \
    "ds_read_b128 v[12:15], v28 offset:768\n" \
    "s_mov_b32 exec_hi, 0\n" \
    "ds_read_b128 v[16:19], v28 offset:1024\n" \
    "s_mov_b64 exec, -1\n" \
    "s_mov_b32 exec_lo, 0\n" \
    "ds_read_b128 v[20:23], v28 offset:896\n" \
    "s_mov_b32 exec_lo, -1\n" \
    "v_readfirstlane_b32 s98, v72\n" \
    "v_add_u32_e32 v54, -4, v31\n" \
    "v_add_u32_e32 v55, -8, v31\n" \
    "v_add_u32_e32 v90, -12, v31\n" \
    "s_add_u32 s98, s98, 1\n" \
    "s_max_u32 s98, s98, 1\n" \
    "v_mov_b32_e32 v25, s98\n" \
    "v_mov_b32_e32 v27, s98\n" \
    "s_waitcnt vmcnt(17)\n" \
    "s_waitcnt lgkmcnt(5)\n" \
    "v_mfma_f32_16x16x32_bf16 v[74:77], v[0:3], v[96:99], 0\n" \
    "s_waitcnt vmcnt(16)\n" \
    "v_mfma_f32_16x16x32_bf16 v[78:81], v[0:3], v[112:115], 0\n" \
    "s_waitcnt vmcnt(15)\n" \
    "v_mfma_f32_16x16x32_bf16 v[82:85], v[0:3], v[128:131], 0\n" \
    "s_waitcnt vmcnt(14)\n" \
    "v_mfma_f32_16x16x32_bf16 v[86:89], v[0:3], v[144:147], 0\n" \
    "s_waitcnt vmcnt(13)\n" \
    "s_waitcnt lgkmcnt(4)\n" \
    "v_mfma_f32_16x16x32_bf16 v[74:77], v[4:7], v[100:103], v[74:77]\n" \
    "s_waitcnt vmcnt(12)\n" \
    "v_mfma_f32_16x16x32_bf16 v[78:81], v[4:7], v[116:119], v[78:81]\n" \
    "s_waitcnt vmcnt(11)\n" \
    "v_mfma_f32_16x16x32_bf16 v[82:85], v[4:7], v[132:135], v[82:85]\n" \
    "s_waitcnt vmcnt(10)\n" \
    "v_mfma_f32_16x16x32_bf16 v[86:89], v[4:7], v[148:151], v[86:89]\n" \
    "s_waitcnt vmcnt(9)\n" \
    "s_waitcnt lgkmcnt(3)\n" \
    "v_mfma_f32_16x16x32_bf16 v[74:77], v[8:11], v[104:107], v[74:77]\n" \
    "s_waitcnt vmcnt(8)\n" \
    "v_mfma_f32_16x16x32_bf16 v[78:81], v[8:11], v[120:123], v[78:81]\n" \
    "s_waitcnt vmcnt(7)\n" \
    "v_mfma_f32_16x16x32_bf16 v[82:85], v[8:11], v[136:139], v[82:85]\n" \
    "s_waitcnt vmcnt(6)\n" \
    "v_mfma_f32_16x16x32_bf16 v[86:89], v[8:11], v[152:155], v[86:89]\n" \
    "s_waitcnt vmcnt(5)\n" \
    "s_waitcnt lgkmcnt(2)\n" \
    "v_mfma_f32_16x16x32_bf16 v[74:77], v[12:15], v[108:111], v[74:77]\n" \
    "s_waitcnt vmcnt(4)\n" \
    "v_mfma_f32_16x16x32_bf16 v[78:81], v[12:15], v[124:127], v[78:81]\n" \
    "s_waitcnt vmcnt(3)\n" \
    "v_mfma_f32_16x16x32_bf16 v[82:85], v[12:15], v[140:143], v[82:85]\n" \
    "s_waitcnt vmcnt(2)\n" \
    "v_mfma_f32_16x16x32_bf16 v[86:89], v[12:15], v[156:159], v[86:89]\n" \
    "s_waitcnt vmcnt(1)\n" \
    "s_waitcnt lgkmcnt(1)\n" \
    "v_mfma_f32_16x16x32_bf16 v[74:77], v[16:19], v[160:163], v[74:77]\n" \
    "s_waitcnt lgkmcnt(0)\n" \
    "v_mfma_f32_16x16x32_bf16 v[78:81], v[20:23], v[160:163], v[78:81]\n" \
    "s_waitcnt vmcnt(0)\n" \
    "v_mfma_f32_16x16x32_bf16 v[82:85], v[16:19], v[164:167], v[82:85]\n" \
    "v_mfma_f32_16x16x32_bf16 v[86:89], v[20:23], v[164:167], v[86:89]\n" \
    "s_nop 2\n" \
    "v_mov_b32_dpp v0, v74 quad_perm:[0,0,0,0] row_mask:0xf bank_mask:0xf\n" \
    "s_nop 0\n" \
    "v_mov_b32_dpp v1, v78 quad_perm:[0,0,0,0] row_mask:0xf bank_mask:0xf\n" \
    "s_nop 0\n" \
    "v_mov_b32_dpp v2, v82 quad_perm:[0,0,0,0] row_mask:0xf bank_mask:0xf\n" \
    "v_mov_b32_dpp v3, v86 quad_perm:[0,0,0,0] row_mask:0xf bank_mask:0xf\n" \
    "v_add_f32_dpp v0, v75, v0 quad_perm:[1,1,1,1] row_mask:0xf bank_mask:0xf\n" \
    "v_add_f32_dpp v1, v79, v1 quad_perm:[1,1,1,1] row_mask:0xf bank_mask:0xf\n" \
    "v_add_f32_dpp v2, v83, v2 quad_perm:[1,1,1,1] row_mask:0xf bank_mask:0xf\n" \
    "v_add_f32_dpp v3, v87, v3 quad_perm:[1,1,1,1] row_mask:0xf bank_mask:0xf\n" \
    "v_add_f32_dpp v0, v76, v0 quad_perm:[2,2,2,2] row_mask:0xf bank_mask:0xf\n" \
    "v_add_f32_dpp v1, v80, v1 quad_perm:[2,2,2,2] row_mask:0xf bank_mask:0xf\n" \
    "v_add_f32_dpp v2, v84, v2 quad_perm:[2,2,2,2] row_mask:0xf bank_mask:0xf\n" \
    "v_add_f32_dpp v3, v88, v3 quad_perm:[2,2,2,2] row_mask:0xf bank_mask:0xf\n" \
    "v_add_f32_dpp v0, v77, v0 quad_perm:[3,3,3,3] row_mask:0xf bank_mask:0xf\n" \
    "v_add_f32_dpp v1, v81, v1 quad_perm:[3,3,3,3] row_mask:0xf bank_mask:0xf\n" \
    "v_add_f32_dpp v2, v85, v2 quad_perm:[3,3,3,3] row_mask:0xf bank_mask:0xf\n" \
    "v_add_f32_dpp v3, v89, v3 quad_perm:[3,3,3,3] row_mask:0xf bank_mask:0xf\n" \
    "v_max_f32_e32 v4, 0xff800000, v0\n" \
    "v_max_f32_e32 v5, 0xff800000, v1\n" \
    "v_max_f32_e32 v6, 0xff800000, v2\n" \
    "v_max_f32_e32 v7, 0xff800000, v3\n" \
    "v_cvt_pk_bf16_f32 v8, v4, v5\n" \
    "v_cvt_pk_bf16_f32 v9, v6, v7\n" \
    "v_pk_ashrrev_i16 v4, 15, v8 op_sel_hi:[0,1]\n" \
    "v_pk_ashrrev_i16 v5, 15, v9 op_sel_hi:[0,1]\n" \
    "v_bitop3_b32 v8, v8, v4, s88 bitop3:0x1e\n" \
    "v_bitop3_b32 v9, v9, v5, s88 bitop3:0x1e\n" \
    "v_lshl_or_b32 v4, v8, 16, v31\n" \
    "v_and_or_b32 v5, v8, s89, v54\n" \
    "v_lshl_or_b32 v6, v9, 16, v55\n" \
    "v_and_or_b32 v7, v9, s89, v90\n" \
    "v_max_u32_e32 v10, v4, v5\n" \
    "v_max_u32_e32 v12, v6, v7\n" \
    "v_min_u32_e32 v11, v4, v5\n" \
    "v_min_u32_e32 v13, v6, v7\n" \
    "v_max_u32_e32 v14, v10, v12\n" \
    "v_min_u32_e32 v17, v10, v12\n" \
    "v_min_u32_e32 v18, v10, v13\n" \
    "v_max3_u32 v15, v17, v11, v13\n" \
    "v_min_u32_e32 v16, v11, v12\n" \
    "v_max_u32_e32 v16, v16, v18\n" \
    "v_min_u32_dpp v4, v14, v14 row_ror:4 row_mask:0xf bank_mask:0xf\n" \
    "v_mov_b32_dpp v5, v15 row_ror:4 row_mask:0xf bank_mask:0xf\n" \
    "v_max_u32_dpp v10, v14, v14 row_ror:4 row_mask:0xf bank_mask:0xf\n" \
    "v_min_u32_dpp v17, v14, v15 row_ror:4 row_mask:0xf bank_mask:0xf\n" \
    "v_max_u32_dpp v6, v16, v16 row_ror:4 row_mask:0xf bank_mask:0xf\n" \
    "v_max3_u32 v11, v4, v15, v5\n" \
    "v_min_u32_e32 v18, v14, v5\n" \
    "v_max3_u32 v12, v17, v18, v6\n" \
    "v_min_u32_dpp v4, v10, v10 row_ror:8 row_mask:0xf bank_mask:0xf\n" \
    "v_mov_b32_dpp v5, v11 row_ror:8 row_mask:0xf bank_mask:0xf\n" \
    "v_max_u32_dpp v14, v10, v10 row_ror:8 row_mask:0xf bank_mask:0xf\n" \
    "v_min_u32_dpp v17, v10, v11 row_ror:8 row_mask:0xf bank_mask:0xf\n" \
    "v_max_u32_dpp v6, v12, v12 row_ror:8 row_mask:0xf bank_mask:0xf\n" \
    "v_max3_u32 v15, v4, v11, v5\n" \
    "v_min_u32_e32 v18, v10, v5\n" \
    "v_max3_u32 v16, v17, v18, v6\n" \
    "s_mov_b64 exec, 0xff\n" \
    "v_perm_b32 v24, v14, v16, s92\n" \
    "v_perm_b32 v26, v15, v16, s93\n" \
    "global_store_dwordx4 v29, v[24:27], %[ws] sc1\n" \
    "ds_write_b32 v91, v92\n" \
    "s_mov_b64 exec, -1\n" \
    "v_mul_f32_e32 v0, 0xbfb8aa3b, v0\n" \
    "v_mul_f32_e32 v1, 0xbfb8aa3b, v1\n" \
    "v_mul_f32_e32 v2, 0xbfb8aa3b, v2\n" \
    "v_mul_f32_e32 v3, 0xbfb8aa3b, v3\n" \
    "v_exp_f32_e32 v0, v0\n" \
    "v_exp_f32_e32 v1, v1\n" \
    "v_exp_f32_e32 v2, v2\n" \
    "v_exp_f32_e32 v3, v3\n" \
    "v_add_f32_e32 v0, 1.0, v0\n" \
    "v_add_f32_e32 v1, 1.0, v1\n" \
    "v_add_f32_e32 v2, 1.0, v2\n" \
    "v_add_f32_e32 v3, 1.0, v3\n" \
    "v_rcp_f32_e32 v0, v0\n" \
    "v_rcp_f32_e32 v1, v1\n" \
    "v_rcp_f32_e32 v2, v2\n" \
    "v_rcp_f32_e32 v3, v3\n" \
    "v_cvt_f16_f32_e32 v0, v0\n" \
    "v_cvt_f16_f32_e32 v1, v1\n" \
    "v_cvt_f16_f32_e32 v2, v2\n" \
    "v_cvt_f16_f32_e32 v3, v3\n" \
    "v_pack_b32_f16 v0, v0, v1\n" \
    "v_pack_b32_f16 v2, v2, v3\n" \
    "v_cndmask_b32_e32 v8, v8, v9, vcc\n" \
    "v_cndmask_b32_e32 v0, v0, v2, vcc\n" \
    "v_mov_b32_e32 v11, s98\n" \
    "s_nop 0\n" \
    "v_permlane16_swap_b32 v0, v1\n" \
    "v_perm_b32 v10, v8, v1, v52\n" \
    "s_mov_b64 exec, 0xffff\n" \
    "global_store_dwordx2 v30, v[10:11], %[ws] sc1\n" \
    "s_mov_b64 exec, -1\n"
#define ITEM_CLOB "v0", "v1", "v2", "v3", "v4", "v5", "v6", "v7", "v8", "v9", "v10", "v11", "v12", "v13", "v14", "v15", "v16", "v17", "v18", "v19", "v20", "v21", "v22", "v23", "v24", "v25", "v26", "v27", "v28", "v29", "v30", "v31", "v52", "v53", "v54", "v55", "v72", "v74", "v75", "v76", "v77", "v78", "v79", "v80", "v81", "v82", "v83", "v84", "v85", "v86", "v87", "v88", "v89", "v90", "v91", "v92", "v96", "v97", "v98", "v99", "v100", "v101", "v102", "v103", "v104", "v105", "v106", "v107", "v108", "v109", "v110", "v111", "v112", "v113", "v114", "v115", "v116", "v117", "v118", "v119", "v120", "v121", "v122", "v123", "v124", "v125", "v126", "v127", "v128", "v129", "v130", "v131", "v132", "v133", "v134", "v135", "v136", "v137", "v138", "v139", "v140", "v141", "v142", "v143", "v144", "v145", "v146", "v147", "v148", "v149", "v150", "v151", "v152", "v153", "v154", "v155", "v156", "v157", "v158", "v159", "v160", "v161", "v162", "v163", "v164", "v165", "v166", "v167"
#define FFN_ASM \
    "v_mov_b64 v[96:97], 0\n" \
    "v_mov_b64 v[98:99], 0\n" \
    "v_mov_b64 v[100:101], 0\n" \
    "v_mov_b64 v[102:103], 0\n" \
    "v_mov_b64 v[104:105], 0\n" \
    "v_mov_b64 v[106:107], 0\n" \
    "v_mov_b64 v[108:109], 0\n" \
    "v_mov_b64 v[110:111], 0\n" \
    "v_mov_b32_e32 v112, 0\n" \
    "v_mov_b32_e32 v113, 2\n" \
    "ds_read_b32 v114, v112 offset:%[tg]\n" \
    "ds_read_b128 v[124:127], %[xa] offset:0\n" \
    "ds_read_b128 v[128:131], %[xa] offset:3072\n" \
    "ds_read_b128 v[132:135], %[xa] offset:6144\n" \
    "s_waitcnt lgkmcnt(3)\n" \
    "v_cmp_gt_u32_e32 vcc, 2, v114\n" \
    "s_cbranch_vccz Lfar_%=\n" \
    "s_mov_b32 s92, 0x100000\n" \
    "Lfaw_%=:\n" \
    "ds_read_b32 v114, v112 offset:%[tg]\n" \
    "s_waitcnt lgkmcnt(0)\n" \
    "v_cmp_gt_u32_e32 vcc, 2, v114\n" \
    "s_cbranch_vccz Lfaq_%=\n" \
    "s_sleep 1\n" \
    "s_sub_u32 s92, s92, 1\n" \
    "s_cbranch_scc0 Lfaw_%=\n" \
    "Lfaq_%=:\n" \
    "ds_read_b128 v[124:127], %[xa] offset:0\n" \
    "ds_read_b128 v[128:131], %[xa] offset:3072\n" \
    "ds_read_b128 v[132:135], %[xa] offset:6144\n" \
    "Lfar_%=:\n" \
    "s_waitcnt lgkmcnt(2)\n" \
    "v_smfmac_f32_16x16x64_bf16 v[96:99], v[124:127], v[0:7], %[idx]\n" \
    "v_smfmac_f32_16x16x64_bf16 v[100:103], v[124:127], v[8:15], %[idx]\n" \
    "v_smfmac_f32_16x16x64_bf16 v[104:107], v[124:127], v[16:23], %[idx]\n" \
    "v_smfmac_f32_16x16x64_bf16 v[108:111], v[124:127], v[24:31], %[idx]\n" \
    "s_waitcnt lgkmcnt(1)\n" \
    "v_smfmac_f32_16x16x64_bf16 v[96:99], v[128:131], v[32:39], %[idx]\n" \
    "v_smfmac_f32_16x16x64_bf16 v[100:103], v[128:131], v[40:47], %[idx]\n" \
    "v_smfmac_f32_16x16x64_bf16 v[104:107], v[128:131], v[48:55], %[idx]\n" \
    "v_smfmac_f32_16x16x64_bf16 v[108:111], v[128:131], v[56:63], %[idx]\n" \
    "s_waitcnt lgkmcnt(0)\n" \
    "v_smfmac_f32_16x16x64_bf16 v[96:99], v[132:135], v[64:71], %[idx]\n" \
    "v_smfmac_f32_16x16x64_bf16 v[100:103], v[132:135], v[72:79], %[idx]\n" \
    "v_smfmac_f32_16x16x64_bf16 v[104:107], v[132:135], v[80:87], %[idx]\n" \
    "v_smfmac_f32_16x16x64_bf16 v[108:111], v[132:135], v[88:95], %[idx]\n" \
    "s_cmp_gt_u32 %[wv], 3\n" \
    "s_nop 3\n" \
    "ds_write_b128 %[rdw], v[96:99] offset:0\n" \
    "ds_write_b128 %[rdw], v[100:103] offset:1024\n" \
    "ds_write_b128 %[rdw], v[104:107] offset:2048\n" \
    "ds_write_b128 %[rdw], v[108:111] offset:3072\n" \
    "ds_write_b32 %[fo], v113\n" \
    "s_cbranch_scc1 Lfe_%=\n" \
    "v_mov_b32_e32 v115, 3\n" \
    "ds_read_b32 v114, %[fa] offset:0\n" \
    "ds_read_b64 v[48:49], %[rdr] offset:0\n" \
    "ds_read_b64 v[50:51], %[rdr] offset:4096\n" \
    "ds_read_b64 v[52:53], %[rdr] offset:8192\n" \
    "ds_read_b64 v[54:55], %[rdr] offset:12288\n" \
    "ds_read_b64 v[56:57], %[rdr] offset:16384\n" \
    "ds_read_b64 v[58:59], %[rdr] offset:20480\n" \
    "ds_read_b64 v[60:61], %[rdr] offset:128\n" \
    "ds_read_b64 v[62:63], %[rdr] offset:4224\n" \
    "ds_read_b64 v[64:65], %[rdr] offset:8320\n" \
    "ds_read_b64 v[66:67], %[rdr] offset:12416\n" \
    "ds_read_b64 v[68:69], %[rdr] offset:16512\n" \
    "ds_read_b64 v[70:71], %[rdr] offset:20608\n" \
    "s_waitcnt lgkmcnt(12)\n" \
    "v_cmp_gt_u32_e32 vcc, 2, v114\n" \
    "s_cbranch_vccz Lfbr_%=\n" \
    "s_mov_b32 s92, 0x100000\n" \
    "Lfbw_%=:\n" \
    "ds_read_b32 v114, %[fa] offset:0\n" \
    "s_waitcnt lgkmcnt(0)\n" \
    "v_cmp_gt_u32_e32 vcc, 2, v114\n" \
    "s_cbranch_vccz Lfbq_%=\n" \
    "s_sleep 1\n" \
    "s_sub_u32 s92, s92, 1\n" \
    "s_cbranch_scc0 Lfbw_%=\n" \
    "Lfbq_%=:\n" \
    "ds_read_b64 v[48:49], %[rdr] offset:0\n" \
    "ds_read_b64 v[50:51], %[rdr] offset:4096\n" \
    "ds_read_b64 v[52:53], %[rdr] offset:8192\n" \
    "ds_read_b64 v[54:55], %[rdr] offset:12288\n" \
    "ds_read_b64 v[56:57], %[rdr] offset:16384\n" \
    "ds_read_b64 v[58:59], %[rdr] offset:20480\n" \
    "ds_read_b64 v[60:61], %[rdr] offset:128\n" \
    "ds_read_b64 v[62:63], %[rdr] offset:4224\n" \
    "ds_read_b64 v[64:65], %[rdr] offset:8320\n" \
    "ds_read_b64 v[66:67], %[rdr] offset:12416\n" \
    "ds_read_b64 v[68:69], %[rdr] offset:16512\n" \
    "ds_read_b64 v[70:71], %[rdr] offset:20608\n" \
    "Lfbr_%=:\n" \
    "s_waitcnt lgkmcnt(10)\n" \
    "v_pk_add_f32 v[48:49], v[48:49], v[50:51]\n" \
    "s_waitcnt lgkmcnt(8)\n" \
    "v_pk_add_f32 v[52:53], v[52:53], v[54:55]\n" \
    "s_waitcnt lgkmcnt(6)\n" \
    "v_pk_add_f32 v[56:57], v[56:57], v[58:59]\n" \
    "v_pk_add_f32 v[48:49], v[48:49], v[52:53]\n" \
    "s_waitcnt lgkmcnt(4)\n" \
    "v_pk_add_f32 v[60:61], v[60:61], v[62:63]\n" \
    "v_pk_add_f32 v[48:49], v[48:49], v[56:57]\n" \
    "s_waitcnt lgkmcnt(2)\n" \
    "v_pk_add_f32 v[64:65], v[64:65], v[66:67]\n" \
    "v_mul_f32_e32 v50, 0xbfb8aa3b, v48\n" \
    "v_mul_f32_e32 v51, 0xbfb8aa3b, v49\n" \
    "v_exp_f32_e32 v50, v50\n" \
    "v_exp_f32_e32 v51, v51\n" \
    "s_waitcnt lgkmcnt(0)\n" \
    "v_pk_add_f32 v[68:69], v[68:69], v[70:71]\n" \
    "v_add_f32_e32 v50, 1.0, v50\n" \
    "v_add_f32_e32 v51, 1.0, v51\n" \
    "v_rcp_f32_e32 v50, v50\n" \
    "v_rcp_f32_e32 v51, v51\n" \
    "v_pk_add_f32 v[60:61], v[60:61], v[64:65]\n" \
    "v_pk_add_f32 v[60:61], v[60:61], v[68:69]\n" \
    "v_pk_mul_f32 v[48:49], v[48:49], v[50:51]\n" \
    "v_pk_mul_f32 v[48:49], v[48:49], v[60:61]\n" \
    "v_cvt_pk_bf16_f32 v48, v48, v49\n" \
    "ds_write_b16 %[hbw], v48\n" \
    "ds_write_b16_d16_hi %[hbw], v48 offset:144\n" \
    "ds_write_b32 %[fo], v115\n" \
    "Lfe_%=:\n" \
    "s_waitcnt lgkmcnt(0)\n"
#define FFN_CLOB "v96", "v97", "v98", "v99", "v100", "v101", "v102", "v103", "v104", "v105", "v106", "v107", "v108", "v109", "v110", "v111", "v112", "v113", "v114", "v115", "v124", "v125", "v126", "v127", "v128", "v129", "v130", "v131", "v132", "v133", "v134", "v135"
#define SEL_ASM \
    "s_mov_b32 %[ps], 0\n" \
    "s_branch Lsn_%=\n" \
    "Lsg_%=:\n" \
    "s_mov_b32 %[ps], 4096\n" \
    "Lsw_%=:\n" \
    "s_memrealtime s[92:93]\n" \
    "s_waitcnt lgkmcnt(0)\n" \
    "s_sub_u32 s92, s92, %[at]\n" \
    "s_cmp_lt_i32 s92, 0\n" \
    "s_cbranch_scc0 Lsr_%=\n" \
    "s_sleep 1\n" \
    "s_sub_u32 %[ps], %[ps], 1\n" \
    "s_cbranch_scc0 Lsw_%=\n" \
    "Lsr_%=:\n" \
    "s_mov_b32 %[ps], 0x20000\n" \
    "Lsp_%=:\n" \
    "global_load_dwordx4 v[0:3], %[co], %[ws] sc1\n" \
    "global_load_dwordx4 v[4:7], %[co2], %[ws] sc1\n" \
    "s_mov_b64 exec, -1\n" \
    "s_waitcnt vmcnt(1)\n" \
    "v_bitop3_b32 v10, v1, v3, %[tag] bitop3:0x7e\n" \
    "s_waitcnt vmcnt(0)\n" \
    "v_bitop3_b32 v11, v5, v7, %[tag] bitop3:0x7e\n" \
    "v_or_b32_e32 v10, v10, v11\n" \
    "v_cmp_ne_u32_e32 vcc, 0, v10\n" \
    "s_cbranch_vccz Lsn_%=\n" \
    "s_sleep %[gap]\n" \
    "s_mov_b64 exec, vcc\n" \
    "s_sub_u32 %[ps], %[ps], 1\n" \
    "s_cbranch_scc0 Lsp_%=\n" \
    "s_mov_b64 exec, -1\n" \
    "s_mov_b32 %[ps], 1\n" \
    "Lsn_%=:\n" \
    "v_and_or_b32 v15, v0, %[mk], %[f0]\n" \
    "v_and_or_b32 v17, v4, %[mk], %[fi]\n" \
    "v_and_or_b32 v16, v2, %[mk], %[f0]\n" \
    "v_and_or_b32 v18, v6, %[mk], %[fi]\n" \
    "v_max_u32_e32 v8, v15, v17\n" \
    "v_min_u32_e32 v9, v15, v17\n" \
    "v_max3_u32 v9, v9, v16, v18\n" \
    "v_mov_b32_dpp v10, v8 quad_perm:[1,0,3,2] row_mask:0xf bank_mask:0xf\n" \
    "v_med3_u32 v8, v8, v10, %[c1]\n" \
    "v_mov_b32_dpp v11, v9 quad_perm:[1,0,3,2] row_mask:0xf bank_mask:0xf\n" \
    "v_med3_u32 v9, v9, v11, %[c1]\n" \
    "v_mov_b32_dpp v10, v8 quad_perm:[3,2,1,0] row_mask:0xf bank_mask:0xf\n" \
    "v_med3_u32 v8, v8, v10, %[c2]\n" \
    "v_mov_b32_dpp v11, v9 quad_perm:[3,2,1,0] row_mask:0xf bank_mask:0xf\n" \
    "v_med3_u32 v9, v9, v11, %[c2]\n" \
    "v_mov_b32_dpp v10, v8 quad_perm:[1,0,3,2] row_mask:0xf bank_mask:0xf\n" \
    "v_med3_u32 v8, v8, v10, %[c1]\n" \
    "v_mov_b32_dpp v11, v9 quad_perm:[1,0,3,2] row_mask:0xf bank_mask:0xf\n" \
    "v_med3_u32 v9, v9, v11, %[c1]\n" \
    "v_mov_b32_dpp v10, v8 row_half_mirror row_mask:0xf bank_mask:0xf\n" \
    "v_med3_u32 v8, v8, v10, %[c4]\n" \
    "v_max_u32_dpp v9, v9, v9 row_half_mirror row_mask:0xf bank_mask:0xf\n" \
    "v_perm_b32 v19, v0, v2, %[pm]\n" \
    "v_mov_b32_dpp v10, v8 quad_perm:[2,3,0,1] row_mask:0xf bank_mask:0xf\n" \
    "v_med3_u32 v8, v8, v10, %[c2]\n" \
    "v_mov_b32_dpp v11, v9 quad_perm:[2,3,0,1] row_mask:0xf bank_mask:0xf\n" \
    "v_med3_u32 v9, v9, v11, %[c2]\n" \
    "v_mov_b32_dpp v10, v8 quad_perm:[1,0,3,2] row_mask:0xf bank_mask:0xf\n" \
    "v_med3_u32 v8, v8, v10, %[c1]\n" \
    "v_mov_b32_dpp v11, v9 quad_perm:[1,0,3,2] row_mask:0xf bank_mask:0xf\n" \
    "v_med3_u32 v9, v9, v11, %[c1]\n" \
    "v_max_u32_dpp v8, v8, v8 row_mirror row_mask:0xf bank_mask:0xf\n" \
    "s_nop 0\n" \
    "v_max_u32_dpp v9, v9, v9 row_mirror row_mask:0xf bank_mask:0xf\n" \
    "v_max_u32_dpp v10, v8, v8 row_shl:4 row_mask:0xf bank_mask:0x5\n" \
    "v_min_u32_dpp v10, v8, v8 row_shr:4 row_mask:0xf bank_mask:0xa\n" \
    "v_mov_b32_dpp v11, v9 quad_perm:[2,3,0,1] row_mask:0xf bank_mask:0xf\n" \
    "v_med3_u32 v9, v9, v11, %[c2]\n" \
    "v_mov_b32_dpp v8, v10 quad_perm:[2,3,0,1] row_mask:0xf bank_mask:0xf\n" \
    "v_med3_u32 v10, v10, v8, %[c2]\n" \
    "v_mov_b32_dpp v11, v9 quad_perm:[1,0,3,2] row_mask:0xf bank_mask:0xf\n" \
    "v_med3_u32 v9, v9, v11, %[c1]\n" \
    "v_mov_b32_dpp v8, v10 quad_perm:[1,0,3,2] row_mask:0xf bank_mask:0xf\n" \
    "v_med3_u32 v10, v10, v8, %[c1]\n" \
    "v_mov_b32_e32 v13, v9\n" \
    "v_perm_b32 v20, v4, v6, %[pm]\n" \
    "v_mov_b32_e32 v12, v10\n" \
    "v_permlane16_swap_b32 v9, v13\n" \
    "v_min_u32_e32 v15, v15, v18\n" \
    "v_permlane16_swap_b32 v10, v12\n" \
    "v_max_u32_dpp v9, v13, v9 quad_perm:[3,2,1,0] row_mask:0xf bank_mask:0xf\n" \
    "v_min_u32_e32 v16, v16, v17\n" \
    "v_max_u32_dpp v10, v12, v10 row_half_mirror row_mask:0xf bank_mask:0xf\n" \
    "v_mov_b32_dpp v11, v9 quad_perm:[2,3,0,1] row_mask:0xf bank_mask:0xf\n" \
    "v_med3_u32 v9, v9, v11, %[c2]\n" \
    "v_mov_b32_dpp v8, v10 row_ror:4 row_mask:0xf bank_mask:0xf\n" \
    "v_med3_u32 v10, v10, v8, %[c4]\n" \
    "v_mov_b32_dpp v11, v9 quad_perm:[1,0,3,2] row_mask:0xf bank_mask:0xf\n" \
    "v_med3_u32 v9, v9, v11, %[c1]\n" \
    "v_mov_b32_dpp v8, v10 quad_perm:[2,3,0,1] row_mask:0xf bank_mask:0xf\n" \
    "v_med3_u32 v10, v10, v8, %[c2]\n" \
    "v_mov_b32_e32 v13, v9\n" \
    "v_max3_u32 v19, v19, v20, v15\n" \
    "v_mov_b32_dpp v8, v10 quad_perm:[1,0,3,2] row_mask:0xf bank_mask:0xf\n" \
    "v_med3_u32 v10, v10, v8, %[c1]\n" \
    "v_permlane32_swap_b32 v9, v13\n" \
    "v_max_u32_e32 %[tc], v19, v16\n" \
    "v_mov_b32_e32 v12, v10\n" \
    "v_max_u32_dpp v9, v13, v9 quad_perm:[3,2,1,0] row_mask:0xf bank_mask:0xf\n" \
    "s_nop 0\n" \
    "v_permlane32_swap_b32 v10, v12\n" \
    "v_mov_b32_dpp v11, v9 quad_perm:[2,3,0,1] row_mask:0xf bank_mask:0xf\n" \
    "v_med3_u32 v9, v9, v11, %[c2]\n" \
    "v_max_u32_dpp v10, v12, v10 row_half_mirror row_mask:0xf bank_mask:0xf\n" \
    "s_nop 0\n" \
    "v_mov_b32_dpp v11, v9 quad_perm:[1,0,3,2] row_mask:0xf bank_mask:0xf\n" \
    "v_med3_u32 v9, v9, v11, %[c1]\n" \
    "v_mov_b32_dpp v8, v10 row_ror:4 row_mask:0xf bank_mask:0xf\n" \
    "v_med3_u32 v10, v10, v8, %[c4]\n" \
    "v_and_b32_e32 v9, v9, %[c4]\n" \
    "s_nop 0\n" \
    "v_mov_b32_dpp v8, v10 quad_perm:[2,3,0,1] row_mask:0xf bank_mask:0xf\n" \
    "v_med3_u32 v10, v10, v8, %[c2]\n" \
    "s_nop 1\n" \
    "v_mov_b32_dpp v8, v10 quad_perm:[1,0,3,2] row_mask:0xf bank_mask:0xf\n" \
    "v_med3_u32 v10, v10, v8, %[c1]\n" \
    "v_max_u32_dpp v10, v9, v10 row_half_mirror row_mask:0xf bank_mask:0xf\n" \
    "s_nop 1\n" \
    "v_mov_b32_dpp v8, v10 row_ror:4 row_mask:0xf bank_mask:0xf\n" \
    "v_med3_u32 v10, v10, v8, %[c4]\n" \
    "s_nop 1\n" \
    "v_mov_b32_dpp v8, v10 quad_perm:[2,3,0,1] row_mask:0xf bank_mask:0xf\n" \
    "v_med3_u32 v10, v10, v8, %[c2]\n" \
    "s_nop 1\n" \
    "v_mov_b32_dpp v8, v10 quad_perm:[1,0,3,2] row_mask:0xf bank_mask:0xf\n" \
    "v_med3_u32 v10, v10, v8, %[c1]\n" \
    "s_cmp_eq_u32 %[ps], 0\n" \
    "s_cbranch_scc1 Lsg_%=\n" \
    "v_mov_b32_dpp v14, v10 row_newbcast:0 row_mask:0x1 bank_mask:0x3\n" \
    "v_mov_b32_dpp v14, v10 row_newbcast:1 row_mask:0x1 bank_mask:0xc\n" \
    "v_mov_b32_dpp v14, v10 row_newbcast:2 row_mask:0x2 bank_mask:0x3\n" \
    "v_mov_b32_dpp v14, v10 row_newbcast:3 row_mask:0x2 bank_mask:0xc\n" \
    "v_mov_b32_dpp v14, v10 row_newbcast:4 row_mask:0x4 bank_mask:0x3\n" \
    "v_mov_b32_dpp v14, v10 row_newbcast:5 row_mask:0x4 bank_mask:0xc\n" \
    "v_mov_b32_dpp v14, v10 row_newbcast:6 row_mask:0x8 bank_mask:0x3\n" \
    "v_mov_b32_dpp v14, v10 row_newbcast:7 row_mask:0x8 bank_mask:0xc\n" \
    "s_mov_b32 m0, %[xs]\n" \
    "v_and_b32_e32 v14, 0x7ff, v14\n" \
    "v_mul_u32_u24_e32 v14, 1152, v14\n" \
    "v_sub_u32_e32 v14, %[xk], v14\n" \
    "global_load_lds_dwordx4 v14, %[x] offset:0\n" \
    "s_addk_i32 m0, 896\n" \
    "s_nop 0\n" \
    "global_load_lds_dwordx4 v14, %[x] offset:128\n" \
    "s_addk_i32 m0, 896\n" \
    "s_nop 0\n" \
    "global_load_lds_dwordx4 v14, %[x] offset:256\n" \
    "s_addk_i32 m0, 896\n" \
    "s_nop 0\n" \
    "global_load_lds_dwordx4 v14, %[x] offset:384\n" \
    "s_addk_i32 m0, 896\n" \
    "s_nop 0\n" \
    "global_load_lds_dwordx4 v14, %[x] offset:512\n" \
    "s_addk_i32 m0, 896\n" \
    "s_nop 0\n" \
    "global_load_lds_dwordx4 v14, %[x] offset:640\n" \
    "s_addk_i32 m0, 896\n" \
    "s_nop 0\n" \
    "global_load_lds_dwordx4 v14, %[x] offset:768\n" \
    "s_addk_i32 m0, 896\n" \
    "s_nop 0\n" \
    "global_load_lds_dwordx4 v14, %[x] offset:896\n" \
    "s_addk_i32 m0, 896\n" \
    "s_nop 0\n" \
    "global_load_lds_dwordx4 v14, %[x] offset:1024\n" \
    "v_mov_b32_e32 %[g8], v10\n"
#define SEL_CLOB "v0", "v1", "v2", "v3", "v4", "v5", "v6", "v7", "v8", "v9", "v10", "v11", "v12", "v13", "v14", "v15", "v16", "v17", "v18", "v19", "v20"
#define NET_ASM \
    "s_nop 1\n" \
    "v_min_u32_e32 v15, v0, v4\n" \
    "v_min_u32_e32 v16, v0, v5\n" \
    "v_min_u32_e32 v17, v1, v4\n" \
    "v_max_u32_e32 v18, v2, v6\n" \
    "v_max_u32_e32 v28, v0, v4\n" \
    "v_max3_u32 v29, v15, v1, v5\n" \
    "v_max3_u32 v30, v16, v17, v18\n" \
    "v_min_u32_e32 v15, v20, v24\n" \
    "v_min_u32_e32 v16, v20, v25\n" \
    "v_min_u32_e32 v17, v21, v24\n" \
    "v_max_u32_e32 v18, v22, v26\n" \
    "v_max_u32_e32 v0, v20, v24\n" \
    "v_max3_u32 v1, v15, v21, v25\n" \
    "v_max3_u32 v2, v16, v17, v18\n" \
    "v_min_u32_e32 v15, v28, v0\n" \
    "v_min_u32_e32 v16, v28, v1\n" \
    "v_min_u32_e32 v17, v29, v0\n" \
    "v_max_u32_e32 v18, v30, v2\n" \
    "v_max_u32_e32 v8, v28, v0\n" \
    "v_max3_u32 v9, v15, v29, v1\n" \
    "v_max3_u32 %[tc], v16, v17, v18\n" \
    "v_mov_b32_dpp v10, v8 quad_perm:[1,0,3,2] row_mask:0xf bank_mask:0xf\n" \
    "v_med3_u32 v8, v8, v10, %[c1]\n" \
    "v_mov_b32_dpp v11, v9 quad_perm:[1,0,3,2] row_mask:0xf bank_mask:0xf\n" \
    "v_med3_u32 v9, v9, v11, %[c1]\n" \
    "v_mov_b32_dpp v10, v8 quad_perm:[3,2,1,0] row_mask:0xf bank_mask:0xf\n" \
    "v_med3_u32 v8, v8, v10, %[c2]\n" \
    "v_mov_b32_dpp v11, v9 quad_perm:[3,2,1,0] row_mask:0xf bank_mask:0xf\n" \
    "v_med3_u32 v9, v9, v11, %[c2]\n" \
    "v_mov_b32_dpp v10, v8 quad_perm:[1,0,3,2] row_mask:0xf bank_mask:0xf\n" \
    "v_med3_u32 v8, v8, v10, %[c1]\n" \
    "v_mov_b32_dpp v11, v9 quad_perm:[1,0,3,2] row_mask:0xf bank_mask:0xf\n" \
    "v_med3_u32 v9, v9, v11, %[c1]\n" \
    "v_mov_b32_dpp v10, v8 row_half_mirror row_mask:0xf bank_mask:0xf\n" \
    "v_med3_u32 v8, v8, v10, %[c4]\n" \
    "v_max_u32_dpp v9, v9, v9 row_half_mirror row_mask:0xf bank_mask:0xf\n" \
    "s_nop 0\n" \
    "v_mov_b32_dpp v10, v8 quad_perm:[2,3,0,1] row_mask:0xf bank_mask:0xf\n" \
    "v_med3_u32 v8, v8, v10, %[c2]\n" \
    "v_mov_b32_dpp v11, v9 quad_perm:[2,3,0,1] row_mask:0xf bank_mask:0xf\n" \
    "v_med3_u32 v9, v9, v11, %[c2]\n" \
    "v_mov_b32_dpp v10, v8 quad_perm:[1,0,3,2] row_mask:0xf bank_mask:0xf\n" \
    "v_med3_u32 v8, v8, v10, %[c1]\n" \
    "v_mov_b32_dpp v11, v9 quad_perm:[1,0,3,2] row_mask:0xf bank_mask:0xf\n" \
    "v_med3_u32 v9, v9, v11, %[c1]\n" \
    "v_max_u32_dpp v8, v8, v8 row_mirror row_mask:0xf bank_mask:0xf\n" \
    "s_nop 0\n" \
    "v_max_u32_dpp v9, v9, v9 row_mirror row_mask:0xf bank_mask:0xf\n" \
    "v_max_u32_dpp v10, v8, v8 row_shl:4 row_mask:0xf bank_mask:0x5\n" \
    "v_min_u32_dpp v10, v8, v8 row_shr:4 row_mask:0xf bank_mask:0xa\n" \
    "v_mov_b32_dpp v11, v9 quad_perm:[2,3,0,1] row_mask:0xf bank_mask:0xf\n" \
    "v_med3_u32 v9, v9, v11, %[c2]\n" \
    "v_mov_b32_dpp v8, v10 quad_perm:[2,3,0,1] row_mask:0xf bank_mask:0xf\n" \
    "v_med3_u32 v10, v10, v8, %[c2]\n" \
    "v_mov_b32_dpp v11, v9 quad_perm:[1,0,3,2] row_mask:0xf bank_mask:0xf\n" \
    "v_med3_u32 v9, v9, v11, %[c1]\n" \
    "v_mov_b32_dpp v8, v10 quad_perm:[1,0,3,2] row_mask:0xf bank_mask:0xf\n" \
    "v_med3_u32 v10, v10, v8, %[c1]\n" \
    "v_mov_b32_e32 v13, v9\n" \
    "v_mov_b32_e32 v12, v10\n" \
    "s_nop 0\n" \
    "v_permlane16_swap_b32 v9, v13\n" \
    "v_permlane16_swap_b32 v10, v12\n" \
    "s_nop 0\n" \
    "v_max_u32_dpp v9, v13, v9 quad_perm:[3,2,1,0] row_mask:0xf bank_mask:0xf\n" \
    "v_max_u32_dpp v10, v12, v10 row_half_mirror row_mask:0xf bank_mask:0xf\n" \
    "s_nop 0\n" \
    "v_mov_b32_dpp v11, v9 quad_perm:[2,3,0,1] row_mask:0xf bank_mask:0xf\n" \
    "v_med3_u32 v9, v9, v11, %[c2]\n" \
    "v_mov_b32_dpp v8, v10 row_ror:4 row_mask:0xf bank_mask:0xf\n" \
    "v_med3_u32 v10, v10, v8, %[c4]\n" \
    "v_mov_b32_dpp v11, v9 quad_perm:[1,0,3,2] row_mask:0xf bank_mask:0xf\n" \
    "v_med3_u32 v9, v9, v11, %[c1]\n" \
    "v_mov_b32_dpp v8, v10 quad_perm:[2,3,0,1] row_mask:0xf bank_mask:0xf\n" \
    "v_med3_u32 v10, v10, v8, %[c2]\n" \
    "v_mov_b32_e32 v13, v9\n" \
    "s_nop 0\n" \
    "v_mov_b32_dpp v8, v10 quad_perm:[1,0,3,2] row_mask:0xf bank_mask:0xf\n" \
    "v_med3_u32 v10, v10, v8, %[c1]\n" \
    "v_permlane32_swap_b32 v9, v13\n" \
    "v_mov_b32_e32 v12, v10\n" \
    "s_nop 0\n" \
    "v_max_u32_dpp v9, v13, v9 quad_perm:[3,2,1,0] row_mask:0xf bank_mask:0xf\n" \
    "v_permlane32_swap_b32 v10, v12\n" \
    "s_nop 0\n" \
    "v_mov_b32_dpp v11, v9 quad_perm:[2,3,0,1] row_mask:0xf bank_mask:0xf\n" \
    "v_med3_u32 v9, v9, v11, %[c2]\n" \
    "v_max_u32_dpp v10, v12, v10 row_half_mirror row_mask:0xf bank_mask:0xf\n" \
    "s_nop 0\n" \
    "v_mov_b32_dpp v11, v9 quad_perm:[1,0,3,2] row_mask:0xf bank_mask:0xf\n" \
    "v_med3_u32 v9, v9, v11, %[c1]\n" \
    "v_mov_b32_dpp v8, v10 row_ror:4 row_mask:0xf bank_mask:0xf\n" \
    "v_med3_u32 v10, v10, v8, %[c4]\n" \
    "v_and_b32_e32 v9, v9, %[c4]\n" \
    "s_nop 0\n" \
    "v_mov_b32_dpp v8, v10 quad_perm:[2,3,0,1] row_mask:0xf bank_mask:0xf\n" \
    "v_med3_u32 v10, v10, v8, %[c2]\n" \
    "s_nop 1\n" \
    "v_mov_b32_dpp v8, v10 quad_perm:[1,0,3,2] row_mask:0xf bank_mask:0xf\n" \
    "v_med3_u32 v10, v10, v8, %[c1]\n" \
    "v_max_u32_dpp v10, v9, v10 row_half_mirror row_mask:0xf bank_mask:0xf\n" \
    "s_nop 1\n" \
    "v_mov_b32_dpp v8, v10 row_ror:4 row_mask:0xf bank_mask:0xf\n" \
    "v_med3_u32 v10, v10, v8, %[c4]\n" \
    "s_nop 1\n" \
    "v_mov_b32_dpp v8, v10 quad_perm:[2,3,0,1] row_mask:0xf bank_mask:0xf\n" \
    "v_med3_u32 v10, v10, v8, %[c2]\n" \
    "s_nop 1\n" \
    "v_mov_b32_dpp v8, v10 quad_perm:[1,0,3,2] row_mask:0xf bank_mask:0xf\n" \
    "v_med3_u32 v10, v10, v8, %[c1]\n" \
    "v_mov_b32_e32 %[g8], v10\n" \
    "s_nop 0\n"
#define NET_CLOB "v8", "v9", "v10", "v11", "v12", "v13", "v15", "v16", "v17", "v18", "v28", "v29", "v30"
#define DOWN_ASM \
    "v_mov_b64 v[128:129], 0\n" \
    "v_mov_b64 v[130:131], 0\n" \
    "v_mov_b64 v[132:133], 0\n" \
    "v_mov_b64 v[134:135], 0\n" \
    "v_mov_b64 v[136:137], 0\n" \
    "v_mov_b64 v[138:139], 0\n" \
    "v_mov_b64 v[140:141], 0\n" \
    "v_mov_b64 v[142:143], 0\n" \
    "v_add_u32_e32 v152, 18432, %[src]\n" \
    "v_add_u32_e32 v153, 36864, %[src]\n" \
    "v_add_u32_e32 v154, 55296, %[src]\n" \
    "v_add_u32_e32 v157, 18432, %[po]\n" \
    "v_add_u32_e32 v158, 36864, %[po]\n" \
    "v_add_u32_e32 v159, 55296, %[po]\n" \
    "s_mov_b32 s93, 0x05040100\n" \
    "s_mov_b32 s94, 0x07060302\n" \
    "s_mov_b32 s92, 0x100000\n" \
    "Ldl0_%=:\n" \
    "ds_read_b32 v155, %[fs] offset:0\n" \
    "ds_read_b32 v156, v128 offset:%[tg]\n" \
    "s_waitcnt lgkmcnt(0)\n" \
    "v_cmp_gt_u32_e32 vcc, 1, v155\n" \
    "s_cbranch_vccz Lde0_%=\n" \
    "s_sleep 2\n" \
    "s_sub_u32 s92, s92, 1\n" \
    "s_cbranch_scc0 Ldl0_%=\n" \
    "Lde0_%=:\n" \
    "global_load_dwordx4 v[64:67], %[src], %[wd] offset:0\n" \
    "global_load_dwordx4 v[68:71], %[src], %[wd] offset:1152\n" \
    "global_load_dwordx4 v[72:75], %[src], %[wd] offset:2304\n" \
    "global_load_dwordx4 v[76:79], %[src], %[wd] offset:3456\n" \
    "global_load_dwordx4 v[80:83], v152, %[wd] offset:0\n" \
    "global_load_dwordx4 v[84:87], v152, %[wd] offset:1152\n" \
    "s_waitcnt vmcnt(5)\n" \
    "global_load_dwordx4 v[88:91], v152, %[wd] offset:2304\n" \
    "s_waitcnt vmcnt(5)\n" \
    "global_load_dwordx4 v[92:95], v152, %[wd] offset:3456\n" \
    "v_perm_b32 v0, v68, v64, s93\n" \
    "v_perm_b32 v8, v68, v64, s94\n" \
    "v_perm_b32 v16, v69, v65, s93\n" \
    "v_perm_b32 v24, v69, v65, s94\n" \
    "v_perm_b32 v1, v70, v66, s93\n" \
    "v_perm_b32 v9, v70, v66, s94\n" \
    "v_perm_b32 v17, v71, v67, s93\n" \
    "v_perm_b32 v25, v71, v67, s94\n" \
    "s_waitcnt vmcnt(5)\n" \
    "global_load_dwordx4 v[96:99], v153, %[wd] offset:0\n" \
    "s_waitcnt vmcnt(5)\n" \
    "global_load_dwordx4 v[100:103], v153, %[wd] offset:1152\n" \
    "v_perm_b32 v2, v76, v72, s93\n" \
    "v_perm_b32 v10, v76, v72, s94\n" \
    "v_perm_b32 v18, v77, v73, s93\n" \
    "v_perm_b32 v26, v77, v73, s94\n" \
    "v_perm_b32 v3, v78, v74, s93\n" \
    "v_perm_b32 v11, v78, v74, s94\n" \
    "v_perm_b32 v19, v79, v75, s93\n" \
    "v_perm_b32 v27, v79, v75, s94\n" \
    "s_waitcnt vmcnt(5)\n" \
    "global_load_dwordx4 v[104:107], v153, %[wd] offset:2304\n" \
    "s_waitcnt vmcnt(5)\n" \
    "global_load_dwordx4 v[108:111], v153, %[wd] offset:3456\n" \
    "v_perm_b32 v4, v84, v80, s93\n" \
    "v_perm_b32 v12, v84, v80, s94\n" \
    "v_perm_b32 v20, v85, v81, s93\n" \
    "v_perm_b32 v28, v85, v81, s94\n" \
    "v_perm_b32 v5, v86, v82, s93\n" \
    "v_perm_b32 v13, v86, v82, s94\n" \
    "v_perm_b32 v21, v87, v83, s93\n" \
    "v_perm_b32 v29, v87, v83, s94\n" \
    "v_mov_b32_e32 v65, v156\n" \
    "v_mov_b32_e32 v67, v156\n" \
    "v_mov_b32_e32 v69, v156\n" \
    "v_mov_b32_e32 v71, v156\n" \
    "v_mov_b32_e32 v73, v156\n" \
    "v_mov_b32_e32 v75, v156\n" \
    "v_mov_b32_e32 v77, v156\n" \
    "v_mov_b32_e32 v79, v156\n" \
    "s_waitcnt vmcnt(5)\n" \
    "global_load_dwordx4 v[112:115], v154, %[wd] offset:0\n" \
    "s_waitcnt vmcnt(5)\n" \
    "global_load_dwordx4 v[116:119], v154, %[wd] offset:1152\n" \
    "v_perm_b32 v6, v92, v88, s93\n" \
    "v_perm_b32 v14, v92, v88, s94\n" \
    "v_perm_b32 v22, v93, v89, s93\n" \
    "v_perm_b32 v30, v93, v89, s94\n" \
    "v_perm_b32 v7, v94, v90, s93\n" \
    "v_perm_b32 v15, v94, v90, s94\n" \
    "v_perm_b32 v23, v95, v91, s93\n" \
    "v_perm_b32 v31, v95, v91, s94\n" \
    "s_waitcnt vmcnt(5)\n" \
    "global_load_dwordx4 v[120:123], v154, %[wd] offset:2304\n" \
    "s_waitcnt vmcnt(5)\n" \
    "global_load_dwordx4 v[124:127], v154, %[wd] offset:3456\n" \
    "v_perm_b32 v32, v100, v96, s93\n" \
    "v_perm_b32 v40, v100, v96, s94\n" \
    "v_perm_b32 v48, v101, v97, s93\n" \
    "v_perm_b32 v56, v101, v97, s94\n" \
    "v_perm_b32 v33, v102, v98, s93\n" \
    "v_perm_b32 v41, v102, v98, s94\n" \
    "v_perm_b32 v49, v103, v99, s93\n" \
    "v_perm_b32 v57, v103, v99, s94\n" \
    "s_waitcnt vmcnt(5)\n" \
    "s_waitcnt vmcnt(4)\n" \
    "v_perm_b32 v34, v108, v104, s93\n" \
    "v_perm_b32 v42, v108, v104, s94\n" \
    "v_perm_b32 v50, v109, v105, s93\n" \
    "v_perm_b32 v58, v109, v105, s94\n" \
    "v_perm_b32 v35, v110, v106, s93\n" \
    "v_perm_b32 v43, v110, v106, s94\n" \
    "v_perm_b32 v51, v111, v107, s93\n" \
    "v_perm_b32 v59, v111, v107, s94\n" \
    "s_waitcnt vmcnt(3)\n" \
    "s_waitcnt vmcnt(2)\n" \
    "v_perm_b32 v36, v116, v112, s93\n" \
    "v_perm_b32 v44, v116, v112, s94\n" \
    "v_perm_b32 v52, v117, v113, s93\n" \
    "v_perm_b32 v60, v117, v113, s94\n" \
    "v_perm_b32 v37, v118, v114, s93\n" \
    "v_perm_b32 v45, v118, v114, s94\n" \
    "v_perm_b32 v53, v119, v115, s93\n" \
    "v_perm_b32 v61, v119, v115, s94\n" \
    "s_waitcnt vmcnt(1)\n" \
    "s_waitcnt vmcnt(0)\n" \
    "s_setprio 3\n" \
    "ds_read_b32 v155, %[fh]\n" \
    "ds_read_b128 v[144:147], %[hs]\n" \
    "ds_read_b128 v[148:151], %[hs] offset:64\n" \
    "v_perm_b32 v38, v124, v120, s93\n" \
    "v_perm_b32 v46, v124, v120, s94\n" \
    "v_perm_b32 v54, v125, v121, s93\n" \
    "v_perm_b32 v62, v125, v121, s94\n" \
    "v_perm_b32 v39, v126, v122, s93\n" \
    "v_perm_b32 v47, v126, v122, s94\n" \
    "v_perm_b32 v55, v127, v123, s93\n" \
    "v_perm_b32 v63, v127, v123, s94\n" \
    "s_waitcnt lgkmcnt(2)\n" \
    "v_cmp_gt_u32_e32 vcc, 3, v155\n" \
    "s_cbranch_vccz Lde1_%=\n" \
    "s_mov_b32 s92, 0x100000\n" \
    "Ldl1_%=:\n" \
    "ds_read_b32 v155, %[fh] offset:0\n" \
    "ds_read_b128 v[144:147], %[hs]\n" \
    "ds_read_b128 v[148:151], %[hs] offset:64\n" \
    "s_waitcnt lgkmcnt(0)\n" \
    "v_cmp_gt_u32_e32 vcc, 3, v155\n" \
    "s_cbranch_vccz Lde1_%=\n" \
    "s_sub_u32 s92, s92, 1\n" \
    "s_cbranch_scc0 Ldl1_%=\n" \
    "Lde1_%=:\n" \
    "s_waitcnt lgkmcnt(1)\n" \
    "v_smfmac_f32_16x16x64_bf16 v[128:131], v[144:147], v[0:7], %[idx]\n" \
    "v_smfmac_f32_16x16x64_bf16 v[132:135], v[144:147], v[8:15], %[idx]\n" \
    "v_smfmac_f32_16x16x64_bf16 v[136:139], v[144:147], v[16:23], %[idx]\n" \
    "v_smfmac_f32_16x16x64_bf16 v[140:143], v[144:147], v[24:31], %[idx]\n" \
    "s_waitcnt lgkmcnt(0)\n" \
    "v_smfmac_f32_16x16x64_bf16 v[128:131], v[148:151], v[32:39], %[idx]\n" \
    "v_smfmac_f32_16x16x64_bf16 v[132:135], v[148:151], v[40:47], %[idx]\n" \
    "v_smfmac_f32_16x16x64_bf16 v[136:139], v[148:151], v[48:55], %[idx]\n" \
    "v_smfmac_f32_16x16x64_bf16 v[140:143], v[148:151], v[56:63], %[idx]\n" \
    "s_mov_b32 vcc_lo, 0x55555555\n" \
    "s_mov_b32 vcc_hi, 0x55555555\n" \
    "s_nop 2\n" \
    "v_cvt_pk_bf16_f32 v80, v128, v129\n" \
    "v_cvt_pk_bf16_f32 v81, v130, v131\n" \
    "v_cvt_pk_bf16_f32 v82, v132, v133\n" \
    "v_cvt_pk_bf16_f32 v83, v134, v135\n" \
    "v_cvt_pk_bf16_f32 v84, v136, v137\n" \
    "v_cvt_pk_bf16_f32 v85, v138, v139\n" \
    "v_cvt_pk_bf16_f32 v86, v140, v141\n" \
    "v_cvt_pk_bf16_f32 v87, v142, v143\n" \
    "v_cndmask_b32_dpp v88, v82, v80, vcc quad_perm:[1,0,3,2] row_mask:0xf bank_mask:0xf\n" \
    "v_cndmask_b32_dpp v89, v83, v81, vcc quad_perm:[1,0,3,2] row_mask:0xf bank_mask:0xf\n" \
    "v_cndmask_b32_dpp v92, v86, v84, vcc quad_perm:[1,0,3,2] row_mask:0xf bank_mask:0xf\n" \
    "v_cndmask_b32_dpp v93, v87, v85, vcc quad_perm:[1,0,3,2] row_mask:0xf bank_mask:0xf\n" \
    "s_not_b64 vcc, vcc\n" \
    "v_cndmask_b32_dpp v90, v80, v82, vcc quad_perm:[1,0,3,2] row_mask:0xf bank_mask:0xf\n" \
    "v_cndmask_b32_dpp v91, v81, v83, vcc quad_perm:[1,0,3,2] row_mask:0xf bank_mask:0xf\n" \
    "v_cndmask_b32_dpp v94, v84, v86, vcc quad_perm:[1,0,3,2] row_mask:0xf bank_mask:0xf\n" \
    "v_cndmask_b32_dpp v95, v85, v87, vcc quad_perm:[1,0,3,2] row_mask:0xf bank_mask:0xf\n" \
    "s_mov_b32 vcc_lo, 0x33333333\n" \
    "s_mov_b32 vcc_hi, 0x33333333\n" \
    "v_cndmask_b32_dpp v64, v92, v88, vcc quad_perm:[2,3,0,1] row_mask:0xf bank_mask:0xf\n" \
    "v_cndmask_b32_dpp v66, v93, v89, vcc quad_perm:[2,3,0,1] row_mask:0xf bank_mask:0xf\n" \
    "v_cndmask_b32_dpp v68, v94, v90, vcc quad_perm:[2,3,0,1] row_mask:0xf bank_mask:0xf\n" \
    "v_cndmask_b32_dpp v70, v95, v91, vcc quad_perm:[2,3,0,1] row_mask:0xf bank_mask:0xf\n" \
    "s_not_b64 vcc, vcc\n" \
    "s_mov_b64 exec, %[em]\n" \
    "global_store_dwordx4 %[po], v[64:67], %[ws] sc1\n" \
    "global_store_dwordx4 v157, v[68:71], %[ws] sc1\n" \
    "v_cndmask_b32_dpp v72, v88, v92, vcc quad_perm:[2,3,0,1] row_mask:0xf bank_mask:0xf\n" \
    "v_cndmask_b32_dpp v74, v89, v93, vcc quad_perm:[2,3,0,1] row_mask:0xf bank_mask:0xf\n" \
    "v_cndmask_b32_dpp v76, v90, v94, vcc quad_perm:[2,3,0,1] row_mask:0xf bank_mask:0xf\n" \
    "v_cndmask_b32_dpp v78, v91, v95, vcc quad_perm:[2,3,0,1] row_mask:0xf bank_mask:0xf\n" \
    "global_store_dwordx4 v158, v[72:75], %[ws] sc1\n" \
    "global_store_dwordx4 v159, v[76:79], %[ws] sc1\n" \
    "s_mov_b64 exec, -1\n"
#define DOWN_CLOB "v0", "v1", "v2", "v3", "v4", "v5", "v6", "v7", "v8", "v9", "v10", "v11", "v12", "v13", "v14", "v15", "v16", "v17", "v18", "v19", "v20", "v21", "v22", "v23", "v24", "v25", "v26", "v27", "v28", "v29", "v30", "v31", "v32", "v33", "v34", "v35", "v36", "v37", "v38", "v39", "v40", "v41", "v42", "v43", "v44", "v45", "v46", "v47", "v48", "v49", "v50", "v51", "v52", "v53", "v54", "v55", "v56", "v57", "v58", "v59", "v60", "v61", "v62", "v63", "v64", "v65", "v66", "v67", "v68", "v69", "v70", "v71", "v72", "v73", "v74", "v75", "v76", "v77", "v78", "v79", "v80", "v81", "v82", "v83", "v84", "v85", "v86", "v87", "v88", "v89", "v90", "v91", "v92", "v93", "v94", "v95", "v96", "v97", "v98", "v99", "v100", "v101", "v102", "v103", "v104", "v105", "v106", "v107", "v108", "v109", "v110", "v111", "v112", "v113", "v114", "v115", "v116", "v117", "v118", "v119", "v120", "v121", "v122", "v123", "v124", "v125", "v126", "v127", "v128", "v129", "v130", "v131", "v132", "v133", "v134", "v135", "v136", "v137", "v138", "v139", "v140", "v141", "v142", "v143", "v144", "v145", "v146", "v147", "v148", "v149", "v150", "v151", "v152", "v153", "v154", "v155", "v156", "v157", "v158", "v159"

DI void conv_final(const Params& P, lptr sm, int cg, int xcd, u32 tag, v2u t1) {
    const int lane = fresh_lane();
    const __amdgpu_buffer_rsrc_t rws = ws_rsrc(P), ro = mk_rsrc(P.out, L_ * D_ * 2);
    const int j = (lane & 4) + (lane >> 4), cc = lane & 3;
    u32 po = WS_PART + (u32)cg * (72u * 128u) + (u32)lane * 16u;
    PIN(po);
    clptr fw = sm + LD_TB + ROW_FU * TR + (lane & 3) * 516 + 256;
    __builtin_amdgcn_s_setprio(FPRIO);
    wait_ge<1>(fw, 10);
    u32 sel;
    {
        v4u e0 = W128(sm + LD_KT + 8448 + lane * 16), e1 = W128(sm + LD_KT + 9472 + lane * 16), e2 = W128(sm + LD_KT + 10496 + lane * 16), e3 = W128(sm + LD_KT + 11520 + lane * 16);
        u32 g8, tc;
        asm volatile(NET_ASM : [g8] "=&v"(g8), [tc] "=&v"(tc), "+{v[0:3]}"(e0), "+{v[4:7]}"(e1), "+{v[20:23]}"(e2), "+{v[24:27]}"(e3)
                     : [c4] "v"((lane & 4) ? 0u : ~0u), [c2] "v"((lane & 2) ? 0u : ~0u), [c1] "v"((lane & 1) ? 0u : ~0u) : NET_CLOB);
        if (__builtin_expect(__any(tc > rdl(g8, 7)), 0)) {
            u32 all[32];
#pragma unroll
            for (int q = 0; q < 32; ++q) all[q] = (W32(sm + LD_KT + lane * 4 + q * 256) & 0xffff0000u) | (u32)(2047 - 64 * q - lane);
            g8 = top8_slow(all, lane);
        }
        sel = from_lane(g8, j);
    }
    const u32 tok = 2047u - (sel & 0x7ffu);
    wait_ge<1>(sm + LD_TB + ROW_FL * TR + (lane & 3) * 516 + (lane & 4) * 64, 18);
    const float rs = sigmoidf_(bf_hi(sel ^ ((~(u32)((int)sel >> 31)) | 0x80000000u)));
    const float x = bf_lo((u32)W16(sm + LD_U + cc * U_CH + 2 * U_PAD + tok * 2u));
    const u32 c = (tok >> 4) & 15u;
    clptr ap = sm + LD_TB + (tok >> 7) * TR + cc * 512 + (c & 7u) * 64u + (((tok >> 2) ^ (c >> 1)) & 3u) * 16u + (tok & 3u) * 4u;
    const float a2 = *(const volatile LAS float*)(ap + (ROW_T0 - 4) * TR);
    const float a = *(const volatile LAS float*)ap + (tok >= 512u ? a2 : 0.f);
    const float g = g_of(W32(sm + LD_KT + tok * 4u));
    float gs = (1.f - g) * rs, base = x + g * (a - x);
    u32 oo = (lane & 9) ? OOB : (tok * D_ + (u32)(4 * cg + cc)) * 2u;
    PIN(base); PIN(gs); PIN(oo); PIN(fw);
    u32 po2 = po + 5120u;
    const u32 at0 = rfl(W32(sm + LD_KT + 8192)) + PART_AT;
    PIN(po2);
#pragma unroll 1
    for (int w = 0; w < 4096; ++w) {
        u32 tg1, at1;
        asm volatile("v_readfirstlane_b32 %0, v108\n\tv_readfirstlane_b32 %1, v109" : "=s"(tg1), "=s"(at1), "+{v[108:109]}"(t1));
        if ((int)((u32)__builtin_amdgcn_s_memrealtime() - (tg1 == tag ? at1 : at0)) >= 0) break;
        __builtin_amdgcn_s_sleep(1);
    }
    float s0, s1, s2, s3;
    u32 n;
#define PLD(k, a, o) "global_load_dwordx4 v[64+4*" #k ":67+4*" #k "], " a ", %[ws] offset:" #o " sc1\n\t"
#define PAD(k, w) "s_waitcnt vmcnt(" #w ")\n\tv_dot2c_f32_bf16 %[s0], %[lo], v[64+4*" #k "]\n\tv_dot2c_f32_bf16 %[s1], %[hi], v[64+4*" #k "]\n\t" \
                  "v_dot2c_f32_bf16 %[s2], %[lo], v[66+4*" #k "]\n\tv_dot2c_f32_bf16 %[s3], %[hi], v[66+4*" #k "]\n\t" \
                  "v_bitop3_b32 v[65+4*" #k "], v[65+4*" #k "], v[67+4*" #k "], %[tag] bitop3:0x7e\n\t"
    asm volatile("s_mov_b32 %[n], 0x20000\n\tv_mov_b32 v100, 1\n"
                 "Lr%=:\n\tv_cmp_ne_u32 vcc, 0, v100\n\tv_mov_b32 %[s0], 0\n\tv_mov_b32 %[s1], 0\n\tv_mov_b32 %[s2], 0\n\tv_mov_b32 %[s3], 0\n\ts_mov_b64 exec, vcc\n\t"
                 PLD(0, "%[p0]", 0) PLD(1, "%[p0]", 1024) PLD(2, "%[p0]", 2048) PLD(3, "%[p0]", 3072) PLD(4, "%[p1]", -1024)
                 PLD(5, "%[p1]", 0) PLD(6, "%[p1]", 1024) PLD(7, "%[p1]", 2048) PLD(8, "%[p1]", 3072) "s_mov_b64 exec, -1\n\t"
                 PAD(0, 8) PAD(1, 7) PAD(2, 6) "v_or3_b32 v100, v65, v69, v73\n\t" PAD(3, 5) PAD(4, 4) "v_or3_b32 v100, v100, v77, v81\n\t"
                 PAD(5, 3) PAD(6, 2) "v_or3_b32 v100, v100, v85, v89\n\t" PAD(7, 1) PAD(8, 0) "v_or3_b32 v100, v100, v93, v97\n\t"
                 "v_cmp_ne_u32 vcc, 0, v100\n\ts_cbranch_vccz Le%=\n\t"
                 "v_mov_b32 v65, %[tag]\n\tv_mov_b32 v69, %[tag]\n\tv_mov_b32 v73, %[tag]\n\tv_mov_b32 v77, %[tag]\n\tv_mov_b32 v81, %[tag]\n\t"
                 "v_mov_b32 v85, %[tag]\n\tv_mov_b32 v89, %[tag]\n\tv_mov_b32 v93, %[tag]\n\tv_mov_b32 v97, %[tag]\n\t"
                 "s_sleep %[gap]\n\ts_sub_u32 %[n], %[n], 1\n\ts_cbranch_scc0 Lr%=\n"
                 "Le%=:\n\ts_nop 1"
                 : [s0] "=&v"(s0), [s1] "=&v"(s1), [s2] "=&v"(s2), [s3] "=&v"(s3), [n] "=&s"(n), "+{v[108:109]}"(t1)
                 : [p0] "v"(po), [p1] "v"(po2), [ws] "s"(P.ws), [tag] "s"(tag), [lo] "s"(0x00003f80u), [hi] "s"(0x3f800000u), [gap] "n"(PART_GAP)
                 : "memory", "vcc", "scc", "v64", "v65", "v66", "v67", "v68", "v69", "v70", "v71", "v72", "v73", "v74", "v75", "v76", "v77", "v78", "v79", "v80", "v81", "v82", "v83",
                   "v84", "v85", "v86", "v87", "v88", "v89", "v90", "v91", "v92", "v93", "v94", "v95", "v96", "v97", "v98", "v99", "v100");
    wait_ge<0>(fw, 20);
    const auto a3 = __builtin_amdgcn_permlane32_swap(__float_as_uint(s0), __float_as_uint(s2), false, false);
    const auto b2 = __builtin_amdgcn_permlane32_swap(__float_as_uint(s1), __float_as_uint(s3), false, false);
    const auto e = __builtin_amdgcn_permlane16_swap(__float_as_uint(__uint_as_float(a3[0]) + __uint_as_float(a3[1])), __float_as_uint(__uint_as_float(b2[0]) + __uint_as_float(b2[1])), false, false);
    float y = __uint_as_float(e[0]) + __uint_as_float(e[1]);
    y += dppf<0x128, 0xf>(y);
    const float o = base + gs * y;
    __builtin_amdgcn_raw_buffer_store_b32(pk_bf16(o, dppf<0xB1, 0xf>(o)), ro, oo, 0, 0);
}

template <int kind> DI void conv_wave(const Params& P, lptr sm, int ch, int xcd, int ii, u32 labw) {
    const int cg = cg_of(xcd, ii);
    const u32 m0a = (u32)(LD_TB + ch * 512 + (kind & 1) * 256);
    u32 epoch;
    if (kind == 0) {
        asm volatile("; claim\n" CONV_A_ASM : : [m0a] "s"(m0a) : "memory", "vcc", "scc", "s90", CONV_A_CLOB);
    } else if (kind == 1) {
        v2u t1;
        asm volatile("; claim\n" CONV_B_ASM : [ep] "=&s"(epoch), "=&{v[108:109]}"(t1) : [m0a] "s"(m0a), [tp] "s"(P.ws + WS_T1 + xcd * 128) : "memory", "vcc", "scc", "s90", "s92", CONV_B_CLOB);
        if (ch == 0) conv_final(P, sm, cg, xcd, tag_of(epoch), t1);
    } else {
        const Lab lb = lab_of(labw);
        const u32 nl = xcd < 2 ? 6u * lb.n : 9u * lb.n, u0 = (u32)(4 * ii + ch);
        const char* pw = (const char*)P.wd + (lb.m0 * 576u + u0 * nl) * 128u;
        asm volatile("; claim\n" CONV_C_ASM : [ep] "=&s"(epoch)
                     : [xb] "s"((const char*)P.x + (umin((xcd < 2 ? 22u : 32u) * u0, xcd < 2 ? 2026u : 2016u) * (D_ * 2) + 128 * xcd)), [pl] "s"(xcd < 2 ? 43 : 31), [ps] "s"(1u >> (xcd >> 1)), [pd] "s"(xcd < 2 ? 1024 - 128 * xcd : 0), [x0] "s"((const char*)P.x + cg * 8), [wd] "s"(pw), [wl] "s"((nl - 1u) * 128u), [o0] "s"((char*)P.out + cg * 8), [ws] "s"(P.ws), [ak] "s"(WS_ACK + (u32)(8 * ii + xcd) * 128u),
                       [bs] "s"((const char*)&conv_tab + 61440 + ch * 1024), [bl] "s"(LD_BD + ch * 1024), [tg] "s"(11 + (ch >> 1)), [am] "s"(1u >> ch), [gt] "n"(LD_KT - (int)WS_KEYS / 2), [kt] "n"(LD_KT + 8192), [kt2] "n"(LD_KT + 8448 - (int)WS_KEYS), [tr] "s"(WS_T0 + (u32)xcd * 128u), [ko] "n"(2047 + (int)WS_KEYS / 8), [pp] "s"(P.ws + cg * (72 * 128))
                     : "memory", "vcc", "scc", "s88", "s89", "s90", "s91", "s92", "s93", "s94", "s95", "s96", "s97", "s98", "s99", CONV_C_CLOB);
    }
}

DI void select8(const Params& P, int lane, int xcd, u32 tag, u32 t_in) {
    const __amdgpu_buffer_rsrc_t rws = ws_rsrc(P);
    const v2u t0 = ld2_sc1(rws, TREF && xcd >= 4 ? WS_T0 + (u32)xcd * 128u : OOB);
    const u32 co = WS_CAND + (u32)xcd * 16384u + (u32)lane * 128u, fi = (u32)(63 - lane) << 4;
    const u32 xk = 2047u * (D_ * 2) + (u32)(((lane & 7) ^ ((lane >> 3) & 6)) * 16);
    const u32 d = rfl(t0[0]) == tag ? t_in - rfl(t0[1]) : 0u, at = t_in + SEL_AT - (d < (u32)SEL_AT ? d : 0u);
    u32 g8, tc, ps;
    asm volatile(SEL_ASM
                 : [g8] "=&v"(g8), [tc] "=&v"(tc), [ps] "=&s"(ps)
                 : [co] "v"(co), [co2] "v"(co + 8192u), [fi] "v"(fi), [f0] "v"(fi | 1024u), [c4] "v"((lane & 4) ? 0u : ~0u), [c2] "v"((lane & 2) ? 0u : ~0u), [c1] "v"((lane & 1) ? 0u : ~0u),
                   [mk] "v"(0xffff000fu), [xk] "v"(xk), [pm] "s"(0x05010d0du), [ws] "s"(P.ws), [x] "s"(P.x), [tag] "s"(tag), [at] "s"(at), [xs] "n"(LD_XS), [gap] "n"(SEL_GAP)
                 : "memory", "vcc", "scc", "s92", "s93", SEL_CLOB);
    if (__builtin_expect(__any(tc > rdl(g8, 7)), 0)) {
        u32 all[32];
#pragma unroll 1
        for (int spin = 0; spin < SPIN_CAP; ++spin) {
            MEMFENCE();
            bool ok = true;
#pragma unroll
            for (int q = 0; q < 16; ++q) {
                const v4u e = ld_sc1(rws, WS_KEYS + (u32)lane * 16u + q * 1024);
                ok &= tag_ok(e, tag);
                all[2 * q] = (e[0] & 0xffff0000u) | (u32)(2047 - 128 * q - 2 * lane); all[2 * q + 1] = (e[2] & 0xffff0000u) | (u32)(2046 - 128 * q - 2 * lane);
            }
            if (__all(ok)) break;
            __builtin_amdgcn_s_sleep(8);
        }
        g8 = top8_slow(all, lane);
        VMWAIT(0);
        dma_rows<1024>(P.x, xk - (from_lane(g8, lane >> 3) & 0x7ffu) * (D_ * 2), LD_XS);
    }
}

DI void moe_weights(const Params& P, lptr sm, int wave, int midx, u32 nit) {
    v4u wb[24];
    u64 base;
    asm volatile("; claim4" : "={v[96:99]}"(wb[0]), "={v[100:103]}"(wb[1]), "={v[104:107]}"(wb[2]), "={v[108:111]}"(wb[3]), "={v[12:13]}"(base));
    const int lane = fresh_lane();
    const int c = lane & 15, kg = lane >> 4;
    auto ldw = [&](int k) {
        asm volatile("global_load_dwordx4 %0, %1, off" : "=v"(wb[k]) : "v"(base) : "memory");
        base += (u64)(((k & 7) == 7 ? 173 : ((k & 7) == 3 ? 13 : 1)) * (F_ * 2));
        SB();
    };
    base += (u64)(12 * (F_ * 2));
#pragma unroll
    for (int k = 4; k < WIN0; ++k) ldw(k);
    lptr rdw = sm + LD_RED + wave * 4096 + lane * 16;
    clptr rdr = sm + LD_RED + (wave & 3) * 1024 + (lane & 55) * 16 + (c >> 3) * 8;
    lptr hbw = sm + LD_HB + (4 * (kg & 1) + 2 * (c >> 3)) * HB_ROW + 2 * (8 * (c & 7) + (wave & 3) + 4 * (kg >> 1));
    const int kq = 4 * wave + kg;
    clptr xa = sm + LD_XS + (kq >> 3) * 1024 + (c & 7) * 128 + (((kq & 7) ^ (c & 6)) * 16);
    u32 idx = c < 8 ? 0x4444u : 0xeeeeu;
    clptr fa = sm + FL_G + 4 * umin(lane, 5);
    lptr fo = sm + FL_G + 4 * wave;
    PIN(rdw); PIN(rdr); PIN(hbw); PIN(xa); PIN(idx); PIN(fa); PIN(fo);
    SB();
    v8u gf[3][4];
    {
        if (WIN0 < WIN && nit) wait_ge<1>(sm + FL_IT + 4 * umin(lane, nit - 1), 1);
#pragma unroll
        for (int k = WIN0; k < WIN; ++k) ldw(k);
        auto done = [&](int d) {
            if (d & 1) {
                asm volatile("" : "+v"(wb[d - 1]), "+v"(wb[d]));
                const v4u lo = wb[d - 1], hi = wb[d];
#pragma unroll
                for (int m = 0; m < 4; ++m) {
                    const int e = (d & 6) + (m >> 1);
                    gf[d >> 3][2 * (m & 1)][e] = perm_lo(hi[m], lo[m]); gf[d >> 3][2 * (m & 1) + 1][e] = perm_hi(hi[m], lo[m]);
                    PIN(gf[d >> 3][2 * (m & 1)][e]); PIN(gf[d >> 3][2 * (m & 1) + 1][e]);
                }
                SB();
            }
        };
        int dn = 0;
#pragma unroll
        for (int k = WIN; k < 24; ++k) {
            const int w = k < WSW ? WIN : WIN2;
            VMWAIT(w - 1);
            ldw(k);
            if (k == GFL) flag1(fo, 1);
#pragma unroll
            for (; dn < k - w + 1; ++dn) done(dn);
        }
#pragma unroll
        for (; dn < 24; ++dn) { VMWAIT(23 - dn); done(dn); }
    }

    asm volatile(FFN_ASM
                 : "+{v[0:7]}"(gf[0][0]), "+{v[8:15]}"(gf[0][1]), "+{v[16:23]}"(gf[0][2]), "+{v[24:31]}"(gf[0][3]), "+{v[32:39]}"(gf[1][0]), "+{v[40:47]}"(gf[1][1]),
                   "+{v[48:55]}"(gf[1][2]), "+{v[56:63]}"(gf[1][3]), "+{v[64:71]}"(gf[2][0]), "+{v[72:79]}"(gf[2][1]), "+{v[80:87]}"(gf[2][2]), "+{v[88:95]}"(gf[2][3])
                 : [xa] "v"(xa), [rdw] "v"(rdw), [rdr] "v"(rdr), [hbw] "v"(hbw), [idx] "v"(idx), [fa] "v"(fa), [fo] "v"(fo), [wv] "s"(wave), [tg] "n"(LD_TAG + 4)
                 : "memory", "scc", "vcc", "s92", FFN_CLOB);
}

DI void moe_down(const Params& P, lptr sm, int d, int midx) {
    asm volatile("s_waitcnt vmcnt(0) ; free" ::: "memory");
    const int lane = fresh_lane();
    const int c = lane & 15, kg = lane >> 4;
    const u32 src = (u32)(midx * 64 + 4 * kg) * (D_ * 2) + umin((u32)(256 * d + 16 * c), D_ * 2 - 16);
    const u32 po = WS_PART + (u32)(((32 * d + 2 * (c & 12) + (kg >> 1)) * 72 + midx) * 8 + 4 * (kg & 1) + (c & 3)) * 16u;
    asm volatile(DOWN_ASM :
                 : [src] "v"(src), [po] "v"(po), [fs] "v"(lane < 6 ? sm + FL_G + 4 * lane : sm + LD_TAG + 4), [fh] "v"(sm + FL_G + 4 * (lane & 3)), [hs] "v"(sm + LD_HB + (c & 7) * HB_ROW + kg * 16),
                   [idx] "v"(c < 8 ? 0x4444u : 0xeeeeu), [wd] "s"(P.wd), [ws] "s"(P.ws), [em] "s"(d < 4 ? ~0ull : 0x00ff00ff00ff00ffull), [tg] "n"(LD_TAG)
                 : "memory", "scc", "vcc", "s92", "s93", "s94", DOWN_CLOB);
}

DI void helper_prefetch(const Params& P, int lane, u32 idx, u32 cnt, Lab lb) {
    const u32 c3 = (3u - (u32)((u64)P.wg >> 7) - lb.m0) & 7u, c3u = (3u - (u32)((u64)P.wu >> 7) - lb.m0) & 7u;
    const u32 n3 = (c3 < lb.n ? 1u : 0u) + (c3 + 8u < lb.n ? 1u : 0u), nl = lb.n - n3;
    const u32 mg2 = (u32)(65536.f * __builtin_amdgcn_rcpf((float)nl)) + 1u;
    const u32 rpu = (64u * mg2) >> 16, r = umin(((u32)lane * mg2) >> 16, rpu - 1u);
    u32 l = umin((u32)lane - nl * r, nl - 1u);
    l += l >= c3 ? 1u : 0u; l += l >= c3 + 8u ? 1u : 0u;
    const u32 go = r * (F_ * 2) + (lb.m0 + l) * 128u;
    constexpr u32 NSG = (D_ - SLOW0) / 64;
    u32 u = idx;
#pragma unroll 1
    for (; u < 2u * NSG * n3; u += cnt) {
        VMWAIT(HWIN - 1);
        const u32 h = n3 == 2u ? u >> 2 : u >> 1;
        touch((u & 1) ? P.wu : P.wg, (SLOW0 + h * 64u + (u32)lane) * (F_ * 2) + umin(lb.m0 + (n3 == 2u && (u & 2u) ? 8u : 0u) + ((u & 1) ? c3u : c3), 71u) * 128u);
    }
#pragma unroll 1
    for (u -= 2u * NSG * n3; HR0 + (u >> 1) * rpu < D_; u += cnt) {
        VMWAIT(HWIN - 1);
        touch((u & 1) ? P.wu : P.wg, go + umin(HR0 + (u >> 1) * rpu, D_ - rpu) * (F_ * 2));
    }
}

#define TAB_CLOB "v148", "v149", "v150", "v151", "v152", "v153", "v154", "v155", "v156", "v157", "v158", "v159", "v160", "v161", "v162", "v163", "v164", "v165", "v166", "v167", "v32", "v33", "v34", "v35", "v36", "v37", "v38", "v39", "v40", "v41", "v42", "v43", "v44", "v45", "v46", "v47", "v48", "v49", "v50", "v51"
#define ENTRY_CLOB TAB_CLOB, "v137", "v138", "v139", "v140", "v141", "v142", "v143", "v144", "v145", "v146", "v147", "v64", "v65", "v66", "v67", "v68", "v69", "v70", "v71", "v72", "v73", "v74", "v75", "v76", "v77", "v78", "v79", "v80", "v81", "v82", "v83", "v84", "v85", "v86", "v87", "v88", "v89", "v90", "v91", "v92", "v93", "v94", "v95", "v96", "v97", "v98", "v99", "v100", "v101", "v102", "v103", "v104", "v105", "v106", "v107", "v108", "v109", "v110", "v111", "v112", "v113", "v114", "v115", "v116", "v117", "v118", "v119", "v120", "v121", "v122", "v123", "v124", "v125", "v126", "v127", "v128", "v129", "v130", "v131", "v132", "v133", "v134", "v135", "v136"
__global__ void __launch_bounds__(NTHR, 3) fused_kernel(Args) {
    __shared__ __attribute__((aligned(16))) unsigned char smem[LDS_BYTES];
    lptr sm = (lptr)smem;
    const int b = blockIdx.x;
    v16u ka0, ka1;
    int wave, item;
    u32 role, labw, tid = threadIdx.x;
    u64 t64;
#define LX(q, a, o) "global_load_dwordx4 v[96+4*(" #q "):99+4*(" #q ")], " a ", s[54:55] offset:" #o "\n\t"
#define LXS(s) LX(s, "v7", -2304+256*s) LX(4+s, "v7", 2304+256*s) LX(8+s, "v9", -2304+256*s) LX(12+s, "v9", 2304+256*s)
#define LG(q) "global_load_dwordx4 v[96+4*" #q ":99+4*" #q "], v[12:13], off\n\tv_lshl_add_u64 v[12:13], v[12:13], 0, s[54:55]\n\t"
#define NPRE 0
#define EARLY_LOADS "s_barrier\n\t" LG(0) LG(1) LG(2) LG(3)
#define LWS(p) "global_load_lds_dwordx4 v2, " p "\n\ts_mov_b64 exec, 0xff\n\tglobal_load_lds_dwordx4 v2, " p " offset:1024\n\ts_mov_b64 exec, -1\n\t"
    asm volatile("s_load_dwordx16 s[36:51], %[kap], 0x0\n\ts_load_dwordx16 s[16:31], %[kap], 0x40\n\t"
                 "v_readfirstlane_b32 %[w], %[tid]\n\ts_lshr_b32 s53, %[b], 3\n\tv_and_b32 v1, 63, %[tid]\n\ts_sub_u32 s56, s53, 24\n\t"
                 "s_cmpk_eq_u32 %[w], 640\n\ts_cselect_b32 s56, s56, -1\n\ts_cmp_lt_u32 s56, 8\n\ts_cbranch_scc0 Lb%=\n\t"
                 "s_getpc_b64 s[54:55]\n\ts_lshl_b32 s56, s56, 12\n\tv_min_u32 v3, 31, v1\n\tv_lshl_add_u32 v3, v3, 7, s56\n\tv_min_u32 v3, %[ce], v3\n\t"
                 "global_load_dword v3, v3, s[54:55]\n"
                 "Lb%=:\n\ts_and_b32 s52, %[b], 7\n\ts_lshl_b32 s57, s52, 7\n\ts_lshl2_add_u32 s55, s53, s57\n\t"
                 "s_load_dword %[rl], %[kap], s55 offset:0x80\n\ts_load_dword %[lb], %[kap], s57 offset:0x80\n\t"
                 "v_mov_b32 v0, 0\n\ts_cmp_lt_u32 s52, 2\n\ts_cselect_b32 s54, 24, 16\n\t"
                 "s_lshr_b32 %[w], %[w], 6\n\tv_lshlrev_b32 v2, 4, v1\n\ts_cmp_ge_u32 s53, s54\n\ts_cbranch_scc1 Ln%=\n\t"
                 "s_mul_i32 s54, %[w], 5120\n\tv_add_u32 v3, s54, v2\n\ts_and_b32 s55, %[w], 3\n\t"
                 "s_cmp_lt_u32 %[w], 8\n\ts_cbranch_scc1 Lt%=\n\ts_setprio 3\n\tv_add_u32 v4, 0x1000, v3\n\t"
                 "global_load_dwordx4 v[44:47], v3, %[tab] offset:3072\n\tglobal_load_dwordx4 v[48:51], v4, %[tab]\n\tglobal_load_dwordx4 v[40:43], v3, %[tab] offset:2048\n"
                 "Lt%=:\n\ts_lshl4_add_u32 s54, s52, s53\n\ts_lshl3_add_u32 s56, s52, s53\n\ts_addk_i32 s56, 112\n\ts_cmp_lt_u32 s53, 16\n\ts_cselect_b32 s54, s54, s56\n\t"
                 "s_lshl2_add_u32 s54, s54, s55\n\t"
                 "s_bfe_u32 s56, %[w], 0x10002\n\ts_lshl1_add_u32 s55, s55, s56\n\ts_lshl_b32 m0, s55, 8\n\t"
                 "s_lshl1_add_u32 s56, s54, s56\n\ts_lshl_b32 s56, s56, 7\n\ts_lshl_b32 s54, s54, 1\n\t"
                 "v_lshl_add_u32 v4, v1, 1, s56\n\tv_mov_b32 v5, s54\n\tv_mov_b32 v6, 0x3f80\n\t"
                 "ds_write_addtid_b32 v0 offset:%[fl]\n\tds_write_addtid_b32 v0 offset:%[fu]\n\tds_write_addtid_b32 v6\n\t"
                 "v_mov_b64 v[64:65], 0\n\tv_mov_b64 v[66:67], 0\n\tv_mov_b64 v[68:69], 0\n\tv_mov_b64 v[70:71], 0\n\ts_waitcnt lgkmcnt(0)\n\t"
                 "s_cmp_ge_u32 %[w], 8\n\ts_cbranch_scc1 Lc%=\n\t"
                 "global_load_short_d16_hi v64, v4, s[38:39]\n\tglobal_load_short_d16_hi v65, v4, s[40:41]\n\tglobal_load_short_d16_hi v70, v5, s[50:51]\n\t"
                 "global_load_short_d16_hi v66, v4, s[42:43]\n\tglobal_load_short_d16_hi v67, v4, s[44:45]\n\tglobal_load_short_d16_hi v68, v4, s[46:47]\n\t"
                 "global_load_short_d16_hi v69, v4, s[48:49]\n"
                 "Lc%=:\n\ts_barrier\n\tglobal_load_short_d16_hi v71, v5, s[16:17]\n\tglobal_load_dword v72, v0, s[30:31] sc1\n\t"
                 "global_load_dwordx4 v[32:35], v3, %[tab]\n\tglobal_load_dwordx4 v[36:39], v3, %[tab] offset:1024\n\ts_branch Lw%=\n"
                 "Ln%=:\n\ts_mov_b32 m0, 0\n\ts_mov_b32 %[it], -1\n\ts_bitcmp1_b32 0x302, %[w]\n\tds_write_addtid_b32 v0\n\ts_cbranch_scc0 Lo%=\n\t"
                 "v_and_b32 v9, 0x330, v2\n\tv_bfe_u32 v7, v1, 2, 2\n\tv_lshrrev_b32 v8, 2, v9\n\tv_and_b32 v9, 48, v9\n\tv_and_or_b32 v9, v8, %[c0c], v9\n\t"
                 "v_mad_u32_u24 v7, v7, %[rb], v9\n\tv_lshrrev_b32 v8, 5, v1\n\tv_add_u32 v7, 2304, v7\n\ts_sub_i32 s56, %[w], 7\n\ts_max_i32 s56, s56, 0\n\t"
                 "v_mul_u32_u24 v8, 4480, v8\n\tv_add_u32 v9, 9216, v7\n\tv_add_u32 v8, v8, v7\n\tv_add_u32 v11, 9216, v8\n\ts_mov_b32 m0, %[wl]\n\t"
                 "s_waitcnt lgkmcnt(0)\n\ts_lshr_b32 s54, %[rl], 16\n\ts_cmp_ge_u32 s56, s54\n\ts_cbranch_scc1 Lx%=\n\t"
                 "s_bfe_u32 s54, %[rl], 0x80008\n\ts_add_u32 %[it], s54, s56\n\ts_mul_i32 s54, %[it], 18432\n\ts_add_u32 s54, s54, s36\n\ts_addc_u32 s55, s37, 0\n\t"
                 "global_load_dword v72, v0, s[30:31] sc1\n\t"
                 LWS("s[18:19]") "s_addk_i32 m0, 1152\n\ts_nop 0\n\t" LWS("s[26:27]")
                 LXS(0) LXS(1) LXS(2) LXS(3) LX(16, "v8", -1280) LX(17, "v11", -1280) "s_barrier\n\ts_branch Lw%=\n"
                 "Lo%=:\n\ts_cmp_lt_u32 %[w], 8\n\ts_cbranch_scc0 Lx%=\n\t"
                 "v_lshrrev_b32 v7, 4, v1\n\ts_lshl_b32 s56, %[w], 3\n\ts_sub_u32 s56, s56, 16\n\tv_add_u32 v7, s56, v7\n\tv_and_b32 v8, 0x70, v2\n\tv_mul_u32_u24 v7, 36864, v7\n\t"
                 "v_add_u32 v7, v7, v8\n\ts_mov_b32 s58, 0xff00ff00\n\ts_mov_b32 s59, s58\n\ts_mov_b64 s[54:55], 9216\n\ts_cmp_lg_u32 %[w], 0\n\t"
                 "s_waitcnt lgkmcnt(0)\n\ts_cbranch_scc1 Lp%=\n\ts_memrealtime %[ti]\n\tglobal_load_dword v136, v0, s[30:31] sc1\n\ts_barrier\n\ts_branch Lw%=\n"
                 "Lp%=:\n\ts_lshl_b32 s56, %[rl], 7\n\ts_and_b32 s56, s56, 0x7f80\n\ts_cbranch_scc0 Lv%=\n\ts_sub_u32 s56, s56, 128\n\t"
                 "v_add_u32 v8, s56, v7\n\tv_mov_b32 v9, 0\n\tv_lshl_add_u64 v[12:13], v[8:9], 0, s[20:21]\n\ts_mov_b64 exec, s[58:59]\n\tv_lshl_add_u64 v[12:13], v[8:9], 0, s[22:23]\n\ts_mov_b64 exec, -1\n\t"
                 EARLY_LOADS "s_branch Lw%=\n"
                 "Lx%=:\n\ts_waitcnt lgkmcnt(0)\n\ts_and_b32 s56, %[rl], 255\n\ts_cbranch_scc1 Lv%=\n\ts_barrier\n\ts_waitcnt vmcnt(0)\n\ts_endpgm\n"
                 "Lv%=:\n\ts_barrier\n"
                 "Lw%=:\n\ts_waitcnt lgkmcnt(0)"
                 : "=&{s[36:51]}"(ka0), "=&{s[16:31]}"(ka1), [w] "=&s"(wave), [it] "=&s"(item), [rl] "=&s"(role), [lb] "=&s"(labw), [ti] "=&s"(t64), [tid] "+{v0}"(tid)
                 : [b] "s"(b), [kap] "s"(__builtin_amdgcn_kernarg_segment_ptr()), [tab] "s"(&conv_tab), [rb] "s"(D_ * 2), [c0c] "s"(0xc0),
                   [fl] "n"(ROW_FL * TR), [fu] "n"(ROW_FU * TR), [ce] "n"(CODE_KB * 1024 - 128), [wl] "n"(LD_KA)
                 : "memory", "scc", "s52", "s53", "s54", "s55", "s56", "s57", "s58", "s59", "v1", "v2", "v3", "v4", "v5", "v6", "v7", "v8", "v9", "v10", "v11", "v12", "v13", ENTRY_CLOB);
    Params P;
#define KARG(T, v, i) (T)(((u64)v[2 * (i) + 1] << 32) | v[2 * (i)])
    P.x = KARG(const u16*, ka0, 0); P.wg = KARG(const u16*, ka1, 2); P.wu = KARG(const u16*, ka1, 3); P.wd = KARG(const u16*, ka1, 4);
    P.out = KARG(u16*, ka1, 6); P.ws = KARG(unsigned char*, ka1, 7);
    const int xcd = b & 7, ii = b >> 3, nconv = xcd < 2 ? 24 : 16;
    if (ii < nconv) {
        if (wave >= 8) { conv_wave<2>(P, sm, wave - 8, xcd, ii, labw); EXIT(); }
        if (wave >= 4) { conv_wave<1>(P, sm, wave - 4, xcd, ii, labw); EXIT(); }
        conv_wave<0>(P, sm, wave, xcd, ii, labw);
        EXIT();
    }
    const bool moe = (role & 255u) != 0u;
    if (wave == 1 || wave >= 8) {
        if (item >= 0) {
            asm volatile("; claim\n\ts_setprio 3\n" ITEM_ASM "\ts_setprio 0" : : [ws] "s"(P.ws), [it] "s"(item), [fl] "s"(FL_IT + 4 * (wave == 1 ? 0 : wave - 7)), [kb] "n"(WS_KEYS), [cb] "n"(WS_CAND), [wl] "n"(LD_KA)
                         : "memory", "vcc", "scc", "s88", "s89", "s92", "s93", "s98", ITEM_CLOB);
        }
        if (moe) moe_down(P, sm, wave == 1 ? 0 : wave - 7, (int)(role & 255u) - 1);
        EXIT();
    }
    if (moe && wave >= 2) { moe_weights(P, sm, wave - 2, (int)(role & 255u) - 1, role >> 16); EXIT(); }
    u32 tag0 = 0;
    if (!moe && wave >= 8 - HWAVES) {
        const int lane = fresh_lane();
        const Lab lb = lab_of(labw);
        if (b == RB) EXIT();
        const u32 nh = (u32)(32 - nconv) - lb.n - (xcd == (RB & 7) ? 1u : 0u), nit = role >> 16;
        if (nit && 7 - wave >= HT0) wait_ge<1>(sm + FL_IT + 4 * umin(lane, nit - 1), 1);
        helper_prefetch(P, lane, (u32)(7 - wave) * nh + (u32)(ii - nconv) - lb.n, HWAVES * nh, lb);
        EXIT();
    }
    if (wave) EXIT();
    const u32 t_in = (u32)t64;
    {
        u32 epoch;
        asm volatile("s_waitcnt vmcnt(0) ; epoch" : "={v136}"(epoch)::"memory");
        tag0 = tag_of(rfl(epoch));
    }
    const int lane = fresh_lane();
    st2_sc1(ws_rsrc(P), lane == 0 ? WS_ACK + (u32)b * 128u : (b == TB && lane >= 8 && lane < 16 ? WS_T0 + (u32)(lane - 8) * 128u : OOB), (v2u){tag0, t_in});
    if (!moe && b != RB) EXIT();
    if (moe) {
        flag1(sm + LD_TAG, tag0);
        select8(P, lane, xcd, tag0, t_in);
        flag1(sm + LD_TAG + 4, 1);
        VMWAIT(0);
        flag1(sm + LD_TAG + 4, 2);
        EXIT();
    }
    {
        const __amdgpu_buffer_rsrc_t rws = ws_rsrc(P);
#pragma unroll 1
        for (int w = 0; w < 4096 && (int)((u32)__builtin_amdgcn_s_memrealtime() - t_in - SEL_AT) < 0; ++w) __builtin_amdgcn_s_sleep(1);
        const v2u a0 = ld2_sc1(rws, (lane & 6) ? WS_ACK + (u32)(128 + lane) * 128u : OOB), a1 = ld2_sc1(rws, WS_ACK + (u32)(192 + lane) * 128u);
#pragma unroll 1
        for (int spin = 0; spin < SPIN_CAP; ++spin) {
            MEMFENCE();
            if (__all((ld2_sc1(rws, WS_CAND + (u32)(xcd * 16384 + lane * 128))[1] == tag0) & (ld2_sc1(rws, WS_CAND + 8192u + (u32)(xcd * 16384 + lane * 128))[1] == tag0))) break;
            __builtin_amdgcn_s_sleep(SEL_GAP);
        }
        const u32 t_sel = (u32)__builtin_amdgcn_s_memrealtime() - t_in;
        const u32 late = wave_max(umax(a0[0] == tag0 ? umin(a0[1] - t_in + 64u, 255u) : 0u, a1[0] == tag0 ? umin(a1[1] - t_in + 64u, 255u) : 0u));
        st2_sc1(rws, lane < 8 ? WS_T1 + (u32)lane * 128u : OOB, (v2u){tag0, t_in + umax(PART_AT1 + (late * PART_K >> 3), t_sel + PART_SEL)});
        __builtin_amdgcn_s_sleep(BUMP_DELAY);
#pragma unroll 1
        for (int spin = 0; spin < SPIN_CAP; ++spin) {
            MEMFENCE();
            u32 bad = 0;
#pragma unroll
            for (int k = 0; k < 4; ++k) bad |= ld2_sc1(rws, WS_ACK + (u32)(k * 64 + lane) * 128u)[0] ^ tag0;
            if (__all(bad == 0u)) break;
            __builtin_amdgcn_s_sleep(10);
        }
        if (lane == 0) __hip_atomic_store((GAS u32*)(P.ws + WS_EPOCH), tag0, RLX_AGENT);
    }
    VMWAIT(0);
}

extern "C" void kernel_launch(void* const* d_in, const int* in_sizes, int n_in, void* d_out, int out_size,
                              void* d_ws, size_t ws_size, hipStream_t stream) {
    Args A = mk_roles();
    Params& P = A.p;
    P.x = (const u16*)d_in[0];
    P.Are = (const u16*)d_in[1]; P.Aim = (const u16*)d_in[2];
    P.Bre = (const u16*)d_in[3]; P.Bim = (const u16*)d_in[4];
    P.Cre = (const u16*)d_in[5]; P.Cim = (const u16*)d_in[6];
    P.logdt = (const u16*)d_in[7]; P.dskip = (const u16*)d_in[8];
    P.wr = (const u16*)d_in[10]; P.wg = (const u16*)d_in[11]; P.wu = (const u16*)d_in[12]; P.wd = (const u16*)d_in[13];
    P.gw = (const u16*)d_in[14];
    P.out = (u16*)d_out;
    P.ws = (unsigned char*)d_ws;
    fused_kernel<<<256, NTHR, 0, stream>>>(A);
}
```
